```python
import torch

def _softmax(x, axis=-1):
    return torch.softmax(x, dim=axis)
def _gelu(x):
    return torch.nn.functional.gelu(x, approximate="tanh")
def _silu(x):
    return torch.nn.functional.silu(x)
def _sigmoid(x):
    return torch.sigmoid(x)
def _layernorm(x, axis=-1, eps=1e-5):
    m = x.mean(dim=axis, keepdim=True)
    v = x.var(dim=axis, keepdim=True, unbiased=False)
    return (x - m) / torch.sqrt(v + eps)
def _scatter_add(tgt, idx, src, axis=0):
    return tgt.clone().index_add_(axis, idx, src)
def _segment_sum(src, seg, n):
    out = torch.zeros((n,) + src.shape[1:], dtype=src.dtype)
    return out.index_add_(0, seg, src)
def _pad(x, pw):
    flat = tuple(v for pair in reversed(pw) for v in pair)
    return torch.nn.functional.pad(x, flat)

kernel_name = 'recurrent_scan__sliding_block_cached__geglu_ffn__prompt'

def setup_inputs():

    _w = (lambda a: (lambda t: t.to(torch.bfloat16) if t.is_floating_point() else t)(torch.as_tensor(a)))
    def _A_setup():
        B, Sq, D = 1, 2048, 512
        Hq, Hkv, Sk, Dh = 32, 8, 2048, 128
        assert Hq % Hkv == 0, (Hq, Hkv)
        return {
            "x": _w((lambda *s: torch.randn(*s, dtype=torch.bfloat16) * 0.05)(B, Sq, D)),
            "k_cache": _w((lambda *s: torch.randn(*s, dtype=torch.bfloat16) * 0.8)(B, Hkv, Sk, Dh)),
            "v_cache": _w((lambda *s: torch.randn(*s, dtype=torch.bfloat16) * 0.2)(B, Hkv, Sk, Dh)),
            "wq": _w((lambda *s: torch.randn(*s, dtype=torch.bfloat16) * 0.8)(D, Hq * Dh) * (D ** -0.5)),
            "wo": _w((lambda *s: torch.randn(*s, dtype=torch.bfloat16) * 0.2)(Hq * Dh, D) * ((Hq * Dh) ** -0.5)),
        }
    _A_in = _A_setup()

    def _B_setup():
        B, S, D, F = 1, 2048, 512, 1792
        return {
            "x": _w((lambda *s: torch.randn(*s, dtype=torch.bfloat16) * 0.05)(B, S, D)),
            "w_gate": _w((lambda *s: torch.randn(*s, dtype=torch.bfloat16) * 0.1)(D, F)),
            "w_up": _w((lambda *s: torch.randn(*s, dtype=torch.bfloat16) * 0.1)(D, F)),
            "w_down": _w((lambda *s: torch.randn(*s, dtype=torch.bfloat16) * 0.1)(F, D)),
        }
    _B_in = _B_setup()

    return {
        "A_x": _A_in["x"],
        "A_k_cache": _A_in["k_cache"],
        "A_v_cache": _A_in["v_cache"],
        "A_wq": _A_in["wq"],
        "A_wo": _A_in["wo"],
        "B_x": _B_in["x"],
        "B_w_gate": _B_in["w_gate"],
        "B_w_up": _B_in["w_up"],
        "B_w_down": _B_in["w_down"],
    }

def reference(A_x, A_k_cache, A_v_cache, A_wq, A_wo, B_x, B_w_gate, B_w_up, B_w_down):
    def _A_ref(x, k_cache, v_cache, wq, wo):
        B, Sq, D = x.shape
        Hq, Dh = 32, 128
        Hkv, Sk = k_cache.shape[1], k_cache.shape[2]
        W = min(4096, max(256, Sk // 4))
        rep = Hq // Hkv
        q = (lambda x, *d: x.permute(*d))((x @ wq).reshape(B, Sq, Hq, Dh), 0, 2, 1, 3)
        k = (lambda x, n, axis: torch.repeat_interleave(x, n, dim=axis))(k_cache[:, :, Sk - W:, :], rep, axis=1)
        v = (lambda x, n, axis: torch.repeat_interleave(x, n, dim=axis))(v_cache[:, :, Sk - W:, :], rep, axis=1)
        scores = torch.einsum("bhqd,bhkd->bhqk", q, k) * (Dh ** -0.5)
        p = _softmax(scores, axis=-1)
        o = torch.einsum("bhqk,bhkd->bhqd", p, v)
        return (lambda x, *d: x.permute(*d))(o, 0, 2, 1, 3).reshape(B, Sq, Hq * Dh) @ wo

    def _B_ref(x, w_gate, w_up, w_down):
        return (_gelu(x @ w_gate) * (x @ w_up)) @ w_down

    x = A_x
    xn = _layernorm(x, axis=-1)
    s = _A_ref(xn, A_k_cache, A_v_cache, A_wq, A_wo)
    g = _silu(_B_ref(xn, B_w_gate, B_w_up, B_w_down))
    return x + s * g
```

```cpp
#include <hip/hip_runtime.h>
#include <hip/hip_bf16.h>
typedef __hip_bfloat16 bf16;
__device__ __forceinline__ float b2f(bf16 v){ return __bfloat162float(v);}
constexpr int S=2048,D=512,HQ=32,HKV=8,SK=2048,DH=128,W=512,F=1792;

__global__ void ln_k(const bf16* x, float* xn){
  int r=blockIdx.x; __shared__ float red[256];
  int t=threadIdx.x; float a=b2f(x[r*D+t]), b=b2f(x[r*D+t+256]);
  red[t]=a+b; __syncthreads(); for(int s=128;s>0;s>>=1){ if(t<s) red[t]+=red[t+s]; __syncthreads();}
  float m=red[0]/D; __syncthreads();
  red[t]=(a-m)*(a-m)+(b-m)*(b-m); __syncthreads(); for(int s=128;s>0;s>>=1){ if(t<s) red[t]+=red[t+s]; __syncthreads();}
  float v=red[0]/D; float rs=1.f/sqrtf(v+1e-5f);
  xn[r*D+t]=(a-m)*rs; xn[r*D+t+256]=(b-m)*rs;
}
__global__ void gemm_k(const float* A, const bf16* B, float* C, int M,int N,int K){
  __shared__ float As[16][17], Bs[16][17];
  int tx=threadIdx.x, ty=threadIdx.y; int row=blockIdx.y*16+ty, col=blockIdx.x*16+tx; float acc=0;
  for(int k0=0;k0<K;k0+=16){ As[ty][tx]=A[(long)row*K+k0+tx]; Bs[ty][tx]=b2f(B[(long)(k0+ty)*N+col]); __syncthreads();
    for(int k=0;k<16;k++) acc+=As[ty][k]*Bs[k][tx]; __syncthreads(); }
  C[(long)row*N+col]=acc;
}
__global__ void attn_k(const float* q, const bf16* kc, const bf16* vc, float* o){
  int r=blockIdx.x, h=blockIdx.y; int g=h/4; int t=threadIdx.x;
  __shared__ float qs[DH]; __shared__ float p[W]; __shared__ float red[256];
  if(t<DH) qs[t]=q[(long)r*(HQ*DH)+h*DH+t]; __syncthreads();
  float mx=-1e30f;
  for(int j=t;j<W;j+=256){ const bf16* kr=kc+((long)g*SK+(SK-W)+j)*DH; float a=0; for(int d=0;d<DH;d++) a+=qs[d]*b2f(kr[d]); a*=0.08838834764831845f; p[j]=a; mx=fmaxf(mx,a);}
  red[t]=mx; __syncthreads(); for(int s=128;s>0;s>>=1){ if(t<s) red[t]=fmaxf(red[t],red[t+s]); __syncthreads();}
  mx=red[0]; __syncthreads(); float sm=0;
  for(int j=t;j<W;j+=256){ float e=expf(p[j]-mx); p[j]=e; sm+=e;} red[t]=sm; __syncthreads(); for(int s=128;s>0;s>>=1){ if(t<s) red[t]+=red[t+s]; __syncthreads();}
  sm=red[0];
  if(t<DH){ float a=0; for(int j=0;j<W;j++) a+=p[j]*b2f(vc[((long)g*SK+(SK-W)+j)*DH+t]); o[(long)r*(HQ*DH)+h*DH+t]=a/sm; }
}
__global__ void geglu_k(const float* g, const float* u, float* h, int n){ int i=blockIdx.x*256+threadIdx.x; if(i<n){ float x=g[i]; float y=0.5f*x*(1.f+tanhf(0.7978845608028654f*(x+0.044715f*x*x*x))); h[i]=y*u[i]; } }
__global__ void fin_k(const bf16* x, const float* s, const float* g, bf16* out, int n){ int i=blockIdx.x*256+threadIdx.x; if(i<n){ float gg=g[i]; float si=gg/(1.f+expf(-gg)); out[i]=__float2bfloat16(b2f(x[i])+s[i]*si);} }

extern "C" void kernel_launch(void* const* d_in, const int* in_sizes, int n_in, void* d_out, int out_size, void* d_ws, size_t ws_size, hipStream_t stream){
  const bf16* x=(const bf16*)d_in[0]; const bf16* kc=(const bf16*)d_in[1]; const bf16* vc=(const bf16*)d_in[2]; const bf16* wq=(const bf16*)d_in[3]; const bf16* wo=(const bf16*)d_in[4];
  const bf16* wg=(const bf16*)d_in[6]; const bf16* wu=(const bf16*)d_in[7]; const bf16* wd=(const bf16*)d_in[8];
  float* ws=(float*)d_ws; float* xn=ws; float* q=xn+S*D; float* o=q+(long)S*4096; float* s=o+(long)S*4096; float* gg=s+S*D; float* uu=gg+(long)S*F; float* hh=uu+(long)S*F; float* g2=hh+(long)S*F;
  ln_k<<<S,256,0,stream>>>(x,xn);
  gemm_k<<<dim3(4096/16,S/16),dim3(16,16),0,stream>>>(xn,wq,q,S,4096,D);
  attn_k<<<dim3(S,HQ),256,0,stream>>>(q,kc,vc,o);
  gemm_k<<<dim3(D/16,S/16),dim3(16,16),0,stream>>>(o,wo,s,S,D,4096);
  gemm_k<<<dim3(F/16,S/16),dim3(16,16),0,stream>>>(xn,wg,gg,S,F,D);
  gemm_k<<<dim3(F/16,S/16),dim3(16,16),0,stream>>>(xn,wu,uu,S,F,D);
  geglu_k<<<(S*F+255)/256,256,0,stream>>>(gg,uu,hh,S*F);
  gemm_k<<<dim3(D/16,S/16),dim3(16,16),0,stream>>>(hh,wd,g2,S,D,F);
  fin_k<<<(S*D+255)/256,256,0,stream>>>(x,s,g2,(bf16*)d_out,S*D);
}
```

```cpp
#include <hip/hip_runtime.h>
#include <hip/hip_bf16.h>
#define GRID 256
__global__ __launch_bounds__(256,1) void fused_main(const void* x, const void* k, const void* v, const void* wq, const void* wo, const void* wg, const void* wu, const void* wd, void* ws, void* out){
  auto ka=__builtin_amdgcn_kernarg_segment_ptr();
  unsigned blk=blockIdx.x; unsigned tid=threadIdx.x;
  asm volatile(
"s_load_dwordx16 s[64:79], %0, 0x0\n"
"s_load_dwordx4 s[80:83], %0, 0x40\n"
"s_mov_b32 s58, %1\n"
"v_and_b32 v1, 63, %2\n"
"v_lshrrev_b32 v2, 6, %2\n"
"s_nop 1\n"
"v_readfirstlane_b32 s42, v2\n"
"s_add_u32 s36, s58, 176\n"
"s_sub_u32 s37, s58, 80\n"
"s_cmp_lt_u32 s58, 80\n"
"s_cselect_b32 s58, s36, s37\n"
"s_getpc_b64 s[84:85]\n"
"LPC_%=:\n"
"s_mov_b32 s86, LFPROG_%=-LPC_%=\n"
"s_mov_b32 s87, LEND_%=-LFPROG_%=\n"
"s_cmp_ge_u32 s58, 176\n"
"s_cselect_b32 s36, s86, 0\n"
"s_cselect_b32 s86, s87, s86\n"
"s_add_u32 s84, s84, s36\n"
"s_addc_u32 s85, s85, 0\n"
"s_and_b32 s85, s85, 0xffff\n"
"s_mov_b32 s87, 0x20000\n"
"v_lshlrev_b32 v3, 6, v1\n"
"s_lshl_b32 s36, s42, 12\n"
"v_add_u32 v3, s36, v3\n"
"s_mov_b32 s36, 0\n"
"buffer_load_dword v63, v3, s[84:87], s36 offen\n"
"s_mov_b32 s36, 16384\n"
"buffer_load_dword v63, v3, s[84:87], s36 offen\n"
"s_mov_b32 s36, 32768\n"
"buffer_load_dword v63, v3, s[84:87], s36 offen\n"
"s_mov_b32 s36, 49152\n"
"buffer_load_dword v63, v3, s[84:87], s36 offen\n"
"s_waitcnt lgkmcnt(0)\n"
"s_nop 4\n"
"s_cmp_ge_u32 s58, 242\n"
"s_cbranch_scc1 LPPROG_%=\n"
"s_cmp_ge_u32 s58, 176\n"
"s_cbranch_scc1 LFPROG_%=\n"
"; ---- end of section: start\n"
"s_and_b32 s53, s58, 3\n"
"s_lshl_b32 s53, s53, 1\n"
"s_bfe_u32 s54, s58, 0x10003\n"
"s_add_u32 s43, s53, s54\n"
"s_bfe_u32 s53, s58, 0x10002\n"
"s_mul_i32 s53, s53, 11\n"
"s_lshr_b32 s54, s58, 4\n"
"s_add_u32 s44, s53, s54\n"
"s_mov_b32 s20, s64\n"
"s_mov_b32 s21, s65\n"
"s_and_b32 s21, s21, 0xffff\n"
"s_mov_b32 s22, 2097152\n"
"s_mov_b32 s23, 0x20000\n"
"v_lshrrev_b32 v2, 4, v1\n"
"v_and_b32 v3, 15, v1\n"
"v_lshlrev_b32 v2, 10, v2\n"
"v_lshl_add_u32 v9, v3, 4, v2\n"
"s_mul_i32 s53, s44, 98304\n"
"s_mul_i32 s54, s42, 24576\n"
"s_add_u32 s40, s53, s54\n"
"s_add_u32 s50, s40, 0\n"
"buffer_load_dwordx4 v[64:67], v9, s[20:23], s50 offen offset:0\n"
"buffer_load_dwordx4 v[68:71], v9, s[20:23], s50 offen offset:256\n"
"buffer_load_dwordx4 v[72:75], v9, s[20:23], s50 offen offset:512\n"
"buffer_load_dwordx4 v[76:79], v9, s[20:23], s50 offen offset:768\n"
"s_add_u32 s50, s40, 4096\n"
"buffer_load_dwordx4 v[80:83], v9, s[20:23], s50 offen offset:0\n"
"buffer_load_dwordx4 v[84:87], v9, s[20:23], s50 offen offset:256\n"
"buffer_load_dwordx4 v[88:91], v9, s[20:23], s50 offen offset:512\n"
"buffer_load_dwordx4 v[92:95], v9, s[20:23], s50 offen offset:768\n"
"s_mov_b32 s24, s70\n"
"s_mov_b32 s25, s71\n"
"s_sub_u32 s24, s24, 4096\n"
"s_subb_u32 s25, s25, 0\n"
"s_and_b32 s25, s25, 0xffff\n"
"s_mov_b32 s26, 4198400\n"
"s_mov_b32 s27, 0x20000\n"
"s_lshl_b32 s49, s42, 14\n"
"s_add_u32 s49, s49, 98304\n"
"s_lshl_b32 s54, s43, 2\n"
"s_add_u32 s54, s54, s42\n"
"s_lshl_b32 s55, s54, 8\n"
"v_lshrrev_b32 v2, 3, v1\n"
"v_and_b32 v3, 7, v1\n"
"v_bfe_u32 v4, v2, 1, 1\n"
"v_lshlrev_b32 v4, 2, v4\n"
"v_xor_b32 v3, v3, v4\n"
"v_lshlrev_b32 v3, 4, v3\n"
"v_lshlrev_b32 v2, 13, v2\n"
"v_add3_u32 v2, v2, v3, s55\n"
"v_add_u32 v22, 4096, v2\n"
"v_add_u32 v23, 68608, v2\n"
"v_add_u32 v24, 133120, v2\n"
"v_add_u32 v25, 197632, v2\n"
"v_add_u32 v26, 4224, v2\n"
"v_add_u32 v27, 68736, v2\n"
"v_add_u32 v28, 133248, v2\n"
"v_add_u32 v29, 197760, v2\n"
"s_mov_b32 s45, 0\n"
"s_mov_b32 s28, s72\n"
"s_mov_b32 s29, s73\n"
"s_sub_u32 s28, s28, 4096\n"
"s_subb_u32 s29, s29, 0\n"
"s_and_b32 s29, s29, 0xffff\n"
"s_mov_b32 s30, 4198400\n"
"s_mov_b32 s31, 0x20000\n"
"s_lshl_b32 s53, s43, 19\n"
"s_add_u32 s53, s53, 393216\n"
"s_mov_b32 s84, s66\n"
"s_mov_b32 s85, s67\n"
"s_add_u32 s50, s40, 8192\n"
"buffer_load_dwordx4 v[96:99], v9, s[20:23], s50 offen offset:0\n"
"buffer_load_dwordx4 v[100:103], v9, s[20:23], s50 offen offset:256\n"
"buffer_load_dwordx4 v[104:107], v9, s[20:23], s50 offen offset:512\n"
"buffer_load_dwordx4 v[108:111], v9, s[20:23], s50 offen offset:768\n"
"s_add_u32 s84, s84, s53\n"
"s_addc_u32 s85, s85, 0\n"
"s_and_b32 s85, s85, 0xffff\n"
"s_mov_b32 s86, 131072\n"
"s_mov_b32 s87, 0x20000\n"
"s_mov_b32 s88, s68\n"
"s_mov_b32 s89, s69\n"
"s_add_u32 s88, s88, s53\n"
"s_addc_u32 s89, s89, 0\n"
"s_sub_u32 s88, s88, 4096\n"
"s_subb_u32 s89, s89, 0\n"
"s_and_b32 s89, s89, 0xffff\n"
"s_mov_b32 s90, 135168\n"
"s_mov_b32 s91, 0x20000\n"
"s_mov_b32 s59, 0x3e0293ee\n"
"s_lshl_b32 s46, s43, 19\n"
"v_lshrrev_b32 v2, 3, v1\n"
"v_bfe_u32 v3, v1, 1, 2\n"
"v_bfe_u32 v4, v2, 1, 2\n"
"v_xor_b32 v3, v3, v4\n"
"v_lshlrev_b32 v3, 5, v3\n"
"v_and_b32 v4, 1, v1\n"
"v_lshl_add_u32 v3, v4, 4, v3\n"
"s_mov_b32 s53, 1024\n"
"v_mul_lo_u32 v2, v2, s53\n"
"v_add_u32 v2, v2, v3\n"
"s_lshl_b32 s54, s42, 8\n"
"v_add_u32 v2, s54, v2\n"
"v_add_u32 v30, 4096, v2\n"
"v_add_u32 v31, 11264, v2\n"
"v_add_u32 v32, 18432, v2\n"
"v_add_u32 v33, 25600, v2\n"
"v_add_u32 v34, 4224, v2\n"
"v_add_u32 v35, 11392, v2\n"
"v_add_u32 v36, 18560, v2\n"
"v_add_u32 v37, 25728, v2\n"
"v_lshrrev_b32 v2, 4, v1\n"
"v_bfe_u32 v3, v1, 2, 2\n"
"v_and_b32 v4, 3, v1\n"
"v_lshlrev_b32 v2, 10, v2\n"
"s_add_u32 s50, s40, 12288\n"
"buffer_load_dwordx4 v[112:115], v9, s[20:23], s50 offen offset:0\n"
"buffer_load_dwordx4 v[116:119], v9, s[20:23], s50 offen offset:256\n"
"buffer_load_dwordx4 v[120:123], v9, s[20:23], s50 offen offset:512\n"
"buffer_load_dwordx4 v[124:127], v9, s[20:23], s50 offen offset:768\n"
"v_lshl_add_u32 v2, v3, 7, v2\n"
"v_lshl_add_u32 v2, v4, 4, v2\n"
"v_add_u32 v2, s49, v2\n"
"v_lshrrev_b32 v3, 1, v3\n"
"v_lshl_add_u32 v10, v3, 6, v2\n"
"v_xor_b32 v3, 1, v3\n"
"v_lshl_add_u32 v11, v3, 6, v2\n"
"v_and_b32 v2, 15, v1\n"
"v_lshrrev_b32 v3, 4, v1\n"
"v_xor_b32 v3, v3, v2\n"
"v_lshlrev_b32 v3, 4, v3\n"
"v_lshl_add_u32 v3, v2, 10, v3\n"
"v_xor_b32 v14, 0, v3\n"
"v_add_u32 v18, 0x10000, v14\n"
"v_xor_b32 v15, 64, v3\n"
"v_add_u32 v19, 0x10000, v15\n"
"v_xor_b32 v16, 128, v3\n"
"v_add_u32 v20, 0x10000, v16\n"
"v_xor_b32 v17, 192, v3\n"
"v_add_u32 v21, 0x10000, v17\n"
"s_memrealtime s[34:35]\n"
"s_waitcnt lgkmcnt(0)\n"
"s_mul_i32 s53, s44, 11\n"
"s_mul_i32 s53, s53, 768\n"
"s_add_u32 s53, s53, 33554432\n"
"s_add_u32 s36, s80, s53\n"
"s_addc_u32 s37, s81, 0\n"
"s_lshl_b32 s53, s43, 2\n"
"s_add_u32 s53, s53, s42\n"
"s_lshl_b32 s53, s53, 3\n"
"v_mul_u32_u24 v56, 768, v1\n"
"v_add_u32 v56, s53, v56\n"
"v_mov_b32 v58, s34\n"
"v_mov_b32 v59, s35\n"
"s_mov_b64 exec, 0x7ff\n"
"global_store_dwordx2 v56, v[58:59], s[36:37] sc1\n"
"s_mov_b64 exec, -1\n"
"s_cmp_lg_u32 s42, 0\n"
"s_cbranch_scc1 LHEA_%=\n"
"s_mul_i32 s53, s44, 11\n"
"s_add_u32 s53, s53, s43\n"
"s_mul_i32 s53, s53, 768\n"
"s_add_u32 s53, s53, 33554432\n"
"s_add_u32 s36, s80, s53\n"
"s_addc_u32 s37, s81, 0\n"
"v_lshlrev_b32 v56, 3, v1\n"
"s_not_b32 s38, s34\n"
"s_not_b32 s39, s35\n"
"v_mov_b32 v58, s38\n"
"v_mov_b32 v59, s39\n"
"s_mov_b32 exec_hi, 0\n"
"global_store_dwordx2 v56, v[58:59], s[36:37] offset:512 sc1\n"
"s_mov_b64 exec, -1\n"
"LHEA_%=:\n"
"; ---- end of section: loads\n"
"v_mov_b32 v6, 0x3f803f80\n"
"v_lshrrev_b32 v7, 4, v1\n"
"s_mul_i32 s54, s42, 24\n"
"v_add_u32 v7, s54, v7\n"
"v_and_b32 v8, 15, v1\n"
"s_waitcnt vmcnt(8)\n"
"v_dot2_f32_bf16 v160, v64, v6, 0\n"
"v_dot2_f32_bf16 v161, v64, v64, 0\n"
"v_dot2_f32_bf16 v162, v80, v6, 0\n"
"v_dot2_f32_bf16 v163, v80, v80, 0\n"
"v_dot2_f32_bf16 v160, v65, v6, v160\n"
"v_dot2_f32_bf16 v161, v65, v65, v161\n"
"v_dot2_f32_bf16 v162, v81, v6, v162\n"
"v_dot2_f32_bf16 v163, v81, v81, v163\n"
"v_dot2_f32_bf16 v160, v66, v6, v160\n"
"v_dot2_f32_bf16 v161, v66, v66, v161\n"
"v_dot2_f32_bf16 v162, v82, v6, v162\n"
"v_dot2_f32_bf16 v163, v82, v82, v163\n"
"v_dot2_f32_bf16 v160, v67, v6, v160\n"
"v_dot2_f32_bf16 v161, v67, v67, v161\n"
"v_dot2_f32_bf16 v162, v83, v6, v162\n"
"v_dot2_f32_bf16 v163, v83, v83, v163\n"
"v_dot2_f32_bf16 v160, v68, v6, v160\n"
"v_dot2_f32_bf16 v161, v68, v68, v161\n"
"v_dot2_f32_bf16 v162, v84, v6, v162\n"
"v_dot2_f32_bf16 v163, v84, v84, v163\n"
"v_dot2_f32_bf16 v160, v69, v6, v160\n"
"v_dot2_f32_bf16 v161, v69, v69, v161\n"
"v_dot2_f32_bf16 v162, v85, v6, v162\n"
"v_dot2_f32_bf16 v163, v85, v85, v163\n"
"v_dot2_f32_bf16 v160, v70, v6, v160\n"
"s_add_u32 s50, s40, 16384\n"
"buffer_load_dwordx4 v[128:131], v9, s[20:23], s50 offen offset:0\n"
"v_dot2_f32_bf16 v161, v70, v70, v161\n"
"v_dot2_f32_bf16 v162, v86, v6, v162\n"
"v_dot2_f32_bf16 v163, v86, v86, v163\n"
"v_dot2_f32_bf16 v160, v71, v6, v160\n"
"v_dot2_f32_bf16 v161, v71, v71, v161\n"
"v_dot2_f32_bf16 v162, v87, v6, v162\n"
"v_dot2_f32_bf16 v163, v87, v87, v163\n"
"v_dot2_f32_bf16 v160, v72, v6, v160\n"
"v_dot2_f32_bf16 v161, v72, v72, v161\n"
"v_dot2_f32_bf16 v162, v88, v6, v162\n"
"v_dot2_f32_bf16 v163, v88, v88, v163\n"
"v_dot2_f32_bf16 v160, v73, v6, v160\n"
"v_dot2_f32_bf16 v161, v73, v73, v161\n"
"v_dot2_f32_bf16 v162, v89, v6, v162\n"
"v_dot2_f32_bf16 v163, v89, v89, v163\n"
"v_dot2_f32_bf16 v160, v74, v6, v160\n"
"v_dot2_f32_bf16 v161, v74, v74, v161\n"
"v_dot2_f32_bf16 v162, v90, v6, v162\n"
"v_dot2_f32_bf16 v163, v90, v90, v163\n"
"v_dot2_f32_bf16 v160, v75, v6, v160\n"
"v_dot2_f32_bf16 v161, v75, v75, v161\n"
"v_dot2_f32_bf16 v162, v91, v6, v162\n"
"v_dot2_f32_bf16 v163, v91, v91, v163\n"
"v_dot2_f32_bf16 v160, v76, v6, v160\n"
"v_dot2_f32_bf16 v161, v76, v76, v161\n"
"buffer_load_dwordx4 v[132:135], v9, s[20:23], s50 offen offset:256\n"
"v_dot2_f32_bf16 v162, v92, v6, v162\n"
"v_dot2_f32_bf16 v163, v92, v92, v163\n"
"v_dot2_f32_bf16 v160, v77, v6, v160\n"
"v_dot2_f32_bf16 v161, v77, v77, v161\n"
"v_dot2_f32_bf16 v162, v93, v6, v162\n"
"v_dot2_f32_bf16 v163, v93, v93, v163\n"
"v_dot2_f32_bf16 v160, v78, v6, v160\n"
"v_dot2_f32_bf16 v161, v78, v78, v161\n"
"v_dot2_f32_bf16 v162, v94, v6, v162\n"
"v_dot2_f32_bf16 v163, v94, v94, v163\n"
"v_dot2_f32_bf16 v160, v79, v6, v160\n"
"v_dot2_f32_bf16 v161, v79, v79, v161\n"
"v_dot2_f32_bf16 v162, v95, v6, v162\n"
"v_dot2_f32_bf16 v163, v95, v95, v163\n"
"v_add_u32 v5, 0, v7\n"
"v_and_b32 v168, 15, v5\n"
"v_xor_b32 v168, v168, v8\n"
"v_lshlrev_b32 v168, 4, v168\n"
"v_lshl_add_u32 v168, v5, 10, v168\n"
"v_add_u32 v5, 4, v7\n"
"v_and_b32 v169, 15, v5\n"
"v_xor_b32 v169, v169, v8\n"
"v_lshlrev_b32 v169, 4, v169\n"
"v_lshl_add_u32 v169, v5, 10, v169\n"
"v_add_f32_dpp v160, v160, v160 row_ror:8 row_mask:0xf bank_mask:0xf\n"
"buffer_load_dwordx4 v[136:139], v9, s[20:23], s50 offen offset:512\n"
"v_add_f32_dpp v161, v161, v161 row_ror:8 row_mask:0xf bank_mask:0xf\n"
"v_add_f32_dpp v162, v162, v162 row_ror:8 row_mask:0xf bank_mask:0xf\n"
"v_add_f32_dpp v163, v163, v163 row_ror:8 row_mask:0xf bank_mask:0xf\n"
"v_add_f32_dpp v160, v160, v160 row_ror:4 row_mask:0xf bank_mask:0xf\n"
"v_add_f32_dpp v161, v161, v161 row_ror:4 row_mask:0xf bank_mask:0xf\n"
"v_add_f32_dpp v162, v162, v162 row_ror:4 row_mask:0xf bank_mask:0xf\n"
"v_add_f32_dpp v163, v163, v163 row_ror:4 row_mask:0xf bank_mask:0xf\n"
"v_add_f32_dpp v160, v160, v160 row_ror:2 row_mask:0xf bank_mask:0xf\n"
"v_add_f32_dpp v161, v161, v161 row_ror:2 row_mask:0xf bank_mask:0xf\n"
"v_add_f32_dpp v162, v162, v162 row_ror:2 row_mask:0xf bank_mask:0xf\n"
"v_add_f32_dpp v163, v163, v163 row_ror:2 row_mask:0xf bank_mask:0xf\n"
"v_add_f32_dpp v160, v160, v160 row_ror:1 row_mask:0xf bank_mask:0xf\n"
"v_add_f32_dpp v161, v161, v161 row_ror:1 row_mask:0xf bank_mask:0xf\n"
"v_add_f32_dpp v162, v162, v162 row_ror:1 row_mask:0xf bank_mask:0xf\n"
"v_add_f32_dpp v163, v163, v163 row_ror:1 row_mask:0xf bank_mask:0xf\n"
"v_mul_f32 v160, 0x3b000000, v160\n"
"v_mul_f32 v161, 0x3b000000, v161\n"
"v_mul_f32 v162, 0x3b000000, v162\n"
"v_mul_f32 v163, 0x3b000000, v163\n"
"v_fma_f32 v161, -v160, v160, v161\n"
"v_fma_f32 v163, -v162, v162, v163\n"
"v_max_f32 v161, 0, v161\n"
"v_max_f32 v163, 0, v163\n"
"v_add_f32 v161, 0x3727c5ac, v161\n"
"v_add_f32 v163, 0x3727c5ac, v163\n"
"buffer_load_dwordx4 v[140:143], v9, s[20:23], s50 offen offset:768\n"
"v_rsq_f32 v164, v161\n"
"v_rsq_f32 v166, v163\n"
"s_nop 0\n"
"v_mul_f32 v164, s59, v164\n"
"v_mul_f32 v166, s59, v166\n"
"v_mul_f32 v165, -v160, v164\n"
"v_mul_f32 v167, -v162, v166\n"
"v_lshlrev_b32 v172, 16, v64\n"
"v_and_b32 v173, 0xffff0000, v64\n"
"v_pk_fma_f32 v[172:173], v[172:173], v[164:165], v[164:165] op_sel:[0,0,1] op_sel_hi:[1,0,1]\n"
"v_cvt_pk_bf16_f32 v64, v172, v173\n"
"v_lshlrev_b32 v174, 16, v65\n"
"v_and_b32 v175, 0xffff0000, v65\n"
"v_pk_fma_f32 v[174:175], v[174:175], v[164:165], v[164:165] op_sel:[0,0,1] op_sel_hi:[1,0,1]\n"
"v_cvt_pk_bf16_f32 v65, v174, v175\n"
"v_lshlrev_b32 v176, 16, v66\n"
"v_and_b32 v177, 0xffff0000, v66\n"
"v_pk_fma_f32 v[176:177], v[176:177], v[164:165], v[164:165] op_sel:[0,0,1] op_sel_hi:[1,0,1]\n"
"v_cvt_pk_bf16_f32 v66, v176, v177\n"
"v_lshlrev_b32 v178, 16, v67\n"
"v_and_b32 v179, 0xffff0000, v67\n"
"v_pk_fma_f32 v[178:179], v[178:179], v[164:165], v[164:165] op_sel:[0,0,1] op_sel_hi:[1,0,1]\n"
"v_cvt_pk_bf16_f32 v67, v178, v179\n"
"ds_write_b128 v168, v[64:67] offset:0\n"
"v_lshlrev_b32 v172, 16, v68\n"
"s_add_u32 s50, s40, 20480\n"
"buffer_load_dwordx4 v[144:147], v9, s[20:23], s50 offen offset:0\n"
"v_and_b32 v173, 0xffff0000, v68\n"
"v_pk_fma_f32 v[172:173], v[172:173], v[164:165], v[164:165] op_sel:[0,0,1] op_sel_hi:[1,0,1]\n"
"v_cvt_pk_bf16_f32 v68, v172, v173\n"
"v_lshlrev_b32 v174, 16, v69\n"
"v_and_b32 v175, 0xffff0000, v69\n"
"v_pk_fma_f32 v[174:175], v[174:175], v[164:165], v[164:165] op_sel:[0,0,1] op_sel_hi:[1,0,1]\n"
"v_cvt_pk_bf16_f32 v69, v174, v175\n"
"v_lshlrev_b32 v176, 16, v70\n"
"v_and_b32 v177, 0xffff0000, v70\n"
"v_pk_fma_f32 v[176:177], v[176:177], v[164:165], v[164:165] op_sel:[0,0,1] op_sel_hi:[1,0,1]\n"
"v_cvt_pk_bf16_f32 v70, v176, v177\n"
"v_lshlrev_b32 v178, 16, v71\n"
"v_and_b32 v179, 0xffff0000, v71\n"
"v_pk_fma_f32 v[178:179], v[178:179], v[164:165], v[164:165] op_sel:[0,0,1] op_sel_hi:[1,0,1]\n"
"v_cvt_pk_bf16_f32 v71, v178, v179\n"
"ds_write_b128 v168, v[68:71] offset:256\n"
"v_lshlrev_b32 v172, 16, v72\n"
"v_and_b32 v173, 0xffff0000, v72\n"
"v_pk_fma_f32 v[172:173], v[172:173], v[164:165], v[164:165] op_sel:[0,0,1] op_sel_hi:[1,0,1]\n"
"v_cvt_pk_bf16_f32 v72, v172, v173\n"
"v_lshlrev_b32 v174, 16, v73\n"
"v_and_b32 v175, 0xffff0000, v73\n"
"v_pk_fma_f32 v[174:175], v[174:175], v[164:165], v[164:165] op_sel:[0,0,1] op_sel_hi:[1,0,1]\n"
"v_cvt_pk_bf16_f32 v73, v174, v175\n"
"v_lshlrev_b32 v176, 16, v74\n"
"buffer_load_dwordx4 v[148:151], v9, s[20:23], s50 offen offset:256\n"
"v_and_b32 v177, 0xffff0000, v74\n"
"v_pk_fma_f32 v[176:177], v[176:177], v[164:165], v[164:165] op_sel:[0,0,1] op_sel_hi:[1,0,1]\n"
"v_cvt_pk_bf16_f32 v74, v176, v177\n"
"v_lshlrev_b32 v178, 16, v75\n"
"v_and_b32 v179, 0xffff0000, v75\n"
"v_pk_fma_f32 v[178:179], v[178:179], v[164:165], v[164:165] op_sel:[0,0,1] op_sel_hi:[1,0,1]\n"
"v_cvt_pk_bf16_f32 v75, v178, v179\n"
"ds_write_b128 v168, v[72:75] offset:512\n"
"v_lshlrev_b32 v172, 16, v76\n"
"v_and_b32 v173, 0xffff0000, v76\n"
"v_pk_fma_f32 v[172:173], v[172:173], v[164:165], v[164:165] op_sel:[0,0,1] op_sel_hi:[1,0,1]\n"
"v_cvt_pk_bf16_f32 v76, v172, v173\n"
"v_lshlrev_b32 v174, 16, v77\n"
"v_and_b32 v175, 0xffff0000, v77\n"
"v_pk_fma_f32 v[174:175], v[174:175], v[164:165], v[164:165] op_sel:[0,0,1] op_sel_hi:[1,0,1]\n"
"v_cvt_pk_bf16_f32 v77, v174, v175\n"
"v_lshlrev_b32 v176, 16, v78\n"
"v_and_b32 v177, 0xffff0000, v78\n"
"v_pk_fma_f32 v[176:177], v[176:177], v[164:165], v[164:165] op_sel:[0,0,1] op_sel_hi:[1,0,1]\n"
"v_cvt_pk_bf16_f32 v78, v176, v177\n"
"v_lshlrev_b32 v178, 16, v79\n"
"v_and_b32 v179, 0xffff0000, v79\n"
"v_pk_fma_f32 v[178:179], v[178:179], v[164:165], v[164:165] op_sel:[0,0,1] op_sel_hi:[1,0,1]\n"
"v_cvt_pk_bf16_f32 v79, v178, v179\n"
"ds_write_b128 v168, v[76:79] offset:768\n"
"buffer_load_dwordx4 v[152:155], v9, s[20:23], s50 offen offset:512\n"
"v_lshlrev_b32 v172, 16, v80\n"
"v_and_b32 v173, 0xffff0000, v80\n"
"v_pk_fma_f32 v[172:173], v[172:173], v[166:167], v[166:167] op_sel:[0,0,1] op_sel_hi:[1,0,1]\n"
"v_cvt_pk_bf16_f32 v80, v172, v173\n"
"v_lshlrev_b32 v174, 16, v81\n"
"v_and_b32 v175, 0xffff0000, v81\n"
"v_pk_fma_f32 v[174:175], v[174:175], v[166:167], v[166:167] op_sel:[0,0,1] op_sel_hi:[1,0,1]\n"
"v_cvt_pk_bf16_f32 v81, v174, v175\n"
"v_lshlrev_b32 v176, 16, v82\n"
"v_and_b32 v177, 0xffff0000, v82\n"
"v_pk_fma_f32 v[176:177], v[176:177], v[166:167], v[166:167] op_sel:[0,0,1] op_sel_hi:[1,0,1]\n"
"v_cvt_pk_bf16_f32 v82, v176, v177\n"
"v_lshlrev_b32 v178, 16, v83\n"
"v_and_b32 v179, 0xffff0000, v83\n"
"v_pk_fma_f32 v[178:179], v[178:179], v[166:167], v[166:167] op_sel:[0,0,1] op_sel_hi:[1,0,1]\n"
"v_cvt_pk_bf16_f32 v83, v178, v179\n"
"ds_write_b128 v169, v[80:83] offset:0\n"
"v_lshlrev_b32 v172, 16, v84\n"
"v_and_b32 v173, 0xffff0000, v84\n"
"v_pk_fma_f32 v[172:173], v[172:173], v[166:167], v[166:167] op_sel:[0,0,1] op_sel_hi:[1,0,1]\n"
"v_cvt_pk_bf16_f32 v84, v172, v173\n"
"v_lshlrev_b32 v174, 16, v85\n"
"v_and_b32 v175, 0xffff0000, v85\n"
"v_pk_fma_f32 v[174:175], v[174:175], v[166:167], v[166:167] op_sel:[0,0,1] op_sel_hi:[1,0,1]\n"
"v_cvt_pk_bf16_f32 v85, v174, v175\n"
"buffer_load_dwordx4 v[156:159], v9, s[20:23], s50 offen offset:768\n"
"v_lshlrev_b32 v176, 16, v86\n"
"v_and_b32 v177, 0xffff0000, v86\n"
"v_pk_fma_f32 v[176:177], v[176:177], v[166:167], v[166:167] op_sel:[0,0,1] op_sel_hi:[1,0,1]\n"
"v_cvt_pk_bf16_f32 v86, v176, v177\n"
"v_lshlrev_b32 v178, 16, v87\n"
"v_and_b32 v179, 0xffff0000, v87\n"
"v_pk_fma_f32 v[178:179], v[178:179], v[166:167], v[166:167] op_sel:[0,0,1] op_sel_hi:[1,0,1]\n"
"v_cvt_pk_bf16_f32 v87, v178, v179\n"
"ds_write_b128 v169, v[84:87] offset:256\n"
"v_lshlrev_b32 v172, 16, v88\n"
"v_and_b32 v173, 0xffff0000, v88\n"
"v_pk_fma_f32 v[172:173], v[172:173], v[166:167], v[166:167] op_sel:[0,0,1] op_sel_hi:[1,0,1]\n"
"v_cvt_pk_bf16_f32 v88, v172, v173\n"
"v_lshlrev_b32 v174, 16, v89\n"
"v_and_b32 v175, 0xffff0000, v89\n"
"v_pk_fma_f32 v[174:175], v[174:175], v[166:167], v[166:167] op_sel:[0,0,1] op_sel_hi:[1,0,1]\n"
"v_cvt_pk_bf16_f32 v89, v174, v175\n"
"v_lshlrev_b32 v176, 16, v90\n"
"v_and_b32 v177, 0xffff0000, v90\n"
"v_pk_fma_f32 v[176:177], v[176:177], v[166:167], v[166:167] op_sel:[0,0,1] op_sel_hi:[1,0,1]\n"
"v_cvt_pk_bf16_f32 v90, v176, v177\n"
"v_lshlrev_b32 v178, 16, v91\n"
"v_and_b32 v179, 0xffff0000, v91\n"
"v_pk_fma_f32 v[178:179], v[178:179], v[166:167], v[166:167] op_sel:[0,0,1] op_sel_hi:[1,0,1]\n"
"v_cvt_pk_bf16_f32 v91, v178, v179\n"
"ds_write_b128 v169, v[88:91] offset:512\n"
"v_lshlrev_b32 v172, 16, v92\n"
"v_and_b32 v173, 0xffff0000, v92\n"
"v_pk_fma_f32 v[172:173], v[172:173], v[166:167], v[166:167] op_sel:[0,0,1] op_sel_hi:[1,0,1]\n"
"v_cvt_pk_bf16_f32 v92, v172, v173\n"
"v_lshlrev_b32 v174, 16, v93\n"
"v_and_b32 v175, 0xffff0000, v93\n"
"v_pk_fma_f32 v[174:175], v[174:175], v[166:167], v[166:167] op_sel:[0,0,1] op_sel_hi:[1,0,1]\n"
"v_cvt_pk_bf16_f32 v93, v174, v175\n"
"v_lshlrev_b32 v176, 16, v94\n"
"v_and_b32 v177, 0xffff0000, v94\n"
"v_pk_fma_f32 v[176:177], v[176:177], v[166:167], v[166:167] op_sel:[0,0,1] op_sel_hi:[1,0,1]\n"
"v_cvt_pk_bf16_f32 v94, v176, v177\n"
"v_lshlrev_b32 v178, 16, v95\n"
"v_and_b32 v179, 0xffff0000, v95\n"
"v_pk_fma_f32 v[178:179], v[178:179], v[166:167], v[166:167] op_sel:[0,0,1] op_sel_hi:[1,0,1]\n"
"v_cvt_pk_bf16_f32 v95, v178, v179\n"
"ds_write_b128 v169, v[92:95] offset:768\n"
"s_waitcnt vmcnt(8)\n"
"v_dot2_f32_bf16 v160, v96, v6, 0\n"
"v_dot2_f32_bf16 v161, v96, v96, 0\n"
"v_dot2_f32_bf16 v162, v112, v6, 0\n"
"v_dot2_f32_bf16 v163, v112, v112, 0\n"
"v_dot2_f32_bf16 v160, v97, v6, v160\n"
"v_dot2_f32_bf16 v161, v97, v97, v161\n"
"v_dot2_f32_bf16 v162, v113, v6, v162\n"
"v_dot2_f32_bf16 v163, v113, v113, v163\n"
"v_dot2_f32_bf16 v160, v98, v6, v160\n"
"v_dot2_f32_bf16 v161, v98, v98, v161\n"
"v_dot2_f32_bf16 v162, v114, v6, v162\n"
"v_dot2_f32_bf16 v163, v114, v114, v163\n"
"s_add_u32 s50, s45, 0\n"
"s_add_u32 m0, s49, 0\n"
"s_nop 0\n"
"buffer_load_dwordx4 v22, s[24:27], s50 offen offset:0 lds\n"
"v_dot2_f32_bf16 v160, v99, v6, v160\n"
"v_dot2_f32_bf16 v161, v99, v99, v161\n"
"v_dot2_f32_bf16 v162, v115, v6, v162\n"
"v_dot2_f32_bf16 v163, v115, v115, v163\n"
"v_dot2_f32_bf16 v160, v100, v6, v160\n"
"v_dot2_f32_bf16 v161, v100, v100, v161\n"
"v_dot2_f32_bf16 v162, v116, v6, v162\n"
"v_dot2_f32_bf16 v163, v116, v116, v163\n"
"v_dot2_f32_bf16 v160, v101, v6, v160\n"
"v_dot2_f32_bf16 v161, v101, v101, v161\n"
"v_dot2_f32_bf16 v162, v117, v6, v162\n"
"v_dot2_f32_bf16 v163, v117, v117, v163\n"
"buffer_load_dwordx4 v23, s[24:27], s50 offen offset:1024 lds\n"
"v_dot2_f32_bf16 v160, v102, v6, v160\n"
"v_dot2_f32_bf16 v161, v102, v102, v161\n"
"v_dot2_f32_bf16 v162, v118, v6, v162\n"
"v_dot2_f32_bf16 v163, v118, v118, v163\n"
"v_dot2_f32_bf16 v160, v103, v6, v160\n"
"v_dot2_f32_bf16 v161, v103, v103, v161\n"
"v_dot2_f32_bf16 v162, v119, v6, v162\n"
"v_dot2_f32_bf16 v163, v119, v119, v163\n"
"v_dot2_f32_bf16 v160, v104, v6, v160\n"
"v_dot2_f32_bf16 v161, v104, v104, v161\n"
"v_dot2_f32_bf16 v162, v120, v6, v162\n"
"v_dot2_f32_bf16 v163, v120, v120, v163\n"
"buffer_load_dwordx4 v24, s[24:27], s50 offen offset:2048 lds\n"
"v_dot2_f32_bf16 v160, v105, v6, v160\n"
"v_dot2_f32_bf16 v161, v105, v105, v161\n"
"v_dot2_f32_bf16 v162, v121, v6, v162\n"
"v_dot2_f32_bf16 v163, v121, v121, v163\n"
"v_dot2_f32_bf16 v160, v106, v6, v160\n"
"v_dot2_f32_bf16 v161, v106, v106, v161\n"
"v_dot2_f32_bf16 v162, v122, v6, v162\n"
"v_dot2_f32_bf16 v163, v122, v122, v163\n"
"v_dot2_f32_bf16 v160, v107, v6, v160\n"
"v_dot2_f32_bf16 v161, v107, v107, v161\n"
"v_dot2_f32_bf16 v162, v123, v6, v162\n"
"v_dot2_f32_bf16 v163, v123, v123, v163\n"
"buffer_load_dwordx4 v25, s[24:27], s50 offen offset:3072 lds\n"
"v_dot2_f32_bf16 v160, v108, v6, v160\n"
"v_dot2_f32_bf16 v161, v108, v108, v161\n"
"v_dot2_f32_bf16 v162, v124, v6, v162\n"
"v_dot2_f32_bf16 v163, v124, v124, v163\n"
"v_dot2_f32_bf16 v160, v109, v6, v160\n"
"v_dot2_f32_bf16 v161, v109, v109, v161\n"
"v_dot2_f32_bf16 v162, v125, v6, v162\n"
"v_dot2_f32_bf16 v163, v125, v125, v163\n"
"v_dot2_f32_bf16 v160, v110, v6, v160\n"
"v_dot2_f32_bf16 v161, v110, v110, v161\n"
"v_dot2_f32_bf16 v162, v126, v6, v162\n"
"v_dot2_f32_bf16 v163, v126, v126, v163\n"
"s_add_u32 s50, s45, 0\n"
"s_add_u32 m0, s49, 4096\n"
"s_nop 0\n"
"buffer_load_dwordx4 v26, s[24:27], s50 offen offset:0 lds\n"
"v_dot2_f32_bf16 v160, v111, v6, v160\n"
"v_dot2_f32_bf16 v161, v111, v111, v161\n"
"v_dot2_f32_bf16 v162, v127, v6, v162\n"
"v_dot2_f32_bf16 v163, v127, v127, v163\n"
"v_add_u32 v5, 8, v7\n"
"v_and_b32 v168, 15, v5\n"
"v_xor_b32 v168, v168, v8\n"
"v_lshlrev_b32 v168, 4, v168\n"
"v_lshl_add_u32 v168, v5, 10, v168\n"
"v_add_u32 v5, 12, v7\n"
"v_and_b32 v169, 15, v5\n"
"v_xor_b32 v169, v169, v8\n"
"buffer_load_dwordx4 v27, s[24:27], s50 offen offset:1024 lds\n"
"v_lshlrev_b32 v169, 4, v169\n"
"v_lshl_add_u32 v169, v5, 10, v169\n"
"v_add_f32_dpp v160, v160, v160 row_ror:8 row_mask:0xf bank_mask:0xf\n"
"v_add_f32_dpp v161, v161, v161 row_ror:8 row_mask:0xf bank_mask:0xf\n"
"v_add_f32_dpp v162, v162, v162 row_ror:8 row_mask:0xf bank_mask:0xf\n"
"v_add_f32_dpp v163, v163, v163 row_ror:8 row_mask:0xf bank_mask:0xf\n"
"v_add_f32_dpp v160, v160, v160 row_ror:4 row_mask:0xf bank_mask:0xf\n"
"v_add_f32_dpp v161, v161, v161 row_ror:4 row_mask:0xf bank_mask:0xf\n"
"v_add_f32_dpp v162, v162, v162 row_ror:4 row_mask:0xf bank_mask:0xf\n"
"v_add_f32_dpp v163, v163, v163 row_ror:4 row_mask:0xf bank_mask:0xf\n"
"v_add_f32_dpp v160, v160, v160 row_ror:2 row_mask:0xf bank_mask:0xf\n"
"v_add_f32_dpp v161, v161, v161 row_ror:2 row_mask:0xf bank_mask:0xf\n"
"buffer_load_dwordx4 v28, s[24:27], s50 offen offset:2048 lds\n"
"v_add_f32_dpp v162, v162, v162 row_ror:2 row_mask:0xf bank_mask:0xf\n"
"v_add_f32_dpp v163, v163, v163 row_ror:2 row_mask:0xf bank_mask:0xf\n"
"v_add_f32_dpp v160, v160, v160 row_ror:1 row_mask:0xf bank_mask:0xf\n"
"v_add_f32_dpp v161, v161, v161 row_ror:1 row_mask:0xf bank_mask:0xf\n"
"v_add_f32_dpp v162, v162, v162 row_ror:1 row_mask:0xf bank_mask:0xf\n"
"v_add_f32_dpp v163, v163, v163 row_ror:1 row_mask:0xf bank_mask:0xf\n"
"v_mul_f32 v160, 0x3b000000, v160\n"
"v_mul_f32 v161, 0x3b000000, v161\n"
"v_mul_f32 v162, 0x3b000000, v162\n"
"v_mul_f32 v163, 0x3b000000, v163\n"
"v_fma_f32 v161, -v160, v160, v161\n"
"v_fma_f32 v163, -v162, v162, v163\n"
"buffer_load_dwordx4 v29, s[24:27], s50 offen offset:3072 lds\n"
"v_max_f32 v161, 0, v161\n"
"v_max_f32 v163, 0, v163\n"
"v_add_f32 v161, 0x3727c5ac, v161\n"
"v_add_f32 v163, 0x3727c5ac, v163\n"
"v_rsq_f32 v164, v161\n"
"v_rsq_f32 v166, v163\n"
"s_nop 0\n"
"v_mul_f32 v164, s59, v164\n"
"v_mul_f32 v166, s59, v166\n"
"v_mul_f32 v165, -v160, v164\n"
"v_mul_f32 v167, -v162, v166\n"
"v_lshlrev_b32 v172, 16, v96\n"
"s_add_u32 s50, s45, 262144\n"
"s_add_u32 m0, s49, 8192\n"
"s_nop 0\n"
"buffer_load_dwordx4 v22, s[24:27], s50 offen offset:0 lds\n"
"v_and_b32 v173, 0xffff0000, v96\n"
"v_pk_fma_f32 v[172:173], v[172:173], v[164:165], v[164:165] op_sel:[0,0,1] op_sel_hi:[1,0,1]\n"
"v_cvt_pk_bf16_f32 v96, v172, v173\n"
"v_lshlrev_b32 v174, 16, v97\n"
"v_and_b32 v175, 0xffff0000, v97\n"
"v_pk_fma_f32 v[174:175], v[174:175], v[164:165], v[164:165] op_sel:[0,0,1] op_sel_hi:[1,0,1]\n"
"v_cvt_pk_bf16_f32 v97, v174, v175\n"
"v_lshlrev_b32 v176, 16, v98\n"
"v_and_b32 v177, 0xffff0000, v98\n"
"v_pk_fma_f32 v[176:177], v[176:177], v[164:165], v[164:165] op_sel:[0,0,1] op_sel_hi:[1,0,1]\n"
"v_cvt_pk_bf16_f32 v98, v176, v177\n"
"v_lshlrev_b32 v178, 16, v99\n"
"buffer_load_dwordx4 v23, s[24:27], s50 offen offset:1024 lds\n"
"v_and_b32 v179, 0xffff0000, v99\n"
"v_pk_fma_f32 v[178:179], v[178:179], v[164:165], v[164:165] op_sel:[0,0,1] op_sel_hi:[1,0,1]\n"
"v_cvt_pk_bf16_f32 v99, v178, v179\n"
"ds_write_b128 v168, v[96:99] offset:0\n"
"v_lshlrev_b32 v172, 16, v100\n"
"v_and_b32 v173, 0xffff0000, v100\n"
"v_pk_fma_f32 v[172:173], v[172:173], v[164:165], v[164:165] op_sel:[0,0,1] op_sel_hi:[1,0,1]\n"
"v_cvt_pk_bf16_f32 v100, v172, v173\n"
"v_lshlrev_b32 v174, 16, v101\n"
"v_and_b32 v175, 0xffff0000, v101\n"
"v_pk_fma_f32 v[174:175], v[174:175], v[164:165], v[164:165] op_sel:[0,0,1] op_sel_hi:[1,0,1]\n"
"v_cvt_pk_bf16_f32 v101, v174, v175\n"
"buffer_load_dwordx4 v24, s[24:27], s50 offen offset:2048 lds\n"
"v_lshlrev_b32 v176, 16, v102\n"
"v_and_b32 v177, 0xffff0000, v102\n"
"v_pk_fma_f32 v[176:177], v[176:177], v[164:165], v[164:165] op_sel:[0,0,1] op_sel_hi:[1,0,1]\n"
"v_cvt_pk_bf16_f32 v102, v176, v177\n"
"v_lshlrev_b32 v178, 16, v103\n"
"v_and_b32 v179, 0xffff0000, v103\n"
"v_pk_fma_f32 v[178:179], v[178:179], v[164:165], v[164:165] op_sel:[0,0,1] op_sel_hi:[1,0,1]\n"
"v_cvt_pk_bf16_f32 v103, v178, v179\n"
"ds_write_b128 v168, v[100:103] offset:256\n"
"v_lshlrev_b32 v172, 16, v104\n"
"v_and_b32 v173, 0xffff0000, v104\n"
"v_pk_fma_f32 v[172:173], v[172:173], v[164:165], v[164:165] op_sel:[0,0,1] op_sel_hi:[1,0,1]\n"
"buffer_load_dwordx4 v25, s[24:27], s50 offen offset:3072 lds\n"
"v_cvt_pk_bf16_f32 v104, v172, v173\n"
"v_lshlrev_b32 v174, 16, v105\n"
"v_and_b32 v175, 0xffff0000, v105\n"
"v_pk_fma_f32 v[174:175], v[174:175], v[164:165], v[164:165] op_sel:[0,0,1] op_sel_hi:[1,0,1]\n"
"v_cvt_pk_bf16_f32 v105, v174, v175\n"
"v_lshlrev_b32 v176, 16, v106\n"
"v_and_b32 v177, 0xffff0000, v106\n"
"v_pk_fma_f32 v[176:177], v[176:177], v[164:165], v[164:165] op_sel:[0,0,1] op_sel_hi:[1,0,1]\n"
"v_cvt_pk_bf16_f32 v106, v176, v177\n"
"v_lshlrev_b32 v178, 16, v107\n"
"v_and_b32 v179, 0xffff0000, v107\n"
"v_pk_fma_f32 v[178:179], v[178:179], v[164:165], v[164:165] op_sel:[0,0,1] op_sel_hi:[1,0,1]\n"
"s_add_u32 s50, s45, 262144\n"
"s_add_u32 m0, s49, 12288\n"
"s_nop 0\n"
"buffer_load_dwordx4 v26, s[24:27], s50 offen offset:0 lds\n"
"v_cvt_pk_bf16_f32 v107, v178, v179\n"
"ds_write_b128 v168, v[104:107] offset:512\n"
"v_lshlrev_b32 v172, 16, v108\n"
"v_and_b32 v173, 0xffff0000, v108\n"
"v_pk_fma_f32 v[172:173], v[172:173], v[164:165], v[164:165] op_sel:[0,0,1] op_sel_hi:[1,0,1]\n"
"v_cvt_pk_bf16_f32 v108, v172, v173\n"
"v_lshlrev_b32 v174, 16, v109\n"
"v_and_b32 v175, 0xffff0000, v109\n"
"v_pk_fma_f32 v[174:175], v[174:175], v[164:165], v[164:165] op_sel:[0,0,1] op_sel_hi:[1,0,1]\n"
"v_cvt_pk_bf16_f32 v109, v174, v175\n"
"v_lshlrev_b32 v176, 16, v110\n"
"v_and_b32 v177, 0xffff0000, v110\n"
"buffer_load_dwordx4 v27, s[24:27], s50 offen offset:1024 lds\n"
"v_pk_fma_f32 v[176:177], v[176:177], v[164:165], v[164:165] op_sel:[0,0,1] op_sel_hi:[1,0,1]\n"
"v_cvt_pk_bf16_f32 v110, v176, v177\n"
"v_lshlrev_b32 v178, 16, v111\n"
"v_and_b32 v179, 0xffff0000, v111\n"
"v_pk_fma_f32 v[178:179], v[178:179], v[164:165], v[164:165] op_sel:[0,0,1] op_sel_hi:[1,0,1]\n"
"v_cvt_pk_bf16_f32 v111, v178, v179\n"
"ds_write_b128 v168, v[108:111] offset:768\n"
"v_lshlrev_b32 v172, 16, v112\n"
"v_and_b32 v173, 0xffff0000, v112\n"
"v_pk_fma_f32 v[172:173], v[172:173], v[166:167], v[166:167] op_sel:[0,0,1] op_sel_hi:[1,0,1]\n"
"v_cvt_pk_bf16_f32 v112, v172, v173\n"
"v_lshlrev_b32 v174, 16, v113\n"
"buffer_load_dwordx4 v28, s[24:27], s50 offen offset:2048 lds\n"
"v_and_b32 v175, 0xffff0000, v113\n"
"v_pk_fma_f32 v[174:175], v[174:175], v[166:167], v[166:167] op_sel:[0,0,1] op_sel_hi:[1,0,1]\n"
"v_cvt_pk_bf16_f32 v113, v174, v175\n"
"v_lshlrev_b32 v176, 16, v114\n"
"v_and_b32 v177, 0xffff0000, v114\n"
"v_pk_fma_f32 v[176:177], v[176:177], v[166:167], v[166:167] op_sel:[0,0,1] op_sel_hi:[1,0,1]\n"
"v_cvt_pk_bf16_f32 v114, v176, v177\n"
"v_lshlrev_b32 v178, 16, v115\n"
"v_and_b32 v179, 0xffff0000, v115\n"
"v_pk_fma_f32 v[178:179], v[178:179], v[166:167], v[166:167] op_sel:[0,0,1] op_sel_hi:[1,0,1]\n"
"v_cvt_pk_bf16_f32 v115, v178, v179\n"
"ds_write_b128 v169, v[112:115] offset:0\n"
"buffer_load_dwordx4 v29, s[24:27], s50 offen offset:3072 lds\n"
"v_lshlrev_b32 v172, 16, v116\n"
"v_and_b32 v173, 0xffff0000, v116\n"
"v_pk_fma_f32 v[172:173], v[172:173], v[166:167], v[166:167] op_sel:[0,0,1] op_sel_hi:[1,0,1]\n"
"v_cvt_pk_bf16_f32 v116, v172, v173\n"
"v_lshlrev_b32 v174, 16, v117\n"
"v_and_b32 v175, 0xffff0000, v117\n"
"v_pk_fma_f32 v[174:175], v[174:175], v[166:167], v[166:167] op_sel:[0,0,1] op_sel_hi:[1,0,1]\n"
"v_cvt_pk_bf16_f32 v117, v174, v175\n"
"v_lshlrev_b32 v176, 16, v118\n"
"v_and_b32 v177, 0xffff0000, v118\n"
"v_pk_fma_f32 v[176:177], v[176:177], v[166:167], v[166:167] op_sel:[0,0,1] op_sel_hi:[1,0,1]\n"
"v_cvt_pk_bf16_f32 v118, v176, v177\n"
"v_lshlrev_b32 v178, 16, v119\n"
"v_and_b32 v179, 0xffff0000, v119\n"
"v_pk_fma_f32 v[178:179], v[178:179], v[166:167], v[166:167] op_sel:[0,0,1] op_sel_hi:[1,0,1]\n"
"v_cvt_pk_bf16_f32 v119, v178, v179\n"
"ds_write_b128 v169, v[116:119] offset:256\n"
"v_lshlrev_b32 v172, 16, v120\n"
"v_and_b32 v173, 0xffff0000, v120\n"
"v_pk_fma_f32 v[172:173], v[172:173], v[166:167], v[166:167] op_sel:[0,0,1] op_sel_hi:[1,0,1]\n"
"v_cvt_pk_bf16_f32 v120, v172, v173\n"
"v_lshlrev_b32 v174, 16, v121\n"
"v_and_b32 v175, 0xffff0000, v121\n"
"v_pk_fma_f32 v[174:175], v[174:175], v[166:167], v[166:167] op_sel:[0,0,1] op_sel_hi:[1,0,1]\n"
"v_cvt_pk_bf16_f32 v121, v174, v175\n"
"v_lshlrev_b32 v176, 16, v122\n"
"v_and_b32 v177, 0xffff0000, v122\n"
"v_pk_fma_f32 v[176:177], v[176:177], v[166:167], v[166:167] op_sel:[0,0,1] op_sel_hi:[1,0,1]\n"
"v_cvt_pk_bf16_f32 v122, v176, v177\n"
"v_lshlrev_b32 v178, 16, v123\n"
"v_and_b32 v179, 0xffff0000, v123\n"
"v_pk_fma_f32 v[178:179], v[178:179], v[166:167], v[166:167] op_sel:[0,0,1] op_sel_hi:[1,0,1]\n"
"v_cvt_pk_bf16_f32 v123, v178, v179\n"
"ds_write_b128 v169, v[120:123] offset:512\n"
"v_lshlrev_b32 v172, 16, v124\n"
"v_and_b32 v173, 0xffff0000, v124\n"
"v_pk_fma_f32 v[172:173], v[172:173], v[166:167], v[166:167] op_sel:[0,0,1] op_sel_hi:[1,0,1]\n"
"v_cvt_pk_bf16_f32 v124, v172, v173\n"
"v_lshlrev_b32 v174, 16, v125\n"
"v_and_b32 v175, 0xffff0000, v125\n"
"v_pk_fma_f32 v[174:175], v[174:175], v[166:167], v[166:167] op_sel:[0,0,1] op_sel_hi:[1,0,1]\n"
"v_cvt_pk_bf16_f32 v125, v174, v175\n"
"v_lshlrev_b32 v176, 16, v126\n"
"v_and_b32 v177, 0xffff0000, v126\n"
"v_pk_fma_f32 v[176:177], v[176:177], v[166:167], v[166:167] op_sel:[0,0,1] op_sel_hi:[1,0,1]\n"
"v_cvt_pk_bf16_f32 v126, v176, v177\n"
"v_lshlrev_b32 v178, 16, v127\n"
"v_and_b32 v179, 0xffff0000, v127\n"
"v_pk_fma_f32 v[178:179], v[178:179], v[166:167], v[166:167] op_sel:[0,0,1] op_sel_hi:[1,0,1]\n"
"v_cvt_pk_bf16_f32 v127, v178, v179\n"
"ds_write_b128 v169, v[124:127] offset:768\n"
"s_waitcnt vmcnt(16)\n"
"v_dot2_f32_bf16 v160, v128, v6, 0\n"
"v_dot2_f32_bf16 v161, v128, v128, 0\n"
"v_dot2_f32_bf16 v162, v144, v6, 0\n"
"v_dot2_f32_bf16 v163, v144, v144, 0\n"
"v_dot2_f32_bf16 v160, v129, v6, v160\n"
"v_dot2_f32_bf16 v161, v129, v129, v161\n"
"v_dot2_f32_bf16 v162, v145, v6, v162\n"
"v_dot2_f32_bf16 v163, v145, v145, v163\n"
"v_dot2_f32_bf16 v160, v130, v6, v160\n"
"v_dot2_f32_bf16 v161, v130, v130, v161\n"
"v_dot2_f32_bf16 v162, v146, v6, v162\n"
"v_dot2_f32_bf16 v163, v146, v146, v163\n"
"v_dot2_f32_bf16 v160, v131, v6, v160\n"
"v_dot2_f32_bf16 v161, v131, v131, v161\n"
"v_dot2_f32_bf16 v162, v147, v6, v162\n"
"v_dot2_f32_bf16 v163, v147, v147, v163\n"
"v_dot2_f32_bf16 v160, v132, v6, v160\n"
"v_dot2_f32_bf16 v161, v132, v132, v161\n"
"v_dot2_f32_bf16 v162, v148, v6, v162\n"
"v_dot2_f32_bf16 v163, v148, v148, v163\n"
"v_dot2_f32_bf16 v160, v133, v6, v160\n"
"v_dot2_f32_bf16 v161, v133, v133, v161\n"
"v_dot2_f32_bf16 v162, v149, v6, v162\n"
"v_dot2_f32_bf16 v163, v149, v149, v163\n"
"v_dot2_f32_bf16 v160, v134, v6, v160\n"
"v_dot2_f32_bf16 v161, v134, v134, v161\n"
"v_dot2_f32_bf16 v162, v150, v6, v162\n"
"v_dot2_f32_bf16 v163, v150, v150, v163\n"
"v_dot2_f32_bf16 v160, v135, v6, v160\n"
"v_dot2_f32_bf16 v161, v135, v135, v161\n"
"v_dot2_f32_bf16 v162, v151, v6, v162\n"
"v_dot2_f32_bf16 v163, v151, v151, v163\n"
"v_dot2_f32_bf16 v160, v136, v6, v160\n"
"v_dot2_f32_bf16 v161, v136, v136, v161\n"
"v_dot2_f32_bf16 v162, v152, v6, v162\n"
"v_dot2_f32_bf16 v163, v152, v152, v163\n"
"v_dot2_f32_bf16 v160, v137, v6, v160\n"
"v_dot2_f32_bf16 v161, v137, v137, v161\n"
"v_dot2_f32_bf16 v162, v153, v6, v162\n"
"v_dot2_f32_bf16 v163, v153, v153, v163\n"
"v_dot2_f32_bf16 v160, v138, v6, v160\n"
"v_dot2_f32_bf16 v161, v138, v138, v161\n"
"v_dot2_f32_bf16 v162, v154, v6, v162\n"
"v_dot2_f32_bf16 v163, v154, v154, v163\n"
"v_dot2_f32_bf16 v160, v139, v6, v160\n"
"v_dot2_f32_bf16 v161, v139, v139, v161\n"
"v_dot2_f32_bf16 v162, v155, v6, v162\n"
"v_dot2_f32_bf16 v163, v155, v155, v163\n"
"v_dot2_f32_bf16 v160, v140, v6, v160\n"
"v_dot2_f32_bf16 v161, v140, v140, v161\n"
"v_dot2_f32_bf16 v162, v156, v6, v162\n"
"v_dot2_f32_bf16 v163, v156, v156, v163\n"
"v_dot2_f32_bf16 v160, v141, v6, v160\n"
"v_dot2_f32_bf16 v161, v141, v141, v161\n"
"v_dot2_f32_bf16 v162, v157, v6, v162\n"
"v_dot2_f32_bf16 v163, v157, v157, v163\n"
"v_dot2_f32_bf16 v160, v142, v6, v160\n"
"v_dot2_f32_bf16 v161, v142, v142, v161\n"
"v_dot2_f32_bf16 v162, v158, v6, v162\n"
"v_dot2_f32_bf16 v163, v158, v158, v163\n"
"v_dot2_f32_bf16 v160, v143, v6, v160\n"
"v_dot2_f32_bf16 v161, v143, v143, v161\n"
"v_dot2_f32_bf16 v162, v159, v6, v162\n"
"v_dot2_f32_bf16 v163, v159, v159, v163\n"
"v_add_u32 v5, 16, v7\n"
"v_and_b32 v168, 15, v5\n"
"v_xor_b32 v168, v168, v8\n"
"v_lshlrev_b32 v168, 4, v168\n"
"v_lshl_add_u32 v168, v5, 10, v168\n"
"v_add_u32 v5, 20, v7\n"
"v_and_b32 v169, 15, v5\n"
"v_xor_b32 v169, v169, v8\n"
"v_lshlrev_b32 v169, 4, v169\n"
"v_lshl_add_u32 v169, v5, 10, v169\n"
"v_add_f32_dpp v160, v160, v160 row_ror:8 row_mask:0xf bank_mask:0xf\n"
"v_add_f32_dpp v161, v161, v161 row_ror:8 row_mask:0xf bank_mask:0xf\n"
"v_add_f32_dpp v162, v162, v162 row_ror:8 row_mask:0xf bank_mask:0xf\n"
"v_add_f32_dpp v163, v163, v163 row_ror:8 row_mask:0xf bank_mask:0xf\n"
"v_add_f32_dpp v160, v160, v160 row_ror:4 row_mask:0xf bank_mask:0xf\n"
"v_add_f32_dpp v161, v161, v161 row_ror:4 row_mask:0xf bank_mask:0xf\n"
"v_add_f32_dpp v162, v162, v162 row_ror:4 row_mask:0xf bank_mask:0xf\n"
"v_add_f32_dpp v163, v163, v163 row_ror:4 row_mask:0xf bank_mask:0xf\n"
"v_add_f32_dpp v160, v160, v160 row_ror:2 row_mask:0xf bank_mask:0xf\n"
"v_add_f32_dpp v161, v161, v161 row_ror:2 row_mask:0xf bank_mask:0xf\n"
"v_add_f32_dpp v162, v162, v162 row_ror:2 row_mask:0xf bank_mask:0xf\n"
"v_add_f32_dpp v163, v163, v163 row_ror:2 row_mask:0xf bank_mask:0xf\n"
"v_add_f32_dpp v160, v160, v160 row_ror:1 row_mask:0xf bank_mask:0xf\n"
"v_add_f32_dpp v161, v161, v161 row_ror:1 row_mask:0xf bank_mask:0xf\n"
"v_add_f32_dpp v162, v162, v162 row_ror:1 row_mask:0xf bank_mask:0xf\n"
"v_add_f32_dpp v163, v163, v163 row_ror:1 row_mask:0xf bank_mask:0xf\n"
"v_mul_f32 v160, 0x3b000000, v160\n"
"v_mul_f32 v161, 0x3b000000, v161\n"
"v_mul_f32 v162, 0x3b000000, v162\n"
"v_mul_f32 v163, 0x3b000000, v163\n"
"v_fma_f32 v161, -v160, v160, v161\n"
"v_fma_f32 v163, -v162, v162, v163\n"
"v_max_f32 v161, 0, v161\n"
"v_max_f32 v163, 0, v163\n"
"v_add_f32 v161, 0x3727c5ac, v161\n"
"v_add_f32 v163, 0x3727c5ac, v163\n"
"v_rsq_f32 v164, v161\n"
"v_rsq_f32 v166, v163\n"
"s_nop 0\n"
"v_mul_f32 v164, s59, v164\n"
"v_mul_f32 v166, s59, v166\n"
"v_mul_f32 v165, -v160, v164\n"
"v_mul_f32 v167, -v162, v166\n"
"v_lshlrev_b32 v172, 16, v128\n"
"v_and_b32 v173, 0xffff0000, v128\n"
"v_pk_fma_f32 v[172:173], v[172:173], v[164:165], v[164:165] op_sel:[0,0,1] op_sel_hi:[1,0,1]\n"
"v_cvt_pk_bf16_f32 v128, v172, v173\n"
"v_lshlrev_b32 v174, 16, v129\n"
"v_and_b32 v175, 0xffff0000, v129\n"
"v_pk_fma_f32 v[174:175], v[174:175], v[164:165], v[164:165] op_sel:[0,0,1] op_sel_hi:[1,0,1]\n"
"v_cvt_pk_bf16_f32 v129, v174, v175\n"
"v_lshlrev_b32 v176, 16, v130\n"
"v_and_b32 v177, 0xffff0000, v130\n"
"v_pk_fma_f32 v[176:177], v[176:177], v[164:165], v[164:165] op_sel:[0,0,1] op_sel_hi:[1,0,1]\n"
"v_cvt_pk_bf16_f32 v130, v176, v177\n"
"v_lshlrev_b32 v178, 16, v131\n"
"v_and_b32 v179, 0xffff0000, v131\n"
"v_pk_fma_f32 v[178:179], v[178:179], v[164:165], v[164:165] op_sel:[0,0,1] op_sel_hi:[1,0,1]\n"
"v_cvt_pk_bf16_f32 v131, v178, v179\n"
"ds_write_b128 v168, v[128:131] offset:0\n"
"v_lshlrev_b32 v172, 16, v132\n"
"v_and_b32 v173, 0xffff0000, v132\n"
"v_pk_fma_f32 v[172:173], v[172:173], v[164:165], v[164:165] op_sel:[0,0,1] op_sel_hi:[1,0,1]\n"
"v_cvt_pk_bf16_f32 v132, v172, v173\n"
"v_lshlrev_b32 v174, 16, v133\n"
"v_and_b32 v175, 0xffff0000, v133\n"
"v_pk_fma_f32 v[174:175], v[174:175], v[164:165], v[164:165] op_sel:[0,0,1] op_sel_hi:[1,0,1]\n"
"v_cvt_pk_bf16_f32 v133, v174, v175\n"
"v_lshlrev_b32 v176, 16, v134\n"
"v_and_b32 v177, 0xffff0000, v134\n"
"v_pk_fma_f32 v[176:177], v[176:177], v[164:165], v[164:165] op_sel:[0,0,1] op_sel_hi:[1,0,1]\n"
"v_cvt_pk_bf16_f32 v134, v176, v177\n"
"v_lshlrev_b32 v178, 16, v135\n"
"v_and_b32 v179, 0xffff0000, v135\n"
"v_pk_fma_f32 v[178:179], v[178:179], v[164:165], v[164:165] op_sel:[0,0,1] op_sel_hi:[1,0,1]\n"
"v_cvt_pk_bf16_f32 v135, v178, v179\n"
"ds_write_b128 v168, v[132:135] offset:256\n"
"v_lshlrev_b32 v172, 16, v136\n"
"v_and_b32 v173, 0xffff0000, v136\n"
"v_pk_fma_f32 v[172:173], v[172:173], v[164:165], v[164:165] op_sel:[0,0,1] op_sel_hi:[1,0,1]\n"
"v_cvt_pk_bf16_f32 v136, v172, v173\n"
"v_lshlrev_b32 v174, 16, v137\n"
"v_and_b32 v175, 0xffff0000, v137\n"
"v_pk_fma_f32 v[174:175], v[174:175], v[164:165], v[164:165] op_sel:[0,0,1] op_sel_hi:[1,0,1]\n"
"v_cvt_pk_bf16_f32 v137, v174, v175\n"
"v_lshlrev_b32 v176, 16, v138\n"
"v_and_b32 v177, 0xffff0000, v138\n"
"v_pk_fma_f32 v[176:177], v[176:177], v[164:165], v[164:165] op_sel:[0,0,1] op_sel_hi:[1,0,1]\n"
"v_cvt_pk_bf16_f32 v138, v176, v177\n"
"v_lshlrev_b32 v178, 16, v139\n"
"v_and_b32 v179, 0xffff0000, v139\n"
"v_pk_fma_f32 v[178:179], v[178:179], v[164:165], v[164:165] op_sel:[0,0,1] op_sel_hi:[1,0,1]\n"
"v_cvt_pk_bf16_f32 v139, v178, v179\n"
"ds_write_b128 v168, v[136:139] offset:512\n"
"v_lshlrev_b32 v172, 16, v140\n"
"v_and_b32 v173, 0xffff0000, v140\n"
"v_pk_fma_f32 v[172:173], v[172:173], v[164:165], v[164:165] op_sel:[0,0,1] op_sel_hi:[1,0,1]\n"
"v_cvt_pk_bf16_f32 v140, v172, v173\n"
"v_lshlrev_b32 v174, 16, v141\n"
"v_and_b32 v175, 0xffff0000, v141\n"
"v_pk_fma_f32 v[174:175], v[174:175], v[164:165], v[164:165] op_sel:[0,0,1] op_sel_hi:[1,0,1]\n"
"v_cvt_pk_bf16_f32 v141, v174, v175\n"
"v_lshlrev_b32 v176, 16, v142\n"
"v_and_b32 v177, 0xffff0000, v142\n"
"v_pk_fma_f32 v[176:177], v[176:177], v[164:165], v[164:165] op_sel:[0,0,1] op_sel_hi:[1,0,1]\n"
"v_cvt_pk_bf16_f32 v142, v176, v177\n"
"v_lshlrev_b32 v178, 16, v143\n"
"v_and_b32 v179, 0xffff0000, v143\n"
"v_pk_fma_f32 v[178:179], v[178:179], v[164:165], v[164:165] op_sel:[0,0,1] op_sel_hi:[1,0,1]\n"
"v_cvt_pk_bf16_f32 v143, v178, v179\n"
"ds_write_b128 v168, v[140:143] offset:768\n"
"v_lshlrev_b32 v172, 16, v144\n"
"v_and_b32 v173, 0xffff0000, v144\n"
"v_pk_fma_f32 v[172:173], v[172:173], v[166:167], v[166:167] op_sel:[0,0,1] op_sel_hi:[1,0,1]\n"
"v_cvt_pk_bf16_f32 v144, v172, v173\n"
"v_lshlrev_b32 v174, 16, v145\n"
"v_and_b32 v175, 0xffff0000, v145\n"
"v_pk_fma_f32 v[174:175], v[174:175], v[166:167], v[166:167] op_sel:[0,0,1] op_sel_hi:[1,0,1]\n"
"v_cvt_pk_bf16_f32 v145, v174, v175\n"
"v_lshlrev_b32 v176, 16, v146\n"
"v_and_b32 v177, 0xffff0000, v146\n"
"v_pk_fma_f32 v[176:177], v[176:177], v[166:167], v[166:167] op_sel:[0,0,1] op_sel_hi:[1,0,1]\n"
"v_cvt_pk_bf16_f32 v146, v176, v177\n"
"v_lshlrev_b32 v178, 16, v147\n"
"v_and_b32 v179, 0xffff0000, v147\n"
"v_pk_fma_f32 v[178:179], v[178:179], v[166:167], v[166:167] op_sel:[0,0,1] op_sel_hi:[1,0,1]\n"
"v_cvt_pk_bf16_f32 v147, v178, v179\n"
"ds_write_b128 v169, v[144:147] offset:0\n"
"v_lshlrev_b32 v172, 16, v148\n"
"v_and_b32 v173, 0xffff0000, v148\n"
"v_pk_fma_f32 v[172:173], v[172:173], v[166:167], v[166:167] op_sel:[0,0,1] op_sel_hi:[1,0,1]\n"
"v_cvt_pk_bf16_f32 v148, v172, v173\n"
"v_lshlrev_b32 v174, 16, v149\n"
"v_and_b32 v175, 0xffff0000, v149\n"
"v_pk_fma_f32 v[174:175], v[174:175], v[166:167], v[166:167] op_sel:[0,0,1] op_sel_hi:[1,0,1]\n"
"v_cvt_pk_bf16_f32 v149, v174, v175\n"
"v_lshlrev_b32 v176, 16, v150\n"
"v_and_b32 v177, 0xffff0000, v150\n"
"v_pk_fma_f32 v[176:177], v[176:177], v[166:167], v[166:167] op_sel:[0,0,1] op_sel_hi:[1,0,1]\n"
"s_mov_b32 s39, 0x20000\n"
"s_lshl_b32 s53, s58, 2\n"
"s_add_u32 s53, s53, s42\n"
"s_cmp_ge_u32 s53, 448\n"
"s_waitcnt lgkmcnt(0)\n"
"s_cbranch_scc1 LPSA%=\n"
"s_add_u32 s53, s53, 512\n"
"s_cmp_lt_u32 s53, 512\n"
"s_cbranch_scc1 LPQAa%=\n"
"s_cmp_lt_u32 s53, 768\n"
"s_cbranch_scc1 LPKAa%=\n"
"s_cmp_lt_u32 s53, 1248\n"
"s_cbranch_scc1 LPGAa%=\n"
"s_cmp_lt_u32 s53, 1472\n"
"s_cbranch_scc1 LPDAa%=\n"
"s_sub_u32 s53, s53, 1472\n"
"s_mov_b32 s36, s72\n"
"s_and_b32 s37, s73, 0xffff\n"
"s_mov_b32 s38, 4194304\n"
"s_lshr_b32 s54, s53, 5\n"
"s_lshl_b32 s54, s54, 15\n"
"s_bfe_u32 s55, s53, 0x30002\n"
"s_lshl_b32 s55, s55, 19\n"
"s_add_u32 s54, s54, s55\n"
"s_and_b32 s53, s53, 3\n"
"s_lshl_b32 s53, s53, 13\n"
"s_add_u32 s54, s54, s53\n"
"v_lshlrev_b32 v56, 7, v1\n"
"s_branch LPLAa%=\n"
"LPQAa%=:\n"
"s_mov_b32 s36, s70\n"
"s_and_b32 s37, s71, 0xffff\n"
"s_mov_b32 s38, 4194304\n"
"s_lshl_b32 s54, s53, 13\n"
"v_lshlrev_b32 v56, 7, v1\n"
"s_branch LPLAa%=\n"
"LPKAa%=:\n"
"s_sub_u32 s53, s53, 512\n"
"s_bitcmp0_b32 s53, 0\n"
"s_cselect_b32 s36, s66, s68\n"
"s_cselect_b32 s37, s67, s69\n"
"s_and_b32 s37, s37, 0xffff\n"
"s_mov_b32 s38, 4194304\n"
"s_lshr_b32 s54, s53, 4\n"
"s_lshl_b32 s54, s54, 13\n"
"s_bfe_u32 s55, s53, 0x30001\n"
"s_lshl_b32 s55, s55, 19\n"
"s_add_u32 s54, s54, s55\n"
"s_add_u32 s54, s54, 393216\n"
"v_lshlrev_b32 v56, 7, v1\n"
"s_branch LPLAa%=\n"
"LPGAa%=:\n"
"s_sub_u32 s53, s53, 768\n"
"s_bitcmp0_b32 s53, 4\n"
"s_cselect_b32 s36, s74, s76\n"
"s_cselect_b32 s37, s75, s77\n"
"s_and_b32 s37, s37, 0xffff\n"
"s_mov_b32 s38, 1835008\n"
"s_and_b32 s54, s53, 15\n"
"s_mul_i32 s54, s54, 114688\n"
"s_lshr_b32 s53, s53, 5\n"
"s_mul_i32 s55, s53, 22\n"
"s_lshr_b32 s55, s55, 6\n"
"s_mul_i32 s56, s55, 3\n"
"s_sub_u32 s53, s53, s56\n"
"s_mul_i32 s53, s53, 1280\n"
"s_lshl_b32 s55, s55, 8\n"
"s_add_u32 s54, s54, s53\n"
"s_add_u32 s54, s54, s55\n"
"v_lshrrev_b32 v56, 1, v1\n"
"v_and_b32 v57, 1, v1\n"
"v_mul_u32_u24 v56, 3584, v56\n"
"v_lshl_add_u32 v56, v57, 7, v56\n"
"s_branch LPLAa%=\n"
"LPDAa%=:\n"
"s_sub_u32 s53, s53, 1248\n"
"s_mov_b32 s36, s78\n"
"s_and_b32 s37, s79, 0xffff\n"
"s_mov_b32 s38, 1835008\n"
"s_lshl_b32 s54, s53, 13\n"
"v_lshlrev_b32 v56, 7, v1\n"
"LPLAa%=:\n"
"buffer_load_dword v63, v56, s[36:39], s54 offen\n"
"LPSA%=:\n"
"v_cvt_pk_bf16_f32 v150, v176, v177\n"
"v_lshlrev_b32 v178, 16, v151\n"
"v_and_b32 v179, 0xffff0000, v151\n"
"v_pk_fma_f32 v[178:179], v[178:179], v[166:167], v[166:167] op_sel:[0,0,1] op_sel_hi:[1,0,1]\n"
"v_cvt_pk_bf16_f32 v151, v178, v179\n"
"ds_write_b128 v169, v[148:151] offset:256\n"
"v_lshlrev_b32 v172, 16, v152\n"
"v_and_b32 v173, 0xffff0000, v152\n"
"v_pk_fma_f32 v[172:173], v[172:173], v[166:167], v[166:167] op_sel:[0,0,1] op_sel_hi:[1,0,1]\n"
"v_cvt_pk_bf16_f32 v152, v172, v173\n"
"v_lshlrev_b32 v174, 16, v153\n"
"v_and_b32 v175, 0xffff0000, v153\n"
"v_pk_fma_f32 v[174:175], v[174:175], v[166:167], v[166:167] op_sel:[0,0,1] op_sel_hi:[1,0,1]\n"
"v_cvt_pk_bf16_f32 v153, v174, v175\n"
"v_lshlrev_b32 v176, 16, v154\n"
"v_and_b32 v177, 0xffff0000, v154\n"
"v_pk_fma_f32 v[176:177], v[176:177], v[166:167], v[166:167] op_sel:[0,0,1] op_sel_hi:[1,0,1]\n"
"v_cvt_pk_bf16_f32 v154, v176, v177\n"
"v_lshlrev_b32 v178, 16, v155\n"
"v_and_b32 v179, 0xffff0000, v155\n"
"v_pk_fma_f32 v[178:179], v[178:179], v[166:167], v[166:167] op_sel:[0,0,1] op_sel_hi:[1,0,1]\n"
"v_cvt_pk_bf16_f32 v155, v178, v179\n"
"ds_write_b128 v169, v[152:155] offset:512\n"
"v_lshlrev_b32 v172, 16, v156\n"
"v_and_b32 v173, 0xffff0000, v156\n"
"v_pk_fma_f32 v[172:173], v[172:173], v[166:167], v[166:167] op_sel:[0,0,1] op_sel_hi:[1,0,1]\n"
"v_cvt_pk_bf16_f32 v156, v172, v173\n"
"v_lshlrev_b32 v174, 16, v157\n"
"v_and_b32 v175, 0xffff0000, v157\n"
"v_pk_fma_f32 v[174:175], v[174:175], v[166:167], v[166:167] op_sel:[0,0,1] op_sel_hi:[1,0,1]\n"
"v_cvt_pk_bf16_f32 v157, v174, v175\n"
"v_lshlrev_b32 v176, 16, v158\n"
"v_and_b32 v177, 0xffff0000, v158\n"
"v_pk_fma_f32 v[176:177], v[176:177], v[166:167], v[166:167] op_sel:[0,0,1] op_sel_hi:[1,0,1]\n"
"v_cvt_pk_bf16_f32 v158, v176, v177\n"
"v_lshlrev_b32 v178, 16, v159\n"
"v_and_b32 v179, 0xffff0000, v159\n"
"v_pk_fma_f32 v[178:179], v[178:179], v[166:167], v[166:167] op_sel:[0,0,1] op_sel_hi:[1,0,1]\n"
"v_cvt_pk_bf16_f32 v159, v178, v179\n"
"ds_write_b128 v169, v[156:159] offset:768\n"
"s_waitcnt lgkmcnt(0)\n"
"s_barrier\n"
"; ---- end of section: ln\n"
"s_waitcnt vmcnt(8)\n"
"ds_read_b64_tr_b16 a[192:193], v10 offset:0\n"
"ds_read_b64_tr_b16 a[194:195], v10 offset:512\n"
"ds_read_b64_tr_b16 a[196:197], v10 offset:8\n"
"ds_read_b64_tr_b16 a[198:199], v10 offset:520\n"
"ds_read_b64_tr_b16 a[200:201], v11 offset:0\n"
"ds_read_b64_tr_b16 a[202:203], v11 offset:512\n"
"ds_read_b64_tr_b16 a[204:205], v11 offset:8\n"
"ds_read_b64_tr_b16 a[206:207], v11 offset:520\n"
"ds_read_b64_tr_b16 a[208:209], v10 offset:4096\n"
"ds_read_b64_tr_b16 a[210:211], v10 offset:4608\n"
"ds_read_b64_tr_b16 a[212:213], v10 offset:4104\n"
"ds_read_b64_tr_b16 a[214:215], v10 offset:4616\n"
"ds_read_b64_tr_b16 a[216:217], v11 offset:4096\n"
"ds_read_b64_tr_b16 a[218:219], v11 offset:4608\n"
"ds_read_b64_tr_b16 a[220:221], v11 offset:4104\n"
"ds_read_b64_tr_b16 a[222:223], v11 offset:4616\n"
"ds_read_b128 a[224:227], v14 offset:0\n"
"ds_read_b128 a[228:231], v14 offset:16384\n"
"ds_read_b128 a[232:235], v14 offset:32768\n"
"ds_read_b128 a[236:239], v14 offset:49152\n"
"ds_read_b128 a[240:243], v18 offset:0\n"
"ds_read_b128 a[244:247], v18 offset:16384\n"
"s_waitcnt lgkmcnt(14)\n"
"s_add_u32 s50, s45, 524288\n"
"s_add_u32 m0, s49, 0\n"
"s_nop 0\n"
"buffer_load_dwordx4 v22, s[24:27], s50 offen offset:0 lds\n"
"buffer_load_dwordx4 v23, s[24:27], s50 offen offset:1024 lds\n"
"buffer_load_dwordx4 v24, s[24:27], s50 offen offset:2048 lds\n"
"buffer_load_dwordx4 v25, s[24:27], s50 offen offset:3072 lds\n"
"s_waitcnt lgkmcnt(5)\n"
"v_mfma_f32_16x16x32_bf16 v[64:67], a[192:195], a[224:227], 0\n"
"v_mfma_f32_16x16x32_bf16 v[68:71], a[196:199], a[224:227], 0\n"
"v_mfma_f32_16x16x32_bf16 v[112:115], a[200:203], a[224:227], 0\n"
"v_mfma_f32_16x16x32_bf16 v[116:119], a[204:207], a[224:227], 0\n"
"s_waitcnt lgkmcnt(4)\n"
"v_mfma_f32_16x16x32_bf16 v[72:75], a[192:195], a[228:231], 0\n"
"v_mfma_f32_16x16x32_bf16 v[76:79], a[196:199], a[228:231], 0\n"
"v_mfma_f32_16x16x32_bf16 v[120:123], a[200:203], a[228:231], 0\n"
"v_mfma_f32_16x16x32_bf16 v[124:127], a[204:207], a[228:231], 0\n"
"s_waitcnt lgkmcnt(3)\n"
"v_mfma_f32_16x16x32_bf16 v[80:83], a[192:195], a[232:235], 0\n"
"s_add_u32 s50, s45, 524288\n"
"s_add_u32 m0, s49, 4096\n"
"v_mfma_f32_16x16x32_bf16 v[84:87], a[196:199], a[232:235], 0\n"
"buffer_load_dwordx4 v26, s[24:27], s50 offen offset:0 lds\n"
"buffer_load_dwordx4 v27, s[24:27], s50 offen offset:1024 lds\n"
"v_mfma_f32_16x16x32_bf16 v[128:131], a[200:203], a[232:235], 0\n"
"buffer_load_dwordx4 v28, s[24:27], s50 offen offset:2048 lds\n"
"buffer_load_dwordx4 v29, s[24:27], s50 offen offset:3072 lds\n"
"v_mfma_f32_16x16x32_bf16 v[132:135], a[204:207], a[232:235], 0\n"
"s_waitcnt lgkmcnt(0)\n"
"v_mfma_f32_16x16x32_bf16 v[88:91], a[192:195], a[236:239], 0\n"
"v_mfma_f32_16x16x32_bf16 v[92:95], a[196:199], a[236:239], 0\n"
"v_mfma_f32_16x16x32_bf16 v[136:139], a[200:203], a[236:239], 0\n"
"v_mfma_f32_16x16x32_bf16 v[140:143], a[204:207], a[236:239], 0\n"
"v_mfma_f32_16x16x32_bf16 v[96:99], a[192:195], a[240:243], 0\n"
"v_mfma_f32_16x16x32_bf16 v[100:103], a[196:199], a[240:243], 0\n"
"v_mfma_f32_16x16x32_bf16 v[144:147], a[200:203], a[240:243], 0\n"
"v_mfma_f32_16x16x32_bf16 v[148:151], a[204:207], a[240:243], 0\n"
"s_waitcnt vmcnt(12)\n"
"v_mfma_f32_16x16x32_bf16 v[104:107], a[192:195], a[244:247], 0\n"
"ds_read_b64_tr_b16 a[192:193], v10 offset:8192\n"
"ds_read_b64_tr_b16 a[194:195], v10 offset:8704\n"
"v_mfma_f32_16x16x32_bf16 v[108:111], a[196:199], a[244:247], 0\n"
"ds_read_b64_tr_b16 a[196:197], v10 offset:8200\n"
"ds_read_b64_tr_b16 a[198:199], v10 offset:8712\n"
"v_mfma_f32_16x16x32_bf16 v[152:155], a[200:203], a[244:247], 0\n"
"ds_read_b64_tr_b16 a[200:201], v11 offset:8192\n"
"ds_read_b64_tr_b16 a[202:203], v11 offset:8704\n"
"v_mfma_f32_16x16x32_bf16 v[156:159], a[204:207], a[244:247], 0\n"
"ds_read_b64_tr_b16 a[204:205], v11 offset:8200\n"
"ds_read_b64_tr_b16 a[206:207], v11 offset:8712\n"
"v_mfma_f32_16x16x32_bf16 v[160:163], a[208:211], a[224:227], 0\n"
"v_mfma_f32_16x16x32_bf16 v[164:167], a[212:215], a[224:227], 0\n"
"v_mfma_f32_16x16x32_bf16 v[208:211], a[216:219], a[224:227], 0\n"
"v_mfma_f32_16x16x32_bf16 v[212:215], a[220:223], a[224:227], 0\n"
"ds_read_b128 a[224:227], v15 offset:0\n"
"v_mfma_f32_16x16x32_bf16 v[168:171], a[208:211], a[228:231], 0\n"
"s_waitcnt lgkmcnt(1)\n"
"v_mfma_f32_16x16x32_bf16 v[172:175], a[212:215], a[228:231], 0\n"
"s_add_u32 s50, s45, 786432\n"
"s_add_u32 m0, s49, 8192\n"
"v_mfma_f32_16x16x32_bf16 v[216:219], a[216:219], a[228:231], 0\n"
"buffer_load_dwordx4 v22, s[24:27], s50 offen offset:0 lds\n"
"buffer_load_dwordx4 v23, s[24:27], s50 offen offset:1024 lds\n"
"v_mfma_f32_16x16x32_bf16 v[220:223], a[220:223], a[228:231], 0\n"
"ds_read_b128 a[228:231], v15 offset:16384\n"
"buffer_load_dwordx4 v24, s[24:27], s50 offen offset:2048 lds\n"
"v_mfma_f32_16x16x32_bf16 v[176:179], a[208:211], a[232:235], 0\n"
"buffer_load_dwordx4 v25, s[24:27], s50 offen offset:3072 lds\n"
"v_mfma_f32_16x16x32_bf16 v[180:183], a[212:215], a[232:235], 0\n"
"v_mfma_f32_16x16x32_bf16 v[224:227], a[216:219], a[232:235], 0\n"
"v_mfma_f32_16x16x32_bf16 v[228:231], a[220:223], a[232:235], 0\n"
"ds_read_b128 a[232:235], v15 offset:32768\n"
"v_mfma_f32_16x16x32_bf16 v[184:187], a[208:211], a[236:239], 0\n"
"v_mfma_f32_16x16x32_bf16 v[188:191], a[212:215], a[236:239], 0\n"
"v_mfma_f32_16x16x32_bf16 v[232:235], a[216:219], a[236:239], 0\n"
"v_mfma_f32_16x16x32_bf16 v[236:239], a[220:223], a[236:239], 0\n"
"ds_read_b128 a[236:239], v15 offset:49152\n"
"v_mfma_f32_16x16x32_bf16 v[192:195], a[208:211], a[240:243], 0\n"
"v_mfma_f32_16x16x32_bf16 v[196:199], a[212:215], a[240:243], 0\n"
"v_mfma_f32_16x16x32_bf16 v[240:243], a[216:219], a[240:243], 0\n"
"v_mfma_f32_16x16x32_bf16 v[244:247], a[220:223], a[240:243], 0\n"
"ds_read_b128 a[240:243], v19 offset:0\n"
"s_waitcnt vmcnt(12)\n"
"v_mfma_f32_16x16x32_bf16 v[200:203], a[208:211], a[244:247], 0\n"
"ds_read_b64_tr_b16 a[208:209], v10 offset:12288\n"
"ds_read_b64_tr_b16 a[210:211], v10 offset:12800\n"
"v_mfma_f32_16x16x32_bf16 v[204:207], a[212:215], a[244:247], 0\n"
"ds_read_b64_tr_b16 a[212:213], v10 offset:12296\n"
"ds_read_b64_tr_b16 a[214:215], v10 offset:12808\n"
"v_mfma_f32_16x16x32_bf16 v[248:251], a[216:219], a[244:247], 0\n"
"ds_read_b64_tr_b16 a[216:217], v11 offset:12288\n"
"ds_read_b64_tr_b16 a[218:219], v11 offset:12800\n"
"v_mfma_f32_16x16x32_bf16 v[252:255], a[220:223], a[244:247], 0\n"
"ds_read_b128 a[244:247], v19 offset:16384\n"
"ds_read_b64_tr_b16 a[220:221], v11 offset:12296\n"
"ds_read_b64_tr_b16 a[222:223], v11 offset:12808\n"
"s_waitcnt lgkmcnt(11)\n"
"v_mfma_f32_16x16x32_bf16 v[64:67], a[192:195], a[224:227], v[64:67]\n"
"v_accvgpr_write_b32 a0, 0\n"
"v_mfma_f32_16x16x32_bf16 v[68:71], a[196:199], a[224:227], v[68:71]\n"
"v_mfma_f32_16x16x32_bf16 v[112:115], a[200:203], a[224:227], v[112:115]\n"
"v_accvgpr_write_b32 a1, 0\n"
"v_mfma_f32_16x16x32_bf16 v[116:119], a[204:207], a[224:227], v[116:119]\n"
"v_accvgpr_write_b32 a2, 0\n"
"v_mfma_f32_16x16x32_bf16 v[72:75], a[192:195], a[228:231], v[72:75]\n"
"v_accvgpr_write_b32 a3, 0\n"
"v_mfma_f32_16x16x32_bf16 v[76:79], a[196:199], a[228:231], v[76:79]\n"
"v_mfma_f32_16x16x32_bf16 v[120:123], a[200:203], a[228:231], v[120:123]\n"
"v_accvgpr_write_b32 a4, 0\n"
"v_mfma_f32_16x16x32_bf16 v[124:127], a[204:207], a[228:231], v[124:127]\n"
"s_waitcnt lgkmcnt(0)\n"
"v_accvgpr_write_b32 a5, 0\n"
"v_mfma_f32_16x16x32_bf16 v[80:83], a[192:195], a[232:235], v[80:83]\n"
"s_add_u32 s50, s45, 786432\n"
"s_add_u32 m0, s49, 12288\n"
"v_mfma_f32_16x16x32_bf16 v[84:87], a[196:199], a[232:235], v[84:87]\n"
"buffer_load_dwordx4 v26, s[24:27], s50 offen offset:0 lds\n"
"buffer_load_dwordx4 v27, s[24:27], s50 offen offset:1024 lds\n"
"v_mfma_f32_16x16x32_bf16 v[128:131], a[200:203], a[232:235], v[128:131]\n"
"buffer_load_dwordx4 v28, s[24:27], s50 offen offset:2048 lds\n"
"buffer_load_dwordx4 v29, s[24:27], s50 offen offset:3072 lds\n"
"v_mfma_f32_16x16x32_bf16 v[132:135], a[204:207], a[232:235], v[132:135]\n"
"v_mfma_f32_16x16x32_bf16 v[88:91], a[192:195], a[236:239], v[88:91]\n"
"v_accvgpr_write_b32 a6, 0\n"
"v_mfma_f32_16x16x32_bf16 v[92:95], a[196:199], a[236:239], v[92:95]\n"
"v_accvgpr_write_b32 a7, 0\n"
"v_mfma_f32_16x16x32_bf16 v[136:139], a[200:203], a[236:239], v[136:139]\n"
"v_accvgpr_write_b32 a8, 0\n"
"v_mfma_f32_16x16x32_bf16 v[140:143], a[204:207], a[236:239], v[140:143]\n"
"v_mfma_f32_16x16x32_bf16 v[96:99], a[192:195], a[240:243], v[96:99]\n"
"v_accvgpr_write_b32 a9, 0\n"
"v_mfma_f32_16x16x32_bf16 v[100:103], a[196:199], a[240:243], v[100:103]\n"
"v_accvgpr_write_b32 a10, 0\n"
"v_mfma_f32_16x16x32_bf16 v[144:147], a[200:203], a[240:243], v[144:147]\n"
"v_mfma_f32_16x16x32_bf16 v[148:151], a[204:207], a[240:243], v[148:151]\n"
"s_waitcnt vmcnt(12)\n"
"v_mfma_f32_16x16x32_bf16 v[104:107], a[192:195], a[244:247], v[104:107]\n"
"ds_read_b64_tr_b16 a[192:193], v10 offset:0\n"
"ds_read_b64_tr_b16 a[194:195], v10 offset:512\n"
"v_mfma_f32_16x16x32_bf16 v[108:111], a[196:199], a[244:247], v[108:111]\n"
"ds_read_b64_tr_b16 a[196:197], v10 offset:8\n"
"ds_read_b64_tr_b16 a[198:199], v10 offset:520\n"
"v_mfma_f32_16x16x32_bf16 v[152:155], a[200:203], a[244:247], v[152:155]\n"
"ds_read_b64_tr_b16 a[200:201], v11 offset:0\n"
"ds_read_b64_tr_b16 a[202:203], v11 offset:512\n"
"v_mfma_f32_16x16x32_bf16 v[156:159], a[204:207], a[244:247], v[156:159]\n"
"ds_read_b64_tr_b16 a[204:205], v11 offset:8\n"
"ds_read_b64_tr_b16 a[206:207], v11 offset:520\n"
"v_mfma_f32_16x16x32_bf16 v[160:163], a[208:211], a[224:227], v[160:163]\n"
"v_accvgpr_write_b32 a11, 0\n"
"v_mfma_f32_16x16x32_bf16 v[164:167], a[212:215], a[224:227], v[164:167]\n"
"v_accvgpr_write_b32 a12, 0\n"
"v_mfma_f32_16x16x32_bf16 v[208:211], a[216:219], a[224:227], v[208:211]\n"
"v_accvgpr_write_b32 a13, 0\n"
"v_mfma_f32_16x16x32_bf16 v[212:215], a[220:223], a[224:227], v[212:215]\n"
"ds_read_b128 a[224:227], v16 offset:0\n"
"v_mfma_f32_16x16x32_bf16 v[168:171], a[208:211], a[228:231], v[168:171]\n"
"s_waitcnt lgkmcnt(1)\n"
"v_mfma_f32_16x16x32_bf16 v[172:175], a[212:215], a[228:231], v[172:175]\n"
"s_add_u32 s50, s45, 1048576\n"
"s_add_u32 m0, s49, 0\n"
"v_mfma_f32_16x16x32_bf16 v[216:219], a[216:219], a[228:231], v[216:219]\n"
"buffer_load_dwordx4 v22, s[24:27], s50 offen offset:0 lds\n"
"buffer_load_dwordx4 v23, s[24:27], s50 offen offset:1024 lds\n"
"v_mfma_f32_16x16x32_bf16 v[220:223], a[220:223], a[228:231], v[220:223]\n"
"ds_read_b128 a[228:231], v16 offset:16384\n"
"buffer_load_dwordx4 v24, s[24:27], s50 offen offset:2048 lds\n"
"v_mfma_f32_16x16x32_bf16 v[176:179], a[208:211], a[232:235], v[176:179]\n"
"buffer_load_dwordx4 v25, s[24:27], s50 offen offset:3072 lds\n"
"v_mfma_f32_16x16x32_bf16 v[180:183], a[212:215], a[232:235], v[180:183]\n"
"v_accvgpr_write_b32 a14, 0\n"
"v_mfma_f32_16x16x32_bf16 v[224:227], a[216:219], a[232:235], v[224:227]\n"
"v_accvgpr_write_b32 a15, 0\n"
"v_mfma_f32_16x16x32_bf16 v[228:231], a[220:223], a[232:235], v[228:231]\n"
"ds_read_b128 a[232:235], v16 offset:32768\n"
"v_mfma_f32_16x16x32_bf16 v[184:187], a[208:211], a[236:239], v[184:187]\n"
"v_mfma_f32_16x16x32_bf16 v[188:191], a[212:215], a[236:239], v[188:191]\n"
"v_accvgpr_write_b32 a16, 0\n"
"v_mfma_f32_16x16x32_bf16 v[232:235], a[216:219], a[236:239], v[232:235]\n"
"v_accvgpr_write_b32 a17, 0\n"
"v_mfma_f32_16x16x32_bf16 v[236:239], a[220:223], a[236:239], v[236:239]\n"
"ds_read_b128 a[236:239], v16 offset:49152\n"
"v_mfma_f32_16x16x32_bf16 v[192:195], a[208:211], a[240:243], v[192:195]\n"
"v_accvgpr_write_b32 a18, 0\n"
"v_mfma_f32_16x16x32_bf16 v[196:199], a[212:215], a[240:243], v[196:199]\n"
"v_mfma_f32_16x16x32_bf16 v[240:243], a[216:219], a[240:243], v[240:243]\n"
"v_accvgpr_write_b32 a19, 0\n"
"v_mfma_f32_16x16x32_bf16 v[244:247], a[220:223], a[240:243], v[244:247]\n"
"ds_read_b128 a[240:243], v20 offset:0\n"
"s_waitcnt vmcnt(12)\n"
"v_mfma_f32_16x16x32_bf16 v[200:203], a[208:211], a[244:247], v[200:203]\n"
"ds_read_b64_tr_b16 a[208:209], v10 offset:4096\n"
"ds_read_b64_tr_b16 a[210:211], v10 offset:4608\n"
"v_mfma_f32_16x16x32_bf16 v[204:207], a[212:215], a[244:247], v[204:207]\n"
"ds_read_b64_tr_b16 a[212:213], v10 offset:4104\n"
"ds_read_b64_tr_b16 a[214:215], v10 offset:4616\n"
"v_mfma_f32_16x16x32_bf16 v[248:251], a[216:219], a[244:247], v[248:251]\n"
"ds_read_b64_tr_b16 a[216:217], v11 offset:4096\n"
"ds_read_b64_tr_b16 a[218:219], v11 offset:4608\n"
"v_mfma_f32_16x16x32_bf16 v[252:255], a[220:223], a[244:247], v[252:255]\n"
"ds_read_b128 a[244:247], v20 offset:16384\n"
"ds_read_b64_tr_b16 a[220:221], v11 offset:4104\n"
"ds_read_b64_tr_b16 a[222:223], v11 offset:4616\n"
"s_waitcnt lgkmcnt(11)\n"
"v_mfma_f32_16x16x32_bf16 v[64:67], a[192:195], a[224:227], v[64:67]\n"
"v_accvgpr_write_b32 a20, 0\n"
"v_mfma_f32_16x16x32_bf16 v[68:71], a[196:199], a[224:227], v[68:71]\n"
"v_mfma_f32_16x16x32_bf16 v[112:115], a[200:203], a[224:227], v[112:115]\n"
"v_accvgpr_write_b32 a21, 0\n"
"v_mfma_f32_16x16x32_bf16 v[116:119], a[204:207], a[224:227], v[116:119]\n"
"v_accvgpr_write_b32 a22, 0\n"
"v_mfma_f32_16x16x32_bf16 v[72:75], a[192:195], a[228:231], v[72:75]\n"
"v_accvgpr_write_b32 a23, 0\n"
"v_mfma_f32_16x16x32_bf16 v[76:79], a[196:199], a[228:231], v[76:79]\n"
"v_mfma_f32_16x16x32_bf16 v[120:123], a[200:203], a[228:231], v[120:123]\n"
"v_accvgpr_write_b32 a24, 0\n"
"v_mfma_f32_16x16x32_bf16 v[124:127], a[204:207], a[228:231], v[124:127]\n"
"s_waitcnt lgkmcnt(0)\n"
"v_accvgpr_write_b32 a25, 0\n"
"v_mfma_f32_16x16x32_bf16 v[80:83], a[192:195], a[232:235], v[80:83]\n"
"s_add_u32 s50, s45, 1048576\n"
"s_add_u32 m0, s49, 4096\n"
"v_mfma_f32_16x16x32_bf16 v[84:87], a[196:199], a[232:235], v[84:87]\n"
"buffer_load_dwordx4 v26, s[24:27], s50 offen offset:0 lds\n"
"buffer_load_dwordx4 v27, s[24:27], s50 offen offset:1024 lds\n"
"v_mfma_f32_16x16x32_bf16 v[128:131], a[200:203], a[232:235], v[128:131]\n"
"buffer_load_dwordx4 v28, s[24:27], s50 offen offset:2048 lds\n"
"buffer_load_dwordx4 v29, s[24:27], s50 offen offset:3072 lds\n"
"v_mfma_f32_16x16x32_bf16 v[132:135], a[204:207], a[232:235], v[132:135]\n"
"v_mfma_f32_16x16x32_bf16 v[88:91], a[192:195], a[236:239], v[88:91]\n"
"v_accvgpr_write_b32 a26, 0\n"
"v_mfma_f32_16x16x32_bf16 v[92:95], a[196:199], a[236:239], v[92:95]\n"
"v_accvgpr_write_b32 a27, 0\n"
"v_mfma_f32_16x16x32_bf16 v[136:139], a[200:203], a[236:239], v[136:139]\n"
"v_accvgpr_write_b32 a28, 0\n"
"v_mfma_f32_16x16x32_bf16 v[140:143], a[204:207], a[236:239], v[140:143]\n"
"v_mfma_f32_16x16x32_bf16 v[96:99], a[192:195], a[240:243], v[96:99]\n"
"v_accvgpr_write_b32 a29, 0\n"
"v_mfma_f32_16x16x32_bf16 v[100:103], a[196:199], a[240:243], v[100:103]\n"
"v_accvgpr_write_b32 a30, 0\n"
"v_mfma_f32_16x16x32_bf16 v[144:147], a[200:203], a[240:243], v[144:147]\n"
"v_mfma_f32_16x16x32_bf16 v[148:151], a[204:207], a[240:243], v[148:151]\n"
"s_waitcnt vmcnt(12)\n"
"v_mfma_f32_16x16x32_bf16 v[104:107], a[192:195], a[244:247], v[104:107]\n"
"ds_read_b64_tr_b16 a[192:193], v10 offset:8192\n"
"ds_read_b64_tr_b16 a[194:195], v10 offset:8704\n"
"v_mfma_f32_16x16x32_bf16 v[108:111], a[196:199], a[244:247], v[108:111]\n"
"ds_read_b64_tr_b16 a[196:197], v10 offset:8200\n"
"ds_read_b64_tr_b16 a[198:199], v10 offset:8712\n"
"v_mfma_f32_16x16x32_bf16 v[152:155], a[200:203], a[244:247], v[152:155]\n"
"ds_read_b64_tr_b16 a[200:201], v11 offset:8192\n"
"ds_read_b64_tr_b16 a[202:203], v11 offset:8704\n"
"v_mfma_f32_16x16x32_bf16 v[156:159], a[204:207], a[244:247], v[156:159]\n"
"ds_read_b64_tr_b16 a[204:205], v11 offset:8200\n"
"ds_read_b64_tr_b16 a[206:207], v11 offset:8712\n"
"v_mfma_f32_16x16x32_bf16 v[160:163], a[208:211], a[224:227], v[160:163]\n"
"v_accvgpr_write_b32 a31, 0\n"
"v_mfma_f32_16x16x32_bf16 v[164:167], a[212:215], a[224:227], v[164:167]\n"
"v_accvgpr_write_b32 a32, 0\n"
"v_mfma_f32_16x16x32_bf16 v[208:211], a[216:219], a[224:227], v[208:211]\n"
"v_accvgpr_write_b32 a33, 0\n"
"v_mfma_f32_16x16x32_bf16 v[212:215], a[220:223], a[224:227], v[212:215]\n"
"ds_read_b128 a[224:227], v17 offset:0\n"
"v_mfma_f32_16x16x32_bf16 v[168:171], a[208:211], a[228:231], v[168:171]\n"
"s_waitcnt lgkmcnt(1)\n"
"v_mfma_f32_16x16x32_bf16 v[172:175], a[212:215], a[228:231], v[172:175]\n"
"s_add_u32 s50, s45, 1310720\n"
"s_add_u32 m0, s49, 8192\n"
"v_mfma_f32_16x16x32_bf16 v[216:219], a[216:219], a[228:231], v[216:219]\n"
"buffer_load_dwordx4 v22, s[24:27], s50 offen offset:0 lds\n"
"buffer_load_dwordx4 v23, s[24:27], s50 offen offset:1024 lds\n"
"v_mfma_f32_16x16x32_bf16 v[220:223], a[220:223], a[228:231], v[220:223]\n"
"ds_read_b128 a[228:231], v17 offset:16384\n"
"buffer_load_dwordx4 v24, s[24:27], s50 offen offset:2048 lds\n"
"v_mfma_f32_16x16x32_bf16 v[176:179], a[208:211], a[232:235], v[176:179]\n"
"buffer_load_dwordx4 v25, s[24:27], s50 offen offset:3072 lds\n"
"v_mfma_f32_16x16x32_bf16 v[180:183], a[212:215], a[232:235], v[180:183]\n"
"v_accvgpr_write_b32 a34, 0\n"
"v_mfma_f32_16x16x32_bf16 v[224:227], a[216:219], a[232:235], v[224:227]\n"
"v_accvgpr_write_b32 a35, 0\n"
"v_mfma_f32_16x16x32_bf16 v[228:231], a[220:223], a[232:235], v[228:231]\n"
"ds_read_b128 a[232:235], v17 offset:32768\n"
"v_mfma_f32_16x16x32_bf16 v[184:187], a[208:211], a[236:239], v[184:187]\n"
"v_mfma_f32_16x16x32_bf16 v[188:191], a[212:215], a[236:239], v[188:191]\n"
"v_accvgpr_write_b32 a36, 0\n"
"v_mfma_f32_16x16x32_bf16 v[232:235], a[216:219], a[236:239], v[232:235]\n"
"v_accvgpr_write_b32 a37, 0\n"
"v_mfma_f32_16x16x32_bf16 v[236:239], a[220:223], a[236:239], v[236:239]\n"
"ds_read_b128 a[236:239], v17 offset:49152\n"
"v_mfma_f32_16x16x32_bf16 v[192:195], a[208:211], a[240:243], v[192:195]\n"
"v_accvgpr_write_b32 a38, 0\n"
"v_mfma_f32_16x16x32_bf16 v[196:199], a[212:215], a[240:243], v[196:199]\n"
"v_mfma_f32_16x16x32_bf16 v[240:243], a[216:219], a[240:243], v[240:243]\n"
"v_accvgpr_write_b32 a39, 0\n"
"v_mfma_f32_16x16x32_bf16 v[244:247], a[220:223], a[240:243], v[244:247]\n"
"ds_read_b128 a[240:243], v21 offset:0\n"
"s_waitcnt vmcnt(12)\n"
"v_mfma_f32_16x16x32_bf16 v[200:203], a[208:211], a[244:247], v[200:203]\n"
"ds_read_b64_tr_b16 a[208:209], v10 offset:12288\n"
"ds_read_b64_tr_b16 a[210:211], v10 offset:12800\n"
"v_mfma_f32_16x16x32_bf16 v[204:207], a[212:215], a[244:247], v[204:207]\n"
"ds_read_b64_tr_b16 a[212:213], v10 offset:12296\n"
"ds_read_b64_tr_b16 a[214:215], v10 offset:12808\n"
"v_mfma_f32_16x16x32_bf16 v[248:251], a[216:219], a[244:247], v[248:251]\n"
"ds_read_b64_tr_b16 a[216:217], v11 offset:12288\n"
"ds_read_b64_tr_b16 a[218:219], v11 offset:12800\n"
"v_mfma_f32_16x16x32_bf16 v[252:255], a[220:223], a[244:247], v[252:255]\n"
"ds_read_b128 a[244:247], v21 offset:16384\n"
"ds_read_b64_tr_b16 a[220:221], v11 offset:12296\n"
"ds_read_b64_tr_b16 a[222:223], v11 offset:12808\n"
"s_waitcnt lgkmcnt(11)\n"
"v_mfma_f32_16x16x32_bf16 v[64:67], a[192:195], a[224:227], v[64:67]\n"
"v_accvgpr_write_b32 a40, 0\n"
"v_mfma_f32_16x16x32_bf16 v[68:71], a[196:199], a[224:227], v[68:71]\n"
"v_mfma_f32_16x16x32_bf16 v[112:115], a[200:203], a[224:227], v[112:115]\n"
"v_accvgpr_write_b32 a41, 0\n"
"v_mfma_f32_16x16x32_bf16 v[116:119], a[204:207], a[224:227], v[116:119]\n"
"v_accvgpr_write_b32 a42, 0\n"
"v_mfma_f32_16x16x32_bf16 v[72:75], a[192:195], a[228:231], v[72:75]\n"
"v_accvgpr_write_b32 a43, 0\n"
"v_mfma_f32_16x16x32_bf16 v[76:79], a[196:199], a[228:231], v[76:79]\n"
"v_mfma_f32_16x16x32_bf16 v[120:123], a[200:203], a[228:231], v[120:123]\n"
"v_accvgpr_write_b32 a44, 0\n"
"v_mfma_f32_16x16x32_bf16 v[124:127], a[204:207], a[228:231], v[124:127]\n"
"s_waitcnt lgkmcnt(0)\n"
"v_accvgpr_write_b32 a45, 0\n"
"v_mfma_f32_16x16x32_bf16 v[80:83], a[192:195], a[232:235], v[80:83]\n"
"s_add_u32 s50, s45, 1310720\n"
"s_add_u32 m0, s49, 12288\n"
"v_mfma_f32_16x16x32_bf16 v[84:87], a[196:199], a[232:235], v[84:87]\n"
"buffer_load_dwordx4 v26, s[24:27], s50 offen offset:0 lds\n"
"buffer_load_dwordx4 v27, s[24:27], s50 offen offset:1024 lds\n"
"v_mfma_f32_16x16x32_bf16 v[128:131], a[200:203], a[232:235], v[128:131]\n"
"buffer_load_dwordx4 v28, s[24:27], s50 offen offset:2048 lds\n"
"buffer_load_dwordx4 v29, s[24:27], s50 offen offset:3072 lds\n"
"v_mfma_f32_16x16x32_bf16 v[132:135], a[204:207], a[232:235], v[132:135]\n"
"v_mfma_f32_16x16x32_bf16 v[88:91], a[192:195], a[236:239], v[88:91]\n"
"v_accvgpr_write_b32 a46, 0\n"
"v_mfma_f32_16x16x32_bf16 v[92:95], a[196:199], a[236:239], v[92:95]\n"
"v_accvgpr_write_b32 a47, 0\n"
"v_mfma_f32_16x16x32_bf16 v[136:139], a[200:203], a[236:239], v[136:139]\n"
"v_accvgpr_write_b32 a48, 0\n"
"v_mfma_f32_16x16x32_bf16 v[140:143], a[204:207], a[236:239], v[140:143]\n"
"v_mfma_f32_16x16x32_bf16 v[96:99], a[192:195], a[240:243], v[96:99]\n"
"v_accvgpr_write_b32 a49, 0\n"
"v_mfma_f32_16x16x32_bf16 v[100:103], a[196:199], a[240:243], v[100:103]\n"
"v_accvgpr_write_b32 a50, 0\n"
"v_mfma_f32_16x16x32_bf16 v[144:147], a[200:203], a[240:243], v[144:147]\n"
"v_mfma_f32_16x16x32_bf16 v[148:151], a[204:207], a[240:243], v[148:151]\n"
"s_waitcnt vmcnt(12)\n"
"v_mfma_f32_16x16x32_bf16 v[104:107], a[192:195], a[244:247], v[104:107]\n"
"ds_read_b64_tr_b16 a[192:193], v10 offset:0\n"
"ds_read_b64_tr_b16 a[194:195], v10 offset:512\n"
"v_mfma_f32_16x16x32_bf16 v[108:111], a[196:199], a[244:247], v[108:111]\n"
"ds_read_b64_tr_b16 a[196:197], v10 offset:8\n"
"ds_read_b64_tr_b16 a[198:199], v10 offset:520\n"
"v_mfma_f32_16x16x32_bf16 v[152:155], a[200:203], a[244:247], v[152:155]\n"
"ds_read_b64_tr_b16 a[200:201], v11 offset:0\n"
"ds_read_b64_tr_b16 a[202:203], v11 offset:512\n"
"v_mfma_f32_16x16x32_bf16 v[156:159], a[204:207], a[244:247], v[156:159]\n"
"ds_read_b64_tr_b16 a[204:205], v11 offset:8\n"
"ds_read_b64_tr_b16 a[206:207], v11 offset:520\n"
"v_mfma_f32_16x16x32_bf16 v[160:163], a[208:211], a[224:227], v[160:163]\n"
"v_accvgpr_write_b32 a51, 0\n"
"v_mfma_f32_16x16x32_bf16 v[164:167], a[212:215], a[224:227], v[164:167]\n"
"v_accvgpr_write_b32 a52, 0\n"
"v_mfma_f32_16x16x32_bf16 v[208:211], a[216:219], a[224:227], v[208:211]\n"
"v_accvgpr_write_b32 a53, 0\n"
"v_mfma_f32_16x16x32_bf16 v[212:215], a[220:223], a[224:227], v[212:215]\n"
"ds_read_b128 a[224:227], v14 offset:256\n"
"v_mfma_f32_16x16x32_bf16 v[168:171], a[208:211], a[228:231], v[168:171]\n"
"s_waitcnt lgkmcnt(1)\n"
"v_mfma_f32_16x16x32_bf16 v[172:175], a[212:215], a[228:231], v[172:175]\n"
"s_add_u32 s50, s45, 1572864\n"
"s_add_u32 m0, s49, 0\n"
"v_mfma_f32_16x16x32_bf16 v[216:219], a[216:219], a[228:231], v[216:219]\n"
"buffer_load_dwordx4 v22, s[24:27], s50 offen offset:0 lds\n"
"buffer_load_dwordx4 v23, s[24:27], s50 offen offset:1024 lds\n"
"v_mfma_f32_16x16x32_bf16 v[220:223], a[220:223], a[228:231], v[220:223]\n"
"ds_read_b128 a[228:231], v14 offset:16640\n"
"buffer_load_dwordx4 v24, s[24:27], s50 offen offset:2048 lds\n"
"v_mfma_f32_16x16x32_bf16 v[176:179], a[208:211], a[232:235], v[176:179]\n"
"buffer_load_dwordx4 v25, s[24:27], s50 offen offset:3072 lds\n"
"v_mfma_f32_16x16x32_bf16 v[180:183], a[212:215], a[232:235], v[180:183]\n"
"v_accvgpr_write_b32 a54, 0\n"
"v_mfma_f32_16x16x32_bf16 v[224:227], a[216:219], a[232:235], v[224:227]\n"
"v_accvgpr_write_b32 a55, 0\n"
"v_mfma_f32_16x16x32_bf16 v[228:231], a[220:223], a[232:235], v[228:231]\n"
"ds_read_b128 a[232:235], v14 offset:33024\n"
"v_mfma_f32_16x16x32_bf16 v[184:187], a[208:211], a[236:239], v[184:187]\n"
"v_mfma_f32_16x16x32_bf16 v[188:191], a[212:215], a[236:239], v[188:191]\n"
"v_accvgpr_write_b32 a56, 0\n"
"v_mfma_f32_16x16x32_bf16 v[232:235], a[216:219], a[236:239], v[232:235]\n"
"v_accvgpr_write_b32 a57, 0\n"
"v_mfma_f32_16x16x32_bf16 v[236:239], a[220:223], a[236:239], v[236:239]\n"
"ds_read_b128 a[236:239], v14 offset:49408\n"
"v_mfma_f32_16x16x32_bf16 v[192:195], a[208:211], a[240:243], v[192:195]\n"
"v_accvgpr_write_b32 a58, 0\n"
"v_mfma_f32_16x16x32_bf16 v[196:199], a[212:215], a[240:243], v[196:199]\n"
"v_mfma_f32_16x16x32_bf16 v[240:243], a[216:219], a[240:243], v[240:243]\n"
"v_accvgpr_write_b32 a59, 0\n"
"v_mfma_f32_16x16x32_bf16 v[244:247], a[220:223], a[240:243], v[244:247]\n"
"ds_read_b128 a[240:243], v18 offset:256\n"
"s_waitcnt vmcnt(12)\n"
"v_mfma_f32_16x16x32_bf16 v[200:203], a[208:211], a[244:247], v[200:203]\n"
"ds_read_b64_tr_b16 a[208:209], v10 offset:4096\n"
"ds_read_b64_tr_b16 a[210:211], v10 offset:4608\n"
"v_mfma_f32_16x16x32_bf16 v[204:207], a[212:215], a[244:247], v[204:207]\n"
"ds_read_b64_tr_b16 a[212:213], v10 offset:4104\n"
"ds_read_b64_tr_b16 a[214:215], v10 offset:4616\n"
"v_mfma_f32_16x16x32_bf16 v[248:251], a[216:219], a[244:247], v[248:251]\n"
"ds_read_b64_tr_b16 a[216:217], v11 offset:4096\n"
"ds_read_b64_tr_b16 a[218:219], v11 offset:4608\n"
"v_mfma_f32_16x16x32_bf16 v[252:255], a[220:223], a[244:247], v[252:255]\n"
"ds_read_b128 a[244:247], v18 offset:16640\n"
"ds_read_b64_tr_b16 a[220:221], v11 offset:4104\n"
"ds_read_b64_tr_b16 a[222:223], v11 offset:4616\n"
"s_waitcnt lgkmcnt(11)\n"
"v_mfma_f32_16x16x32_bf16 v[64:67], a[192:195], a[224:227], v[64:67]\n"
"v_accvgpr_write_b32 a60, 0\n"
"v_mfma_f32_16x16x32_bf16 v[68:71], a[196:199], a[224:227], v[68:71]\n"
"v_mfma_f32_16x16x32_bf16 v[112:115], a[200:203], a[224:227], v[112:115]\n"
"v_accvgpr_write_b32 a61, 0\n"
"v_mfma_f32_16x16x32_bf16 v[116:119], a[204:207], a[224:227], v[116:119]\n"
"v_accvgpr_write_b32 a62, 0\n"
"v_mfma_f32_16x16x32_bf16 v[72:75], a[192:195], a[228:231], v[72:75]\n"
"v_accvgpr_write_b32 a63, 0\n"
"v_mfma_f32_16x16x32_bf16 v[76:79], a[196:199], a[228:231], v[76:79]\n"
"v_mfma_f32_16x16x32_bf16 v[120:123], a[200:203], a[228:231], v[120:123]\n"
"v_accvgpr_write_b32 a64, 0\n"
"v_mfma_f32_16x16x32_bf16 v[124:127], a[204:207], a[228:231], v[124:127]\n"
"s_waitcnt lgkmcnt(0)\n"
"v_accvgpr_write_b32 a65, 0\n"
"v_mfma_f32_16x16x32_bf16 v[80:83], a[192:195], a[232:235], v[80:83]\n"
"s_add_u32 s50, s45, 1572864\n"
"s_add_u32 m0, s49, 4096\n"
"v_mfma_f32_16x16x32_bf16 v[84:87], a[196:199], a[232:235], v[84:87]\n"
"buffer_load_dwordx4 v26, s[24:27], s50 offen offset:0 lds\n"
"buffer_load_dwordx4 v27, s[24:27], s50 offen offset:1024 lds\n"
"v_mfma_f32_16x16x32_bf16 v[128:131], a[200:203], a[232:235], v[128:131]\n"
"buffer_load_dwordx4 v28, s[24:27], s50 offen offset:2048 lds\n"
"buffer_load_dwordx4 v29, s[24:27], s50 offen offset:3072 lds\n"
"v_mfma_f32_16x16x32_bf16 v[132:135], a[204:207], a[232:235], v[132:135]\n"
"v_mfma_f32_16x16x32_bf16 v[88:91], a[192:195], a[236:239], v[88:91]\n"
"v_accvgpr_write_b32 a66, 0\n"
"v_mfma_f32_16x16x32_bf16 v[92:95], a[196:199], a[236:239], v[92:95]\n"
"v_accvgpr_write_b32 a67, 0\n"
"v_mfma_f32_16x16x32_bf16 v[136:139], a[200:203], a[236:239], v[136:139]\n"
"v_accvgpr_write_b32 a68, 0\n"
"v_mfma_f32_16x16x32_bf16 v[140:143], a[204:207], a[236:239], v[140:143]\n"
"v_mfma_f32_16x16x32_bf16 v[96:99], a[192:195], a[240:243], v[96:99]\n"
"v_accvgpr_write_b32 a69, 0\n"
"v_mfma_f32_16x16x32_bf16 v[100:103], a[196:199], a[240:243], v[100:103]\n"
"v_accvgpr_write_b32 a70, 0\n"
"v_mfma_f32_16x16x32_bf16 v[144:147], a[200:203], a[240:243], v[144:147]\n"
"v_mfma_f32_16x16x32_bf16 v[148:151], a[204:207], a[240:243], v[148:151]\n"
"s_waitcnt vmcnt(12)\n"
"v_mfma_f32_16x16x32_bf16 v[104:107], a[192:195], a[244:247], v[104:107]\n"
"ds_read_b64_tr_b16 a[192:193], v10 offset:8192\n"
"ds_read_b64_tr_b16 a[194:195], v10 offset:8704\n"
"v_mfma_f32_16x16x32_bf16 v[108:111], a[196:199], a[244:247], v[108:111]\n"
"ds_read_b64_tr_b16 a[196:197], v10 offset:8200\n"
"ds_read_b64_tr_b16 a[198:199], v10 offset:8712\n"
"v_mfma_f32_16x16x32_bf16 v[152:155], a[200:203], a[244:247], v[152:155]\n"
"ds_read_b64_tr_b16 a[200:201], v11 offset:8192\n"
"ds_read_b64_tr_b16 a[202:203], v11 offset:8704\n"
"v_mfma_f32_16x16x32_bf16 v[156:159], a[204:207], a[244:247], v[156:159]\n"
"ds_read_b64_tr_b16 a[204:205], v11 offset:8200\n"
"ds_read_b64_tr_b16 a[206:207], v11 offset:8712\n"
"v_mfma_f32_16x16x32_bf16 v[160:163], a[208:211], a[224:227], v[160:163]\n"
"v_accvgpr_write_b32 a71, 0\n"
"v_mfma_f32_16x16x32_bf16 v[164:167], a[212:215], a[224:227], v[164:167]\n"
"v_accvgpr_write_b32 a72, 0\n"
"v_mfma_f32_16x16x32_bf16 v[208:211], a[216:219], a[224:227], v[208:211]\n"
"v_accvgpr_write_b32 a73, 0\n"
"v_mfma_f32_16x16x32_bf16 v[212:215], a[220:223], a[224:227], v[212:215]\n"
"ds_read_b128 a[224:227], v15 offset:256\n"
"v_mfma_f32_16x16x32_bf16 v[168:171], a[208:211], a[228:231], v[168:171]\n"
"s_waitcnt lgkmcnt(1)\n"
"v_mfma_f32_16x16x32_bf16 v[172:175], a[212:215], a[228:231], v[172:175]\n"
"s_add_u32 s50, s45, 1835008\n"
"s_add_u32 m0, s49, 8192\n"
"v_mfma_f32_16x16x32_bf16 v[216:219], a[216:219], a[228:231], v[216:219]\n"
"buffer_load_dwordx4 v22, s[24:27], s50 offen offset:0 lds\n"
"buffer_load_dwordx4 v23, s[24:27], s50 offen offset:1024 lds\n"
"v_mfma_f32_16x16x32_bf16 v[220:223], a[220:223], a[228:231], v[220:223]\n"
"ds_read_b128 a[228:231], v15 offset:16640\n"
"buffer_load_dwordx4 v24, s[24:27], s50 offen offset:2048 lds\n"
"v_mfma_f32_16x16x32_bf16 v[176:179], a[208:211], a[232:235], v[176:179]\n"
"buffer_load_dwordx4 v25, s[24:27], s50 offen offset:3072 lds\n"
"v_mfma_f32_16x16x32_bf16 v[180:183], a[212:215], a[232:235], v[180:183]\n"
"v_accvgpr_write_b32 a74, 0\n"
"v_mfma_f32_16x16x32_bf16 v[224:227], a[216:219], a[232:235], v[224:227]\n"
"v_accvgpr_write_b32 a75, 0\n"
"v_mfma_f32_16x16x32_bf16 v[228:231], a[220:223], a[232:235], v[228:231]\n"
"ds_read_b128 a[232:235], v15 offset:33024\n"
"v_mfma_f32_16x16x32_bf16 v[184:187], a[208:211], a[236:239], v[184:187]\n"
"v_mfma_f32_16x16x32_bf16 v[188:191], a[212:215], a[236:239], v[188:191]\n"
"v_accvgpr_write_b32 a76, 0\n"
"v_mfma_f32_16x16x32_bf16 v[232:235], a[216:219], a[236:239], v[232:235]\n"
"v_accvgpr_write_b32 a77, 0\n"
"v_mfma_f32_16x16x32_bf16 v[236:239], a[220:223], a[236:239], v[236:239]\n"
"ds_read_b128 a[236:239], v15 offset:49408\n"
"v_mfma_f32_16x16x32_bf16 v[192:195], a[208:211], a[240:243], v[192:195]\n"
"v_accvgpr_write_b32 a78, 0\n"
"v_mfma_f32_16x16x32_bf16 v[196:199], a[212:215], a[240:243], v[196:199]\n"
"v_mfma_f32_16x16x32_bf16 v[240:243], a[216:219], a[240:243], v[240:243]\n"
"v_accvgpr_write_b32 a79, 0\n"
"v_mfma_f32_16x16x32_bf16 v[244:247], a[220:223], a[240:243], v[244:247]\n"
"ds_read_b128 a[240:243], v19 offset:256\n"
"s_waitcnt vmcnt(12)\n"
"v_mfma_f32_16x16x32_bf16 v[200:203], a[208:211], a[244:247], v[200:203]\n"
"ds_read_b64_tr_b16 a[208:209], v10 offset:12288\n"
"ds_read_b64_tr_b16 a[210:211], v10 offset:12800\n"
"v_mfma_f32_16x16x32_bf16 v[204:207], a[212:215], a[244:247], v[204:207]\n"
"ds_read_b64_tr_b16 a[212:213], v10 offset:12296\n"
"ds_read_b64_tr_b16 a[214:215], v10 offset:12808\n"
"v_mfma_f32_16x16x32_bf16 v[248:251], a[216:219], a[244:247], v[248:251]\n"
"ds_read_b64_tr_b16 a[216:217], v11 offset:12288\n"
"ds_read_b64_tr_b16 a[218:219], v11 offset:12800\n"
"v_mfma_f32_16x16x32_bf16 v[252:255], a[220:223], a[244:247], v[252:255]\n"
"ds_read_b128 a[244:247], v19 offset:16640\n"
"ds_read_b64_tr_b16 a[220:221], v11 offset:12296\n"
"ds_read_b64_tr_b16 a[222:223], v11 offset:12808\n"
"s_waitcnt lgkmcnt(11)\n"
"v_mfma_f32_16x16x32_bf16 v[64:67], a[192:195], a[224:227], v[64:67]\n"
"v_accvgpr_write_b32 a80, 0\n"
"v_mfma_f32_16x16x32_bf16 v[68:71], a[196:199], a[224:227], v[68:71]\n"
"v_mfma_f32_16x16x32_bf16 v[112:115], a[200:203], a[224:227], v[112:115]\n"
"v_accvgpr_write_b32 a81, 0\n"
"v_mfma_f32_16x16x32_bf16 v[116:119], a[204:207], a[224:227], v[116:119]\n"
"v_accvgpr_write_b32 a82, 0\n"
"v_mfma_f32_16x16x32_bf16 v[72:75], a[192:195], a[228:231], v[72:75]\n"
"v_accvgpr_write_b32 a83, 0\n"
"v_mfma_f32_16x16x32_bf16 v[76:79], a[196:199], a[228:231], v[76:79]\n"
"v_mfma_f32_16x16x32_bf16 v[120:123], a[200:203], a[228:231], v[120:123]\n"
"v_accvgpr_write_b32 a84, 0\n"
"v_mfma_f32_16x16x32_bf16 v[124:127], a[204:207], a[228:231], v[124:127]\n"
"s_waitcnt lgkmcnt(0)\n"
"v_accvgpr_write_b32 a85, 0\n"
"v_mfma_f32_16x16x32_bf16 v[80:83], a[192:195], a[232:235], v[80:83]\n"
"s_add_u32 s50, s45, 1835008\n"
"s_add_u32 m0, s49, 12288\n"
"v_mfma_f32_16x16x32_bf16 v[84:87], a[196:199], a[232:235], v[84:87]\n"
"buffer_load_dwordx4 v26, s[24:27], s50 offen offset:0 lds\n"
"buffer_load_dwordx4 v27, s[24:27], s50 offen offset:1024 lds\n"
"v_mfma_f32_16x16x32_bf16 v[128:131], a[200:203], a[232:235], v[128:131]\n"
"buffer_load_dwordx4 v28, s[24:27], s50 offen offset:2048 lds\n"
"buffer_load_dwordx4 v29, s[24:27], s50 offen offset:3072 lds\n"
"v_mfma_f32_16x16x32_bf16 v[132:135], a[204:207], a[232:235], v[132:135]\n"
"v_mfma_f32_16x16x32_bf16 v[88:91], a[192:195], a[236:239], v[88:91]\n"
"v_accvgpr_write_b32 a86, 0\n"
"v_mfma_f32_16x16x32_bf16 v[92:95], a[196:199], a[236:239], v[92:95]\n"
"v_accvgpr_write_b32 a87, 0\n"
"v_mfma_f32_16x16x32_bf16 v[136:139], a[200:203], a[236:239], v[136:139]\n"
"v_accvgpr_write_b32 a88, 0\n"
"v_mfma_f32_16x16x32_bf16 v[140:143], a[204:207], a[236:239], v[140:143]\n"
"v_mfma_f32_16x16x32_bf16 v[96:99], a[192:195], a[240:243], v[96:99]\n"
"v_accvgpr_write_b32 a89, 0\n"
"v_mfma_f32_16x16x32_bf16 v[100:103], a[196:199], a[240:243], v[100:103]\n"
"v_accvgpr_write_b32 a90, 0\n"
"v_mfma_f32_16x16x32_bf16 v[144:147], a[200:203], a[240:243], v[144:147]\n"
"v_mfma_f32_16x16x32_bf16 v[148:151], a[204:207], a[240:243], v[148:151]\n"
"s_waitcnt vmcnt(12)\n"
"v_mfma_f32_16x16x32_bf16 v[104:107], a[192:195], a[244:247], v[104:107]\n"
"ds_read_b64_tr_b16 a[192:193], v10 offset:0\n"
"ds_read_b64_tr_b16 a[194:195], v10 offset:512\n"
"v_mfma_f32_16x16x32_bf16 v[108:111], a[196:199], a[244:247], v[108:111]\n"
"ds_read_b64_tr_b16 a[196:197], v10 offset:8\n"
"ds_read_b64_tr_b16 a[198:199], v10 offset:520\n"
"v_mfma_f32_16x16x32_bf16 v[152:155], a[200:203], a[244:247], v[152:155]\n"
"ds_read_b64_tr_b16 a[200:201], v11 offset:0\n"
"ds_read_b64_tr_b16 a[202:203], v11 offset:512\n"
"v_mfma_f32_16x16x32_bf16 v[156:159], a[204:207], a[244:247], v[156:159]\n"
"ds_read_b64_tr_b16 a[204:205], v11 offset:8\n"
"ds_read_b64_tr_b16 a[206:207], v11 offset:520\n"
"v_mfma_f32_16x16x32_bf16 v[160:163], a[208:211], a[224:227], v[160:163]\n"
"v_accvgpr_write_b32 a91, 0\n"
"v_mfma_f32_16x16x32_bf16 v[164:167], a[212:215], a[224:227], v[164:167]\n"
"v_accvgpr_write_b32 a92, 0\n"
"v_mfma_f32_16x16x32_bf16 v[208:211], a[216:219], a[224:227], v[208:211]\n"
"v_accvgpr_write_b32 a93, 0\n"
"v_mfma_f32_16x16x32_bf16 v[212:215], a[220:223], a[224:227], v[212:215]\n"
"ds_read_b128 a[224:227], v16 offset:256\n"
"v_mfma_f32_16x16x32_bf16 v[168:171], a[208:211], a[228:231], v[168:171]\n"
"s_waitcnt lgkmcnt(1)\n"
"v_mfma_f32_16x16x32_bf16 v[172:175], a[212:215], a[228:231], v[172:175]\n"
"s_add_u32 s50, s45, 2097152\n"
"s_add_u32 m0, s49, 0\n"
"v_mfma_f32_16x16x32_bf16 v[216:219], a[216:219], a[228:231], v[216:219]\n"
"buffer_load_dwordx4 v22, s[24:27], s50 offen offset:0 lds\n"
"buffer_load_dwordx4 v23, s[24:27], s50 offen offset:1024 lds\n"
"v_mfma_f32_16x16x32_bf16 v[220:223], a[220:223], a[228:231], v[220:223]\n"
"ds_read_b128 a[228:231], v16 offset:16640\n"
"buffer_load_dwordx4 v24, s[24:27], s50 offen offset:2048 lds\n"
"v_mfma_f32_16x16x32_bf16 v[176:179], a[208:211], a[232:235], v[176:179]\n"
"buffer_load_dwordx4 v25, s[24:27], s50 offen offset:3072 lds\n"
"v_mfma_f32_16x16x32_bf16 v[180:183], a[212:215], a[232:235], v[180:183]\n"
"v_accvgpr_write_b32 a94, 0\n"
"v_mfma_f32_16x16x32_bf16 v[224:227], a[216:219], a[232:235], v[224:227]\n"
"v_accvgpr_write_b32 a95, 0\n"
"v_mfma_f32_16x16x32_bf16 v[228:231], a[220:223], a[232:235], v[228:231]\n"
"ds_read_b128 a[232:235], v16 offset:33024\n"
"v_mfma_f32_16x16x32_bf16 v[184:187], a[208:211], a[236:239], v[184:187]\n"
"v_mfma_f32_16x16x32_bf16 v[188:191], a[212:215], a[236:239], v[188:191]\n"
"v_accvgpr_write_b32 a96, 0\n"
"v_mfma_f32_16x16x32_bf16 v[232:235], a[216:219], a[236:239], v[232:235]\n"
"v_accvgpr_write_b32 a97, 0\n"
"v_mfma_f32_16x16x32_bf16 v[236:239], a[220:223], a[236:239], v[236:239]\n"
"ds_read_b128 a[236:239], v16 offset:49408\n"
"v_mfma_f32_16x16x32_bf16 v[192:195], a[208:211], a[240:243], v[192:195]\n"
"v_accvgpr_write_b32 a98, 0\n"
"v_mfma_f32_16x16x32_bf16 v[196:199], a[212:215], a[240:243], v[196:199]\n"
"v_mfma_f32_16x16x32_bf16 v[240:243], a[216:219], a[240:243], v[240:243]\n"
"v_accvgpr_write_b32 a99, 0\n"
"v_mfma_f32_16x16x32_bf16 v[244:247], a[220:223], a[240:243], v[244:247]\n"
"ds_read_b128 a[240:243], v20 offset:256\n"
"s_waitcnt vmcnt(12)\n"
"v_mfma_f32_16x16x32_bf16 v[200:203], a[208:211], a[244:247], v[200:203]\n"
"ds_read_b64_tr_b16 a[208:209], v10 offset:4096\n"
"ds_read_b64_tr_b16 a[210:211], v10 offset:4608\n"
"v_mfma_f32_16x16x32_bf16 v[204:207], a[212:215], a[244:247], v[204:207]\n"
"ds_read_b64_tr_b16 a[212:213], v10 offset:4104\n"
"ds_read_b64_tr_b16 a[214:215], v10 offset:4616\n"
"v_mfma_f32_16x16x32_bf16 v[248:251], a[216:219], a[244:247], v[248:251]\n"
"ds_read_b64_tr_b16 a[216:217], v11 offset:4096\n"
"ds_read_b64_tr_b16 a[218:219], v11 offset:4608\n"
"v_mfma_f32_16x16x32_bf16 v[252:255], a[220:223], a[244:247], v[252:255]\n"
"ds_read_b128 a[244:247], v20 offset:16640\n"
"ds_read_b64_tr_b16 a[220:221], v11 offset:4104\n"
"ds_read_b64_tr_b16 a[222:223], v11 offset:4616\n"
"s_waitcnt lgkmcnt(11)\n"
"v_mfma_f32_16x16x32_bf16 v[64:67], a[192:195], a[224:227], v[64:67]\n"
"v_accvgpr_write_b32 a100, 0\n"
"v_mfma_f32_16x16x32_bf16 v[68:71], a[196:199], a[224:227], v[68:71]\n"
"v_mfma_f32_16x16x32_bf16 v[112:115], a[200:203], a[224:227], v[112:115]\n"
"v_accvgpr_write_b32 a101, 0\n"
"v_mfma_f32_16x16x32_bf16 v[116:119], a[204:207], a[224:227], v[116:119]\n"
"v_accvgpr_write_b32 a102, 0\n"
"v_mfma_f32_16x16x32_bf16 v[72:75], a[192:195], a[228:231], v[72:75]\n"
"v_accvgpr_write_b32 a103, 0\n"
"v_mfma_f32_16x16x32_bf16 v[76:79], a[196:199], a[228:231], v[76:79]\n"
"v_mfma_f32_16x16x32_bf16 v[120:123], a[200:203], a[228:231], v[120:123]\n"
"v_accvgpr_write_b32 a104, 0\n"
"v_mfma_f32_16x16x32_bf16 v[124:127], a[204:207], a[228:231], v[124:127]\n"
"s_waitcnt lgkmcnt(0)\n"
"v_accvgpr_write_b32 a105, 0\n"
"v_mfma_f32_16x16x32_bf16 v[80:83], a[192:195], a[232:235], v[80:83]\n"
"s_add_u32 s50, s45, 2097152\n"
"s_add_u32 m0, s49, 4096\n"
"v_mfma_f32_16x16x32_bf16 v[84:87], a[196:199], a[232:235], v[84:87]\n"
"buffer_load_dwordx4 v26, s[24:27], s50 offen offset:0 lds\n"
"buffer_load_dwordx4 v27, s[24:27], s50 offen offset:1024 lds\n"
"v_mfma_f32_16x16x32_bf16 v[128:131], a[200:203], a[232:235], v[128:131]\n"
"buffer_load_dwordx4 v28, s[24:27], s50 offen offset:2048 lds\n"
"buffer_load_dwordx4 v29, s[24:27], s50 offen offset:3072 lds\n"
"v_mfma_f32_16x16x32_bf16 v[132:135], a[204:207], a[232:235], v[132:135]\n"
"v_mfma_f32_16x16x32_bf16 v[88:91], a[192:195], a[236:239], v[88:91]\n"
"v_accvgpr_write_b32 a106, 0\n"
"v_mfma_f32_16x16x32_bf16 v[92:95], a[196:199], a[236:239], v[92:95]\n"
"v_accvgpr_write_b32 a107, 0\n"
"v_mfma_f32_16x16x32_bf16 v[136:139], a[200:203], a[236:239], v[136:139]\n"
"v_accvgpr_write_b32 a108, 0\n"
"v_mfma_f32_16x16x32_bf16 v[140:143], a[204:207], a[236:239], v[140:143]\n"
"v_mfma_f32_16x16x32_bf16 v[96:99], a[192:195], a[240:243], v[96:99]\n"
"v_accvgpr_write_b32 a109, 0\n"
"v_mfma_f32_16x16x32_bf16 v[100:103], a[196:199], a[240:243], v[100:103]\n"
"v_accvgpr_write_b32 a110, 0\n"
"v_mfma_f32_16x16x32_bf16 v[144:147], a[200:203], a[240:243], v[144:147]\n"
"v_mfma_f32_16x16x32_bf16 v[148:151], a[204:207], a[240:243], v[148:151]\n"
"s_waitcnt vmcnt(12)\n"
"v_mfma_f32_16x16x32_bf16 v[104:107], a[192:195], a[244:247], v[104:107]\n"
"ds_read_b64_tr_b16 a[192:193], v10 offset:8192\n"
"ds_read_b64_tr_b16 a[194:195], v10 offset:8704\n"
"v_mfma_f32_16x16x32_bf16 v[108:111], a[196:199], a[244:247], v[108:111]\n"
"ds_read_b64_tr_b16 a[196:197], v10 offset:8200\n"
"ds_read_b64_tr_b16 a[198:199], v10 offset:8712\n"
"v_mfma_f32_16x16x32_bf16 v[152:155], a[200:203], a[244:247], v[152:155]\n"
"ds_read_b64_tr_b16 a[200:201], v11 offset:8192\n"
"ds_read_b64_tr_b16 a[202:203], v11 offset:8704\n"
"v_mfma_f32_16x16x32_bf16 v[156:159], a[204:207], a[244:247], v[156:159]\n"
"ds_read_b64_tr_b16 a[204:205], v11 offset:8200\n"
"ds_read_b64_tr_b16 a[206:207], v11 offset:8712\n"
"v_mfma_f32_16x16x32_bf16 v[160:163], a[208:211], a[224:227], v[160:163]\n"
"v_accvgpr_write_b32 a111, 0\n"
"v_mfma_f32_16x16x32_bf16 v[164:167], a[212:215], a[224:227], v[164:167]\n"
"v_accvgpr_write_b32 a112, 0\n"
"v_mfma_f32_16x16x32_bf16 v[208:211], a[216:219], a[224:227], v[208:211]\n"
"v_accvgpr_write_b32 a113, 0\n"
"v_mfma_f32_16x16x32_bf16 v[212:215], a[220:223], a[224:227], v[212:215]\n"
"ds_read_b128 a[224:227], v17 offset:256\n"
"v_mfma_f32_16x16x32_bf16 v[168:171], a[208:211], a[228:231], v[168:171]\n"
"s_waitcnt lgkmcnt(1)\n"
"v_mfma_f32_16x16x32_bf16 v[172:175], a[212:215], a[228:231], v[172:175]\n"
"s_add_u32 s50, s45, 2359296\n"
"s_add_u32 m0, s49, 8192\n"
"v_mfma_f32_16x16x32_bf16 v[216:219], a[216:219], a[228:231], v[216:219]\n"
"buffer_load_dwordx4 v22, s[24:27], s50 offen offset:0 lds\n"
"buffer_load_dwordx4 v23, s[24:27], s50 offen offset:1024 lds\n"
"v_mfma_f32_16x16x32_bf16 v[220:223], a[220:223], a[228:231], v[220:223]\n"
"ds_read_b128 a[228:231], v17 offset:16640\n"
"buffer_load_dwordx4 v24, s[24:27], s50 offen offset:2048 lds\n"
"v_mfma_f32_16x16x32_bf16 v[176:179], a[208:211], a[232:235], v[176:179]\n"
"buffer_load_dwordx4 v25, s[24:27], s50 offen offset:3072 lds\n"
"v_mfma_f32_16x16x32_bf16 v[180:183], a[212:215], a[232:235], v[180:183]\n"
"v_accvgpr_write_b32 a114, 0\n"
"v_mfma_f32_16x16x32_bf16 v[224:227], a[216:219], a[232:235], v[224:227]\n"
"v_accvgpr_write_b32 a115, 0\n"
"v_mfma_f32_16x16x32_bf16 v[228:231], a[220:223], a[232:235], v[228:231]\n"
"ds_read_b128 a[232:235], v17 offset:33024\n"
"v_mfma_f32_16x16x32_bf16 v[184:187], a[208:211], a[236:239], v[184:187]\n"
"v_mfma_f32_16x16x32_bf16 v[188:191], a[212:215], a[236:239], v[188:191]\n"
"v_accvgpr_write_b32 a116, 0\n"
"v_mfma_f32_16x16x32_bf16 v[232:235], a[216:219], a[236:239], v[232:235]\n"
"v_accvgpr_write_b32 a117, 0\n"
"v_mfma_f32_16x16x32_bf16 v[236:239], a[220:223], a[236:239], v[236:239]\n"
"ds_read_b128 a[236:239], v17 offset:49408\n"
"v_mfma_f32_16x16x32_bf16 v[192:195], a[208:211], a[240:243], v[192:195]\n"
"v_accvgpr_write_b32 a118, 0\n"
"v_mfma_f32_16x16x32_bf16 v[196:199], a[212:215], a[240:243], v[196:199]\n"
"v_mfma_f32_16x16x32_bf16 v[240:243], a[216:219], a[240:243], v[240:243]\n"
"v_accvgpr_write_b32 a119, 0\n"
"v_mfma_f32_16x16x32_bf16 v[244:247], a[220:223], a[240:243], v[244:247]\n"
"ds_read_b128 a[240:243], v21 offset:256\n"
"s_waitcnt vmcnt(12)\n"
"v_mfma_f32_16x16x32_bf16 v[200:203], a[208:211], a[244:247], v[200:203]\n"
"ds_read_b64_tr_b16 a[208:209], v10 offset:12288\n"
"ds_read_b64_tr_b16 a[210:211], v10 offset:12800\n"
"v_mfma_f32_16x16x32_bf16 v[204:207], a[212:215], a[244:247], v[204:207]\n"
"ds_read_b64_tr_b16 a[212:213], v10 offset:12296\n"
"ds_read_b64_tr_b16 a[214:215], v10 offset:12808\n"
"v_mfma_f32_16x16x32_bf16 v[248:251], a[216:219], a[244:247], v[248:251]\n"
"ds_read_b64_tr_b16 a[216:217], v11 offset:12288\n"
"ds_read_b64_tr_b16 a[218:219], v11 offset:12800\n"
"v_mfma_f32_16x16x32_bf16 v[252:255], a[220:223], a[244:247], v[252:255]\n"
"ds_read_b128 a[244:247], v21 offset:16640\n"
"ds_read_b64_tr_b16 a[220:221], v11 offset:12296\n"
"ds_read_b64_tr_b16 a[222:223], v11 offset:12808\n"
"s_waitcnt lgkmcnt(11)\n"
"v_mfma_f32_16x16x32_bf16 v[64:67], a[192:195], a[224:227], v[64:67]\n"
"v_accvgpr_write_b32 a120, 0\n"
"v_mfma_f32_16x16x32_bf16 v[68:71], a[196:199], a[224:227], v[68:71]\n"
"v_mfma_f32_16x16x32_bf16 v[112:115], a[200:203], a[224:227], v[112:115]\n"
"v_accvgpr_write_b32 a121, 0\n"
"v_mfma_f32_16x16x32_bf16 v[116:119], a[204:207], a[224:227], v[116:119]\n"
"v_accvgpr_write_b32 a122, 0\n"
"v_mfma_f32_16x16x32_bf16 v[72:75], a[192:195], a[228:231], v[72:75]\n"
"v_accvgpr_write_b32 a123, 0\n"
"v_mfma_f32_16x16x32_bf16 v[76:79], a[196:199], a[228:231], v[76:79]\n"
"v_mfma_f32_16x16x32_bf16 v[120:123], a[200:203], a[228:231], v[120:123]\n"
"v_accvgpr_write_b32 a124, 0\n"
"v_mfma_f32_16x16x32_bf16 v[124:127], a[204:207], a[228:231], v[124:127]\n"
"s_waitcnt lgkmcnt(0)\n"
"v_accvgpr_write_b32 a125, 0\n"
"v_mfma_f32_16x16x32_bf16 v[80:83], a[192:195], a[232:235], v[80:83]\n"
"s_add_u32 s50, s45, 2359296\n"
"s_add_u32 m0, s49, 12288\n"
"v_mfma_f32_16x16x32_bf16 v[84:87], a[196:199], a[232:235], v[84:87]\n"
"buffer_load_dwordx4 v26, s[24:27], s50 offen offset:0 lds\n"
"buffer_load_dwordx4 v27, s[24:27], s50 offen offset:1024 lds\n"
"v_mfma_f32_16x16x32_bf16 v[128:131], a[200:203], a[232:235], v[128:131]\n"
"buffer_load_dwordx4 v28, s[24:27], s50 offen offset:2048 lds\n"
"buffer_load_dwordx4 v29, s[24:27], s50 offen offset:3072 lds\n"
"v_mfma_f32_16x16x32_bf16 v[132:135], a[204:207], a[232:235], v[132:135]\n"
"v_mfma_f32_16x16x32_bf16 v[88:91], a[192:195], a[236:239], v[88:91]\n"
"v_accvgpr_write_b32 a126, 0\n"
"v_mfma_f32_16x16x32_bf16 v[92:95], a[196:199], a[236:239], v[92:95]\n"
"v_accvgpr_write_b32 a127, 0\n"
"v_mfma_f32_16x16x32_bf16 v[136:139], a[200:203], a[236:239], v[136:139]\n"
"v_accvgpr_write_b32 a128, 0\n"
"v_mfma_f32_16x16x32_bf16 v[140:143], a[204:207], a[236:239], v[140:143]\n"
"v_mfma_f32_16x16x32_bf16 v[96:99], a[192:195], a[240:243], v[96:99]\n"
"v_accvgpr_write_b32 a129, 0\n"
"v_mfma_f32_16x16x32_bf16 v[100:103], a[196:199], a[240:243], v[100:103]\n"
"v_accvgpr_write_b32 a130, 0\n"
"v_mfma_f32_16x16x32_bf16 v[144:147], a[200:203], a[240:243], v[144:147]\n"
"v_mfma_f32_16x16x32_bf16 v[148:151], a[204:207], a[240:243], v[148:151]\n"
"s_waitcnt vmcnt(12)\n"
"v_mfma_f32_16x16x32_bf16 v[104:107], a[192:195], a[244:247], v[104:107]\n"
"ds_read_b64_tr_b16 a[192:193], v10 offset:0\n"
"ds_read_b64_tr_b16 a[194:195], v10 offset:512\n"
"v_mfma_f32_16x16x32_bf16 v[108:111], a[196:199], a[244:247], v[108:111]\n"
"ds_read_b64_tr_b16 a[196:197], v10 offset:8\n"
"ds_read_b64_tr_b16 a[198:199], v10 offset:520\n"
"v_mfma_f32_16x16x32_bf16 v[152:155], a[200:203], a[244:247], v[152:155]\n"
"ds_read_b64_tr_b16 a[200:201], v11 offset:0\n"
"ds_read_b64_tr_b16 a[202:203], v11 offset:512\n"
"v_mfma_f32_16x16x32_bf16 v[156:159], a[204:207], a[244:247], v[156:159]\n"
"ds_read_b64_tr_b16 a[204:205], v11 offset:8\n"
"ds_read_b64_tr_b16 a[206:207], v11 offset:520\n"
"v_mfma_f32_16x16x32_bf16 v[160:163], a[208:211], a[224:227], v[160:163]\n"
"v_accvgpr_write_b32 a131, 0\n"
"v_mfma_f32_16x16x32_bf16 v[164:167], a[212:215], a[224:227], v[164:167]\n"
"v_accvgpr_write_b32 a132, 0\n"
"v_mfma_f32_16x16x32_bf16 v[208:211], a[216:219], a[224:227], v[208:211]\n"
"v_accvgpr_write_b32 a133, 0\n"
"v_mfma_f32_16x16x32_bf16 v[212:215], a[220:223], a[224:227], v[212:215]\n"
"ds_read_b128 a[224:227], v14 offset:512\n"
"v_mfma_f32_16x16x32_bf16 v[168:171], a[208:211], a[228:231], v[168:171]\n"
"s_waitcnt lgkmcnt(1)\n"
"v_mfma_f32_16x16x32_bf16 v[172:175], a[212:215], a[228:231], v[172:175]\n"
"s_add_u32 s50, s45, 2621440\n"
"s_add_u32 m0, s49, 0\n"
"v_mfma_f32_16x16x32_bf16 v[216:219], a[216:219], a[228:231], v[216:219]\n"
"buffer_load_dwordx4 v22, s[24:27], s50 offen offset:0 lds\n"
"buffer_load_dwordx4 v23, s[24:27], s50 offen offset:1024 lds\n"
"v_mfma_f32_16x16x32_bf16 v[220:223], a[220:223], a[228:231], v[220:223]\n"
"ds_read_b128 a[228:231], v14 offset:16896\n"
"buffer_load_dwordx4 v24, s[24:27], s50 offen offset:2048 lds\n"
"v_mfma_f32_16x16x32_bf16 v[176:179], a[208:211], a[232:235], v[176:179]\n"
"buffer_load_dwordx4 v25, s[24:27], s50 offen offset:3072 lds\n"
"v_mfma_f32_16x16x32_bf16 v[180:183], a[212:215], a[232:235], v[180:183]\n"
"v_accvgpr_write_b32 a134, 0\n"
"v_mfma_f32_16x16x32_bf16 v[224:227], a[216:219], a[232:235], v[224:227]\n"
"v_accvgpr_write_b32 a135, 0\n"
"v_mfma_f32_16x16x32_bf16 v[228:231], a[220:223], a[232:235], v[228:231]\n"
"ds_read_b128 a[232:235], v14 offset:33280\n"
"v_mfma_f32_16x16x32_bf16 v[184:187], a[208:211], a[236:239], v[184:187]\n"
"v_mfma_f32_16x16x32_bf16 v[188:191], a[212:215], a[236:239], v[188:191]\n"
"v_accvgpr_write_b32 a136, 0\n"
"v_mfma_f32_16x16x32_bf16 v[232:235], a[216:219], a[236:239], v[232:235]\n"
"v_accvgpr_write_b32 a137, 0\n"
"v_mfma_f32_16x16x32_bf16 v[236:239], a[220:223], a[236:239], v[236:239]\n"
"ds_read_b128 a[236:239], v14 offset:49664\n"
"v_mfma_f32_16x16x32_bf16 v[192:195], a[208:211], a[240:243], v[192:195]\n"
"v_accvgpr_write_b32 a138, 0\n"
"v_mfma_f32_16x16x32_bf16 v[196:199], a[212:215], a[240:243], v[196:199]\n"
"v_mfma_f32_16x16x32_bf16 v[240:243], a[216:219], a[240:243], v[240:243]\n"
"v_accvgpr_write_b32 a139, 0\n"
"v_mfma_f32_16x16x32_bf16 v[244:247], a[220:223], a[240:243], v[244:247]\n"
"ds_read_b128 a[240:243], v18 offset:512\n"
"s_waitcnt vmcnt(12)\n"
"v_mfma_f32_16x16x32_bf16 v[200:203], a[208:211], a[244:247], v[200:203]\n"
"ds_read_b64_tr_b16 a[208:209], v10 offset:4096\n"
"ds_read_b64_tr_b16 a[210:211], v10 offset:4608\n"
"v_mfma_f32_16x16x32_bf16 v[204:207], a[212:215], a[244:247], v[204:207]\n"
"ds_read_b64_tr_b16 a[212:213], v10 offset:4104\n"
"ds_read_b64_tr_b16 a[214:215], v10 offset:4616\n"
"v_mfma_f32_16x16x32_bf16 v[248:251], a[216:219], a[244:247], v[248:251]\n"
"ds_read_b64_tr_b16 a[216:217], v11 offset:4096\n"
"ds_read_b64_tr_b16 a[218:219], v11 offset:4608\n"
"v_mfma_f32_16x16x32_bf16 v[252:255], a[220:223], a[244:247], v[252:255]\n"
"ds_read_b128 a[244:247], v18 offset:16896\n"
"ds_read_b64_tr_b16 a[220:221], v11 offset:4104\n"
"ds_read_b64_tr_b16 a[222:223], v11 offset:4616\n"
"s_waitcnt lgkmcnt(11)\n"
"v_mfma_f32_16x16x32_bf16 v[64:67], a[192:195], a[224:227], v[64:67]\n"
"v_accvgpr_write_b32 a140, 0\n"
"v_mfma_f32_16x16x32_bf16 v[68:71], a[196:199], a[224:227], v[68:71]\n"
"v_mfma_f32_16x16x32_bf16 v[112:115], a[200:203], a[224:227], v[112:115]\n"
"v_accvgpr_write_b32 a141, 0\n"
"v_mfma_f32_16x16x32_bf16 v[116:119], a[204:207], a[224:227], v[116:119]\n"
"v_accvgpr_write_b32 a142, 0\n"
"v_mfma_f32_16x16x32_bf16 v[72:75], a[192:195], a[228:231], v[72:75]\n"
"v_accvgpr_write_b32 a143, 0\n"
"v_mfma_f32_16x16x32_bf16 v[76:79], a[196:199], a[228:231], v[76:79]\n"
"v_mfma_f32_16x16x32_bf16 v[120:123], a[200:203], a[228:231], v[120:123]\n"
"v_accvgpr_write_b32 a144, 0\n"
"v_mfma_f32_16x16x32_bf16 v[124:127], a[204:207], a[228:231], v[124:127]\n"
"s_waitcnt lgkmcnt(0)\n"
"v_accvgpr_write_b32 a145, 0\n"
"v_mfma_f32_16x16x32_bf16 v[80:83], a[192:195], a[232:235], v[80:83]\n"
"s_add_u32 s50, s45, 2621440\n"
"s_add_u32 m0, s49, 4096\n"
"v_mfma_f32_16x16x32_bf16 v[84:87], a[196:199], a[232:235], v[84:87]\n"
"buffer_load_dwordx4 v26, s[24:27], s50 offen offset:0 lds\n"
"buffer_load_dwordx4 v27, s[24:27], s50 offen offset:1024 lds\n"
"v_mfma_f32_16x16x32_bf16 v[128:131], a[200:203], a[232:235], v[128:131]\n"
"buffer_load_dwordx4 v28, s[24:27], s50 offen offset:2048 lds\n"
"buffer_load_dwordx4 v29, s[24:27], s50 offen offset:3072 lds\n"
"v_mfma_f32_16x16x32_bf16 v[132:135], a[204:207], a[232:235], v[132:135]\n"
"v_mfma_f32_16x16x32_bf16 v[88:91], a[192:195], a[236:239], v[88:91]\n"
"v_accvgpr_write_b32 a146, 0\n"
"v_mfma_f32_16x16x32_bf16 v[92:95], a[196:199], a[236:239], v[92:95]\n"
"v_accvgpr_write_b32 a147, 0\n"
"v_mfma_f32_16x16x32_bf16 v[136:139], a[200:203], a[236:239], v[136:139]\n"
"v_accvgpr_write_b32 a148, 0\n"
"v_mfma_f32_16x16x32_bf16 v[140:143], a[204:207], a[236:239], v[140:143]\n"
"v_mfma_f32_16x16x32_bf16 v[96:99], a[192:195], a[240:243], v[96:99]\n"
"v_accvgpr_write_b32 a149, 0\n"
"v_mfma_f32_16x16x32_bf16 v[100:103], a[196:199], a[240:243], v[100:103]\n"
"v_accvgpr_write_b32 a150, 0\n"
"v_mfma_f32_16x16x32_bf16 v[144:147], a[200:203], a[240:243], v[144:147]\n"
"v_mfma_f32_16x16x32_bf16 v[148:151], a[204:207], a[240:243], v[148:151]\n"
"s_waitcnt vmcnt(12)\n"
"v_mfma_f32_16x16x32_bf16 v[104:107], a[192:195], a[244:247], v[104:107]\n"
"ds_read_b64_tr_b16 a[192:193], v10 offset:8192\n"
"ds_read_b64_tr_b16 a[194:195], v10 offset:8704\n"
"v_mfma_f32_16x16x32_bf16 v[108:111], a[196:199], a[244:247], v[108:111]\n"
"ds_read_b64_tr_b16 a[196:197], v10 offset:8200\n"
"ds_read_b64_tr_b16 a[198:199], v10 offset:8712\n"
"v_mfma_f32_16x16x32_bf16 v[152:155], a[200:203], a[244:247], v[152:155]\n"
"ds_read_b64_tr_b16 a[200:201], v11 offset:8192\n"
"ds_read_b64_tr_b16 a[202:203], v11 offset:8704\n"
"v_mfma_f32_16x16x32_bf16 v[156:159], a[204:207], a[244:247], v[156:159]\n"
"ds_read_b64_tr_b16 a[204:205], v11 offset:8200\n"
"ds_read_b64_tr_b16 a[206:207], v11 offset:8712\n"
"v_mfma_f32_16x16x32_bf16 v[160:163], a[208:211], a[224:227], v[160:163]\n"
"v_accvgpr_write_b32 a151, 0\n"
"v_mfma_f32_16x16x32_bf16 v[164:167], a[212:215], a[224:227], v[164:167]\n"
"v_accvgpr_write_b32 a152, 0\n"
"v_mfma_f32_16x16x32_bf16 v[208:211], a[216:219], a[224:227], v[208:211]\n"
"v_accvgpr_write_b32 a153, 0\n"
"v_mfma_f32_16x16x32_bf16 v[212:215], a[220:223], a[224:227], v[212:215]\n"
"ds_read_b128 a[224:227], v15 offset:512\n"
"v_mfma_f32_16x16x32_bf16 v[168:171], a[208:211], a[228:231], v[168:171]\n"
"s_waitcnt lgkmcnt(1)\n"
"v_mfma_f32_16x16x32_bf16 v[172:175], a[212:215], a[228:231], v[172:175]\n"
"s_add_u32 s50, s45, 2883584\n"
"s_add_u32 m0, s49, 8192\n"
"v_mfma_f32_16x16x32_bf16 v[216:219], a[216:219], a[228:231], v[216:219]\n"
"buffer_load_dwordx4 v22, s[24:27], s50 offen offset:0 lds\n"
"buffer_load_dwordx4 v23, s[24:27], s50 offen offset:1024 lds\n"
"v_mfma_f32_16x16x32_bf16 v[220:223], a[220:223], a[228:231], v[220:223]\n"
"ds_read_b128 a[228:231], v15 offset:16896\n"
"buffer_load_dwordx4 v24, s[24:27], s50 offen offset:2048 lds\n"
"v_mfma_f32_16x16x32_bf16 v[176:179], a[208:211], a[232:235], v[176:179]\n"
"buffer_load_dwordx4 v25, s[24:27], s50 offen offset:3072 lds\n"
"v_mfma_f32_16x16x32_bf16 v[180:183], a[212:215], a[232:235], v[180:183]\n"
"v_accvgpr_write_b32 a154, 0\n"
"v_mfma_f32_16x16x32_bf16 v[224:227], a[216:219], a[232:235], v[224:227]\n"
"v_accvgpr_write_b32 a155, 0\n"
"v_mfma_f32_16x16x32_bf16 v[228:231], a[220:223], a[232:235], v[228:231]\n"
"ds_read_b128 a[232:235], v15 offset:33280\n"
"v_mfma_f32_16x16x32_bf16 v[184:187], a[208:211], a[236:239], v[184:187]\n"
"v_mfma_f32_16x16x32_bf16 v[188:191], a[212:215], a[236:239], v[188:191]\n"
"v_accvgpr_write_b32 a156, 0\n"
"v_mfma_f32_16x16x32_bf16 v[232:235], a[216:219], a[236:239], v[232:235]\n"
"v_accvgpr_write_b32 a157, 0\n"
"v_mfma_f32_16x16x32_bf16 v[236:239], a[220:223], a[236:239], v[236:239]\n"
"ds_read_b128 a[236:239], v15 offset:49664\n"
"v_mfma_f32_16x16x32_bf16 v[192:195], a[208:211], a[240:243], v[192:195]\n"
"v_accvgpr_write_b32 a158, 0\n"
"v_mfma_f32_16x16x32_bf16 v[196:199], a[212:215], a[240:243], v[196:199]\n"
"v_mfma_f32_16x16x32_bf16 v[240:243], a[216:219], a[240:243], v[240:243]\n"
"v_accvgpr_write_b32 a159, 0\n"
"v_mfma_f32_16x16x32_bf16 v[244:247], a[220:223], a[240:243], v[244:247]\n"
"ds_read_b128 a[240:243], v19 offset:512\n"
"s_waitcnt vmcnt(12)\n"
"v_mfma_f32_16x16x32_bf16 v[200:203], a[208:211], a[244:247], v[200:203]\n"
"ds_read_b64_tr_b16 a[208:209], v10 offset:12288\n"
"ds_read_b64_tr_b16 a[210:211], v10 offset:12800\n"
"v_mfma_f32_16x16x32_bf16 v[204:207], a[212:215], a[244:247], v[204:207]\n"
"ds_read_b64_tr_b16 a[212:213], v10 offset:12296\n"
"ds_read_b64_tr_b16 a[214:215], v10 offset:12808\n"
"v_mfma_f32_16x16x32_bf16 v[248:251], a[216:219], a[244:247], v[248:251]\n"
"ds_read_b64_tr_b16 a[216:217], v11 offset:12288\n"
"ds_read_b64_tr_b16 a[218:219], v11 offset:12800\n"
"v_mfma_f32_16x16x32_bf16 v[252:255], a[220:223], a[244:247], v[252:255]\n"
"ds_read_b128 a[244:247], v19 offset:16896\n"
"ds_read_b64_tr_b16 a[220:221], v11 offset:12296\n"
"ds_read_b64_tr_b16 a[222:223], v11 offset:12808\n"
"s_waitcnt lgkmcnt(11)\n"
"v_mfma_f32_16x16x32_bf16 v[64:67], a[192:195], a[224:227], v[64:67]\n"
"v_accvgpr_write_b32 a160, 0\n"
"v_mfma_f32_16x16x32_bf16 v[68:71], a[196:199], a[224:227], v[68:71]\n"
"v_mfma_f32_16x16x32_bf16 v[112:115], a[200:203], a[224:227], v[112:115]\n"
"v_accvgpr_write_b32 a161, 0\n"
"v_mfma_f32_16x16x32_bf16 v[116:119], a[204:207], a[224:227], v[116:119]\n"
"v_accvgpr_write_b32 a162, 0\n"
"v_mfma_f32_16x16x32_bf16 v[72:75], a[192:195], a[228:231], v[72:75]\n"
"v_accvgpr_write_b32 a163, 0\n"
"v_mfma_f32_16x16x32_bf16 v[76:79], a[196:199], a[228:231], v[76:79]\n"
"v_mfma_f32_16x16x32_bf16 v[120:123], a[200:203], a[228:231], v[120:123]\n"
"v_accvgpr_write_b32 a164, 0\n"
"v_mfma_f32_16x16x32_bf16 v[124:127], a[204:207], a[228:231], v[124:127]\n"
"s_waitcnt lgkmcnt(0)\n"
"v_accvgpr_write_b32 a165, 0\n"
"v_mfma_f32_16x16x32_bf16 v[80:83], a[192:195], a[232:235], v[80:83]\n"
"s_add_u32 s50, s45, 2883584\n"
"s_add_u32 m0, s49, 12288\n"
"v_mfma_f32_16x16x32_bf16 v[84:87], a[196:199], a[232:235], v[84:87]\n"
"buffer_load_dwordx4 v26, s[24:27], s50 offen offset:0 lds\n"
"buffer_load_dwordx4 v27, s[24:27], s50 offen offset:1024 lds\n"
"v_mfma_f32_16x16x32_bf16 v[128:131], a[200:203], a[232:235], v[128:131]\n"
"buffer_load_dwordx4 v28, s[24:27], s50 offen offset:2048 lds\n"
"buffer_load_dwordx4 v29, s[24:27], s50 offen offset:3072 lds\n"
"v_mfma_f32_16x16x32_bf16 v[132:135], a[204:207], a[232:235], v[132:135]\n"
"v_mfma_f32_16x16x32_bf16 v[88:91], a[192:195], a[236:239], v[88:91]\n"
"v_accvgpr_write_b32 a166, 0\n"
"v_mfma_f32_16x16x32_bf16 v[92:95], a[196:199], a[236:239], v[92:95]\n"
"v_accvgpr_write_b32 a167, 0\n"
"v_mfma_f32_16x16x32_bf16 v[136:139], a[200:203], a[236:239], v[136:139]\n"
"v_accvgpr_write_b32 a168, 0\n"
"v_mfma_f32_16x16x32_bf16 v[140:143], a[204:207], a[236:239], v[140:143]\n"
"v_mfma_f32_16x16x32_bf16 v[96:99], a[192:195], a[240:243], v[96:99]\n"
"v_accvgpr_write_b32 a169, 0\n"
"v_mfma_f32_16x16x32_bf16 v[100:103], a[196:199], a[240:243], v[100:103]\n"
"v_accvgpr_write_b32 a170, 0\n"
"v_mfma_f32_16x16x32_bf16 v[144:147], a[200:203], a[240:243], v[144:147]\n"
"v_mfma_f32_16x16x32_bf16 v[148:151], a[204:207], a[240:243], v[148:151]\n"
"s_waitcnt vmcnt(12)\n"
"v_mfma_f32_16x16x32_bf16 v[104:107], a[192:195], a[244:247], v[104:107]\n"
"ds_read_b64_tr_b16 a[192:193], v10 offset:0\n"
"ds_read_b64_tr_b16 a[194:195], v10 offset:512\n"
"v_mfma_f32_16x16x32_bf16 v[108:111], a[196:199], a[244:247], v[108:111]\n"
"ds_read_b64_tr_b16 a[196:197], v10 offset:8\n"
"ds_read_b64_tr_b16 a[198:199], v10 offset:520\n"
"v_mfma_f32_16x16x32_bf16 v[152:155], a[200:203], a[244:247], v[152:155]\n"
"ds_read_b64_tr_b16 a[200:201], v11 offset:0\n"
"ds_read_b64_tr_b16 a[202:203], v11 offset:512\n"
"v_mfma_f32_16x16x32_bf16 v[156:159], a[204:207], a[244:247], v[156:159]\n"
"ds_read_b64_tr_b16 a[204:205], v11 offset:8\n"
"ds_read_b64_tr_b16 a[206:207], v11 offset:520\n"
"v_mfma_f32_16x16x32_bf16 v[160:163], a[208:211], a[224:227], v[160:163]\n"
"v_accvgpr_write_b32 a171, 0\n"
"v_mfma_f32_16x16x32_bf16 v[164:167], a[212:215], a[224:227], v[164:167]\n"
"v_accvgpr_write_b32 a172, 0\n"
"v_mfma_f32_16x16x32_bf16 v[208:211], a[216:219], a[224:227], v[208:211]\n"
"v_accvgpr_write_b32 a173, 0\n"
"v_mfma_f32_16x16x32_bf16 v[212:215], a[220:223], a[224:227], v[212:215]\n"
"ds_read_b128 a[224:227], v16 offset:512\n"
"v_mfma_f32_16x16x32_bf16 v[168:171], a[208:211], a[228:231], v[168:171]\n"
"s_waitcnt lgkmcnt(1)\n"
"v_mfma_f32_16x16x32_bf16 v[172:175], a[212:215], a[228:231], v[172:175]\n"
"s_add_u32 s50, s45, 3145728\n"
"s_add_u32 m0, s49, 0\n"
"v_mfma_f32_16x16x32_bf16 v[216:219], a[216:219], a[228:231], v[216:219]\n"
"buffer_load_dwordx4 v22, s[24:27], s50 offen offset:0 lds\n"
"buffer_load_dwordx4 v23, s[24:27], s50 offen offset:1024 lds\n"
"v_mfma_f32_16x16x32_bf16 v[220:223], a[220:223], a[228:231], v[220:223]\n"
"ds_read_b128 a[228:231], v16 offset:16896\n"
"buffer_load_dwordx4 v24, s[24:27], s50 offen offset:2048 lds\n"
"v_mfma_f32_16x16x32_bf16 v[176:179], a[208:211], a[232:235], v[176:179]\n"
"buffer_load_dwordx4 v25, s[24:27], s50 offen offset:3072 lds\n"
"v_mfma_f32_16x16x32_bf16 v[180:183], a[212:215], a[232:235], v[180:183]\n"
"v_accvgpr_write_b32 a174, 0\n"
"v_mfma_f32_16x16x32_bf16 v[224:227], a[216:219], a[232:235], v[224:227]\n"
"v_accvgpr_write_b32 a175, 0\n"
"v_mfma_f32_16x16x32_bf16 v[228:231], a[220:223], a[232:235], v[228:231]\n"
"ds_read_b128 a[232:235], v16 offset:33280\n"
"v_mfma_f32_16x16x32_bf16 v[184:187], a[208:211], a[236:239], v[184:187]\n"
"v_mfma_f32_16x16x32_bf16 v[188:191], a[212:215], a[236:239], v[188:191]\n"
"v_accvgpr_write_b32 a176, 0\n"
"v_mfma_f32_16x16x32_bf16 v[232:235], a[216:219], a[236:239], v[232:235]\n"
"v_accvgpr_write_b32 a177, 0\n"
"v_mfma_f32_16x16x32_bf16 v[236:239], a[220:223], a[236:239], v[236:239]\n"
"ds_read_b128 a[236:239], v16 offset:49664\n"
"v_mfma_f32_16x16x32_bf16 v[192:195], a[208:211], a[240:243], v[192:195]\n"
"v_accvgpr_write_b32 a178, 0\n"
"v_mfma_f32_16x16x32_bf16 v[196:199], a[212:215], a[240:243], v[196:199]\n"
"v_mfma_f32_16x16x32_bf16 v[240:243], a[216:219], a[240:243], v[240:243]\n"
"v_accvgpr_write_b32 a179, 0\n"
"v_mfma_f32_16x16x32_bf16 v[244:247], a[220:223], a[240:243], v[244:247]\n"
"ds_read_b128 a[240:243], v20 offset:512\n"
"s_waitcnt vmcnt(12)\n"
"v_mfma_f32_16x16x32_bf16 v[200:203], a[208:211], a[244:247], v[200:203]\n"
"ds_read_b64_tr_b16 a[208:209], v10 offset:4096\n"
"ds_read_b64_tr_b16 a[210:211], v10 offset:4608\n"
"v_mfma_f32_16x16x32_bf16 v[204:207], a[212:215], a[244:247], v[204:207]\n"
"ds_read_b64_tr_b16 a[212:213], v10 offset:4104\n"
"ds_read_b64_tr_b16 a[214:215], v10 offset:4616\n"
"v_mfma_f32_16x16x32_bf16 v[248:251], a[216:219], a[244:247], v[248:251]\n"
"ds_read_b64_tr_b16 a[216:217], v11 offset:4096\n"
"ds_read_b64_tr_b16 a[218:219], v11 offset:4608\n"
"v_mfma_f32_16x16x32_bf16 v[252:255], a[220:223], a[244:247], v[252:255]\n"
"ds_read_b128 a[244:247], v20 offset:16896\n"
"ds_read_b64_tr_b16 a[220:221], v11 offset:4104\n"
"ds_read_b64_tr_b16 a[222:223], v11 offset:4616\n"
"s_waitcnt lgkmcnt(11)\n"
"v_mfma_f32_16x16x32_bf16 v[64:67], a[192:195], a[224:227], v[64:67]\n"
"v_accvgpr_write_b32 a180, 0\n"
"v_mfma_f32_16x16x32_bf16 v[68:71], a[196:199], a[224:227], v[68:71]\n"
"v_mfma_f32_16x16x32_bf16 v[112:115], a[200:203], a[224:227], v[112:115]\n"
"v_accvgpr_write_b32 a181, 0\n"
"v_mfma_f32_16x16x32_bf16 v[116:119], a[204:207], a[224:227], v[116:119]\n"
"v_accvgpr_write_b32 a182, 0\n"
"v_mfma_f32_16x16x32_bf16 v[72:75], a[192:195], a[228:231], v[72:75]\n"
"v_accvgpr_write_b32 a183, 0\n"
"v_mfma_f32_16x16x32_bf16 v[76:79], a[196:199], a[228:231], v[76:79]\n"
"v_mfma_f32_16x16x32_bf16 v[120:123], a[200:203], a[228:231], v[120:123]\n"
"v_accvgpr_write_b32 a184, 0\n"
"v_mfma_f32_16x16x32_bf16 v[124:127], a[204:207], a[228:231], v[124:127]\n"
"s_waitcnt lgkmcnt(0)\n"
"v_accvgpr_write_b32 a185, 0\n"
"v_mfma_f32_16x16x32_bf16 v[80:83], a[192:195], a[232:235], v[80:83]\n"
"s_add_u32 s50, s45, 3145728\n"
"s_add_u32 m0, s49, 4096\n"
"v_mfma_f32_16x16x32_bf16 v[84:87], a[196:199], a[232:235], v[84:87]\n"
"buffer_load_dwordx4 v26, s[24:27], s50 offen offset:0 lds\n"
"buffer_load_dwordx4 v27, s[24:27], s50 offen offset:1024 lds\n"
"v_mfma_f32_16x16x32_bf16 v[128:131], a[200:203], a[232:235], v[128:131]\n"
"buffer_load_dwordx4 v28, s[24:27], s50 offen offset:2048 lds\n"
"buffer_load_dwordx4 v29, s[24:27], s50 offen offset:3072 lds\n"
"v_mfma_f32_16x16x32_bf16 v[132:135], a[204:207], a[232:235], v[132:135]\n"
"v_mfma_f32_16x16x32_bf16 v[88:91], a[192:195], a[236:239], v[88:91]\n"
"v_accvgpr_write_b32 a186, 0\n"
"v_mfma_f32_16x16x32_bf16 v[92:95], a[196:199], a[236:239], v[92:95]\n"
"v_accvgpr_write_b32 a187, 0\n"
"v_mfma_f32_16x16x32_bf16 v[136:139], a[200:203], a[236:239], v[136:139]\n"
"v_accvgpr_write_b32 a188, 0\n"
"v_mfma_f32_16x16x32_bf16 v[140:143], a[204:207], a[236:239], v[140:143]\n"
"v_mfma_f32_16x16x32_bf16 v[96:99], a[192:195], a[240:243], v[96:99]\n"
"v_accvgpr_write_b32 a189, 0\n"
"v_mfma_f32_16x16x32_bf16 v[100:103], a[196:199], a[240:243], v[100:103]\n"
"v_accvgpr_write_b32 a190, 0\n"
"v_mfma_f32_16x16x32_bf16 v[144:147], a[200:203], a[240:243], v[144:147]\n"
"v_mfma_f32_16x16x32_bf16 v[148:151], a[204:207], a[240:243], v[148:151]\n"
"s_waitcnt vmcnt(12)\n"
"v_mfma_f32_16x16x32_bf16 v[104:107], a[192:195], a[244:247], v[104:107]\n"
"ds_read_b64_tr_b16 a[192:193], v10 offset:8192\n"
"ds_read_b64_tr_b16 a[194:195], v10 offset:8704\n"
"v_mfma_f32_16x16x32_bf16 v[108:111], a[196:199], a[244:247], v[108:111]\n"
"ds_read_b64_tr_b16 a[196:197], v10 offset:8200\n"
"ds_read_b64_tr_b16 a[198:199], v10 offset:8712\n"
"v_mfma_f32_16x16x32_bf16 v[152:155], a[200:203], a[244:247], v[152:155]\n"
"ds_read_b64_tr_b16 a[200:201], v11 offset:8192\n"
"ds_read_b64_tr_b16 a[202:203], v11 offset:8704\n"
"v_mfma_f32_16x16x32_bf16 v[156:159], a[204:207], a[244:247], v[156:159]\n"
"ds_read_b64_tr_b16 a[204:205], v11 offset:8200\n"
"ds_read_b64_tr_b16 a[206:207], v11 offset:8712\n"
"v_mfma_f32_16x16x32_bf16 v[160:163], a[208:211], a[224:227], v[160:163]\n"
"v_accvgpr_write_b32 a191, 0\n"
"v_mfma_f32_16x16x32_bf16 v[164:167], a[212:215], a[224:227], v[164:167]\n"
"v_and_b32 v2, 15, v1\n"
"v_mfma_f32_16x16x32_bf16 v[208:211], a[216:219], a[224:227], v[208:211]\n"
"v_lshrrev_b32 v3, 4, v1\n"
"v_mfma_f32_16x16x32_bf16 v[212:215], a[220:223], a[224:227], v[212:215]\n"
"ds_read_b128 a[224:227], v17 offset:512\n"
"v_mfma_f32_16x16x32_bf16 v[168:171], a[208:211], a[228:231], v[168:171]\n"
"s_waitcnt lgkmcnt(1)\n"
"v_mfma_f32_16x16x32_bf16 v[172:175], a[212:215], a[228:231], v[172:175]\n"
"s_add_u32 s50, s45, 3407872\n"
"s_add_u32 m0, s49, 8192\n"
"v_mfma_f32_16x16x32_bf16 v[216:219], a[216:219], a[228:231], v[216:219]\n"
"buffer_load_dwordx4 v22, s[24:27], s50 offen offset:0 lds\n"
"buffer_load_dwordx4 v23, s[24:27], s50 offen offset:1024 lds\n"
"v_mfma_f32_16x16x32_bf16 v[220:223], a[220:223], a[228:231], v[220:223]\n"
"ds_read_b128 a[228:231], v17 offset:16896\n"
"buffer_load_dwordx4 v24, s[24:27], s50 offen offset:2048 lds\n"
"v_mfma_f32_16x16x32_bf16 v[176:179], a[208:211], a[232:235], v[176:179]\n"
"buffer_load_dwordx4 v25, s[24:27], s50 offen offset:3072 lds\n"
"v_mfma_f32_16x16x32_bf16 v[180:183], a[212:215], a[232:235], v[180:183]\n"
"v_xor_b32 v3, v3, v2\n"
"v_mfma_f32_16x16x32_bf16 v[224:227], a[216:219], a[232:235], v[224:227]\n"
"v_lshlrev_b32 v3, 4, v3\n"
"v_mfma_f32_16x16x32_bf16 v[228:231], a[220:223], a[232:235], v[228:231]\n"
"ds_read_b128 a[232:235], v17 offset:33280\n"
"v_mfma_f32_16x16x32_bf16 v[184:187], a[208:211], a[236:239], v[184:187]\n"
"v_mfma_f32_16x16x32_bf16 v[188:191], a[212:215], a[236:239], v[188:191]\n"
"v_lshl_add_u32 v3, v2, 8, v3\n"
"v_mfma_f32_16x16x32_bf16 v[232:235], a[216:219], a[236:239], v[232:235]\n"
"v_xor_b32 v38, 0, v3\n"
"v_mfma_f32_16x16x32_bf16 v[236:239], a[220:223], a[236:239], v[236:239]\n"
"ds_read_b128 a[236:239], v17 offset:49664\n"
"v_mfma_f32_16x16x32_bf16 v[192:195], a[208:211], a[240:243], v[192:195]\n"
"v_xor_b32 v39, 64, v3\n"
"v_mfma_f32_16x16x32_bf16 v[196:199], a[212:215], a[240:243], v[196:199]\n"
"v_mfma_f32_16x16x32_bf16 v[240:243], a[216:219], a[240:243], v[240:243]\n"
"v_xor_b32 v40, 128, v3\n"
"v_mfma_f32_16x16x32_bf16 v[244:247], a[220:223], a[240:243], v[244:247]\n"
"ds_read_b128 a[240:243], v21 offset:512\n"
"s_waitcnt vmcnt(12)\n"
"v_mfma_f32_16x16x32_bf16 v[200:203], a[208:211], a[244:247], v[200:203]\n"
"ds_read_b64_tr_b16 a[208:209], v10 offset:12288\n"
"ds_read_b64_tr_b16 a[210:211], v10 offset:12800\n"
"v_mfma_f32_16x16x32_bf16 v[204:207], a[212:215], a[244:247], v[204:207]\n"
"ds_read_b64_tr_b16 a[212:213], v10 offset:12296\n"
"ds_read_b64_tr_b16 a[214:215], v10 offset:12808\n"
"v_mfma_f32_16x16x32_bf16 v[248:251], a[216:219], a[244:247], v[248:251]\n"
"ds_read_b64_tr_b16 a[216:217], v11 offset:12288\n"
"ds_read_b64_tr_b16 a[218:219], v11 offset:12800\n"
"v_mfma_f32_16x16x32_bf16 v[252:255], a[220:223], a[244:247], v[252:255]\n"
"ds_read_b128 a[244:247], v21 offset:16896\n"
"ds_read_b64_tr_b16 a[220:221], v11 offset:12296\n"
"ds_read_b64_tr_b16 a[222:223], v11 offset:12808\n"
"s_waitcnt lgkmcnt(11)\n"
"v_mfma_f32_16x16x32_bf16 v[64:67], a[192:195], a[224:227], v[64:67]\n"
"v_xor_b32 v41, 192, v3\n"
"v_mfma_f32_16x16x32_bf16 v[68:71], a[196:199], a[224:227], v[68:71]\n"
"v_mfma_f32_16x16x32_bf16 v[112:115], a[200:203], a[224:227], v[112:115]\n"
"v_bfe_u32 v2, v1, 2, 3\n"
"v_mfma_f32_16x16x32_bf16 v[116:119], a[204:207], a[224:227], v[116:119]\n"
"v_lshrrev_b32 v3, 5, v1\n"
"v_mfma_f32_16x16x32_bf16 v[72:75], a[192:195], a[228:231], v[72:75]\n"
"v_and_b32 v4, 3, v1\n"
"v_mfma_f32_16x16x32_bf16 v[76:79], a[196:199], a[228:231], v[76:79]\n"
"v_mfma_f32_16x16x32_bf16 v[120:123], a[200:203], a[228:231], v[120:123]\n"
"v_lshlrev_b32 v3, 11, v3\n"
"v_mfma_f32_16x16x32_bf16 v[124:127], a[204:207], a[228:231], v[124:127]\n"
"s_waitcnt lgkmcnt(0)\n"
"v_lshl_add_u32 v3, v2, 7, v3\n"
"v_mfma_f32_16x16x32_bf16 v[80:83], a[192:195], a[232:235], v[80:83]\n"
"s_add_u32 s50, s45, 3407872\n"
"s_add_u32 m0, s49, 12288\n"
"v_mfma_f32_16x16x32_bf16 v[84:87], a[196:199], a[232:235], v[84:87]\n"
"buffer_load_dwordx4 v26, s[24:27], s50 offen offset:0 lds\n"
"buffer_load_dwordx4 v27, s[24:27], s50 offen offset:1024 lds\n"
"v_mfma_f32_16x16x32_bf16 v[128:131], a[200:203], a[232:235], v[128:131]\n"
"buffer_load_dwordx4 v28, s[24:27], s50 offen offset:2048 lds\n"
"buffer_load_dwordx4 v29, s[24:27], s50 offen offset:3072 lds\n"
"v_mfma_f32_16x16x32_bf16 v[132:135], a[204:207], a[232:235], v[132:135]\n"
"v_mfma_f32_16x16x32_bf16 v[88:91], a[192:195], a[236:239], v[88:91]\n"
"v_lshl_add_u32 v3, v4, 3, v3\n"
"v_mfma_f32_16x16x32_bf16 v[92:95], a[196:199], a[236:239], v[92:95]\n"
"v_add_u32 v3, 90112, v3\n"
"v_mfma_f32_16x16x32_bf16 v[136:139], a[200:203], a[236:239], v[136:139]\n"
"v_bfe_u32 v2, v2, 1, 2\n"
"v_mfma_f32_16x16x32_bf16 v[140:143], a[204:207], a[236:239], v[140:143]\n"
"v_mfma_f32_16x16x32_bf16 v[96:99], a[192:195], a[240:243], v[96:99]\n"
"v_xor_b32 v4, 0, v2\n"
"v_mfma_f32_16x16x32_bf16 v[100:103], a[196:199], a[240:243], v[100:103]\n"
"v_lshl_add_u32 v42, v4, 5, v3\n"
"v_mfma_f32_16x16x32_bf16 v[144:147], a[200:203], a[240:243], v[144:147]\n"
"v_mfma_f32_16x16x32_bf16 v[148:151], a[204:207], a[240:243], v[148:151]\n"
"s_waitcnt vmcnt(12)\n"
"v_mfma_f32_16x16x32_bf16 v[104:107], a[192:195], a[244:247], v[104:107]\n"
"ds_read_b64_tr_b16 a[192:193], v10 offset:0\n"
"ds_read_b64_tr_b16 a[194:195], v10 offset:512\n"
"v_mfma_f32_16x16x32_bf16 v[108:111], a[196:199], a[244:247], v[108:111]\n"
"ds_read_b64_tr_b16 a[196:197], v10 offset:8\n"
"ds_read_b64_tr_b16 a[198:199], v10 offset:520\n"
"v_mfma_f32_16x16x32_bf16 v[152:155], a[200:203], a[244:247], v[152:155]\n"
"ds_read_b64_tr_b16 a[200:201], v11 offset:0\n"
"ds_read_b64_tr_b16 a[202:203], v11 offset:512\n"
"v_mfma_f32_16x16x32_bf16 v[156:159], a[204:207], a[244:247], v[156:159]\n"
"ds_read_b64_tr_b16 a[204:205], v11 offset:8\n"
"ds_read_b64_tr_b16 a[206:207], v11 offset:520\n"
"v_mfma_f32_16x16x32_bf16 v[160:163], a[208:211], a[224:227], v[160:163]\n"
"v_xor_b32 v4, 1, v2\n"
"v_mfma_f32_16x16x32_bf16 v[164:167], a[212:215], a[224:227], v[164:167]\n"
"v_lshl_add_u32 v43, v4, 5, v3\n"
"v_mfma_f32_16x16x32_bf16 v[208:211], a[216:219], a[224:227], v[208:211]\n"
"v_xor_b32 v4, 2, v2\n"
"v_mfma_f32_16x16x32_bf16 v[212:215], a[220:223], a[224:227], v[212:215]\n"
"ds_read_b128 a[224:227], v14 offset:768\n"
"v_mfma_f32_16x16x32_bf16 v[168:171], a[208:211], a[228:231], v[168:171]\n"
"s_waitcnt lgkmcnt(1)\n"
"v_mfma_f32_16x16x32_bf16 v[172:175], a[212:215], a[228:231], v[172:175]\n"
"s_add_u32 s50, s45, 3670016\n"
"s_add_u32 m0, s49, 0\n"
"v_mfma_f32_16x16x32_bf16 v[216:219], a[216:219], a[228:231], v[216:219]\n"
"buffer_load_dwordx4 v22, s[24:27], s50 offen offset:0 lds\n"
"buffer_load_dwordx4 v23, s[24:27], s50 offen offset:1024 lds\n"
"v_mfma_f32_16x16x32_bf16 v[220:223], a[220:223], a[228:231], v[220:223]\n"
"ds_read_b128 a[228:231], v14 offset:17152\n"
"buffer_load_dwordx4 v24, s[24:27], s50 offen offset:2048 lds\n"
"v_mfma_f32_16x16x32_bf16 v[176:179], a[208:211], a[232:235], v[176:179]\n"
"buffer_load_dwordx4 v25, s[24:27], s50 offen offset:3072 lds\n"
"v_mfma_f32_16x16x32_bf16 v[180:183], a[212:215], a[232:235], v[180:183]\n"
"v_lshl_add_u32 v44, v4, 5, v3\n"
"v_mfma_f32_16x16x32_bf16 v[224:227], a[216:219], a[232:235], v[224:227]\n"
"v_xor_b32 v4, 3, v2\n"
"v_mfma_f32_16x16x32_bf16 v[228:231], a[220:223], a[232:235], v[228:231]\n"
"ds_read_b128 a[232:235], v14 offset:33536\n"
"v_mfma_f32_16x16x32_bf16 v[184:187], a[208:211], a[236:239], v[184:187]\n"
"v_mfma_f32_16x16x32_bf16 v[188:191], a[212:215], a[236:239], v[188:191]\n"
"v_lshl_add_u32 v45, v4, 5, v3\n"
"v_mfma_f32_16x16x32_bf16 v[232:235], a[216:219], a[236:239], v[232:235]\n"
"v_lshrrev_b32 v2, 4, v1\n"
"v_mfma_f32_16x16x32_bf16 v[236:239], a[220:223], a[236:239], v[236:239]\n"
"ds_read_b128 a[236:239], v14 offset:49920\n"
"v_mfma_f32_16x16x32_bf16 v[192:195], a[208:211], a[240:243], v[192:195]\n"
"s_lshl_b32 s53, s42, 3\n"
"v_mfma_f32_16x16x32_bf16 v[196:199], a[212:215], a[240:243], v[196:199]\n"
"v_mfma_f32_16x16x32_bf16 v[240:243], a[216:219], a[240:243], v[240:243]\n"
"v_add_u32 v2, s53, v2\n"
"v_mfma_f32_16x16x32_bf16 v[244:247], a[220:223], a[240:243], v[244:247]\n"
"ds_read_b128 a[240:243], v18 offset:768\n"
"s_waitcnt vmcnt(12)\n"
"v_mfma_f32_16x16x32_bf16 v[200:203], a[208:211], a[244:247], v[200:203]\n"
"ds_read_b64_tr_b16 a[208:209], v10 offset:4096\n"
"ds_read_b64_tr_b16 a[210:211], v10 offset:4608\n"
"v_mfma_f32_16x16x32_bf16 v[204:207], a[212:215], a[244:247], v[204:207]\n"
"ds_read_b64_tr_b16 a[212:213], v10 offset:4104\n"
"ds_read_b64_tr_b16 a[214:215], v10 offset:4616\n"
"v_mfma_f32_16x16x32_bf16 v[248:251], a[216:219], a[244:247], v[248:251]\n"
"ds_read_b64_tr_b16 a[216:217], v11 offset:4096\n"
"ds_read_b64_tr_b16 a[218:219], v11 offset:4608\n"
"v_mfma_f32_16x16x32_bf16 v[252:255], a[220:223], a[244:247], v[252:255]\n"
"ds_read_b128 a[244:247], v18 offset:17152\n"
"ds_read_b64_tr_b16 a[220:221], v11 offset:4104\n"
"ds_read_b64_tr_b16 a[222:223], v11 offset:4616\n"
"s_waitcnt lgkmcnt(11)\n"
"v_mfma_f32_16x16x32_bf16 v[64:67], a[192:195], a[224:227], v[64:67]\n"
"v_and_b32 v3, 15, v1\n"
"v_mfma_f32_16x16x32_bf16 v[68:71], a[196:199], a[224:227], v[68:71]\n"
"v_mfma_f32_16x16x32_bf16 v[112:115], a[200:203], a[224:227], v[112:115]\n"
"v_add_u32 v4, 0, v2\n"
"v_mfma_f32_16x16x32_bf16 v[116:119], a[204:207], a[224:227], v[116:119]\n"
"v_and_b32 v5, 15, v4\n"
"v_mfma_f32_16x16x32_bf16 v[72:75], a[192:195], a[228:231], v[72:75]\n"
"v_xor_b32 v5, v5, v3\n"
"v_mfma_f32_16x16x32_bf16 v[76:79], a[196:199], a[228:231], v[76:79]\n"
"v_mfma_f32_16x16x32_bf16 v[120:123], a[200:203], a[228:231], v[120:123]\n"
"v_lshlrev_b32 v5, 4, v5\n"
"v_mfma_f32_16x16x32_bf16 v[124:127], a[204:207], a[228:231], v[124:127]\n"
"s_waitcnt lgkmcnt(0)\n"
"v_lshl_add_u32 v5, v4, 8, v5\n"
"v_mfma_f32_16x16x32_bf16 v[80:83], a[192:195], a[232:235], v[80:83]\n"
"s_add_u32 s50, s45, 3670016\n"
"s_add_u32 m0, s49, 4096\n"
"v_mfma_f32_16x16x32_bf16 v[84:87], a[196:199], a[232:235], v[84:87]\n"
"buffer_load_dwordx4 v26, s[24:27], s50 offen offset:0 lds\n"
"buffer_load_dwordx4 v27, s[24:27], s50 offen offset:1024 lds\n"
"v_mfma_f32_16x16x32_bf16 v[128:131], a[200:203], a[232:235], v[128:131]\n"
"buffer_load_dwordx4 v28, s[24:27], s50 offen offset:2048 lds\n"
"buffer_load_dwordx4 v29, s[24:27], s50 offen offset:3072 lds\n"
"v_mfma_f32_16x16x32_bf16 v[132:135], a[204:207], a[232:235], v[132:135]\n"
"v_mfma_f32_16x16x32_bf16 v[88:91], a[192:195], a[236:239], v[88:91]\n"
"v_subrev_u32 v46, 0, v5\n"
"v_mfma_f32_16x16x32_bf16 v[92:95], a[196:199], a[236:239], v[92:95]\n"
"v_add_u32 v4, 4, v2\n"
"v_mfma_f32_16x16x32_bf16 v[136:139], a[200:203], a[236:239], v[136:139]\n"
"v_and_b32 v5, 15, v4\n"
"v_mfma_f32_16x16x32_bf16 v[140:143], a[204:207], a[236:239], v[140:143]\n"
"v_mfma_f32_16x16x32_bf16 v[96:99], a[192:195], a[240:243], v[96:99]\n"
"v_xor_b32 v5, v5, v3\n"
"v_mfma_f32_16x16x32_bf16 v[100:103], a[196:199], a[240:243], v[100:103]\n"
"v_lshlrev_b32 v5, 4, v5\n"
"v_mfma_f32_16x16x32_bf16 v[144:147], a[200:203], a[240:243], v[144:147]\n"
"v_mfma_f32_16x16x32_bf16 v[148:151], a[204:207], a[240:243], v[148:151]\n"
"s_waitcnt vmcnt(12)\n"
"v_mfma_f32_16x16x32_bf16 v[104:107], a[192:195], a[244:247], v[104:107]\n"
"ds_read_b64_tr_b16 a[192:193], v10 offset:8192\n"
"ds_read_b64_tr_b16 a[194:195], v10 offset:8704\n"
"v_mfma_f32_16x16x32_bf16 v[108:111], a[196:199], a[244:247], v[108:111]\n"
"ds_read_b64_tr_b16 a[196:197], v10 offset:8200\n"
"ds_read_b64_tr_b16 a[198:199], v10 offset:8712\n"
"v_mfma_f32_16x16x32_bf16 v[152:155], a[200:203], a[244:247], v[152:155]\n"
"ds_read_b64_tr_b16 a[200:201], v11 offset:8192\n"
"ds_read_b64_tr_b16 a[202:203], v11 offset:8704\n"
"v_mfma_f32_16x16x32_bf16 v[156:159], a[204:207], a[244:247], v[156:159]\n"
"ds_read_b64_tr_b16 a[204:205], v11 offset:8200\n"
"ds_read_b64_tr_b16 a[206:207], v11 offset:8712\n"
"v_mfma_f32_16x16x32_bf16 v[160:163], a[208:211], a[224:227], v[160:163]\n"
"v_lshl_add_u32 v5, v4, 8, v5\n"
"v_mfma_f32_16x16x32_bf16 v[164:167], a[212:215], a[224:227], v[164:167]\n"
"v_subrev_u32 v47, 1024, v5\n"
"v_mfma_f32_16x16x32_bf16 v[208:211], a[216:219], a[224:227], v[208:211]\n"
"v_lshrrev_b32 v2, 3, v1\n"
"v_mfma_f32_16x16x32_bf16 v[212:215], a[220:223], a[224:227], v[212:215]\n"
"ds_read_b128 a[224:227], v15 offset:768\n"
"v_mfma_f32_16x16x32_bf16 v[168:171], a[208:211], a[228:231], v[168:171]\n"
"s_waitcnt lgkmcnt(1)\n"
"v_mfma_f32_16x16x32_bf16 v[172:175], a[212:215], a[228:231], v[172:175]\n"
"s_add_u32 s50, s45, 3932160\n"
"s_add_u32 m0, s49, 8192\n"
"v_mfma_f32_16x16x32_bf16 v[216:219], a[216:219], a[228:231], v[216:219]\n"
"buffer_load_dwordx4 v22, s[24:27], s50 offen offset:0 lds\n"
"buffer_load_dwordx4 v23, s[24:27], s50 offen offset:1024 lds\n"
"v_mfma_f32_16x16x32_bf16 v[220:223], a[220:223], a[228:231], v[220:223]\n"
"ds_read_b128 a[228:231], v15 offset:17152\n"
"buffer_load_dwordx4 v24, s[24:27], s50 offen offset:2048 lds\n"
"v_mfma_f32_16x16x32_bf16 v[176:179], a[208:211], a[232:235], v[176:179]\n"
"buffer_load_dwordx4 v25, s[24:27], s50 offen offset:3072 lds\n"
"v_mfma_f32_16x16x32_bf16 v[180:183], a[212:215], a[232:235], v[180:183]\n"
"v_bfe_u32 v3, v1, 1, 2\n"
"v_mfma_f32_16x16x32_bf16 v[224:227], a[216:219], a[232:235], v[224:227]\n"
"v_bfe_u32 v4, v2, 1, 2\n"
"v_mfma_f32_16x16x32_bf16 v[228:231], a[220:223], a[232:235], v[228:231]\n"
"ds_read_b128 a[232:235], v15 offset:33536\n"
"v_mfma_f32_16x16x32_bf16 v[184:187], a[208:211], a[236:239], v[184:187]\n"
"v_mfma_f32_16x16x32_bf16 v[188:191], a[212:215], a[236:239], v[188:191]\n"
"v_xor_b32 v3, v3, v4\n"
"v_mfma_f32_16x16x32_bf16 v[232:235], a[216:219], a[236:239], v[232:235]\n"
"v_lshlrev_b32 v3, 5, v3\n"
"v_mfma_f32_16x16x32_bf16 v[236:239], a[220:223], a[236:239], v[236:239]\n"
"ds_read_b128 a[236:239], v15 offset:49920\n"
"v_mfma_f32_16x16x32_bf16 v[192:195], a[208:211], a[240:243], v[192:195]\n"
"v_and_b32 v4, 1, v1\n"
"v_mfma_f32_16x16x32_bf16 v[196:199], a[212:215], a[240:243], v[196:199]\n"
"v_mfma_f32_16x16x32_bf16 v[240:243], a[216:219], a[240:243], v[240:243]\n"
"v_lshl_add_u32 v3, v4, 4, v3\n"
"v_mfma_f32_16x16x32_bf16 v[244:247], a[220:223], a[240:243], v[244:247]\n"
"ds_read_b128 a[240:243], v19 offset:768\n"
"s_waitcnt vmcnt(12)\n"
"v_mfma_f32_16x16x32_bf16 v[200:203], a[208:211], a[244:247], v[200:203]\n"
"ds_read_b64_tr_b16 a[208:209], v10 offset:12288\n"
"ds_read_b64_tr_b16 a[210:211], v10 offset:12800\n"
"v_mfma_f32_16x16x32_bf16 v[204:207], a[212:215], a[244:247], v[204:207]\n"
"ds_read_b64_tr_b16 a[212:213], v10 offset:12296\n"
"ds_read_b64_tr_b16 a[214:215], v10 offset:12808\n"
"v_mfma_f32_16x16x32_bf16 v[248:251], a[216:219], a[244:247], v[248:251]\n"
"ds_read_b64_tr_b16 a[216:217], v11 offset:12288\n"
"ds_read_b64_tr_b16 a[218:219], v11 offset:12800\n"
"v_mfma_f32_16x16x32_bf16 v[252:255], a[220:223], a[244:247], v[252:255]\n"
"ds_read_b128 a[244:247], v19 offset:17152\n"
"ds_read_b64_tr_b16 a[220:221], v11 offset:12296\n"
"ds_read_b64_tr_b16 a[222:223], v11 offset:12808\n"
"s_waitcnt lgkmcnt(11)\n"
"v_mfma_f32_16x16x32_bf16 v[64:67], a[192:195], a[224:227], v[64:67]\n"
"s_mov_b32 s53, 256\n"
"v_mfma_f32_16x16x32_bf16 v[68:71], a[196:199], a[224:227], v[68:71]\n"
"v_mfma_f32_16x16x32_bf16 v[112:115], a[200:203], a[224:227], v[112:115]\n"
"v_mul_lo_u32 v2, v2, s53\n"
"v_mfma_f32_16x16x32_bf16 v[116:119], a[204:207], a[224:227], v[116:119]\n"
"v_add_u32 v2, v2, v3\n"
"v_mfma_f32_16x16x32_bf16 v[72:75], a[192:195], a[228:231], v[72:75]\n"
"v_mfma_f32_16x16x32_bf16 v[76:79], a[196:199], a[228:231], v[76:79]\n"
"s_lshl_b32 s53, s42, 11\n"
"v_mfma_f32_16x16x32_bf16 v[120:123], a[200:203], a[228:231], v[120:123]\n"
"v_add_u32 v2, s53, v2\n"
"v_mfma_f32_16x16x32_bf16 v[124:127], a[204:207], a[228:231], v[124:127]\n"
"s_waitcnt lgkmcnt(0)\n"
"v_mfma_f32_16x16x32_bf16 v[80:83], a[192:195], a[232:235], v[80:83]\n"
"s_add_u32 s50, s45, 3932160\n"
"s_add_u32 m0, s49, 12288\n"
"v_mfma_f32_16x16x32_bf16 v[84:87], a[196:199], a[232:235], v[84:87]\n"
"buffer_load_dwordx4 v26, s[24:27], s50 offen offset:0 lds\n"
"buffer_load_dwordx4 v27, s[24:27], s50 offen offset:1024 lds\n"
"v_mfma_f32_16x16x32_bf16 v[128:131], a[200:203], a[232:235], v[128:131]\n"
"buffer_load_dwordx4 v28, s[24:27], s50 offen offset:2048 lds\n"
"buffer_load_dwordx4 v29, s[24:27], s50 offen offset:3072 lds\n"
"v_mfma_f32_16x16x32_bf16 v[132:135], a[204:207], a[232:235], v[132:135]\n"
"v_add_u32 v48, 4096, v2\n"
"v_mfma_f32_16x16x32_bf16 v[88:91], a[192:195], a[236:239], v[88:91]\n"
"v_mfma_f32_16x16x32_bf16 v[92:95], a[196:199], a[236:239], v[92:95]\n"
"v_add_u32 v49, 3200, v2\n"
"v_mfma_f32_16x16x32_bf16 v[136:139], a[200:203], a[236:239], v[136:139]\n"
"s_lshl_b32 s57, s42, 11\n"
"v_mfma_f32_16x16x32_bf16 v[140:143], a[204:207], a[236:239], v[140:143]\n"
"v_mfma_f32_16x16x32_bf16 v[96:99], a[192:195], a[240:243], v[96:99]\n"
"s_mov_b32 s60, 0\n"
"v_mfma_f32_16x16x32_bf16 v[100:103], a[196:199], a[240:243], v[100:103]\n"
"s_mov_b32 s61, 0\n"
"v_mfma_f32_16x16x32_bf16 v[144:147], a[200:203], a[240:243], v[144:147]\n"
"v_mfma_f32_16x16x32_bf16 v[148:151], a[204:207], a[240:243], v[148:151]\n"
"s_waitcnt vmcnt(12)\n"
"v_mfma_f32_16x16x32_bf16 v[104:107], a[192:195], a[244:247], v[104:107]\n"
"ds_read_b64_tr_b16 a[192:193], v10 offset:0\n"
"ds_read_b64_tr_b16 a[194:195], v10 offset:512\n"
"v_mfma_f32_16x16x32_bf16 v[108:111], a[196:199], a[244:247], v[108:111]\n"
"ds_read_b64_tr_b16 a[196:197], v10 offset:8\n"
"ds_read_b64_tr_b16 a[198:199], v10 offset:520\n"
"v_mfma_f32_16x16x32_bf16 v[152:155], a[200:203], a[244:247], v[152:155]\n"
"ds_read_b64_tr_b16 a[200:201], v11 offset:0\n"
"ds_read_b64_tr_b16 a[202:203], v11 offset:512\n"
"v_mfma_f32_16x16x32_bf16 v[156:159], a[204:207], a[244:247], v[156:159]\n"
"ds_read_b64_tr_b16 a[204:205], v11 offset:8\n"
"ds_read_b64_tr_b16 a[206:207], v11 offset:520\n"
"v_mfma_f32_16x16x32_bf16 v[160:163], a[208:211], a[224:227], v[160:163]\n"
"s_mov_b32 s55, 4294885376\n"
"v_mfma_f32_16x16x32_bf16 v[164:167], a[212:215], a[224:227], v[164:167]\n"
"s_mov_b32 s56, 16384\n"
"v_mfma_f32_16x16x32_bf16 v[208:211], a[216:219], a[224:227], v[208:211]\n"
"v_mfma_f32_16x16x32_bf16 v[212:215], a[220:223], a[224:227], v[212:215]\n"
"ds_read_b128 a[224:227], v16 offset:768\n"
"v_mfma_f32_16x16x32_bf16 v[168:171], a[208:211], a[228:231], v[168:171]\n"
"s_waitcnt lgkmcnt(1)\n"
"s_mov_b32 s62, 0\n"
"v_mfma_f32_16x16x32_bf16 v[172:175], a[212:215], a[228:231], v[172:175]\n"
"s_add_u32 s50, s46, 0\n"
"s_add_u32 m0, s49, 0\n"
"v_mfma_f32_16x16x32_bf16 v[216:219], a[216:219], a[228:231], v[216:219]\n"
"buffer_load_dwordx4 v30, s[28:31], s50 offen offset:0 lds\n"
"buffer_load_dwordx4 v31, s[28:31], s50 offen offset:1024 lds\n"
"v_mfma_f32_16x16x32_bf16 v[220:223], a[220:223], a[228:231], v[220:223]\n"
"ds_read_b128 a[228:231], v16 offset:17152\n"
"buffer_load_dwordx4 v32, s[28:31], s50 offen offset:2048 lds\n"
"v_mfma_f32_16x16x32_bf16 v[176:179], a[208:211], a[232:235], v[176:179]\n"
"buffer_load_dwordx4 v33, s[28:31], s50 offen offset:3072 lds\n"
"v_mfma_f32_16x16x32_bf16 v[180:183], a[212:215], a[232:235], v[180:183]\n"
"s_mov_b32 s63, 5\n"
"v_mfma_f32_16x16x32_bf16 v[224:227], a[216:219], a[232:235], v[224:227]\n"
"v_mfma_f32_16x16x32_bf16 v[228:231], a[220:223], a[232:235], v[228:231]\n"
"ds_read_b128 a[232:235], v16 offset:33536\n"
"v_mfma_f32_16x16x32_bf16 v[184:187], a[208:211], a[236:239], v[184:187]\n"
"v_mov_b32 v50, 0x3f803f80\n"
"v_mfma_f32_16x16x32_bf16 v[188:191], a[212:215], a[236:239], v[188:191]\n"
"v_mfma_f32_16x16x32_bf16 v[232:235], a[216:219], a[236:239], v[232:235]\n"
"v_mov_b32 v51, 0x3f803f80\n"
"v_mfma_f32_16x16x32_bf16 v[236:239], a[220:223], a[236:239], v[236:239]\n"
"ds_read_b128 a[236:239], v16 offset:49920\n"
"v_mfma_f32_16x16x32_bf16 v[192:195], a[208:211], a[240:243], v[192:195]\n"
"v_mov_b32 v52, 0x3f803f80\n"
"v_mfma_f32_16x16x32_bf16 v[196:199], a[212:215], a[240:243], v[196:199]\n"
"v_mfma_f32_16x16x32_bf16 v[240:243], a[216:219], a[240:243], v[240:243]\n"
"v_mov_b32 v53, 0x3f803f80\n"
"v_mfma_f32_16x16x32_bf16 v[244:247], a[220:223], a[240:243], v[244:247]\n"
"ds_read_b128 a[240:243], v20 offset:768\n"
"s_waitcnt vmcnt(12)\n"
"v_mfma_f32_16x16x32_bf16 v[200:203], a[208:211], a[244:247], v[200:203]\n"
"ds_read_b64_tr_b16 a[208:209], v10 offset:4096\n"
"ds_read_b64_tr_b16 a[210:211], v10 offset:4608\n"
"v_mfma_f32_16x16x32_bf16 v[204:207], a[212:215], a[244:247], v[204:207]\n"
"ds_read_b64_tr_b16 a[212:213], v10 offset:4104\n"
"ds_read_b64_tr_b16 a[214:215], v10 offset:4616\n"
"v_mfma_f32_16x16x32_bf16 v[248:251], a[216:219], a[244:247], v[248:251]\n"
"ds_read_b64_tr_b16 a[216:217], v11 offset:4096\n"
"ds_read_b64_tr_b16 a[218:219], v11 offset:4608\n"
"v_mfma_f32_16x16x32_bf16 v[252:255], a[220:223], a[244:247], v[252:255]\n"
"ds_read_b128 a[244:247], v20 offset:17152\n"
"ds_read_b64_tr_b16 a[220:221], v11 offset:4104\n"
"ds_read_b64_tr_b16 a[222:223], v11 offset:4616\n"
"s_waitcnt lgkmcnt(11)\n"
"v_mfma_f32_16x16x32_bf16 v[64:67], a[192:195], a[224:227], v[64:67]\n"
"v_mfma_f32_16x16x32_bf16 v[68:71], a[196:199], a[224:227], v[68:71]\n"
"v_mfma_f32_16x16x32_bf16 v[112:115], a[200:203], a[224:227], v[112:115]\n"
"v_mfma_f32_16x16x32_bf16 v[116:119], a[204:207], a[224:227], v[116:119]\n"
"v_mfma_f32_16x16x32_bf16 v[72:75], a[192:195], a[228:231], v[72:75]\n"
"v_mfma_f32_16x16x32_bf16 v[76:79], a[196:199], a[228:231], v[76:79]\n"
"v_mfma_f32_16x16x32_bf16 v[120:123], a[200:203], a[228:231], v[120:123]\n"
"v_mfma_f32_16x16x32_bf16 v[124:127], a[204:207], a[228:231], v[124:127]\n"
"s_waitcnt lgkmcnt(0)\n"
"v_mfma_f32_16x16x32_bf16 v[80:83], a[192:195], a[232:235], v[80:83]\n"
"s_add_u32 s50, s46, 32768\n"
"s_add_u32 m0, s49, 4096\n"
"v_mfma_f32_16x16x32_bf16 v[84:87], a[196:199], a[232:235], v[84:87]\n"
"buffer_load_dwordx4 v30, s[28:31], s50 offen offset:0 lds\n"
"buffer_load_dwordx4 v31, s[28:31], s50 offen offset:1024 lds\n"
"v_mfma_f32_16x16x32_bf16 v[128:131], a[200:203], a[232:235], v[128:131]\n"
"buffer_load_dwordx4 v32, s[28:31], s50 offen offset:2048 lds\n"
"buffer_load_dwordx4 v33, s[28:31], s50 offen offset:3072 lds\n"
"v_mfma_f32_16x16x32_bf16 v[132:135], a[204:207], a[232:235], v[132:135]\n"
"v_mfma_f32_16x16x32_bf16 v[88:91], a[192:195], a[236:239], v[88:91]\n"
"v_mfma_f32_16x16x32_bf16 v[92:95], a[196:199], a[236:239], v[92:95]\n"
"v_mfma_f32_16x16x32_bf16 v[136:139], a[200:203], a[236:239], v[136:139]\n"
"v_mfma_f32_16x16x32_bf16 v[140:143], a[204:207], a[236:239], v[140:143]\n"
"v_mfma_f32_16x16x32_bf16 v[96:99], a[192:195], a[240:243], v[96:99]\n"
"v_mfma_f32_16x16x32_bf16 v[100:103], a[196:199], a[240:243], v[100:103]\n"
"v_mfma_f32_16x16x32_bf16 v[144:147], a[200:203], a[240:243], v[144:147]\n"
"v_mfma_f32_16x16x32_bf16 v[148:151], a[204:207], a[240:243], v[148:151]\n"
"s_waitcnt vmcnt(12)\n"
"v_mfma_f32_16x16x32_bf16 v[104:107], a[192:195], a[244:247], v[104:107]\n"
"ds_read_b64_tr_b16 a[192:193], v10 offset:8192\n"
"ds_read_b64_tr_b16 a[194:195], v10 offset:8704\n"
"v_mfma_f32_16x16x32_bf16 v[108:111], a[196:199], a[244:247], v[108:111]\n"
"ds_read_b64_tr_b16 a[196:197], v10 offset:8200\n"
"ds_read_b64_tr_b16 a[198:199], v10 offset:8712\n"
"v_mfma_f32_16x16x32_bf16 v[152:155], a[200:203], a[244:247], v[152:155]\n"
"ds_read_b64_tr_b16 a[200:201], v11 offset:8192\n"
"ds_read_b64_tr_b16 a[202:203], v11 offset:8704\n"
"v_mfma_f32_16x16x32_bf16 v[156:159], a[204:207], a[244:247], v[156:159]\n"
"ds_read_b64_tr_b16 a[204:205], v11 offset:8200\n"
"ds_read_b64_tr_b16 a[206:207], v11 offset:8712\n"
"v_mfma_f32_16x16x32_bf16 v[160:163], a[208:211], a[224:227], v[160:163]\n"
"v_mfma_f32_16x16x32_bf16 v[164:167], a[212:215], a[224:227], v[164:167]\n"
"v_mfma_f32_16x16x32_bf16 v[208:211], a[216:219], a[224:227], v[208:211]\n"
"v_mfma_f32_16x16x32_bf16 v[212:215], a[220:223], a[224:227], v[212:215]\n"
"ds_read_b128 a[224:227], v17 offset:768\n"
"v_mfma_f32_16x16x32_bf16 v[168:171], a[208:211], a[228:231], v[168:171]\n"
"s_waitcnt lgkmcnt(1)\n"
"v_mfma_f32_16x16x32_bf16 v[172:175], a[212:215], a[228:231], v[172:175]\n"
"s_add_u32 s50, s46, 65536\n"
"s_add_u32 m0, s49, 8192\n"
"v_mfma_f32_16x16x32_bf16 v[216:219], a[216:219], a[228:231], v[216:219]\n"
"buffer_load_dwordx4 v30, s[28:31], s50 offen offset:0 lds\n"
"buffer_load_dwordx4 v31, s[28:31], s50 offen offset:1024 lds\n"
"v_mfma_f32_16x16x32_bf16 v[220:223], a[220:223], a[228:231], v[220:223]\n"
"ds_read_b128 a[228:231], v17 offset:17152\n"
"buffer_load_dwordx4 v32, s[28:31], s50 offen offset:2048 lds\n"
"v_mfma_f32_16x16x32_bf16 v[176:179], a[208:211], a[232:235], v[176:179]\n"
"buffer_load_dwordx4 v33, s[28:31], s50 offen offset:3072 lds\n"
"v_mfma_f32_16x16x32_bf16 v[180:183], a[212:215], a[232:235], v[180:183]\n"
"v_mfma_f32_16x16x32_bf16 v[224:227], a[216:219], a[232:235], v[224:227]\n"
"v_mfma_f32_16x16x32_bf16 v[228:231], a[220:223], a[232:235], v[228:231]\n"
"ds_read_b128 a[232:235], v17 offset:33536\n"
"v_mfma_f32_16x16x32_bf16 v[184:187], a[208:211], a[236:239], v[184:187]\n"
"v_mfma_f32_16x16x32_bf16 v[188:191], a[212:215], a[236:239], v[188:191]\n"
"v_mfma_f32_16x16x32_bf16 v[232:235], a[216:219], a[236:239], v[232:235]\n"
"v_mfma_f32_16x16x32_bf16 v[236:239], a[220:223], a[236:239], v[236:239]\n"
"ds_read_b128 a[236:239], v17 offset:49920\n"
"v_mfma_f32_16x16x32_bf16 v[192:195], a[208:211], a[240:243], v[192:195]\n"
"v_mfma_f32_16x16x32_bf16 v[196:199], a[212:215], a[240:243], v[196:199]\n"
"v_mfma_f32_16x16x32_bf16 v[240:243], a[216:219], a[240:243], v[240:243]\n"
"v_mfma_f32_16x16x32_bf16 v[244:247], a[220:223], a[240:243], v[244:247]\n"
"ds_read_b128 a[240:243], v21 offset:768\n"
"s_waitcnt vmcnt(12)\n"
"v_mfma_f32_16x16x32_bf16 v[200:203], a[208:211], a[244:247], v[200:203]\n"
"ds_read_b64_tr_b16 a[208:209], v10 offset:12288\n"
"ds_read_b64_tr_b16 a[210:211], v10 offset:12800\n"
"v_mfma_f32_16x16x32_bf16 v[204:207], a[212:215], a[244:247], v[204:207]\n"
"ds_read_b64_tr_b16 a[212:213], v10 offset:12296\n"
"ds_read_b64_tr_b16 a[214:215], v10 offset:12808\n"
"v_mfma_f32_16x16x32_bf16 v[248:251], a[216:219], a[244:247], v[248:251]\n"
"ds_read_b64_tr_b16 a[216:217], v11 offset:12288\n"
"ds_read_b64_tr_b16 a[218:219], v11 offset:12800\n"
"v_mfma_f32_16x16x32_bf16 v[252:255], a[220:223], a[244:247], v[252:255]\n"
"ds_read_b128 a[244:247], v21 offset:17152\n"
"ds_read_b64_tr_b16 a[220:221], v11 offset:12296\n"
"ds_read_b64_tr_b16 a[222:223], v11 offset:12808\n"
"s_waitcnt lgkmcnt(11)\n"
"v_mfma_f32_16x16x32_bf16 v[64:67], a[192:195], a[224:227], v[64:67]\n"
"v_mfma_f32_16x16x32_bf16 v[68:71], a[196:199], a[224:227], v[68:71]\n"
"v_mfma_f32_16x16x32_bf16 v[112:115], a[200:203], a[224:227], v[112:115]\n"
"v_mfma_f32_16x16x32_bf16 v[116:119], a[204:207], a[224:227], v[116:119]\n"
"v_mfma_f32_16x16x32_bf16 v[72:75], a[192:195], a[228:231], v[72:75]\n"
"v_mfma_f32_16x16x32_bf16 v[76:79], a[196:199], a[228:231], v[76:79]\n"
"s_waitcnt lgkmcnt(2)\n"
"s_barrier\n"
"v_mfma_f32_16x16x32_bf16 v[120:123], a[200:203], a[228:231], v[120:123]\n"
"v_mfma_f32_16x16x32_bf16 v[124:127], a[204:207], a[228:231], v[124:127]\n"
"s_waitcnt lgkmcnt(0)\n"
"v_mfma_f32_16x16x32_bf16 v[80:83], a[192:195], a[232:235], v[80:83]\n"
"s_add_u32 s50, s46, 98304\n"
"s_add_u32 m0, s49, 12288\n"
"v_mfma_f32_16x16x32_bf16 v[84:87], a[196:199], a[232:235], v[84:87]\n"
"buffer_load_dwordx4 v30, s[28:31], s50 offen offset:0 lds\n"
"buffer_load_dwordx4 v31, s[28:31], s50 offen offset:1024 lds\n"
"v_mfma_f32_16x16x32_bf16 v[128:131], a[200:203], a[232:235], v[128:131]\n"
"buffer_load_dwordx4 v32, s[28:31], s50 offen offset:2048 lds\n"
"buffer_load_dwordx4 v33, s[28:31], s50 offen offset:3072 lds\n"
"v_mfma_f32_16x16x32_bf16 v[132:135], a[204:207], a[232:235], v[132:135]\n"
"s_add_u32 m0, s61, s57\n"
"v_mfma_f32_16x16x32_bf16 v[88:91], a[192:195], a[236:239], v[88:91]\n"
"buffer_load_dwordx4 v46, s[84:87], s60 offen lds\n"
"v_mfma_f32_16x16x32_bf16 v[92:95], a[196:199], a[236:239], v[92:95]\n"
"buffer_load_dwordx4 v47, s[84:87], s60 offen offset:1024 lds\n"
"v_mfma_f32_16x16x32_bf16 v[136:139], a[200:203], a[236:239], v[136:139]\n"
"s_add_u32 m0, m0, 8192\n"
"v_mfma_f32_16x16x32_bf16 v[140:143], a[204:207], a[236:239], v[140:143]\n"
"buffer_load_dwordx4 v48, s[88:91], s60 offen lds\n"
"v_mfma_f32_16x16x32_bf16 v[96:99], a[192:195], a[240:243], v[96:99]\n"
"buffer_load_dwordx4 v49, s[88:91], s60 offen offset:1024 lds\n"
"v_mfma_f32_16x16x32_bf16 v[100:103], a[196:199], a[240:243], v[100:103]\n"
"s_add_u32 s60, s60, 8192\n"
"s_add_u32 s61, s61, 16384\n"
"v_mfma_f32_16x16x32_bf16 v[144:147], a[200:203], a[240:243], v[144:147]\n"
"s_cmp_eq_u32 s61, 98304\n"
"v_mfma_f32_16x16x32_bf16 v[148:151], a[204:207], a[240:243], v[148:151]\n"
"s_cselect_b32 s61, 0, s61\n"
"v_mfma_f32_16x16x32_bf16 v[104:107], a[192:195], a[244:247], v[104:107]\n"
"s_add_u32 m0, s61, s57\n"
"v_mfma_f32_16x16x32_bf16 v[108:111], a[196:199], a[244:247], v[108:111]\n"
"buffer_load_dwordx4 v46, s[84:87], s60 offen lds\n"
"v_mfma_f32_16x16x32_bf16 v[152:155], a[200:203], a[244:247], v[152:155]\n"
"buffer_load_dwordx4 v47, s[84:87], s60 offen offset:1024 lds\n"
"v_mfma_f32_16x16x32_bf16 v[156:159], a[204:207], a[244:247], v[156:159]\n"
"s_add_u32 m0, m0, 8192\n"
"v_mfma_f32_16x16x32_bf16 v[160:163], a[208:211], a[224:227], v[160:163]\n"
"buffer_load_dwordx4 v48, s[88:91], s60 offen lds\n"
"v_mfma_f32_16x16x32_bf16 v[164:167], a[212:215], a[224:227], v[164:167]\n"
"buffer_load_dwordx4 v49, s[88:91], s60 offen offset:1024 lds\n"
"v_mfma_f32_16x16x32_bf16 v[208:211], a[216:219], a[224:227], v[208:211]\n"
"s_add_u32 s60, s60, 8192\n"
"v_cvt_pk_bf16_f32 v64, v64, v65\n"
"v_mfma_f32_16x16x32_bf16 v[212:215], a[220:223], a[224:227], v[212:215]\n"
"s_add_u32 s61, s61, 16384\n"
"v_cvt_pk_bf16_f32 v65, v66, v67\n"
"v_mfma_f32_16x16x32_bf16 v[168:171], a[208:211], a[228:231], v[168:171]\n"
"s_cmp_eq_u32 s61, 98304\n"
"v_cvt_pk_bf16_f32 v66, v68, v69\n"
"v_cvt_pk_bf16_f32 v67, v70, v71\n"
"v_mfma_f32_16x16x32_bf16 v[172:175], a[212:215], a[228:231], v[172:175]\n"
"s_cselect_b32 s61, 0, s61\n"
"s_add_u32 m0, s61, s57\n"
"v_mfma_f32_16x16x32_bf16 v[216:219], a[216:219], a[228:231], v[216:219]\n"
"buffer_load_dwordx4 v46, s[84:87], s60 offen lds\n"
"v_cvt_pk_bf16_f32 v112, v112, v113\n"
"v_mfma_f32_16x16x32_bf16 v[220:223], a[220:223], a[228:231], v[220:223]\n"
"buffer_load_dwordx4 v47, s[84:87], s60 offen offset:1024 lds\n"
"v_cvt_pk_bf16_f32 v113, v114, v115\n"
"v_mfma_f32_16x16x32_bf16 v[176:179], a[208:211], a[232:235], v[176:179]\n"
"s_add_u32 m0, m0, 8192\n"
"v_cvt_pk_bf16_f32 v114, v116, v117\n"
"v_mfma_f32_16x16x32_bf16 v[180:183], a[212:215], a[232:235], v[180:183]\n"
"buffer_load_dwordx4 v48, s[88:91], s60 offen lds\n"
"v_cvt_pk_bf16_f32 v115, v118, v119\n"
"v_mfma_f32_16x16x32_bf16 v[224:227], a[216:219], a[232:235], v[224:227]\n"
"buffer_load_dwordx4 v49, s[88:91], s60 offen offset:1024 lds\n"
"v_cvt_pk_bf16_f32 v72, v72, v73\n"
"v_cvt_pk_bf16_f32 v73, v74, v75\n"
"v_mfma_f32_16x16x32_bf16 v[228:231], a[220:223], a[232:235], v[228:231]\n"
"s_add_u32 s60, s60, 8192\n"
"v_cvt_pk_bf16_f32 v74, v76, v77\n"
"v_mfma_f32_16x16x32_bf16 v[184:187], a[208:211], a[236:239], v[184:187]\n"
"s_add_u32 s61, s61, 16384\n"
"v_cvt_pk_bf16_f32 v75, v78, v79\n"
"v_cvt_pk_bf16_f32 v88, v88, v89\n"
"v_cvt_pk_bf16_f32 v89, v90, v91\n"
"v_mfma_f32_16x16x32_bf16 v[188:191], a[212:215], a[236:239], v[188:191]\n"
"s_cmp_eq_u32 s61, 98304\n"
"v_cvt_pk_bf16_f32 v120, v120, v121\n"
"v_cvt_pk_bf16_f32 v90, v92, v93\n"
"v_cvt_pk_bf16_f32 v91, v94, v95\n"
"v_mfma_f32_16x16x32_bf16 v[232:235], a[216:219], a[236:239], v[232:235]\n"
"s_cselect_b32 s61, 0, s61\n"
"v_cvt_pk_bf16_f32 v121, v122, v123\n"
"v_cvt_pk_bf16_f32 v136, v136, v137\n"
"v_cvt_pk_bf16_f32 v137, v138, v139\n"
"v_cvt_pk_bf16_f32 v138, v140, v141\n"
"v_mfma_f32_16x16x32_bf16 v[236:239], a[220:223], a[236:239], v[236:239]\n"
"s_add_u32 m0, s61, s57\n"
"v_cvt_pk_bf16_f32 v122, v124, v125\n"
"v_cvt_pk_bf16_f32 v123, v126, v127\n"
"v_cvt_pk_bf16_f32 v139, v142, v143\n"
"v_mfma_f32_16x16x32_bf16 v[192:195], a[208:211], a[240:243], v[192:195]\n"
"buffer_load_dwordx4 v46, s[84:87], s60 offen lds\n"
"v_cvt_pk_bf16_f32 v80, v80, v81\n"
"v_cvt_pk_bf16_f32 v96, v96, v97\n"
"v_cvt_pk_bf16_f32 v97, v98, v99\n"
"v_mfma_f32_16x16x32_bf16 v[196:199], a[212:215], a[240:243], v[196:199]\n"
"buffer_load_dwordx4 v47, s[84:87], s60 offen offset:1024 lds\n"
"s_add_u32 m0, m0, 8192\n"
"v_cvt_pk_bf16_f32 v98, v100, v101\n"
"v_cvt_pk_bf16_f32 v99, v102, v103\n"
"v_mfma_f32_16x16x32_bf16 v[240:243], a[216:219], a[240:243], v[240:243]\n"
"buffer_load_dwordx4 v48, s[88:91], s60 offen lds\n"
"v_cvt_pk_bf16_f32 v81, v82, v83\n"
"v_cvt_pk_bf16_f32 v144, v144, v145\n"
"v_cvt_pk_bf16_f32 v145, v146, v147\n"
"v_mfma_f32_16x16x32_bf16 v[244:247], a[220:223], a[240:243], v[244:247]\n"
"buffer_load_dwordx4 v49, s[88:91], s60 offen offset:1024 lds\n"
"v_cvt_pk_bf16_f32 v82, v84, v85\n"
"v_cvt_pk_bf16_f32 v146, v148, v149\n"
"v_cvt_pk_bf16_f32 v147, v150, v151\n"
"v_mfma_f32_16x16x32_bf16 v[200:203], a[208:211], a[244:247], v[200:203]\n"
"s_add_u32 s60, s60, 8192\n"
"v_cvt_pk_bf16_f32 v83, v86, v87\n"
"v_cvt_pk_bf16_f32 v104, v104, v105\n"
"v_cvt_pk_bf16_f32 v105, v106, v107\n"
"v_cvt_pk_bf16_f32 v106, v108, v109\n"
"v_mfma_f32_16x16x32_bf16 v[204:207], a[212:215], a[244:247], v[204:207]\n"
"s_add_u32 s61, s61, 16384\n"
"v_cvt_pk_bf16_f32 v128, v128, v129\n"
"v_cvt_pk_bf16_f32 v129, v130, v131\n"
"v_cvt_pk_bf16_f32 v107, v110, v111\n"
"v_mfma_f32_16x16x32_bf16 v[248:251], a[216:219], a[244:247], v[248:251]\n"
"s_cmp_eq_u32 s61, 98304\n"
"v_cvt_pk_bf16_f32 v130, v132, v133\n"
"v_cvt_pk_bf16_f32 v152, v152, v153\n"
"v_cvt_pk_bf16_f32 v153, v154, v155\n"
"v_mfma_f32_16x16x32_bf16 v[252:255], a[220:223], a[244:247], v[252:255]\n"
"s_cselect_b32 s61, 0, s61\n"
"v_cvt_pk_bf16_f32 v131, v134, v135\n"
"v_cvt_pk_bf16_f32 v154, v156, v157\n"
"v_cvt_pk_bf16_f32 v155, v158, v159\n"
"; ---- end of section: qproj\n"
"v_cvt_pk_bf16_f32 v160, v160, v161\n"
"v_cvt_pk_bf16_f32 v161, v162, v163\n"
"v_cvt_pk_bf16_f32 v162, v164, v165\n"
"v_cvt_pk_bf16_f32 v163, v166, v167\n"
"v_cvt_pk_bf16_f32 v208, v208, v209\n"
"v_cvt_pk_bf16_f32 v209, v210, v211\n"
"v_cvt_pk_bf16_f32 v210, v212, v213\n"
"v_cvt_pk_bf16_f32 v211, v214, v215\n"
"v_cvt_pk_bf16_f32 v168, v168, v169\n"
"v_cvt_pk_bf16_f32 v169, v170, v171\n"
"v_cvt_pk_bf16_f32 v170, v172, v173\n"
"v_cvt_pk_bf16_f32 v171, v174, v175\n"
"v_cvt_pk_bf16_f32 v216, v216, v217\n"
"v_cvt_pk_bf16_f32 v217, v218, v219\n"
"v_cvt_pk_bf16_f32 v218, v220, v221\n"
"v_cvt_pk_bf16_f32 v219, v222, v223\n"
"v_cvt_pk_bf16_f32 v176, v176, v177\n"
"v_cvt_pk_bf16_f32 v177, v178, v179\n"
"v_cvt_pk_bf16_f32 v178, v180, v181\n"
"v_cvt_pk_bf16_f32 v179, v182, v183\n"
"v_cvt_pk_bf16_f32 v224, v224, v225\n"
"v_cvt_pk_bf16_f32 v225, v226, v227\n"
"v_cvt_pk_bf16_f32 v226, v228, v229\n"
"v_cvt_pk_bf16_f32 v227, v230, v231\n"
"v_cvt_pk_bf16_f32 v184, v184, v185\n"
"v_cvt_pk_bf16_f32 v185, v186, v187\n"
"v_cvt_pk_bf16_f32 v186, v188, v189\n"
"v_cvt_pk_bf16_f32 v187, v190, v191\n"
"v_cvt_pk_bf16_f32 v232, v232, v233\n"
"v_cvt_pk_bf16_f32 v233, v234, v235\n"
"v_cvt_pk_bf16_f32 v234, v236, v237\n"
"v_cvt_pk_bf16_f32 v235, v238, v239\n"
"v_cvt_pk_bf16_f32 v192, v192, v193\n"
"v_cvt_pk_bf16_f32 v193, v194, v195\n"
"v_cvt_pk_bf16_f32 v194, v196, v197\n"
"v_cvt_pk_bf16_f32 v195, v198, v199\n"
"v_cvt_pk_bf16_f32 v240, v240, v241\n"
"v_cvt_pk_bf16_f32 v241, v242, v243\n"
"v_cvt_pk_bf16_f32 v242, v244, v245\n"
"v_cvt_pk_bf16_f32 v243, v246, v247\n"
"v_cvt_pk_bf16_f32 v200, v200, v201\n"
"v_cvt_pk_bf16_f32 v201, v202, v203\n"
"v_cvt_pk_bf16_f32 v202, v204, v205\n"
"v_cvt_pk_bf16_f32 v203, v206, v207\n"
"v_cvt_pk_bf16_f32 v248, v248, v249\n"
"v_cvt_pk_bf16_f32 v249, v250, v251\n"
"v_cvt_pk_bf16_f32 v250, v252, v253\n"
"v_cvt_pk_bf16_f32 v251, v254, v255\n"
"v_mov_b32 v10, 0\n"
"v_mov_b32 v11, 0\n"
"v_mov_b32 v12, 0\n"
"v_mov_b32 v13, 0\n"
"v_mov_b32 v14, 0\n"
"v_mov_b32 v15, 0\n"
"v_mov_b32 v16, 0\n"
"v_mov_b32 v17, 0\n"
"v_mov_b32 v18, 0\n"
"v_mov_b32 v19, 0\n"
"v_mov_b32 v20, 0\n"
"v_mov_b32 v21, 0\n"
"v_mov_b32 v22, 0\n"
"v_mov_b32 v23, 0\n"
"v_mov_b32 v24, 0\n"
"v_mov_b32 v25, 0\n"
"v_mov_b32 v26, 0\n"
"v_mov_b32 v27, 0\n"
"v_mov_b32 v28, 0\n"
"v_mov_b32 v29, 0\n"
"v_mov_b32 v54, 0\n"
"v_mov_b32 v55, 0\n"
"v_mov_b32 v56, 0\n"
"v_mov_b32 v57, 0\n"
"s_waitcnt vmcnt(8)\n"
"s_barrier\n"
"ds_read_b128 a[192:195], v38 offset:0\n"
"ds_read_b128 a[196:199], v39 offset:0\n"
"ds_read_b128 a[200:203], v40 offset:0\n"
"ds_read_b128 a[204:207], v41 offset:0\n"
"ds_read_b128 a[208:211], v38 offset:4096\n"
"ds_read_b128 a[212:215], v39 offset:4096\n"
"ds_read_b128 a[216:219], v40 offset:4096\n"
"ds_read_b128 a[220:223], v41 offset:4096\n"
"; ---- end of section: att_pre\n"
"s_waitcnt lgkmcnt(7)\n"
"v_mfma_f32_16x16x32_bf16 v[68:71], a[192:195], v[64:67], 0\n"
"s_add_u32 s62, s62, 1\n"
"s_cmp_eq_u32 s62, 6\n"
"s_waitcnt lgkmcnt(6)\n"
"v_mfma_f32_16x16x32_bf16 v[68:71], a[196:199], v[112:115], v[68:71]\n"
"s_cselect_b32 s51, s55, s56\n"
"s_cselect_b32 s62, 0, s62\n"
"s_waitcnt lgkmcnt(5)\n"
"v_mfma_f32_16x16x32_bf16 v[68:71], a[200:203], v[160:163], v[68:71]\n"
"v_add_u32 v38, s51, v38\n"
"v_add_u32 v39, s51, v39\n"
"s_waitcnt lgkmcnt(4)\n"
"v_mfma_f32_16x16x32_bf16 v[68:71], a[204:207], v[208:211], v[68:71]\n"
"v_add_u32 v40, s51, v40\n"
"v_add_u32 v41, s51, v41\n"
"v_mfma_f32_16x16x32_bf16 v[76:79], a[192:195], v[72:75], 0\n"
"s_add_u32 s63, s63, 1\n"
"s_cmp_eq_u32 s63, 6\n"
"v_mfma_f32_16x16x32_bf16 v[76:79], a[196:199], v[120:123], v[76:79]\n"
"s_cselect_b32 s52, s55, s56\n"
"s_cselect_b32 s63, 0, s63\n"
"v_mfma_f32_16x16x32_bf16 v[76:79], a[200:203], v[168:171], v[76:79]\n"
"v_add_u32 v42, s52, v42\n"
"v_add_u32 v43, s52, v43\n"
"v_mfma_f32_16x16x32_bf16 v[76:79], a[204:207], v[216:219], v[76:79]\n"
"v_add_u32 v44, s52, v44\n"
"v_add_u32 v45, s52, v45\n"
"v_exp_f32 v68, v68\n"
"v_mfma_f32_16x16x32_bf16 v[84:87], a[192:195], v[80:83], 0\n"
"ds_read_b64_tr_b16 a[224:225], v42 offset:0\n"
"ds_read_b64_tr_b16 a[226:227], v42 offset:4096\n"
"v_exp_f32 v69, v69\n"
"v_mfma_f32_16x16x32_bf16 v[84:87], a[196:199], v[128:131], v[84:87]\n"
"v_exp_f32 v70, v70\n"
"v_cvt_pk_bf16_f32 v164, v68, v69\n"
"v_mfma_f32_16x16x32_bf16 v[84:87], a[200:203], v[176:179], v[84:87]\n"
"s_add_u32 m0, s61, s57\n"
"s_nop 0\n"
"buffer_load_dwordx4 v46, s[84:87], s60 offen lds\n"
"v_exp_f32 v71, v71\n"
"v_mfma_f32_16x16x32_bf16 v[84:87], a[204:207], v[224:227], v[84:87]\n"
"buffer_load_dwordx4 v47, s[84:87], s60 offen offset:1024 lds\n"
"s_add_u32 m0, m0, 8192\n"
"v_exp_f32 v76, v76\n"
"v_mfma_f32_16x16x32_bf16 v[92:95], a[192:195], v[88:91], 0\n"
"ds_read_b64_tr_b16 a[228:229], v43 offset:0\n"
"ds_read_b64_tr_b16 a[230:231], v43 offset:4096\n"
"v_cvt_pk_bf16_f32 v165, v70, v71\n"
"v_mfma_f32_16x16x32_bf16 v[92:95], a[196:199], v[136:139], v[92:95]\n"
"buffer_load_dwordx4 v48, s[88:91], s60 offen lds\n"
"buffer_load_dwordx4 v49, s[88:91], s60 offen offset:1024 lds\n"
"v_exp_f32 v77, v77\n"
"v_mfma_f32_16x16x32_bf16 v[92:95], a[200:203], v[184:187], v[92:95]\n"
"s_add_u32 s60, s60, 8192\n"
"s_add_u32 s61, s61, 16384\n"
"v_exp_f32 v78, v78\n"
"v_mfma_f32_16x16x32_bf16 v[92:95], a[204:207], v[232:235], v[92:95]\n"
"s_cmp_eq_u32 s61, 98304\n"
"s_cselect_b32 s61, 0, s61\n"
"v_cvt_pk_bf16_f32 v172, v76, v77\n"
"v_mfma_f32_16x16x32_bf16 v[100:103], a[192:195], v[96:99], 0\n"
"ds_read_b64_tr_b16 a[232:233], v44 offset:0\n"
"ds_read_b64_tr_b16 a[234:235], v44 offset:4096\n"
"v_exp_f32 v79, v79\n"
"v_mfma_f32_16x16x32_bf16 v[100:103], a[196:199], v[144:147], v[100:103]\n"
"v_exp_f32 v84, v84\n"
"v_cvt_pk_bf16_f32 v173, v78, v79\n"
"v_mfma_f32_16x16x32_bf16 v[100:103], a[200:203], v[192:195], v[100:103]\n"
"v_exp_f32 v85, v85\n"
"v_exp_f32 v86, v86\n"
"v_mfma_f32_16x16x32_bf16 v[100:103], a[204:207], v[240:243], v[100:103]\n"
"v_cvt_pk_bf16_f32 v180, v84, v85\n"
"v_exp_f32 v87, v87\n"
"v_mfma_f32_16x16x32_bf16 v[108:111], a[192:195], v[104:107], 0\n"
"ds_read_b64_tr_b16 a[236:237], v45 offset:0\n"
"ds_read_b64_tr_b16 a[238:239], v45 offset:4096\n"
"v_exp_f32 v92, v92\n"
"v_mfma_f32_16x16x32_bf16 v[108:111], a[196:199], v[152:155], v[108:111]\n"
"v_cvt_pk_bf16_f32 v181, v86, v87\n"
"v_exp_f32 v93, v93\n"
"v_mfma_f32_16x16x32_bf16 v[108:111], a[200:203], v[200:203], v[108:111]\n"
"v_exp_f32 v94, v94\n"
"v_cvt_pk_bf16_f32 v188, v92, v93\n"
"v_mfma_f32_16x16x32_bf16 v[108:111], a[204:207], v[248:251], v[108:111]\n"
"v_exp_f32 v95, v95\n"
"v_exp_f32 v100, v100\n"
"s_waitcnt lgkmcnt(4)\n"
"v_mfma_f32_16x16x32_bf16 v[116:119], a[208:211], v[64:67], 0\n"
"ds_read_b128 a[192:195], v38 offset:0\n"
"ds_read_b64_tr_b16 a[240:241], v42 offset:1024\n"
"ds_read_b64_tr_b16 a[242:243], v42 offset:5120\n"
"v_cvt_pk_bf16_f32 v189, v94, v95\n"
"v_mfma_f32_16x16x32_bf16 v[116:119], a[212:215], v[112:115], v[116:119]\n"
"v_exp_f32 v101, v101\n"
"v_exp_f32 v102, v102\n"
"v_mfma_f32_16x16x32_bf16 v[116:119], a[216:219], v[160:163], v[116:119]\n"
"ds_read_b128 a[196:199], v39 offset:0\n"
"v_cvt_pk_bf16_f32 v196, v100, v101\n"
"v_exp_f32 v103, v103\n"
"v_mfma_f32_16x16x32_bf16 v[116:119], a[220:223], v[208:211], v[116:119]\n"
"v_exp_f32 v108, v108\n"
"v_cvt_pk_bf16_f32 v197, v102, v103\n"
"v_mfma_f32_16x16x32_bf16 v[124:127], a[208:211], v[72:75], 0\n"
"ds_read_b128 a[200:203], v40 offset:0\n"
"ds_read_b64_tr_b16 a[244:245], v43 offset:1024\n"
"ds_read_b64_tr_b16 a[246:247], v43 offset:5120\n"
"v_mfma_f32_16x16x32_bf16 v[124:127], a[212:215], v[120:123], v[124:127]\n"
"v_exp_f32 v109, v109\n"
"v_exp_f32 v110, v110\n"
"v_mfma_f32_16x16x32_bf16 v[124:127], a[216:219], v[168:171], v[124:127]\n"
"ds_read_b128 a[204:207], v41 offset:0\n"
"v_cvt_pk_bf16_f32 v204, v108, v109\n"
"v_exp_f32 v111, v111\n"
"v_mfma_f32_16x16x32_bf16 v[124:127], a[220:223], v[216:219], v[124:127]\n"
"v_exp_f32 v116, v116\n"
"v_cvt_pk_bf16_f32 v205, v110, v111\n"
"v_mfma_f32_16x16x32_bf16 v[132:135], a[208:211], v[80:83], 0\n"
"ds_read_b64_tr_b16 a[248:249], v44 offset:1024\n"
"ds_read_b64_tr_b16 a[250:251], v44 offset:5120\n"
"v_exp_f32 v117, v117\n"
"v_mfma_f32_16x16x32_bf16 v[132:135], a[212:215], v[128:131], v[132:135]\n"
"v_exp_f32 v118, v118\n"
"v_cvt_pk_bf16_f32 v166, v116, v117\n"
"v_mfma_f32_16x16x32_bf16 v[132:135], a[216:219], v[176:179], v[132:135]\n"
"v_exp_f32 v119, v119\n"
"v_mfma_f32_16x16x32_bf16 v[132:135], a[220:223], v[224:227], v[132:135]\n"
"v_exp_f32 v124, v124\n"
"v_cvt_pk_bf16_f32 v167, v118, v119\n"
"v_mfma_f32_16x16x32_bf16 v[140:143], a[208:211], v[88:91], 0\n"
"ds_read_b64_tr_b16 a[252:253], v45 offset:1024\n"
"ds_read_b64_tr_b16 a[254:255], v45 offset:5120\n"
"v_exp_f32 v125, v125\n"
"v_mfma_f32_16x16x32_bf16 v[140:143], a[212:215], v[136:139], v[140:143]\n"
"v_exp_f32 v126, v126\n"
"v_cvt_pk_bf16_f32 v174, v124, v125\n"
"v_mfma_f32_16x16x32_bf16 v[140:143], a[216:219], v[184:187], v[140:143]\n"
"v_exp_f32 v127, v127\n"
"v_mfma_f32_16x16x32_bf16 v[140:143], a[220:223], v[232:235], v[140:143]\n"
"v_exp_f32 v132, v132\n"
"v_cvt_pk_bf16_f32 v175, v126, v127\n"
"v_mfma_f32_16x16x32_bf16 v[148:151], a[208:211], v[96:99], 0\n"
"v_exp_f32 v133, v133\n"
"v_exp_f32 v134, v134\n"
"v_mfma_f32_16x16x32_bf16 v[148:151], a[212:215], v[144:147], v[148:151]\n"
"v_cvt_pk_bf16_f32 v182, v132, v133\n"
"v_exp_f32 v135, v135\n"
"v_mfma_f32_16x16x32_bf16 v[148:151], a[216:219], v[192:195], v[148:151]\n"
"v_mfma_f32_16x16x32_bf16 v[148:151], a[220:223], v[240:243], v[148:151]\n"
"v_exp_f32 v140, v140\n"
"v_cvt_pk_bf16_f32 v183, v134, v135\n"
"v_mfma_f32_16x16x32_bf16 v[156:159], a[208:211], v[104:107], 0\n"
"v_exp_f32 v141, v141\n"
"v_exp_f32 v142, v142\n"
"v_mfma_f32_16x16x32_bf16 v[156:159], a[212:215], v[152:155], v[156:159]\n"
"v_cvt_pk_bf16_f32 v190, v140, v141\n"
"v_exp_f32 v143, v143\n"
"v_mfma_f32_16x16x32_bf16 v[156:159], a[216:219], v[200:203], v[156:159]\n"
"v_mfma_f32_16x16x32_bf16 v[156:159], a[220:223], v[248:251], v[156:159]\n"
"ds_read_b128 a[208:211], v38 offset:4096\n"
"ds_read_b128 a[212:215], v39 offset:4096\n"
"ds_read_b128 a[216:219], v40 offset:4096\n"
"ds_read_b128 a[220:223], v41 offset:4096\n"
"v_exp_f32 v148, v148\n"
"v_cvt_pk_bf16_f32 v191, v142, v143\n"
"v_exp_f32 v149, v149\n"
"v_exp_f32 v150, v150\n"
"v_cvt_pk_bf16_f32 v198, v148, v149\n"
"v_exp_f32 v151, v151\n"
"v_exp_f32 v156, v156\n"
"v_cvt_pk_bf16_f32 v199, v150, v151\n"
"v_exp_f32 v157, v157\n"
"v_exp_f32 v158, v158\n"
"v_cvt_pk_bf16_f32 v206, v156, v157\n"
"v_exp_f32 v159, v159\n"
"s_nop 0\n"
"v_cvt_pk_bf16_f32 v207, v158, v159\n"
"s_waitcnt vmcnt(4)\n"
"s_mov_b32 s47, 7\n"
"s_waitcnt lgkmcnt(0)\n"
"LAT_%=:\n"
"s_barrier\n"
"v_mfma_f32_16x16x32_bf16 v[68:71], a[192:195], v[64:67], 0\n"
"s_add_u32 s62, s62, 1\n"
"s_cmp_eq_u32 s62, 6\n"
"v_mfma_f32_16x16x32_bf16 v[68:71], a[196:199], v[112:115], v[68:71]\n"
"s_cselect_b32 s51, s55, s56\n"
"s_cselect_b32 s62, 0, s62\n"
"v_mfma_f32_16x16x32_bf16 v[68:71], a[200:203], v[160:163], v[68:71]\n"
"v_add_u32 v38, s51, v38\n"
"v_add_u32 v39, s51, v39\n"
"v_mfma_f32_16x16x32_bf16 v[68:71], a[204:207], v[208:211], v[68:71]\n"
"v_add_u32 v40, s51, v40\n"
"v_add_u32 v41, s51, v41\n"
"v_mfma_f32_16x16x32_bf16 v[76:79], a[192:195], v[72:75], 0\n"
"s_add_u32 s63, s63, 1\n"
"s_cmp_eq_u32 s63, 6\n"
"v_mfma_f32_16x16x32_bf16 v[76:79], a[196:199], v[120:123], v[76:79]\n"
"s_cselect_b32 s52, s55, s56\n"
"s_cselect_b32 s63, 0, s63\n"
"v_mfma_f32_16x16x32_bf16 v[76:79], a[200:203], v[168:171], v[76:79]\n"
"v_add_u32 v42, s52, v42\n"
"v_add_u32 v43, s52, v43\n"
"v_mfma_f32_16x16x32_bf16 v[76:79], a[204:207], v[216:219], v[76:79]\n"
"v_add_u32 v44, s52, v44\n"
"v_add_u32 v45, s52, v45\n"
"v_mfma_f32_16x16x32_bf16 v[84:87], a[192:195], v[80:83], 0\n"
"v_exp_f32 v68, v68\n"
"v_mfma_f32_16x16x32_bf16 v[84:87], a[196:199], v[128:131], v[84:87]\n"
"v_exp_f32 v69, v69\n"
"v_mfma_f32_16x16x32_bf16 v[84:87], a[200:203], v[176:179], v[84:87]\n"
"s_add_u32 m0, s61, s57\n"
"s_nop 0\n"
"buffer_load_dwordx4 v46, s[84:87], s60 offen lds\n"
"v_mfma_f32_16x16x32_bf16 v[84:87], a[204:207], v[224:227], v[84:87]\n"
"buffer_load_dwordx4 v47, s[84:87], s60 offen offset:1024 lds\n"
"s_add_u32 m0, m0, 8192\n"
"v_mfma_f32_16x16x32_bf16 v[92:95], a[192:195], v[88:91], 0\n"
"buffer_load_dwordx4 v48, s[88:91], s60 offen lds\n"
"buffer_load_dwordx4 v49, s[88:91], s60 offen offset:1024 lds\n"
"v_mfma_f32_16x16x32_bf16 v[92:95], a[196:199], v[136:139], v[92:95]\n"
"s_add_u32 s60, s60, 8192\n"
"s_add_u32 s61, s61, 16384\n"
"v_mfma_f32_16x16x32_bf16 v[92:95], a[200:203], v[184:187], v[92:95]\n"
"s_cmp_eq_u32 s61, 98304\n"
"s_cselect_b32 s61, 0, s61\n"
"v_mfma_f32_16x16x32_bf16 v[92:95], a[204:207], v[232:235], v[92:95]\n"
"v_exp_f32 v70, v70\n"
"v_mfma_f32_16x16x32_bf16 v[100:103], a[192:195], v[96:99], 0\n"
"v_cvt_pk_bf16_f32 v212, v68, v69\n"
"v_mfma_f32_16x16x32_bf16 v[100:103], a[196:199], v[144:147], v[100:103]\n"
"v_exp_f32 v71, v71\n"
"v_mfma_f32_16x16x32_bf16 v[100:103], a[200:203], v[192:195], v[100:103]\n"
"v_exp_f32 v76, v76\n"
"v_mfma_f32_16x16x32_bf16 v[100:103], a[204:207], v[240:243], v[100:103]\n"
"v_cvt_pk_bf16_f32 v213, v70, v71\n"
"v_mfma_f32_16x16x32_bf16 v[108:111], a[192:195], v[104:107], 0\n"
"v_exp_f32 v77, v77\n"
"v_mfma_f32_16x16x32_bf16 v[108:111], a[196:199], v[152:155], v[108:111]\n"
"v_exp_f32 v78, v78\n"
"v_mfma_f32_16x16x32_bf16 v[108:111], a[200:203], v[200:203], v[108:111]\n"
"v_cvt_pk_bf16_f32 v220, v76, v77\n"
"v_mfma_f32_16x16x32_bf16 v[108:111], a[204:207], v[248:251], v[108:111]\n"
"v_exp_f32 v79, v79\n"
"v_mfma_f32_16x16x32_bf16 v[116:119], a[208:211], v[64:67], 0\n"
"ds_read_b128 a[192:195], v38 offset:0\n"
"v_mfma_f32_16x16x32_bf16 v[116:119], a[212:215], v[112:115], v[116:119]\n"
"v_exp_f32 v84, v84\n"
"s_waitcnt lgkmcnt(1)\n"
"v_mfma_f32_16x16x32_bf16 v[116:119], a[216:219], v[160:163], v[116:119]\n"
"ds_read_b128 a[196:199], v39 offset:0\n"
"v_cvt_pk_bf16_f32 v221, v78, v79\n"
"v_mfma_f32_16x16x32_bf16 v[116:119], a[220:223], v[208:211], v[116:119]\n"
"v_exp_f32 v85, v85\n"
"v_mfma_f32_16x16x32_bf16 v[124:127], a[208:211], v[72:75], 0\n"
"ds_read_b128 a[200:203], v40 offset:0\n"
"v_mfma_f32_16x16x32_bf16 v[124:127], a[212:215], v[120:123], v[124:127]\n"
"v_exp_f32 v86, v86\n"
"v_mfma_f32_16x16x32_bf16 v[124:127], a[216:219], v[168:171], v[124:127]\n"
"ds_read_b128 a[204:207], v41 offset:0\n"
"v_cvt_pk_bf16_f32 v228, v84, v85\n"
"v_mfma_f32_16x16x32_bf16 v[124:127], a[220:223], v[216:219], v[124:127]\n"
"v_exp_f32 v87, v87\n"
"v_mfma_f32_16x16x32_bf16 v[132:135], a[208:211], v[80:83], 0\n"
"v_exp_f32 v92, v92\n"
"v_mfma_f32_16x16x32_bf16 v[132:135], a[212:215], v[128:131], v[132:135]\n"
"v_cvt_pk_bf16_f32 v229, v86, v87\n"
"v_mfma_f32_16x16x32_bf16 v[132:135], a[216:219], v[176:179], v[132:135]\n"
"v_exp_f32 v93, v93\n"
"v_mfma_f32_16x16x32_bf16 v[132:135], a[220:223], v[224:227], v[132:135]\n"
"v_exp_f32 v94, v94\n"
"v_mfma_f32_16x16x32_bf16 v[140:143], a[208:211], v[88:91], 0\n"
"v_cvt_pk_bf16_f32 v236, v92, v93\n"
"v_mfma_f32_16x16x32_bf16 v[140:143], a[212:215], v[136:139], v[140:143]\n"
"v_exp_f32 v95, v95\n"
"v_mfma_f32_16x16x32_bf16 v[140:143], a[216:219], v[184:187], v[140:143]\n"
"v_exp_f32 v100, v100\n"
"v_mfma_f32_16x16x32_bf16 v[140:143], a[220:223], v[232:235], v[140:143]\n"
"v_cvt_pk_bf16_f32 v237, v94, v95\n"
"v_mfma_f32_16x16x32_bf16 v[148:151], a[208:211], v[96:99], 0\n"
"v_exp_f32 v101, v101\n"
"v_mfma_f32_16x16x32_bf16 v[148:151], a[212:215], v[144:147], v[148:151]\n"
"v_exp_f32 v102, v102\n"
"v_mfma_f32_16x16x32_bf16 v[148:151], a[216:219], v[192:195], v[148:151]\n"
"v_cvt_pk_bf16_f32 v244, v100, v101\n"
"v_mfma_f32_16x16x32_bf16 v[148:151], a[220:223], v[240:243], v[148:151]\n"
"v_exp_f32 v103, v103\n"
"v_mfma_f32_16x16x32_bf16 v[156:159], a[208:211], v[104:107], 0\n"
"v_exp_f32 v108, v108\n"
"v_mfma_f32_16x16x32_bf16 v[156:159], a[212:215], v[152:155], v[156:159]\n"
"v_cvt_pk_bf16_f32 v245, v102, v103\n"
"v_mfma_f32_16x16x32_bf16 v[156:159], a[216:219], v[200:203], v[156:159]\n"
"v_exp_f32 v109, v109\n"
"v_mfma_f32_16x16x32_bf16 v[156:159], a[220:223], v[248:251], v[156:159]\n"
"v_exp_f32 v110, v110\n"
"v_mfma_f32_16x16x32_bf16 a[0:3], a[224:227], v[164:167], a[0:3]\n"
"ds_read_b128 a[208:211], v38 offset:4096\n"
"v_cvt_pk_bf16_f32 v252, v108, v109\n"
"v_mfma_f32_16x16x32_bf16 a[4:7], a[224:227], v[172:175], a[4:7]\n"
"v_exp_f32 v111, v111\n"
"v_mfma_f32_16x16x32_bf16 a[8:11], a[224:227], v[180:183], a[8:11]\n"
"ds_read_b128 a[212:215], v39 offset:4096\n"
"v_mfma_f32_16x16x32_bf16 a[12:15], a[224:227], v[188:191], a[12:15]\n"
"v_exp_f32 v116, v116\n"
"v_mfma_f32_16x16x32_bf16 a[16:19], a[224:227], v[196:199], a[16:19]\n"
"ds_read_b128 a[216:219], v40 offset:4096\n"
"v_cvt_pk_bf16_f32 v253, v110, v111\n"
"v_mfma_f32_16x16x32_bf16 a[20:23], a[224:227], v[204:207], a[20:23]\n"
"ds_read_b64_tr_b16 a[224:225], v42 offset:0\n"
"ds_read_b64_tr_b16 a[226:227], v42 offset:4096\n"
"v_mfma_f32_16x16x32_bf16 v[10:13], v[50:53], v[164:167], v[10:13]\n"
"ds_read_b128 a[220:223], v41 offset:4096\n"
"v_mfma_f32_16x16x32_bf16 a[24:27], a[228:231], v[164:167], a[24:27]\n"
"v_exp_f32 v117, v117\n"
"v_mfma_f32_16x16x32_bf16 a[28:31], a[228:231], v[172:175], a[28:31]\n"
"v_exp_f32 v118, v118\n"
"v_mfma_f32_16x16x32_bf16 a[32:35], a[228:231], v[180:183], a[32:35]\n"
"v_cvt_pk_bf16_f32 v214, v116, v117\n"
"v_mfma_f32_16x16x32_bf16 a[36:39], a[228:231], v[188:191], a[36:39]\n"
"v_exp_f32 v119, v119\n"
"v_mfma_f32_16x16x32_bf16 a[40:43], a[228:231], v[196:199], a[40:43]\n"
"v_exp_f32 v124, v124\n"
"v_mfma_f32_16x16x32_bf16 a[44:47], a[228:231], v[204:207], a[44:47]\n"
"ds_read_b64_tr_b16 a[228:229], v43 offset:0\n"
"ds_read_b64_tr_b16 a[230:231], v43 offset:4096\n"
"v_mfma_f32_16x16x32_bf16 v[14:17], v[50:53], v[172:175], v[14:17]\n"
"v_cvt_pk_bf16_f32 v215, v118, v119\n"
"v_mfma_f32_16x16x32_bf16 a[48:51], a[232:235], v[164:167], a[48:51]\n"
"v_exp_f32 v125, v125\n"
"v_mfma_f32_16x16x32_bf16 a[52:55], a[232:235], v[172:175], a[52:55]\n"
"v_exp_f32 v126, v126\n"
"v_mfma_f32_16x16x32_bf16 a[56:59], a[232:235], v[180:183], a[56:59]\n"
"v_cvt_pk_bf16_f32 v222, v124, v125\n"
"v_mfma_f32_16x16x32_bf16 a[60:63], a[232:235], v[188:191], a[60:63]\n"
"v_exp_f32 v127, v127\n"
"v_mfma_f32_16x16x32_bf16 a[64:67], a[232:235], v[196:199], a[64:67]\n"
"v_exp_f32 v132, v132\n"
"v_mfma_f32_16x16x32_bf16 a[68:71], a[232:235], v[204:207], a[68:71]\n"
"ds_read_b64_tr_b16 a[232:233], v44 offset:0\n"
"ds_read_b64_tr_b16 a[234:235], v44 offset:4096\n"
"v_mfma_f32_16x16x32_bf16 v[18:21], v[50:53], v[180:183], v[18:21]\n"
"v_cvt_pk_bf16_f32 v223, v126, v127\n"
"v_mfma_f32_16x16x32_bf16 a[72:75], a[236:239], v[164:167], a[72:75]\n"
"v_exp_f32 v133, v133\n"
"v_mfma_f32_16x16x32_bf16 a[76:79], a[236:239], v[172:175], a[76:79]\n"
"v_exp_f32 v134, v134\n"
"v_mfma_f32_16x16x32_bf16 a[80:83], a[236:239], v[180:183], a[80:83]\n"
"v_cvt_pk_bf16_f32 v230, v132, v133\n"
"v_mfma_f32_16x16x32_bf16 a[84:87], a[236:239], v[188:191], a[84:87]\n"
"v_exp_f32 v135, v135\n"
"v_mfma_f32_16x16x32_bf16 a[88:91], a[236:239], v[196:199], a[88:91]\n"
"v_exp_f32 v140, v140\n"
"v_mfma_f32_16x16x32_bf16 a[92:95], a[236:239], v[204:207], a[92:95]\n"
"ds_read_b64_tr_b16 a[236:237], v45 offset:0\n"
"ds_read_b64_tr_b16 a[238:239], v45 offset:4096\n"
"v_mfma_f32_16x16x32_bf16 v[22:25], v[50:53], v[188:191], v[22:25]\n"
"v_cvt_pk_bf16_f32 v231, v134, v135\n"
"s_waitcnt lgkmcnt(4)\n"
"v_mfma_f32_16x16x32_bf16 a[96:99], a[240:243], v[164:167], a[96:99]\n"
"v_exp_f32 v141, v141\n"
"v_mfma_f32_16x16x32_bf16 a[100:103], a[240:243], v[172:175], a[100:103]\n"
"v_exp_f32 v142, v142\n"
"v_mfma_f32_16x16x32_bf16 a[104:107], a[240:243], v[180:183], a[104:107]\n"
"v_cvt_pk_bf16_f32 v238, v140, v141\n"
"v_mfma_f32_16x16x32_bf16 a[108:111], a[240:243], v[188:191], a[108:111]\n"
"v_exp_f32 v143, v143\n"
"v_mfma_f32_16x16x32_bf16 a[112:115], a[240:243], v[196:199], a[112:115]\n"
"v_exp_f32 v148, v148\n"
"v_mfma_f32_16x16x32_bf16 a[116:119], a[240:243], v[204:207], a[116:119]\n"
"ds_read_b64_tr_b16 a[240:241], v42 offset:1024\n"
"ds_read_b64_tr_b16 a[242:243], v42 offset:5120\n"
"v_mfma_f32_16x16x32_bf16 v[26:29], v[50:53], v[196:199], v[26:29]\n"
"v_cvt_pk_bf16_f32 v239, v142, v143\n"
"v_mfma_f32_16x16x32_bf16 a[120:123], a[244:247], v[164:167], a[120:123]\n"
"v_exp_f32 v149, v149\n"
"v_mfma_f32_16x16x32_bf16 a[124:127], a[244:247], v[172:175], a[124:127]\n"
"v_exp_f32 v150, v150\n"
"v_mfma_f32_16x16x32_bf16 a[128:131], a[244:247], v[180:183], a[128:131]\n"
"v_cvt_pk_bf16_f32 v246, v148, v149\n"
"v_mfma_f32_16x16x32_bf16 a[132:135], a[244:247], v[188:191], a[132:135]\n"
"v_exp_f32 v151, v151\n"
"v_mfma_f32_16x16x32_bf16 a[136:139], a[244:247], v[196:199], a[136:139]\n"
"v_exp_f32 v156, v156\n"
"v_mfma_f32_16x16x32_bf16 a[140:143], a[244:247], v[204:207], a[140:143]\n"
"ds_read_b64_tr_b16 a[244:245], v43 offset:1024\n"
"ds_read_b64_tr_b16 a[246:247], v43 offset:5120\n"
"v_mfma_f32_16x16x32_bf16 v[54:57], v[50:53], v[204:207], v[54:57]\n"
"v_cvt_pk_bf16_f32 v247, v150, v151\n"
"v_mfma_f32_16x16x32_bf16 a[144:147], a[248:251], v[164:167], a[144:147]\n"
"v_exp_f32 v157, v157\n"
"v_mfma_f32_16x16x32_bf16 a[148:151], a[248:251], v[172:175], a[148:151]\n"
"v_exp_f32 v158, v158\n"
"v_mfma_f32_16x16x32_bf16 a[152:155], a[248:251], v[180:183], a[152:155]\n"
"v_cvt_pk_bf16_f32 v254, v156, v157\n"
"v_mfma_f32_16x16x32_bf16 a[156:159], a[248:251], v[188:191], a[156:159]\n"
"v_exp_f32 v159, v159\n"
"v_mfma_f32_16x16x32_bf16 a[160:163], a[248:251], v[196:199], a[160:163]\n"
"s_nop 0\n"
"v_cvt_pk_bf16_f32 v255, v158, v159\n"
"v_mfma_f32_16x16x32_bf16 a[164:167], a[248:251], v[204:207], a[164:167]\n"
"ds_read_b64_tr_b16 a[248:249], v44 offset:1024\n"
"ds_read_b64_tr_b16 a[250:251], v44 offset:5120\n"
"v_mfma_f32_16x16x32_bf16 a[168:171], a[252:255], v[164:167], a[168:171]\n"
"v_mfma_f32_16x16x32_bf16 a[172:175], a[252:255], v[172:175], a[172:175]\n"
"v_mfma_f32_16x16x32_bf16 a[176:179], a[252:255], v[180:183], a[176:179]\n"
"v_mfma_f32_16x16x32_bf16 a[180:183], a[252:255], v[188:191], a[180:183]\n"
"v_mfma_f32_16x16x32_bf16 a[184:187], a[252:255], v[196:199], a[184:187]\n"
"v_mfma_f32_16x16x32_bf16 a[188:191], a[252:255], v[204:207], a[188:191]\n"
"ds_read_b64_tr_b16 a[252:253], v45 offset:1024\n"
"ds_read_b64_tr_b16 a[254:255], v45 offset:5120\n"
"v_mfma_f32_16x16x32_bf16 v[68:71], a[192:195], v[64:67], 0\n"
"s_add_u32 s62, s62, 1\n"
"s_cmp_eq_u32 s62, 6\n"
"v_mfma_f32_16x16x32_bf16 v[68:71], a[196:199], v[112:115], v[68:71]\n"
"s_cselect_b32 s51, s55, s56\n"
"s_cselect_b32 s62, 0, s62\n"
"v_mfma_f32_16x16x32_bf16 v[68:71], a[200:203], v[160:163], v[68:71]\n"
"v_add_u32 v38, s51, v38\n"
"v_add_u32 v39, s51, v39\n"
"v_mfma_f32_16x16x32_bf16 v[68:71], a[204:207], v[208:211], v[68:71]\n"
"v_add_u32 v40, s51, v40\n"
"v_add_u32 v41, s51, v41\n"
"v_mfma_f32_16x16x32_bf16 v[76:79], a[192:195], v[72:75], 0\n"
"s_add_u32 s63, s63, 1\n"
"s_cmp_eq_u32 s63, 6\n"
"v_mfma_f32_16x16x32_bf16 v[76:79], a[196:199], v[120:123], v[76:79]\n"
"s_cselect_b32 s52, s55, s56\n"
"s_cselect_b32 s63, 0, s63\n"
"v_mfma_f32_16x16x32_bf16 v[76:79], a[200:203], v[168:171], v[76:79]\n"
"v_add_u32 v42, s52, v42\n"
"v_add_u32 v43, s52, v43\n"
"v_mfma_f32_16x16x32_bf16 v[76:79], a[204:207], v[216:219], v[76:79]\n"
"v_add_u32 v44, s52, v44\n"
"v_add_u32 v45, s52, v45\n"
"v_mfma_f32_16x16x32_bf16 v[84:87], a[192:195], v[80:83], 0\n"
"v_exp_f32 v68, v68\n"
"v_mfma_f32_16x16x32_bf16 v[84:87], a[196:199], v[128:131], v[84:87]\n"
"v_exp_f32 v69, v69\n"
"v_mfma_f32_16x16x32_bf16 v[84:87], a[200:203], v[176:179], v[84:87]\n"
"s_add_u32 m0, s61, s57\n"
"s_nop 0\n"
"buffer_load_dwordx4 v46, s[84:87], s60 offen lds\n"
"v_mfma_f32_16x16x32_bf16 v[84:87], a[204:207], v[224:227], v[84:87]\n"
"buffer_load_dwordx4 v47, s[84:87], s60 offen offset:1024 lds\n"
"s_add_u32 m0, m0, 8192\n"
"v_mfma_f32_16x16x32_bf16 v[92:95], a[192:195], v[88:91], 0\n"
"buffer_load_dwordx4 v48, s[88:91], s60 offen lds\n"
"buffer_load_dwordx4 v49, s[88:91], s60 offen offset:1024 lds\n"
"v_mfma_f32_16x16x32_bf16 v[92:95], a[196:199], v[136:139], v[92:95]\n"
"s_add_u32 s60, s60, 8192\n"
"s_add_u32 s61, s61, 16384\n"
"v_mfma_f32_16x16x32_bf16 v[92:95], a[200:203], v[184:187], v[92:95]\n"
"s_cmp_eq_u32 s61, 98304\n"
"s_cselect_b32 s61, 0, s61\n"
"v_mfma_f32_16x16x32_bf16 v[92:95], a[204:207], v[232:235], v[92:95]\n"
"v_exp_f32 v70, v70\n"
"v_mfma_f32_16x16x32_bf16 v[100:103], a[192:195], v[96:99], 0\n"
"v_cvt_pk_bf16_f32 v164, v68, v69\n"
"v_mfma_f32_16x16x32_bf16 v[100:103], a[196:199], v[144:147], v[100:103]\n"
"v_exp_f32 v71, v71\n"
"v_mfma_f32_16x16x32_bf16 v[100:103], a[200:203], v[192:195], v[100:103]\n"
"v_exp_f32 v76, v76\n"
"v_mfma_f32_16x16x32_bf16 v[100:103], a[204:207], v[240:243], v[100:103]\n"
"v_cvt_pk_bf16_f32 v165, v70, v71\n"
"v_mfma_f32_16x16x32_bf16 v[108:111], a[192:195], v[104:107], 0\n"
"v_exp_f32 v77, v77\n"
"v_mfma_f32_16x16x32_bf16 v[108:111], a[196:199], v[152:155], v[108:111]\n"
"v_exp_f32 v78, v78\n"
"v_mfma_f32_16x16x32_bf16 v[108:111], a[200:203], v[200:203], v[108:111]\n"
"v_cvt_pk_bf16_f32 v172, v76, v77\n"
"v_mfma_f32_16x16x32_bf16 v[108:111], a[204:207], v[248:251], v[108:111]\n"
"v_exp_f32 v79, v79\n"
"v_mfma_f32_16x16x32_bf16 v[116:119], a[208:211], v[64:67], 0\n"
"ds_read_b128 a[192:195], v38 offset:0\n"
"v_mfma_f32_16x16x32_bf16 v[116:119], a[212:215], v[112:115], v[116:119]\n"
"v_exp_f32 v84, v84\n"
"v_mfma_f32_16x16x32_bf16 v[116:119], a[216:219], v[160:163], v[116:119]\n"
"ds_read_b128 a[196:199], v39 offset:0\n"
"v_cvt_pk_bf16_f32 v173, v78, v79\n"
"v_mfma_f32_16x16x32_bf16 v[116:119], a[220:223], v[208:211], v[116:119]\n"
"v_exp_f32 v85, v85\n"
"v_mfma_f32_16x16x32_bf16 v[124:127], a[208:211], v[72:75], 0\n"
"ds_read_b128 a[200:203], v40 offset:0\n"
"v_mfma_f32_16x16x32_bf16 v[124:127], a[212:215], v[120:123], v[124:127]\n"
"v_exp_f32 v86, v86\n"
"v_mfma_f32_16x16x32_bf16 v[124:127], a[216:219], v[168:171], v[124:127]\n"
"ds_read_b128 a[204:207], v41 offset:0\n"
"v_cvt_pk_bf16_f32 v180, v84, v85\n"
"v_mfma_f32_16x16x32_bf16 v[124:127], a[220:223], v[216:219], v[124:127]\n"
"v_exp_f32 v87, v87\n"
"v_mfma_f32_16x16x32_bf16 v[132:135], a[208:211], v[80:83], 0\n"
"v_exp_f32 v92, v92\n"
"v_mfma_f32_16x16x32_bf16 v[132:135], a[212:215], v[128:131], v[132:135]\n"
"v_cvt_pk_bf16_f32 v181, v86, v87\n"
"v_mfma_f32_16x16x32_bf16 v[132:135], a[216:219], v[176:179], v[132:135]\n"
"v_exp_f32 v93, v93\n"
"v_mfma_f32_16x16x32_bf16 v[132:135], a[220:223], v[224:227], v[132:135]\n"
"v_exp_f32 v94, v94\n"
"v_mfma_f32_16x16x32_bf16 v[140:143], a[208:211], v[88:91], 0\n"
"v_cvt_pk_bf16_f32 v188, v92, v93\n"
"v_mfma_f32_16x16x32_bf16 v[140:143], a[212:215], v[136:139], v[140:143]\n"
"v_exp_f32 v95, v95\n"
"v_mfma_f32_16x16x32_bf16 v[140:143], a[216:219], v[184:187], v[140:143]\n"
"v_exp_f32 v100, v100\n"
"v_mfma_f32_16x16x32_bf16 v[140:143], a[220:223], v[232:235], v[140:143]\n"
"v_cvt_pk_bf16_f32 v189, v94, v95\n"
"v_mfma_f32_16x16x32_bf16 v[148:151], a[208:211], v[96:99], 0\n"
"v_exp_f32 v101, v101\n"
"v_mfma_f32_16x16x32_bf16 v[148:151], a[212:215], v[144:147], v[148:151]\n"
"v_exp_f32 v102, v102\n"
"v_mfma_f32_16x16x32_bf16 v[148:151], a[216:219], v[192:195], v[148:151]\n"
"v_cvt_pk_bf16_f32 v196, v100, v101\n"
"v_mfma_f32_16x16x32_bf16 v[148:151], a[220:223], v[240:243], v[148:151]\n"
"v_exp_f32 v103, v103\n"
"v_mfma_f32_16x16x32_bf16 v[156:159], a[208:211], v[104:107], 0\n"
"v_exp_f32 v108, v108\n"
"v_mfma_f32_16x16x32_bf16 v[156:159], a[212:215], v[152:155], v[156:159]\n"
"v_cvt_pk_bf16_f32 v197, v102, v103\n"
"v_mfma_f32_16x16x32_bf16 v[156:159], a[216:219], v[200:203], v[156:159]\n"
"v_exp_f32 v109, v109\n"
"v_mfma_f32_16x16x32_bf16 v[156:159], a[220:223], v[248:251], v[156:159]\n"
"v_exp_f32 v110, v110\n"
"v_mfma_f32_16x16x32_bf16 a[0:3], a[224:227], v[212:215], a[0:3]\n"
"ds_read_b128 a[208:211], v38 offset:4096\n"
"v_cvt_pk_bf16_f32 v204, v108, v109\n"
"v_mfma_f32_16x16x32_bf16 a[4:7], a[224:227], v[220:223], a[4:7]\n"
"v_exp_f32 v111, v111\n"
"v_mfma_f32_16x16x32_bf16 a[8:11], a[224:227], v[228:231], a[8:11]\n"
"ds_read_b128 a[212:215], v39 offset:4096\n"
"v_mfma_f32_16x16x32_bf16 a[12:15], a[224:227], v[236:239], a[12:15]\n"
"v_exp_f32 v116, v116\n"
"v_mfma_f32_16x16x32_bf16 a[16:19], a[224:227], v[244:247], a[16:19]\n"
"ds_read_b128 a[216:219], v40 offset:4096\n"
"v_cvt_pk_bf16_f32 v205, v110, v111\n"
"v_mfma_f32_16x16x32_bf16 a[20:23], a[224:227], v[252:255], a[20:23]\n"
"ds_read_b64_tr_b16 a[224:225], v42 offset:0\n"
"ds_read_b64_tr_b16 a[226:227], v42 offset:4096\n"
"v_mfma_f32_16x16x32_bf16 v[10:13], v[50:53], v[212:215], v[10:13]\n"
"ds_read_b128 a[220:223], v41 offset:4096\n"
"v_mfma_f32_16x16x32_bf16 a[24:27], a[228:231], v[212:215], a[24:27]\n"
"v_exp_f32 v117, v117\n"
"v_mfma_f32_16x16x32_bf16 a[28:31], a[228:231], v[220:223], a[28:31]\n"
"v_exp_f32 v118, v118\n"
"v_mfma_f32_16x16x32_bf16 a[32:35], a[228:231], v[228:231], a[32:35]\n"
"v_cvt_pk_bf16_f32 v166, v116, v117\n"
"v_mfma_f32_16x16x32_bf16 a[36:39], a[228:231], v[236:239], a[36:39]\n"
"v_exp_f32 v119, v119\n"
"v_mfma_f32_16x16x32_bf16 a[40:43], a[228:231], v[244:247], a[40:43]\n"
"v_exp_f32 v124, v124\n"
"v_mfma_f32_16x16x32_bf16 a[44:47], a[228:231], v[252:255], a[44:47]\n"
"ds_read_b64_tr_b16 a[228:229], v43 offset:0\n"
"ds_read_b64_tr_b16 a[230:231], v43 offset:4096\n"
"v_mfma_f32_16x16x32_bf16 v[14:17], v[50:53], v[220:223], v[14:17]\n"
"v_cvt_pk_bf16_f32 v167, v118, v119\n"
"s_waitcnt lgkmcnt(6)\n"
"v_mfma_f32_16x16x32_bf16 a[48:51], a[232:235], v[212:215], a[48:51]\n"
"v_exp_f32 v125, v125\n"
"v_mfma_f32_16x16x32_bf16 a[52:55], a[232:235], v[220:223], a[52:55]\n"
"v_exp_f32 v126, v126\n"
"v_mfma_f32_16x16x32_bf16 a[56:59], a[232:235], v[228:231], a[56:59]\n"
"v_cvt_pk_bf16_f32 v174, v124, v125\n"
"v_mfma_f32_16x16x32_bf16 a[60:63], a[232:235], v[236:239], a[60:63]\n"
"v_exp_f32 v127, v127\n"
"v_mfma_f32_16x16x32_bf16 a[64:67], a[232:235], v[244:247], a[64:67]\n"
"v_exp_f32 v132, v132\n"
"v_mfma_f32_16x16x32_bf16 a[68:71], a[232:235], v[252:255], a[68:71]\n"
"ds_read_b64_tr_b16 a[232:233], v44 offset:0\n"
"ds_read_b64_tr_b16 a[234:235], v44 offset:4096\n"
"v_mfma_f32_16x16x32_bf16 v[18:21], v[50:53], v[228:231], v[18:21]\n"
"v_cvt_pk_bf16_f32 v175, v126, v127\n"
"v_mfma_f32_16x16x32_bf16 a[72:75], a[236:239], v[212:215], a[72:75]\n"
"v_exp_f32 v133, v133\n"
"v_mfma_f32_16x16x32_bf16 a[76:79], a[236:239], v[220:223], a[76:79]\n"
"v_exp_f32 v134, v134\n"
"v_mfma_f32_16x16x32_bf16 a[80:83], a[236:239], v[228:231], a[80:83]\n"
"v_cvt_pk_bf16_f32 v182, v132, v133\n"
"v_mfma_f32_16x16x32_bf16 a[84:87], a[236:239], v[236:239], a[84:87]\n"
"v_exp_f32 v135, v135\n"
"v_mfma_f32_16x16x32_bf16 a[88:91], a[236:239], v[244:247], a[88:91]\n"
"v_exp_f32 v140, v140\n"
"v_mfma_f32_16x16x32_bf16 a[92:95], a[236:239], v[252:255], a[92:95]\n"
"ds_read_b64_tr_b16 a[236:237], v45 offset:0\n"
"ds_read_b64_tr_b16 a[238:239], v45 offset:4096\n"
"v_mfma_f32_16x16x32_bf16 v[22:25], v[50:53], v[236:239], v[22:25]\n"
"v_cvt_pk_bf16_f32 v183, v134, v135\n"
"v_mfma_f32_16x16x32_bf16 a[96:99], a[240:243], v[212:215], a[96:99]\n"
"v_exp_f32 v141, v141\n"
"v_mfma_f32_16x16x32_bf16 a[100:103], a[240:243], v[220:223], a[100:103]\n"
"v_exp_f32 v142, v142\n"
"v_mfma_f32_16x16x32_bf16 a[104:107], a[240:243], v[228:231], a[104:107]\n"
"v_cvt_pk_bf16_f32 v190, v140, v141\n"
"v_mfma_f32_16x16x32_bf16 a[108:111], a[240:243], v[236:239], a[108:111]\n"
"v_exp_f32 v143, v143\n"
"v_mfma_f32_16x16x32_bf16 a[112:115], a[240:243], v[244:247], a[112:115]\n"
"v_exp_f32 v148, v148\n"
"v_mfma_f32_16x16x32_bf16 a[116:119], a[240:243], v[252:255], a[116:119]\n"
"ds_read_b64_tr_b16 a[240:241], v42 offset:1024\n"
"ds_read_b64_tr_b16 a[242:243], v42 offset:5120\n"
"v_mfma_f32_16x16x32_bf16 v[26:29], v[50:53], v[244:247], v[26:29]\n"
"v_cvt_pk_bf16_f32 v191, v142, v143\n"
"v_mfma_f32_16x16x32_bf16 a[120:123], a[244:247], v[212:215], a[120:123]\n"
"v_exp_f32 v149, v149\n"
"v_mfma_f32_16x16x32_bf16 a[124:127], a[244:247], v[220:223], a[124:127]\n"
"v_exp_f32 v150, v150\n"
"v_mfma_f32_16x16x32_bf16 a[128:131], a[244:247], v[228:231], a[128:131]\n"
"v_cvt_pk_bf16_f32 v198, v148, v149\n"
"v_mfma_f32_16x16x32_bf16 a[132:135], a[244:247], v[236:239], a[132:135]\n"
"v_exp_f32 v151, v151\n"
"v_mfma_f32_16x16x32_bf16 a[136:139], a[244:247], v[244:247], a[136:139]\n"
"v_exp_f32 v156, v156\n"
"v_mfma_f32_16x16x32_bf16 a[140:143], a[244:247], v[252:255], a[140:143]\n"
"ds_read_b64_tr_b16 a[244:245], v43 offset:1024\n"
"ds_read_b64_tr_b16 a[246:247], v43 offset:5120\n"
"v_mfma_f32_16x16x32_bf16 v[54:57], v[50:53], v[252:255], v[54:57]\n"
"v_cvt_pk_bf16_f32 v199, v150, v151\n"
"v_mfma_f32_16x16x32_bf16 a[144:147], a[248:251], v[212:215], a[144:147]\n"
"v_exp_f32 v157, v157\n"
"v_mfma_f32_16x16x32_bf16 a[148:151], a[248:251], v[220:223], a[148:151]\n"
"v_exp_f32 v158, v158\n"
"v_mfma_f32_16x16x32_bf16 a[152:155], a[248:251], v[228:231], a[152:155]\n"
"v_cvt_pk_bf16_f32 v206, v156, v157\n"
"v_mfma_f32_16x16x32_bf16 a[156:159], a[248:251], v[236:239], a[156:159]\n"
"v_exp_f32 v159, v159\n"
"v_mfma_f32_16x16x32_bf16 a[160:163], a[248:251], v[244:247], a[160:163]\n"
"s_nop 0\n"
"v_cvt_pk_bf16_f32 v207, v158, v159\n"
"v_mfma_f32_16x16x32_bf16 a[164:167], a[248:251], v[252:255], a[164:167]\n"
"ds_read_b64_tr_b16 a[248:249], v44 offset:1024\n"
"ds_read_b64_tr_b16 a[250:251], v44 offset:5120\n"
"v_mfma_f32_16x16x32_bf16 a[168:171], a[252:255], v[212:215], a[168:171]\n"
"v_mfma_f32_16x16x32_bf16 a[172:175], a[252:255], v[220:223], a[172:175]\n"
"v_mfma_f32_16x16x32_bf16 a[176:179], a[252:255], v[228:231], a[176:179]\n"
"v_mfma_f32_16x16x32_bf16 a[180:183], a[252:255], v[236:239], a[180:183]\n"
"v_mfma_f32_16x16x32_bf16 a[184:187], a[252:255], v[244:247], a[184:187]\n"
"v_mfma_f32_16x16x32_bf16 a[188:191], a[252:255], v[252:255], a[188:191]\n"
"ds_read_b64_tr_b16 a[252:253], v45 offset:1024\n"
"ds_read_b64_tr_b16 a[254:255], v45 offset:5120\n"
"s_waitcnt vmcnt(4)\n"
"s_sub_u32 s47, s47, 1\n"
"s_cmp_lg_u32 s47, 0\n"
"s_cbranch_scc1 LAT_%=\n"
"s_barrier\n"
"v_mfma_f32_16x16x32_bf16 v[68:71], a[192:195], v[64:67], 0\n"
"s_add_u32 s63, s63, 1\n"
"s_cmp_eq_u32 s63, 6\n"
"v_mfma_f32_16x16x32_bf16 v[68:71], a[196:199], v[112:115], v[68:71]\n"
"s_cselect_b32 s52, s55, s56\n"
"s_cselect_b32 s63, 0, s63\n"
"v_mfma_f32_16x16x32_bf16 v[68:71], a[200:203], v[160:163], v[68:71]\n"
"v_add_u32 v42, s52, v42\n"
"v_add_u32 v43, s52, v43\n"
"v_mfma_f32_16x16x32_bf16 v[68:71], a[204:207], v[208:211], v[68:71]\n"
"v_add_u32 v44, s52, v44\n"
"v_add_u32 v45, s52, v45\n"
"v_mfma_f32_16x16x32_bf16 v[76:79], a[192:195], v[72:75], 0\n"
"v_mfma_f32_16x16x32_bf16 v[76:79], a[196:199], v[120:123], v[76:79]\n"
"v_mfma_f32_16x16x32_bf16 v[76:79], a[200:203], v[168:171], v[76:79]\n"
"v_mfma_f32_16x16x32_bf16 v[76:79], a[204:207], v[216:219], v[76:79]\n"
"s_nop 1\n"
"v_exp_f32 v68, v68\n"
"v_mfma_f32_16x16x32_bf16 v[84:87], a[192:195], v[80:83], 0\n"
"v_exp_f32 v69, v69\n"
"v_mfma_f32_16x16x32_bf16 v[84:87], a[196:199], v[128:131], v[84:87]\n"
"v_exp_f32 v70, v70\n"
"v_mfma_f32_16x16x32_bf16 v[84:87], a[200:203], v[176:179], v[84:87]\n"
"v_cvt_pk_bf16_f32 v212, v68, v69\n"
"v_mfma_f32_16x16x32_bf16 v[84:87], a[204:207], v[224:227], v[84:87]\n"
"v_exp_f32 v71, v71\n"
"v_mfma_f32_16x16x32_bf16 v[92:95], a[192:195], v[88:91], 0\n"
"v_exp_f32 v76, v76\n"
"v_mfma_f32_16x16x32_bf16 v[92:95], a[196:199], v[136:139], v[92:95]\n"
"v_cvt_pk_bf16_f32 v213, v70, v71\n"
"v_mfma_f32_16x16x32_bf16 v[92:95], a[200:203], v[184:187], v[92:95]\n"
"v_exp_f32 v77, v77\n"
"v_mfma_f32_16x16x32_bf16 v[92:95], a[204:207], v[232:235], v[92:95]\n"
"v_exp_f32 v78, v78\n"
"v_mfma_f32_16x16x32_bf16 v[100:103], a[192:195], v[96:99], 0\n"
"v_cvt_pk_bf16_f32 v220, v76, v77\n"
"v_mfma_f32_16x16x32_bf16 v[100:103], a[196:199], v[144:147], v[100:103]\n"
"v_exp_f32 v79, v79\n"
"v_mfma_f32_16x16x32_bf16 v[100:103], a[200:203], v[192:195], v[100:103]\n"
"v_exp_f32 v84, v84\n"
"v_mfma_f32_16x16x32_bf16 v[100:103], a[204:207], v[240:243], v[100:103]\n"
"v_cvt_pk_bf16_f32 v221, v78, v79\n"
"v_mfma_f32_16x16x32_bf16 v[108:111], a[192:195], v[104:107], 0\n"
"v_exp_f32 v85, v85\n"
"v_mfma_f32_16x16x32_bf16 v[108:111], a[196:199], v[152:155], v[108:111]\n"
"v_exp_f32 v86, v86\n"
"v_mfma_f32_16x16x32_bf16 v[108:111], a[200:203], v[200:203], v[108:111]\n"
"v_cvt_pk_bf16_f32 v228, v84, v85\n"
"v_mfma_f32_16x16x32_bf16 v[108:111], a[204:207], v[248:251], v[108:111]\n"
"v_exp_f32 v87, v87\n"
"v_mfma_f32_16x16x32_bf16 v[116:119], a[208:211], v[64:67], 0\n"
"v_exp_f32 v92, v92\n"
"v_mfma_f32_16x16x32_bf16 v[116:119], a[212:215], v[112:115], v[116:119]\n"
"v_cvt_pk_bf16_f32 v229, v86, v87\n"
"s_waitcnt lgkmcnt(0)\n"
"v_mfma_f32_16x16x32_bf16 v[116:119], a[216:219], v[160:163], v[116:119]\n"
"v_exp_f32 v93, v93\n"
"v_mfma_f32_16x16x32_bf16 v[116:119], a[220:223], v[208:211], v[116:119]\n"
"v_exp_f32 v94, v94\n"
"v_mfma_f32_16x16x32_bf16 v[124:127], a[208:211], v[72:75], 0\n"
"v_cvt_pk_bf16_f32 v236, v92, v93\n"
"v_mfma_f32_16x16x32_bf16 v[124:127], a[212:215], v[120:123], v[124:127]\n"
"v_exp_f32 v95, v95\n"
"v_mfma_f32_16x16x32_bf16 v[124:127], a[216:219], v[168:171], v[124:127]\n"
"v_exp_f32 v100, v100\n"
"v_mfma_f32_16x16x32_bf16 v[124:127], a[220:223], v[216:219], v[124:127]\n"
"v_cvt_pk_bf16_f32 v237, v94, v95\n"
"v_mfma_f32_16x16x32_bf16 v[132:135], a[208:211], v[80:83], 0\n"
"v_exp_f32 v101, v101\n"
"v_mfma_f32_16x16x32_bf16 v[132:135], a[212:215], v[128:131], v[132:135]\n"
"v_exp_f32 v102, v102\n"
"v_mfma_f32_16x16x32_bf16 v[132:135], a[216:219], v[176:179], v[132:135]\n"
"v_cvt_pk_bf16_f32 v244, v100, v101\n"
"v_mfma_f32_16x16x32_bf16 v[132:135], a[220:223], v[224:227], v[132:135]\n"
"v_exp_f32 v103, v103\n"
"v_mfma_f32_16x16x32_bf16 v[140:143], a[208:211], v[88:91], 0\n"
"v_exp_f32 v108, v108\n"
"v_mfma_f32_16x16x32_bf16 v[140:143], a[212:215], v[136:139], v[140:143]\n"
"v_cvt_pk_bf16_f32 v245, v102, v103\n"
"v_mfma_f32_16x16x32_bf16 v[140:143], a[216:219], v[184:187], v[140:143]\n"
"v_exp_f32 v109, v109\n"
"v_mfma_f32_16x16x32_bf16 v[140:143], a[220:223], v[232:235], v[140:143]\n"
"v_exp_f32 v110, v110\n"
"v_mfma_f32_16x16x32_bf16 v[148:151], a[208:211], v[96:99], 0\n"
"v_cvt_pk_bf16_f32 v252, v108, v109\n"
"v_mfma_f32_16x16x32_bf16 v[148:151], a[212:215], v[144:147], v[148:151]\n"
"v_exp_f32 v111, v111\n"
"v_mfma_f32_16x16x32_bf16 v[148:151], a[216:219], v[192:195], v[148:151]\n"
"v_exp_f32 v116, v116\n"
"v_mfma_f32_16x16x32_bf16 v[148:151], a[220:223], v[240:243], v[148:151]\n"
"v_cvt_pk_bf16_f32 v253, v110, v111\n"
"v_mfma_f32_16x16x32_bf16 v[156:159], a[208:211], v[104:107], 0\n"
"v_exp_f32 v117, v117\n"
"v_mfma_f32_16x16x32_bf16 v[156:159], a[212:215], v[152:155], v[156:159]\n"
"v_exp_f32 v118, v118\n"
"v_mfma_f32_16x16x32_bf16 v[156:159], a[216:219], v[200:203], v[156:159]\n"
"v_cvt_pk_bf16_f32 v214, v116, v117\n"
"v_mfma_f32_16x16x32_bf16 v[156:159], a[220:223], v[248:251], v[156:159]\n"
"v_exp_f32 v119, v119\n"
"v_mfma_f32_16x16x32_bf16 a[0:3], a[224:227], v[164:167], a[0:3]\n"
"v_exp_f32 v124, v124\n"
"v_mfma_f32_16x16x32_bf16 a[4:7], a[224:227], v[172:175], a[4:7]\n"
"v_cvt_pk_bf16_f32 v215, v118, v119\n"
"v_mfma_f32_16x16x32_bf16 a[8:11], a[224:227], v[180:183], a[8:11]\n"
"v_exp_f32 v125, v125\n"
"v_mfma_f32_16x16x32_bf16 a[12:15], a[224:227], v[188:191], a[12:15]\n"
"v_exp_f32 v126, v126\n"
"v_mfma_f32_16x16x32_bf16 a[16:19], a[224:227], v[196:199], a[16:19]\n"
"v_cvt_pk_bf16_f32 v222, v124, v125\n"
"v_mfma_f32_16x16x32_bf16 a[20:23], a[224:227], v[204:207], a[20:23]\n"
"ds_read_b64_tr_b16 a[224:225], v42 offset:0\n"
"ds_read_b64_tr_b16 a[226:227], v42 offset:4096\n"
"v_mfma_f32_16x16x32_bf16 v[10:13], v[50:53], v[164:167], v[10:13]\n"
"v_exp_f32 v127, v127\n"
"v_mfma_f32_16x16x32_bf16 a[24:27], a[228:231], v[164:167], a[24:27]\n"
"v_exp_f32 v132, v132\n"
"v_mfma_f32_16x16x32_bf16 a[28:31], a[228:231], v[172:175], a[28:31]\n"
"v_cvt_pk_bf16_f32 v223, v126, v127\n"
"v_mfma_f32_16x16x32_bf16 a[32:35], a[228:231], v[180:183], a[32:35]\n"
"v_exp_f32 v133, v133\n"
"v_mfma_f32_16x16x32_bf16 a[36:39], a[228:231], v[188:191], a[36:39]\n"
"v_exp_f32 v134, v134\n"
"v_mfma_f32_16x16x32_bf16 a[40:43], a[228:231], v[196:199], a[40:43]\n"
"v_cvt_pk_bf16_f32 v230, v132, v133\n"
"v_mfma_f32_16x16x32_bf16 a[44:47], a[228:231], v[204:207], a[44:47]\n"
"ds_read_b64_tr_b16 a[228:229], v43 offset:0\n"
"ds_read_b64_tr_b16 a[230:231], v43 offset:4096\n"
"v_mfma_f32_16x16x32_bf16 v[14:17], v[50:53], v[172:175], v[14:17]\n"
"v_exp_f32 v135, v135\n"
"v_mfma_f32_16x16x32_bf16 a[48:51], a[232:235], v[164:167], a[48:51]\n"
"v_exp_f32 v140, v140\n"
"v_mfma_f32_16x16x32_bf16 a[52:55], a[232:235], v[172:175], a[52:55]\n"
"v_cvt_pk_bf16_f32 v231, v134, v135\n"
"v_mfma_f32_16x16x32_bf16 a[56:59], a[232:235], v[180:183], a[56:59]\n"
"v_exp_f32 v141, v141\n"
"v_mfma_f32_16x16x32_bf16 a[60:63], a[232:235], v[188:191], a[60:63]\n"
"v_exp_f32 v142, v142\n"
"v_mfma_f32_16x16x32_bf16 a[64:67], a[232:235], v[196:199], a[64:67]\n"
"v_cvt_pk_bf16_f32 v238, v140, v141\n"
"v_mfma_f32_16x16x32_bf16 a[68:71], a[232:235], v[204:207], a[68:71]\n"
"ds_read_b64_tr_b16 a[232:233], v44 offset:0\n"
"ds_read_b64_tr_b16 a[234:235], v44 offset:4096\n"
"v_mfma_f32_16x16x32_bf16 v[18:21], v[50:53], v[180:183], v[18:21]\n"
"v_exp_f32 v143, v143\n"
"v_mfma_f32_16x16x32_bf16 a[72:75], a[236:239], v[164:167], a[72:75]\n"
"v_exp_f32 v148, v148\n"
"v_mfma_f32_16x16x32_bf16 a[76:79], a[236:239], v[172:175], a[76:79]\n"
"v_cvt_pk_bf16_f32 v239, v142, v143\n"
"v_mfma_f32_16x16x32_bf16 a[80:83], a[236:239], v[180:183], a[80:83]\n"
"v_exp_f32 v149, v149\n"
"v_mfma_f32_16x16x32_bf16 a[84:87], a[236:239], v[188:191], a[84:87]\n"
"v_exp_f32 v150, v150\n"
"v_mfma_f32_16x16x32_bf16 a[88:91], a[236:239], v[196:199], a[88:91]\n"
"v_cvt_pk_bf16_f32 v246, v148, v149\n"
"v_mfma_f32_16x16x32_bf16 a[92:95], a[236:239], v[204:207], a[92:95]\n"
"ds_read_b64_tr_b16 a[236:237], v45 offset:0\n"
"ds_read_b64_tr_b16 a[238:239], v45 offset:4096\n"
"v_mfma_f32_16x16x32_bf16 v[22:25], v[50:53], v[188:191], v[22:25]\n"
"v_exp_f32 v151, v151\n"
"v_mfma_f32_16x16x32_bf16 a[96:99], a[240:243], v[164:167], a[96:99]\n"
"v_exp_f32 v156, v156\n"
"v_mfma_f32_16x16x32_bf16 a[100:103], a[240:243], v[172:175], a[100:103]\n"
"v_cvt_pk_bf16_f32 v247, v150, v151\n"
"v_mfma_f32_16x16x32_bf16 a[104:107], a[240:243], v[180:183], a[104:107]\n"
"v_exp_f32 v157, v157\n"
"v_mfma_f32_16x16x32_bf16 a[108:111], a[240:243], v[188:191], a[108:111]\n"
"v_exp_f32 v158, v158\n"
"v_mfma_f32_16x16x32_bf16 a[112:115], a[240:243], v[196:199], a[112:115]\n"
"v_cvt_pk_bf16_f32 v254, v156, v157\n"
"v_mfma_f32_16x16x32_bf16 a[116:119], a[240:243], v[204:207], a[116:119]\n"
"ds_read_b64_tr_b16 a[240:241], v42 offset:1024\n"
"ds_read_b64_tr_b16 a[242:243], v42 offset:5120\n"
"v_mfma_f32_16x16x32_bf16 v[26:29], v[50:53], v[196:199], v[26:29]\n"
"v_exp_f32 v159, v159\n"
"v_mfma_f32_16x16x32_bf16 a[120:123], a[244:247], v[164:167], a[120:123]\n"
"s_nop 0\n"
"v_cvt_pk_bf16_f32 v255, v158, v159\n"
"v_mfma_f32_16x16x32_bf16 a[124:127], a[244:247], v[172:175], a[124:127]\n"
"v_mfma_f32_16x16x32_bf16 a[128:131], a[244:247], v[180:183], a[128:131]\n"
"v_mfma_f32_16x16x32_bf16 a[132:135], a[244:247], v[188:191], a[132:135]\n"
"v_mfma_f32_16x16x32_bf16 a[136:139], a[244:247], v[196:199], a[136:139]\n"
"v_mfma_f32_16x16x32_bf16 a[140:143], a[244:247], v[204:207], a[140:143]\n"
"ds_read_b64_tr_b16 a[244:245], v43 offset:1024\n"
"ds_read_b64_tr_b16 a[246:247], v43 offset:5120\n"
"v_mfma_f32_16x16x32_bf16 v[54:57], v[50:53], v[204:207], v[54:57]\n"
"v_mfma_f32_16x16x32_bf16 a[144:147], a[248:251], v[164:167], a[144:147]\n"
"v_mfma_f32_16x16x32_bf16 a[148:151], a[248:251], v[172:175], a[148:151]\n"
"v_mfma_f32_16x16x32_bf16 a[152:155], a[248:251], v[180:183], a[152:155]\n"
"v_mfma_f32_16x16x32_bf16 a[156:159], a[248:251], v[188:191], a[156:159]\n"
"v_mfma_f32_16x16x32_bf16 a[160:163], a[248:251], v[196:199], a[160:163]\n"
"v_mfma_f32_16x16x32_bf16 a[164:167], a[248:251], v[204:207], a[164:167]\n"
"ds_read_b64_tr_b16 a[248:249], v44 offset:1024\n"
"ds_read_b64_tr_b16 a[250:251], v44 offset:5120\n"
"v_mfma_f32_16x16x32_bf16 a[168:171], a[252:255], v[164:167], a[168:171]\n"
"v_mfma_f32_16x16x32_bf16 a[172:175], a[252:255], v[172:175], a[172:175]\n"
"v_mfma_f32_16x16x32_bf16 a[176:179], a[252:255], v[180:183], a[176:179]\n"
"v_mfma_f32_16x16x32_bf16 a[180:183], a[252:255], v[188:191], a[180:183]\n"
"v_mfma_f32_16x16x32_bf16 a[184:187], a[252:255], v[196:199], a[184:187]\n"
"v_mfma_f32_16x16x32_bf16 a[188:191], a[252:255], v[204:207], a[188:191]\n"
"ds_read_b64_tr_b16 a[252:253], v45 offset:1024\n"
"ds_read_b64_tr_b16 a[254:255], v45 offset:5120\n"
"s_waitcnt vmcnt(0)\n"
"s_waitcnt lgkmcnt(0)\n"
"s_barrier\n"
"v_mfma_f32_16x16x32_bf16 v[10:13], v[50:53], v[212:215], v[10:13]\n"
"v_and_b32 v9, 15, v1\n"
"v_lshrrev_b32 v8, 4, v1\n"
"v_mfma_f32_16x16x32_bf16 v[14:17], v[50:53], v[220:223], v[14:17]\n"
"v_xor_b32 v8, v8, v9\n"
"v_lshlrev_b32 v8, 4, v8\n"
"v_mfma_f32_16x16x32_bf16 v[18:21], v[50:53], v[228:231], v[18:21]\n"
"v_lshl_add_u32 v8, v9, 10, v8\n"
"s_lshl_b32 s53, s42, 8\n"
"v_mfma_f32_16x16x32_bf16 v[22:25], v[50:53], v[236:239], v[22:25]\n"
"v_add_u32 v8, s53, v8\n"
"v_mfma_f32_16x16x32_bf16 v[26:29], v[50:53], v[244:247], v[26:29]\n"
"v_mfma_f32_16x16x32_bf16 v[54:57], v[50:53], v[252:255], v[54:57]\n"
"v_mfma_f32_16x16x32_bf16 a[0:3], a[224:227], v[212:215], a[0:3]\n"
"v_mfma_f32_16x16x32_bf16 a[4:7], a[224:227], v[220:223], a[4:7]\n"
"v_mfma_f32_16x16x32_bf16 a[8:11], a[224:227], v[228:231], a[8:11]\n"
"v_mfma_f32_16x16x32_bf16 a[12:15], a[224:227], v[236:239], a[12:15]\n"
"v_rcp_f32 v2, v10\n"
"v_mfma_f32_16x16x32_bf16 a[16:19], a[224:227], v[244:247], a[16:19]\n"
"v_rcp_f32 v3, v14\n"
"v_mfma_f32_16x16x32_bf16 a[20:23], a[224:227], v[252:255], a[20:23]\n"
"v_rcp_f32 v4, v18\n"
"v_mfma_f32_16x16x32_bf16 a[24:27], a[228:231], v[212:215], a[24:27]\n"
"v_rcp_f32 v5, v22\n"
"v_mfma_f32_16x16x32_bf16 a[28:31], a[228:231], v[220:223], a[28:31]\n"
"v_rcp_f32 v6, v26\n"
"v_mfma_f32_16x16x32_bf16 a[32:35], a[228:231], v[228:231], a[32:35]\n"
"v_rcp_f32 v7, v54\n"
"v_mfma_f32_16x16x32_bf16 a[36:39], a[228:231], v[236:239], a[36:39]\n"
"v_min3_f32 v11, v10, v14, v18\n"
"v_max3_f32 v12, v10, v14, v18\n"
"v_mfma_f32_16x16x32_bf16 a[40:43], a[228:231], v[244:247], a[40:43]\n"
"v_min3_f32 v11, v11, v22, v26\n"
"v_max3_f32 v12, v12, v22, v26\n"
"v_mfma_f32_16x16x32_bf16 a[44:47], a[228:231], v[252:255], a[44:47]\n"
"v_min_f32 v11, v11, v54\n"
"v_max_f32 v12, v12, v54\n"
"v_mfma_f32_16x16x32_bf16 a[48:51], a[232:235], v[212:215], a[48:51]\n"
"v_cmp_gt_f32 vcc, 0x2b800000, v11\n"
"s_nop 0\n"
"s_mov_b64 s[36:37], vcc\n"
"v_mfma_f32_16x16x32_bf16 a[52:55], a[232:235], v[220:223], a[52:55]\n"
"v_cmp_nge_f32 vcc, 0x53800000, v12\n"
"s_nop 0\n"
"s_or_b64 s[36:37], s[36:37], vcc\n"
"v_mfma_f32_16x16x32_bf16 a[56:59], a[232:235], v[228:231], a[56:59]\n"
"s_or_b32 s36, s36, s37\n"
"s_lshl_b32 s37, s42, 2\n"
"v_mfma_f32_16x16x32_bf16 a[60:63], a[232:235], v[236:239], a[60:63]\n"
"s_add_u32 s37, s37, 97280\n"
"v_mov_b32 v13, s37\n"
"v_mfma_f32_16x16x32_bf16 a[64:67], a[232:235], v[244:247], a[64:67]\n"
"v_mov_b32 v12, s36\n"
"ds_write_b32 v13, v12\n"
"v_mfma_f32_16x16x32_bf16 a[68:71], a[232:235], v[252:255], a[68:71]\n"
"v_xor_b32 v62, 0, v8\n"
"v_add_u32 v63, 0x10000, v62\n"
"v_mfma_f32_16x16x32_bf16 a[72:75], a[236:239], v[212:215], a[72:75]\n"
"v_accvgpr_read_b32 v38, a0\n"
"v_accvgpr_read_b32 v39, a1\n"
"v_mfma_f32_16x16x32_bf16 a[76:79], a[236:239], v[220:223], a[76:79]\n"
"v_accvgpr_read_b32 v40, a2\n"
"v_accvgpr_read_b32 v41, a3\n"
"v_mfma_f32_16x16x32_bf16 a[80:83], a[236:239], v[228:231], a[80:83]\n"
"v_accvgpr_read_b32 v42, a24\n"
"v_accvgpr_read_b32 v43, a25\n"
"v_mfma_f32_16x16x32_bf16 a[84:87], a[236:239], v[236:239], a[84:87]\n"
"v_accvgpr_read_b32 v44, a26\n"
"v_accvgpr_read_b32 v45, a27\n"
"v_mfma_f32_16x16x32_bf16 a[88:91], a[236:239], v[244:247], a[88:91]\n"
"v_mul_f32 v38, v38, v2\n"
"v_mul_f32 v39, v39, v2\n"
"v_mfma_f32_16x16x32_bf16 a[92:95], a[236:239], v[252:255], a[92:95]\n"
"v_mul_f32 v40, v40, v2\n"
"v_mul_f32 v41, v41, v2\n"
"v_mfma_f32_16x16x32_bf16 a[96:99], a[240:243], v[212:215], a[96:99]\n"
"v_mul_f32 v42, v42, v2\n"
"v_mul_f32 v43, v43, v2\n"
"v_mfma_f32_16x16x32_bf16 a[100:103], a[240:243], v[220:223], a[100:103]\n"
"v_mul_f32 v44, v44, v2\n"
"v_mul_f32 v45, v45, v2\n"
"v_mfma_f32_16x16x32_bf16 a[104:107], a[240:243], v[228:231], a[104:107]\n"
"v_cvt_pk_bf16_f32 v46, v38, v39\n"
"v_cvt_pk_bf16_f32 v47, v40, v41\n"
"v_mfma_f32_16x16x32_bf16 a[108:111], a[240:243], v[236:239], a[108:111]\n"
"v_cvt_pk_bf16_f32 v48, v42, v43\n"
"v_cvt_pk_bf16_f32 v49, v44, v45\n"
"v_mfma_f32_16x16x32_bf16 a[112:115], a[240:243], v[244:247], a[112:115]\n"
"ds_write_b128 v62, v[46:49] offset:0\n"
"v_accvgpr_read_b32 v50, a4\n"
"v_mfma_f32_16x16x32_bf16 a[116:119], a[240:243], v[252:255], a[116:119]\n"
"v_accvgpr_read_b32 v51, a5\n"
"v_accvgpr_read_b32 v52, a6\n"
"v_mfma_f32_16x16x32_bf16 a[120:123], a[244:247], v[212:215], a[120:123]\n"
"v_accvgpr_read_b32 v53, a7\n"
"v_accvgpr_read_b32 v54, a28\n"
"v_mfma_f32_16x16x32_bf16 a[124:127], a[244:247], v[220:223], a[124:127]\n"
"v_accvgpr_read_b32 v55, a29\n"
"v_accvgpr_read_b32 v56, a30\n"
"v_mfma_f32_16x16x32_bf16 a[128:131], a[244:247], v[228:231], a[128:131]\n"
"v_accvgpr_read_b32 v57, a31\n"
"v_mul_f32 v50, v50, v3\n"
"v_mfma_f32_16x16x32_bf16 a[132:135], a[244:247], v[236:239], a[132:135]\n"
"v_mul_f32 v51, v51, v3\n"
"v_mul_f32 v52, v52, v3\n"
"v_mfma_f32_16x16x32_bf16 a[136:139], a[244:247], v[244:247], a[136:139]\n"
"v_mul_f32 v53, v53, v3\n"
"v_mul_f32 v54, v54, v3\n"
"v_mfma_f32_16x16x32_bf16 a[140:143], a[244:247], v[252:255], a[140:143]\n"
"v_mul_f32 v55, v55, v3\n"
"v_mul_f32 v56, v56, v3\n"
"v_mfma_f32_16x16x32_bf16 a[144:147], a[248:251], v[212:215], a[144:147]\n"
"v_mul_f32 v57, v57, v3\n"
"v_cvt_pk_bf16_f32 v58, v50, v51\n"
"v_mfma_f32_16x16x32_bf16 a[148:151], a[248:251], v[220:223], a[148:151]\n"
"v_cvt_pk_bf16_f32 v59, v52, v53\n"
"v_cvt_pk_bf16_f32 v60, v54, v55\n"
"v_mfma_f32_16x16x32_bf16 a[152:155], a[248:251], v[228:231], a[152:155]\n"
"v_cvt_pk_bf16_f32 v61, v56, v57\n"
"ds_write_b128 v62, v[58:61] offset:16384\n"
"v_mfma_f32_16x16x32_bf16 a[156:159], a[248:251], v[236:239], a[156:159]\n"
"v_accvgpr_read_b32 v38, a8\n"
"v_accvgpr_read_b32 v39, a9\n"
"v_mfma_f32_16x16x32_bf16 a[160:163], a[248:251], v[244:247], a[160:163]\n"
"v_accvgpr_read_b32 v40, a10\n"
"v_accvgpr_read_b32 v41, a11\n"
"v_mfma_f32_16x16x32_bf16 a[164:167], a[248:251], v[252:255], a[164:167]\n"
"v_accvgpr_read_b32 v42, a32\n"
"v_accvgpr_read_b32 v43, a33\n"
"v_mfma_f32_16x16x32_bf16 a[168:171], a[252:255], v[212:215], a[168:171]\n"
"v_accvgpr_read_b32 v44, a34\n"
"v_accvgpr_read_b32 v45, a35\n"
"v_mfma_f32_16x16x32_bf16 a[172:175], a[252:255], v[220:223], a[172:175]\n"
"v_mul_f32 v38, v38, v4\n"
"v_mul_f32 v39, v39, v4\n"
"v_mfma_f32_16x16x32_bf16 a[176:179], a[252:255], v[228:231], a[176:179]\n"
"v_mul_f32 v40, v40, v4\n"
"v_mul_f32 v41, v41, v4\n"
"v_mfma_f32_16x16x32_bf16 a[180:183], a[252:255], v[236:239], a[180:183]\n"
"v_mul_f32 v42, v42, v4\n"
"v_mul_f32 v43, v43, v4\n"
"v_mfma_f32_16x16x32_bf16 a[184:187], a[252:255], v[244:247], a[184:187]\n"
"v_mul_f32 v44, v44, v4\n"
"v_mul_f32 v45, v45, v4\n"
"v_mfma_f32_16x16x32_bf16 a[188:191], a[252:255], v[252:255], a[188:191]\n"
"v_cvt_pk_bf16_f32 v46, v38, v39\n"
"v_cvt_pk_bf16_f32 v47, v40, v41\n"
"v_cvt_pk_bf16_f32 v48, v42, v43\n"
"v_cvt_pk_bf16_f32 v49, v44, v45\n"
"ds_write_b128 v62, v[46:49] offset:32768\n"
"s_waitcnt lgkmcnt(0)\n"
"s_barrier\n"
"v_mov_b32 v10, 97280\n"
"ds_read_b128 v[12:15], v10\n"
"s_waitcnt lgkmcnt(0)\n"
"v_or3_b32 v12, v12, v13, v14\n"
"v_or_b32 v12, v12, v15\n"
"s_nop 1\n"
"v_readfirstlane_b32 s36, v12\n"
"s_barrier\n"
"s_nop 3\n"
"s_cmp_lg_u32 s36, 0\n"
"s_cbranch_scc1 LSLOW_%=\n"
"LJOIN_%=:\n"
"s_nop 7\n"
"s_nop 7\n"
"; ---- end of section: att\n"
"v_accvgpr_read_b32 v50, a12\n"
"v_accvgpr_read_b32 v51, a13\n"
"v_accvgpr_read_b32 v52, a14\n"
"v_accvgpr_read_b32 v53, a15\n"
"v_accvgpr_read_b32 v54, a36\n"
"v_accvgpr_read_b32 v55, a37\n"
"v_accvgpr_read_b32 v56, a38\n"
"v_accvgpr_read_b32 v57, a39\n"
"v_pk_mul_f32 v[50:51], v[50:51], v[4:5] op_sel:[0,1] op_sel_hi:[1,1]\n"
"v_pk_mul_f32 v[52:53], v[52:53], v[4:5] op_sel:[0,1] op_sel_hi:[1,1]\n"
"v_pk_mul_f32 v[54:55], v[54:55], v[4:5] op_sel:[0,1] op_sel_hi:[1,1]\n"
"v_pk_mul_f32 v[56:57], v[56:57], v[4:5] op_sel:[0,1] op_sel_hi:[1,1]\n"
"v_cvt_pk_bf16_f32 v58, v50, v51\n"
"v_cvt_pk_bf16_f32 v59, v52, v53\n"
"v_cvt_pk_bf16_f32 v60, v54, v55\n"
"v_cvt_pk_bf16_f32 v61, v56, v57\n"
"ds_write_b128 v62, v[58:61] offset:49152\n"
"v_accvgpr_read_b32 v38, a16\n"
"v_accvgpr_read_b32 v39, a17\n"
"v_accvgpr_read_b32 v40, a18\n"
"v_accvgpr_read_b32 v41, a19\n"
"v_accvgpr_read_b32 v42, a40\n"
"v_accvgpr_read_b32 v43, a41\n"
"v_accvgpr_read_b32 v44, a42\n"
"v_accvgpr_read_b32 v45, a43\n"
"v_pk_mul_f32 v[38:39], v[38:39], v[6:7] op_sel:[0,0] op_sel_hi:[1,0]\n"
"v_pk_mul_f32 v[40:41], v[40:41], v[6:7] op_sel:[0,0] op_sel_hi:[1,0]\n"
"v_pk_mul_f32 v[42:43], v[42:43], v[6:7] op_sel:[0,0] op_sel_hi:[1,0]\n"
"v_pk_mul_f32 v[44:45], v[44:45], v[6:7] op_sel:[0,0] op_sel_hi:[1,0]\n"
"v_cvt_pk_bf16_f32 v46, v38, v39\n"
"v_cvt_pk_bf16_f32 v47, v40, v41\n"
"v_cvt_pk_bf16_f32 v48, v42, v43\n"
"v_cvt_pk_bf16_f32 v49, v44, v45\n"
"ds_write_b128 v63, v[46:49] offset:0\n"
"v_accvgpr_read_b32 v50, a20\n"
"v_accvgpr_read_b32 v51, a21\n"
"v_accvgpr_read_b32 v52, a22\n"
"v_accvgpr_read_b32 v53, a23\n"
"v_accvgpr_read_b32 v54, a44\n"
"v_accvgpr_read_b32 v55, a45\n"
"v_accvgpr_read_b32 v56, a46\n"
"v_accvgpr_read_b32 v57, a47\n"
"v_pk_mul_f32 v[50:51], v[50:51], v[6:7] op_sel:[0,1] op_sel_hi:[1,1]\n"
"v_pk_mul_f32 v[52:53], v[52:53], v[6:7] op_sel:[0,1] op_sel_hi:[1,1]\n"
"v_pk_mul_f32 v[54:55], v[54:55], v[6:7] op_sel:[0,1] op_sel_hi:[1,1]\n"
"v_pk_mul_f32 v[56:57], v[56:57], v[6:7] op_sel:[0,1] op_sel_hi:[1,1]\n"
"v_cvt_pk_bf16_f32 v58, v50, v51\n"
"v_cvt_pk_bf16_f32 v59, v52, v53\n"
"v_cvt_pk_bf16_f32 v60, v54, v55\n"
"v_cvt_pk_bf16_f32 v61, v56, v57\n"
"ds_write_b128 v63, v[58:61] offset:16384\n"
"v_xor_b32 v62, 64, v8\n"
"v_add_u32 v63, 0x10000, v62\n"
"v_accvgpr_read_b32 v38, a48\n"
"v_accvgpr_read_b32 v39, a49\n"
"v_accvgpr_read_b32 v40, a50\n"
"v_accvgpr_read_b32 v41, a51\n"
"v_accvgpr_read_b32 v42, a72\n"
"v_accvgpr_read_b32 v43, a73\n"
"v_accvgpr_read_b32 v44, a74\n"
"v_accvgpr_read_b32 v45, a75\n"
"v_pk_mul_f32 v[38:39], v[38:39], v[2:3] op_sel:[0,0] op_sel_hi:[1,0]\n"
"v_pk_mul_f32 v[40:41], v[40:41], v[2:3] op_sel:[0,0] op_sel_hi:[1,0]\n"
"v_pk_mul_f32 v[42:43], v[42:43], v[2:3] op_sel:[0,0] op_sel_hi:[1,0]\n"
"v_pk_mul_f32 v[44:45], v[44:45], v[2:3] op_sel:[0,0] op_sel_hi:[1,0]\n"
"v_cvt_pk_bf16_f32 v46, v38, v39\n"
"v_cvt_pk_bf16_f32 v47, v40, v41\n"
"v_cvt_pk_bf16_f32 v48, v42, v43\n"
"v_cvt_pk_bf16_f32 v49, v44, v45\n"
"ds_write_b128 v62, v[46:49] offset:0\n"
"v_accvgpr_read_b32 v50, a52\n"
"v_accvgpr_read_b32 v51, a53\n"
"v_accvgpr_read_b32 v52, a54\n"
"v_accvgpr_read_b32 v53, a55\n"
"v_accvgpr_read_b32 v54, a76\n"
"v_accvgpr_read_b32 v55, a77\n"
"v_accvgpr_read_b32 v56, a78\n"
"v_accvgpr_read_b32 v57, a79\n"
"v_pk_mul_f32 v[50:51], v[50:51], v[2:3] op_sel:[0,1] op_sel_hi:[1,1]\n"
"v_pk_mul_f32 v[52:53], v[52:53], v[2:3] op_sel:[0,1] op_sel_hi:[1,1]\n"
"v_pk_mul_f32 v[54:55], v[54:55], v[2:3] op_sel:[0,1] op_sel_hi:[1,1]\n"
"v_pk_mul_f32 v[56:57], v[56:57], v[2:3] op_sel:[0,1] op_sel_hi:[1,1]\n"
"v_cvt_pk_bf16_f32 v58, v50, v51\n"
"v_cvt_pk_bf16_f32 v59, v52, v53\n"
"v_cvt_pk_bf16_f32 v60, v54, v55\n"
"v_cvt_pk_bf16_f32 v61, v56, v57\n"
"ds_write_b128 v62, v[58:61] offset:16384\n"
"v_accvgpr_read_b32 v38, a56\n"
"v_accvgpr_read_b32 v39, a57\n"
"v_accvgpr_read_b32 v40, a58\n"
"v_accvgpr_read_b32 v41, a59\n"
"v_accvgpr_read_b32 v42, a80\n"
"v_accvgpr_read_b32 v43, a81\n"
"v_accvgpr_read_b32 v44, a82\n"
"v_accvgpr_read_b32 v45, a83\n"
"v_pk_mul_f32 v[38:39], v[38:39], v[4:5] op_sel:[0,0] op_sel_hi:[1,0]\n"
"v_pk_mul_f32 v[40:41], v[40:41], v[4:5] op_sel:[0,0] op_sel_hi:[1,0]\n"
"v_pk_mul_f32 v[42:43], v[42:43], v[4:5] op_sel:[0,0] op_sel_hi:[1,0]\n"
"v_pk_mul_f32 v[44:45], v[44:45], v[4:5] op_sel:[0,0] op_sel_hi:[1,0]\n"
"v_cvt_pk_bf16_f32 v46, v38, v39\n"
"v_cvt_pk_bf16_f32 v47, v40, v41\n"
"v_cvt_pk_bf16_f32 v48, v42, v43\n"
"v_cvt_pk_bf16_f32 v49, v44, v45\n"
"ds_write_b128 v62, v[46:49] offset:32768\n"
"v_accvgpr_read_b32 v50, a60\n"
"v_accvgpr_read_b32 v51, a61\n"
"v_accvgpr_read_b32 v52, a62\n"
"v_accvgpr_read_b32 v53, a63\n"
"v_accvgpr_read_b32 v54, a84\n"
"v_accvgpr_read_b32 v55, a85\n"
"v_accvgpr_read_b32 v56, a86\n"
"v_accvgpr_read_b32 v57, a87\n"
"v_pk_mul_f32 v[50:51], v[50:51], v[4:5] op_sel:[0,1] op_sel_hi:[1,1]\n"
"v_pk_mul_f32 v[52:53], v[52:53], v[4:5] op_sel:[0,1] op_sel_hi:[1,1]\n"
"v_pk_mul_f32 v[54:55], v[54:55], v[4:5] op_sel:[0,1] op_sel_hi:[1,1]\n"
"v_pk_mul_f32 v[56:57], v[56:57], v[4:5] op_sel:[0,1] op_sel_hi:[1,1]\n"
"v_cvt_pk_bf16_f32 v58, v50, v51\n"
"v_cvt_pk_bf16_f32 v59, v52, v53\n"
"v_cvt_pk_bf16_f32 v60, v54, v55\n"
"v_cvt_pk_bf16_f32 v61, v56, v57\n"
"ds_write_b128 v62, v[58:61] offset:49152\n"
"v_accvgpr_read_b32 v38, a64\n"
"v_accvgpr_read_b32 v39, a65\n"
"v_accvgpr_read_b32 v40, a66\n"
"v_accvgpr_read_b32 v41, a67\n"
"v_accvgpr_read_b32 v42, a88\n"
"v_accvgpr_read_b32 v43, a89\n"
"v_accvgpr_read_b32 v44, a90\n"
"v_accvgpr_read_b32 v45, a91\n"
"v_pk_mul_f32 v[38:39], v[38:39], v[6:7] op_sel:[0,0] op_sel_hi:[1,0]\n"
"v_pk_mul_f32 v[40:41], v[40:41], v[6:7] op_sel:[0,0] op_sel_hi:[1,0]\n"
"v_pk_mul_f32 v[42:43], v[42:43], v[6:7] op_sel:[0,0] op_sel_hi:[1,0]\n"
"v_pk_mul_f32 v[44:45], v[44:45], v[6:7] op_sel:[0,0] op_sel_hi:[1,0]\n"
"v_cvt_pk_bf16_f32 v46, v38, v39\n"
"v_cvt_pk_bf16_f32 v47, v40, v41\n"
"v_cvt_pk_bf16_f32 v48, v42, v43\n"
"v_cvt_pk_bf16_f32 v49, v44, v45\n"
"ds_write_b128 v63, v[46:49] offset:0\n"
"v_accvgpr_read_b32 v50, a68\n"
"v_accvgpr_read_b32 v51, a69\n"
"v_accvgpr_read_b32 v52, a70\n"
"v_accvgpr_read_b32 v53, a71\n"
"v_accvgpr_read_b32 v54, a92\n"
"v_accvgpr_read_b32 v55, a93\n"
"v_accvgpr_read_b32 v56, a94\n"
"v_accvgpr_read_b32 v57, a95\n"
"v_pk_mul_f32 v[50:51], v[50:51], v[6:7] op_sel:[0,1] op_sel_hi:[1,1]\n"
"v_pk_mul_f32 v[52:53], v[52:53], v[6:7] op_sel:[0,1] op_sel_hi:[1,1]\n"
"v_pk_mul_f32 v[54:55], v[54:55], v[6:7] op_sel:[0,1] op_sel_hi:[1,1]\n"
"v_pk_mul_f32 v[56:57], v[56:57], v[6:7] op_sel:[0,1] op_sel_hi:[1,1]\n"
"v_cvt_pk_bf16_f32 v58, v50, v51\n"
"v_cvt_pk_bf16_f32 v59, v52, v53\n"
"v_cvt_pk_bf16_f32 v60, v54, v55\n"
"v_cvt_pk_bf16_f32 v61, v56, v57\n"
"ds_write_b128 v63, v[58:61] offset:16384\n"
"v_xor_b32 v62, 128, v8\n"
"v_add_u32 v63, 0x10000, v62\n"
"v_accvgpr_read_b32 v38, a96\n"
"v_accvgpr_read_b32 v39, a97\n"
"v_accvgpr_read_b32 v40, a98\n"
"v_accvgpr_read_b32 v41, a99\n"
"v_accvgpr_read_b32 v42, a120\n"
"v_accvgpr_read_b32 v43, a121\n"
"v_accvgpr_read_b32 v44, a122\n"
"v_accvgpr_read_b32 v45, a123\n"
"v_pk_mul_f32 v[38:39], v[38:39], v[2:3] op_sel:[0,0] op_sel_hi:[1,0]\n"
"v_pk_mul_f32 v[40:41], v[40:41], v[2:3] op_sel:[0,0] op_sel_hi:[1,0]\n"
"v_pk_mul_f32 v[42:43], v[42:43], v[2:3] op_sel:[0,0] op_sel_hi:[1,0]\n"
"v_pk_mul_f32 v[44:45], v[44:45], v[2:3] op_sel:[0,0] op_sel_hi:[1,0]\n"
"v_cvt_pk_bf16_f32 v46, v38, v39\n"
"v_cvt_pk_bf16_f32 v47, v40, v41\n"
"v_cvt_pk_bf16_f32 v48, v42, v43\n"
"v_cvt_pk_bf16_f32 v49, v44, v45\n"
"ds_write_b128 v62, v[46:49] offset:0\n"
"v_accvgpr_read_b32 v50, a100\n"
"v_accvgpr_read_b32 v51, a101\n"
"v_accvgpr_read_b32 v52, a102\n"
"v_accvgpr_read_b32 v53, a103\n"
"v_accvgpr_read_b32 v54, a124\n"
"v_accvgpr_read_b32 v55, a125\n"
"v_accvgpr_read_b32 v56, a126\n"
"v_accvgpr_read_b32 v57, a127\n"
"v_pk_mul_f32 v[50:51], v[50:51], v[2:3] op_sel:[0,1] op_sel_hi:[1,1]\n"
"v_pk_mul_f32 v[52:53], v[52:53], v[2:3] op_sel:[0,1] op_sel_hi:[1,1]\n"
"v_pk_mul_f32 v[54:55], v[54:55], v[2:3] op_sel:[0,1] op_sel_hi:[1,1]\n"
"v_pk_mul_f32 v[56:57], v[56:57], v[2:3] op_sel:[0,1] op_sel_hi:[1,1]\n"
"v_cvt_pk_bf16_f32 v58, v50, v51\n"
"v_cvt_pk_bf16_f32 v59, v52, v53\n"
"v_cvt_pk_bf16_f32 v60, v54, v55\n"
"v_cvt_pk_bf16_f32 v61, v56, v57\n"
"ds_write_b128 v62, v[58:61] offset:16384\n"
"v_accvgpr_read_b32 v38, a104\n"
"v_accvgpr_read_b32 v39, a105\n"
"v_accvgpr_read_b32 v40, a106\n"
"v_accvgpr_read_b32 v41, a107\n"
"v_accvgpr_read_b32 v42, a128\n"
"v_accvgpr_read_b32 v43, a129\n"
"v_accvgpr_read_b32 v44, a130\n"
"v_accvgpr_read_b32 v45, a131\n"
"v_pk_mul_f32 v[38:39], v[38:39], v[4:5] op_sel:[0,0] op_sel_hi:[1,0]\n"
"v_pk_mul_f32 v[40:41], v[40:41], v[4:5] op_sel:[0,0] op_sel_hi:[1,0]\n"
"v_pk_mul_f32 v[42:43], v[42:43], v[4:5] op_sel:[0,0] op_sel_hi:[1,0]\n"
"v_pk_mul_f32 v[44:45], v[44:45], v[4:5] op_sel:[0,0] op_sel_hi:[1,0]\n"
"v_cvt_pk_bf16_f32 v46, v38, v39\n"
"v_cvt_pk_bf16_f32 v47, v40, v41\n"
"v_cvt_pk_bf16_f32 v48, v42, v43\n"
"v_cvt_pk_bf16_f32 v49, v44, v45\n"
"ds_write_b128 v62, v[46:49] offset:32768\n"
"v_accvgpr_read_b32 v50, a108\n"
"v_accvgpr_read_b32 v51, a109\n"
"v_accvgpr_read_b32 v52, a110\n"
"v_accvgpr_read_b32 v53, a111\n"
"v_accvgpr_read_b32 v54, a132\n"
"v_accvgpr_read_b32 v55, a133\n"
"v_accvgpr_read_b32 v56, a134\n"
"v_accvgpr_read_b32 v57, a135\n"
"v_pk_mul_f32 v[50:51], v[50:51], v[4:5] op_sel:[0,1] op_sel_hi:[1,1]\n"
"v_pk_mul_f32 v[52:53], v[52:53], v[4:5] op_sel:[0,1] op_sel_hi:[1,1]\n"
"v_pk_mul_f32 v[54:55], v[54:55], v[4:5] op_sel:[0,1] op_sel_hi:[1,1]\n"
"v_pk_mul_f32 v[56:57], v[56:57], v[4:5] op_sel:[0,1] op_sel_hi:[1,1]\n"
"v_cvt_pk_bf16_f32 v58, v50, v51\n"
"v_cvt_pk_bf16_f32 v59, v52, v53\n"
"v_cvt_pk_bf16_f32 v60, v54, v55\n"
"v_cvt_pk_bf16_f32 v61, v56, v57\n"
"ds_write_b128 v62, v[58:61] offset:49152\n"
"v_accvgpr_read_b32 v38, a112\n"
"v_accvgpr_read_b32 v39, a113\n"
"v_accvgpr_read_b32 v40, a114\n"
"v_accvgpr_read_b32 v41, a115\n"
"v_accvgpr_read_b32 v42, a136\n"
"v_accvgpr_read_b32 v43, a137\n"
"v_accvgpr_read_b32 v44, a138\n"
"v_accvgpr_read_b32 v45, a139\n"
"v_pk_mul_f32 v[38:39], v[38:39], v[6:7] op_sel:[0,0] op_sel_hi:[1,0]\n"
"v_pk_mul_f32 v[40:41], v[40:41], v[6:7] op_sel:[0,0] op_sel_hi:[1,0]\n"
"v_pk_mul_f32 v[42:43], v[42:43], v[6:7] op_sel:[0,0] op_sel_hi:[1,0]\n"
"v_pk_mul_f32 v[44:45], v[44:45], v[6:7] op_sel:[0,0] op_sel_hi:[1,0]\n"
"v_cvt_pk_bf16_f32 v46, v38, v39\n"
"v_cvt_pk_bf16_f32 v47, v40, v41\n"
"v_cvt_pk_bf16_f32 v48, v42, v43\n"
"v_cvt_pk_bf16_f32 v49, v44, v45\n"
"ds_write_b128 v63, v[46:49] offset:0\n"
"v_accvgpr_read_b32 v50, a116\n"
"v_accvgpr_read_b32 v51, a117\n"
"v_accvgpr_read_b32 v52, a118\n"
"v_accvgpr_read_b32 v53, a119\n"
"v_accvgpr_read_b32 v54, a140\n"
"v_accvgpr_read_b32 v55, a141\n"
"v_accvgpr_read_b32 v56, a142\n"
"v_accvgpr_read_b32 v57, a143\n"
"v_pk_mul_f32 v[50:51], v[50:51], v[6:7] op_sel:[0,1] op_sel_hi:[1,1]\n"
"v_pk_mul_f32 v[52:53], v[52:53], v[6:7] op_sel:[0,1] op_sel_hi:[1,1]\n"
"v_pk_mul_f32 v[54:55], v[54:55], v[6:7] op_sel:[0,1] op_sel_hi:[1,1]\n"
"v_pk_mul_f32 v[56:57], v[56:57], v[6:7] op_sel:[0,1] op_sel_hi:[1,1]\n"
"v_cvt_pk_bf16_f32 v58, v50, v51\n"
"v_cvt_pk_bf16_f32 v59, v52, v53\n"
"v_cvt_pk_bf16_f32 v60, v54, v55\n"
"v_cvt_pk_bf16_f32 v61, v56, v57\n"
"ds_write_b128 v63, v[58:61] offset:16384\n"
"v_xor_b32 v62, 192, v8\n"
"v_add_u32 v63, 0x10000, v62\n"
"v_accvgpr_read_b32 v38, a144\n"
"v_accvgpr_read_b32 v39, a145\n"
"v_accvgpr_read_b32 v40, a146\n"
"v_accvgpr_read_b32 v41, a147\n"
"v_accvgpr_read_b32 v42, a168\n"
"v_accvgpr_read_b32 v43, a169\n"
"v_accvgpr_read_b32 v44, a170\n"
"v_accvgpr_read_b32 v45, a171\n"
"v_pk_mul_f32 v[38:39], v[38:39], v[2:3] op_sel:[0,0] op_sel_hi:[1,0]\n"
"v_pk_mul_f32 v[40:41], v[40:41], v[2:3] op_sel:[0,0] op_sel_hi:[1,0]\n"
"v_pk_mul_f32 v[42:43], v[42:43], v[2:3] op_sel:[0,0] op_sel_hi:[1,0]\n"
"v_pk_mul_f32 v[44:45], v[44:45], v[2:3] op_sel:[0,0] op_sel_hi:[1,0]\n"
"v_cvt_pk_bf16_f32 v46, v38, v39\n"
"v_cvt_pk_bf16_f32 v47, v40, v41\n"
"v_cvt_pk_bf16_f32 v48, v42, v43\n"
"v_cvt_pk_bf16_f32 v49, v44, v45\n"
"ds_write_b128 v62, v[46:49] offset:0\n"
"v_accvgpr_read_b32 v50, a148\n"
"v_accvgpr_read_b32 v51, a149\n"
"v_accvgpr_read_b32 v52, a150\n"
"v_accvgpr_read_b32 v53, a151\n"
"v_accvgpr_read_b32 v54, a172\n"
"v_accvgpr_read_b32 v55, a173\n"
"v_accvgpr_read_b32 v56, a174\n"
"v_accvgpr_read_b32 v57, a175\n"
"v_pk_mul_f32 v[50:51], v[50:51], v[2:3] op_sel:[0,1] op_sel_hi:[1,1]\n"
"v_pk_mul_f32 v[52:53], v[52:53], v[2:3] op_sel:[0,1] op_sel_hi:[1,1]\n"
"v_pk_mul_f32 v[54:55], v[54:55], v[2:3] op_sel:[0,1] op_sel_hi:[1,1]\n"
"v_pk_mul_f32 v[56:57], v[56:57], v[2:3] op_sel:[0,1] op_sel_hi:[1,1]\n"
"v_cvt_pk_bf16_f32 v58, v50, v51\n"
"v_cvt_pk_bf16_f32 v59, v52, v53\n"
"v_cvt_pk_bf16_f32 v60, v54, v55\n"
"v_cvt_pk_bf16_f32 v61, v56, v57\n"
"ds_write_b128 v62, v[58:61] offset:16384\n"
"v_accvgpr_read_b32 v38, a152\n"
"v_accvgpr_read_b32 v39, a153\n"
"v_accvgpr_read_b32 v40, a154\n"
"v_accvgpr_read_b32 v41, a155\n"
"v_accvgpr_read_b32 v42, a176\n"
"v_accvgpr_read_b32 v43, a177\n"
"v_accvgpr_read_b32 v44, a178\n"
"v_accvgpr_read_b32 v45, a179\n"
"v_pk_mul_f32 v[38:39], v[38:39], v[4:5] op_sel:[0,0] op_sel_hi:[1,0]\n"
"v_pk_mul_f32 v[40:41], v[40:41], v[4:5] op_sel:[0,0] op_sel_hi:[1,0]\n"
"v_pk_mul_f32 v[42:43], v[42:43], v[4:5] op_sel:[0,0] op_sel_hi:[1,0]\n"
"v_pk_mul_f32 v[44:45], v[44:45], v[4:5] op_sel:[0,0] op_sel_hi:[1,0]\n"
"v_cvt_pk_bf16_f32 v46, v38, v39\n"
"v_cvt_pk_bf16_f32 v47, v40, v41\n"
"v_cvt_pk_bf16_f32 v48, v42, v43\n"
"v_cvt_pk_bf16_f32 v49, v44, v45\n"
"ds_write_b128 v62, v[46:49] offset:32768\n"
"v_accvgpr_read_b32 v50, a156\n"
"v_accvgpr_read_b32 v51, a157\n"
"v_accvgpr_read_b32 v52, a158\n"
"v_accvgpr_read_b32 v53, a159\n"
"v_accvgpr_read_b32 v54, a180\n"
"v_accvgpr_read_b32 v55, a181\n"
"v_accvgpr_read_b32 v56, a182\n"
"v_accvgpr_read_b32 v57, a183\n"
"v_pk_mul_f32 v[50:51], v[50:51], v[4:5] op_sel:[0,1] op_sel_hi:[1,1]\n"
"v_pk_mul_f32 v[52:53], v[52:53], v[4:5] op_sel:[0,1] op_sel_hi:[1,1]\n"
"v_pk_mul_f32 v[54:55], v[54:55], v[4:5] op_sel:[0,1] op_sel_hi:[1,1]\n"
"v_pk_mul_f32 v[56:57], v[56:57], v[4:5] op_sel:[0,1] op_sel_hi:[1,1]\n"
"v_cvt_pk_bf16_f32 v58, v50, v51\n"
"v_cvt_pk_bf16_f32 v59, v52, v53\n"
"v_cvt_pk_bf16_f32 v60, v54, v55\n"
"v_cvt_pk_bf16_f32 v61, v56, v57\n"
"ds_write_b128 v62, v[58:61] offset:49152\n"
"v_accvgpr_read_b32 v38, a160\n"
"v_accvgpr_read_b32 v39, a161\n"
"v_accvgpr_read_b32 v40, a162\n"
"v_accvgpr_read_b32 v41, a163\n"
"v_accvgpr_read_b32 v42, a184\n"
"v_accvgpr_read_b32 v43, a185\n"
"v_accvgpr_read_b32 v44, a186\n"
"v_accvgpr_read_b32 v45, a187\n"
"v_pk_mul_f32 v[38:39], v[38:39], v[6:7] op_sel:[0,0] op_sel_hi:[1,0]\n"
"v_pk_mul_f32 v[40:41], v[40:41], v[6:7] op_sel:[0,0] op_sel_hi:[1,0]\n"
"v_pk_mul_f32 v[42:43], v[42:43], v[6:7] op_sel:[0,0] op_sel_hi:[1,0]\n"
"v_pk_mul_f32 v[44:45], v[44:45], v[6:7] op_sel:[0,0] op_sel_hi:[1,0]\n"
"v_cvt_pk_bf16_f32 v46, v38, v39\n"
"v_cvt_pk_bf16_f32 v47, v40, v41\n"
"v_cvt_pk_bf16_f32 v48, v42, v43\n"
"v_cvt_pk_bf16_f32 v49, v44, v45\n"
"ds_write_b128 v63, v[46:49] offset:0\n"
"v_accvgpr_read_b32 v50, a164\n"
"v_accvgpr_read_b32 v51, a165\n"
"v_accvgpr_read_b32 v52, a166\n"
"v_accvgpr_read_b32 v53, a167\n"
"v_accvgpr_read_b32 v54, a188\n"
"v_accvgpr_read_b32 v55, a189\n"
"v_accvgpr_read_b32 v56, a190\n"
"v_accvgpr_read_b32 v57, a191\n"
"v_pk_mul_f32 v[50:51], v[50:51], v[6:7] op_sel:[0,1] op_sel_hi:[1,1]\n"
"v_pk_mul_f32 v[52:53], v[52:53], v[6:7] op_sel:[0,1] op_sel_hi:[1,1]\n"
"v_pk_mul_f32 v[54:55], v[54:55], v[6:7] op_sel:[0,1] op_sel_hi:[1,1]\n"
"v_pk_mul_f32 v[56:57], v[56:57], v[6:7] op_sel:[0,1] op_sel_hi:[1,1]\n"
"v_cvt_pk_bf16_f32 v58, v50, v51\n"
"v_cvt_pk_bf16_f32 v59, v52, v53\n"
"v_cvt_pk_bf16_f32 v60, v54, v55\n"
"v_cvt_pk_bf16_f32 v61, v56, v57\n"
"ds_write_b128 v63, v[58:61] offset:16384\n"
"v_bfe_u32 v60, v1, 2, 3\n"
"v_lshrrev_b32 v61, 5, v1\n"
"v_and_b32 v59, 3, v1\n"
"v_lshlrev_b32 v61, 10, v61\n"
"v_lshl_add_u32 v61, v60, 7, v61\n"
"v_lshl_add_u32 v61, v59, 3, v61\n"
"v_add_u32 v61, s49, v61\n"
"v_bfe_u32 v60, v60, 1, 2\n"
"v_xor_b32 v59, 0, v60\n"
"v_lshl_add_u32 v10, v59, 5, v61\n"
"v_xor_b32 v59, 1, v60\n"
"v_lshl_add_u32 v11, v59, 5, v61\n"
"v_xor_b32 v59, 2, v60\n"
"v_lshl_add_u32 v12, v59, 5, v61\n"
"v_xor_b32 v59, 3, v60\n"
"v_lshl_add_u32 v13, v59, 5, v61\n"
"v_and_b32 v60, 15, v1\n"
"v_lshrrev_b32 v61, 4, v1\n"
"v_xor_b32 v61, v61, v60\n"
"v_lshlrev_b32 v61, 4, v61\n"
"v_lshl_add_u32 v61, v60, 10, v61\n"
"v_xor_b32 v14, 0, v61\n"
"v_add_u32 v18, 0x10000, v14\n"
"v_xor_b32 v15, 64, v61\n"
"v_add_u32 v19, 0x10000, v15\n"
"v_xor_b32 v16, 128, v61\n"
"v_add_u32 v20, 0x10000, v16\n"
"v_xor_b32 v17, 192, v61\n"
"v_add_u32 v21, 0x10000, v17\n"
"s_waitcnt lgkmcnt(0)\n"
"s_barrier\n"
"ds_read_b64_tr_b16 a[192:193], v10 offset:0\n"
"ds_read_b64_tr_b16 a[194:195], v10 offset:2048\n"
"ds_read_b64_tr_b16 a[196:197], v11 offset:0\n"
"ds_read_b64_tr_b16 a[198:199], v11 offset:2048\n"
"ds_read_b64_tr_b16 a[200:201], v12 offset:0\n"
"ds_read_b64_tr_b16 a[202:203], v12 offset:2048\n"
"ds_read_b64_tr_b16 a[204:205], v13 offset:0\n"
"ds_read_b64_tr_b16 a[206:207], v13 offset:2048\n"
"ds_read_b128 a[224:227], v14 offset:0\n"
"ds_read_b128 a[228:231], v14 offset:16384\n"
"ds_read_b128 a[232:235], v14 offset:32768\n"
"ds_read_b128 a[236:239], v14 offset:49152\n"
"ds_read_b128 a[240:243], v18 offset:0\n"
"ds_read_b128 a[244:247], v18 offset:16384\n"
"s_waitcnt lgkmcnt(0)\n"
"s_cmp_lg_u32 s42, 0\n"
"s_cbranch_scc1 LHRA_%=\n"
"s_mul_i32 s53, s44, 11\n"
"s_add_u32 s53, s53, s43\n"
"s_mul_i32 s53, s53, 768\n"
"s_add_u32 s53, s53, 33554432\n"
"s_add_u32 s86, s80, s53\n"
"s_addc_u32 s87, s81, 0\n"
"v_lshlrev_b32 v60, 3, v1\n"
"s_mov_b32 exec_hi, 0\n"
"global_load_dwordx2 v[58:59], v60, s[86:87] sc1\n"
"s_mov_b64 exec, -1\n"
"LHRA_%=:\n"
"s_lshl_b32 s54, s43, 2\n"
"s_add_u32 s54, s54, s42\n"
"v_and_b32 v52, 15, v1\n"
"v_lshrrev_b32 v53, 4, v1\n"
"v_lshlrev_b32 v52, 10, v52\n"
"v_lshl_add_u32 v52, v53, 3, v52\n"
"s_lshr_b32 s55, s54, 1\n"
"s_lshl_b32 s55, s55, 6\n"
"v_add_u32 v52, s55, v52\n"
"s_and_b32 s55, s54, 1\n"
"s_mul_i32 s55, s55, 49152\n"
"s_mul_i32 s56, s44, 98304\n"
"s_add_u32 s48, s55, s56\n"
"s_add_u32 s50, s48, 0\n"
"buffer_load_dwordx2 v[40:41], v52, s[20:23], s50 offen\n"
"buffer_load_dwordx2 v[42:43], v52, s[20:23], s50 offen offset:32\n"
"s_add_u32 s50, s48, 16384\n"
"buffer_load_dwordx2 v[44:45], v52, s[20:23], s50 offen\n"
"buffer_load_dwordx2 v[46:47], v52, s[20:23], s50 offen offset:32\n"
"s_add_u32 s50, s48, 32768\n"
"buffer_load_dwordx2 v[48:49], v52, s[20:23], s50 offen\n"
"buffer_load_dwordx2 v[50:51], v52, s[20:23], s50 offen offset:32\n"
"s_mov_b32 s55, 2162688\n"
"v_mul_lo_u32 v53, v1, s55\n"
"s_mul_i32 s55, s44, 98304\n"
"v_add_u32 v53, s55, v53\n"
"s_mov_b64 exec, 0x7ff\n"
"global_load_dword v54, v53, s[80:81]\n"
"s_mov_b64 exec, -1\n"
"s_mov_b32 s36, s82\n"
"s_and_b32 s37, s83, 0xffff\n"
"s_mov_b32 s38, 2097152\n"
"s_mov_b32 s39, 0x20000\n"
"s_mov_b64 exec, 1\n"
"buffer_load_dword v55, v52, s[36:39], s48 offen\n"
"s_mov_b64 exec, -1\n"
"; ---- end of section: oepi\n"
"s_nop 3\n"
"v_mfma_f32_16x16x32_bf16 v[64:67], a[192:195], a[224:227], 0\n"
"v_mfma_f32_16x16x32_bf16 v[68:71], a[196:199], a[224:227], 0\n"
"v_mfma_f32_16x16x32_bf16 v[112:115], a[200:203], a[224:227], 0\n"
"ds_read_b64_tr_b16 a[208:209], v10 offset:4096\n"
"ds_read_b64_tr_b16 a[210:211], v10 offset:6144\n"
"v_mfma_f32_16x16x32_bf16 v[116:119], a[204:207], a[224:227], 0\n"
"ds_read_b128 a[224:227], v15 offset:0\n"
"v_mfma_f32_16x16x32_bf16 v[72:75], a[192:195], a[228:231], 0\n"
"ds_read_b64_tr_b16 a[212:213], v11 offset:4096\n"
"ds_read_b64_tr_b16 a[214:215], v11 offset:6144\n"
"v_mfma_f32_16x16x32_bf16 v[76:79], a[196:199], a[228:231], 0\n"
"v_mfma_f32_16x16x32_bf16 v[120:123], a[200:203], a[228:231], 0\n"
"ds_read_b64_tr_b16 a[216:217], v12 offset:4096\n"
"ds_read_b64_tr_b16 a[218:219], v12 offset:6144\n"
"v_mfma_f32_16x16x32_bf16 v[124:127], a[204:207], a[228:231], 0\n"
"ds_read_b128 a[228:231], v15 offset:16384\n"
"v_mfma_f32_16x16x32_bf16 v[80:83], a[192:195], a[232:235], 0\n"
"ds_read_b64_tr_b16 a[220:221], v13 offset:4096\n"
"ds_read_b64_tr_b16 a[222:223], v13 offset:6144\n"
"v_mfma_f32_16x16x32_bf16 v[84:87], a[196:199], a[232:235], 0\n"
"v_mfma_f32_16x16x32_bf16 v[128:131], a[200:203], a[232:235], 0\n"
"s_add_u32 s50, s46, 131072\n"
"s_add_u32 m0, s49, 0\n"
"v_mfma_f32_16x16x32_bf16 v[132:135], a[204:207], a[232:235], 0\n"
"ds_read_b128 a[232:235], v15 offset:32768\n"
"buffer_load_dwordx4 v30, s[28:31], s50 offen offset:0 lds\n"
"v_mfma_f32_16x16x32_bf16 v[88:91], a[192:195], a[236:239], 0\n"
"buffer_load_dwordx4 v31, s[28:31], s50 offen offset:1024 lds\n"
"buffer_load_dwordx4 v32, s[28:31], s50 offen offset:2048 lds\n"
"v_mfma_f32_16x16x32_bf16 v[92:95], a[196:199], a[236:239], 0\n"
"buffer_load_dwordx4 v33, s[28:31], s50 offen offset:3072 lds\n"
"v_mfma_f32_16x16x32_bf16 v[136:139], a[200:203], a[236:239], 0\n"
"v_mfma_f32_16x16x32_bf16 v[140:143], a[204:207], a[236:239], 0\n"
"ds_read_b128 a[236:239], v15 offset:49152\n"
"v_mfma_f32_16x16x32_bf16 v[96:99], a[192:195], a[240:243], 0\n"
"v_mfma_f32_16x16x32_bf16 v[100:103], a[196:199], a[240:243], 0\n"
"v_mfma_f32_16x16x32_bf16 v[144:147], a[200:203], a[240:243], 0\n"
"v_mfma_f32_16x16x32_bf16 v[148:151], a[204:207], a[240:243], 0\n"
"ds_read_b128 a[240:243], v19 offset:0\n"
"v_mfma_f32_16x16x32_bf16 v[104:107], a[192:195], a[244:247], 0\n"
"v_mfma_f32_16x16x32_bf16 v[108:111], a[196:199], a[244:247], 0\n"
"v_mfma_f32_16x16x32_bf16 v[152:155], a[200:203], a[244:247], 0\n"
"v_mfma_f32_16x16x32_bf16 v[156:159], a[204:207], a[244:247], 0\n"
"ds_read_b128 a[244:247], v19 offset:16384\n"
"s_waitcnt lgkmcnt(3)\n"
"v_mfma_f32_16x16x32_bf16 v[64:67], a[208:211], a[224:227], v[64:67]\n"
"v_mfma_f32_16x16x32_bf16 v[68:71], a[212:215], a[224:227], v[68:71]\n"
"v_mfma_f32_16x16x32_bf16 v[112:115], a[216:219], a[224:227], v[112:115]\n"
"ds_read_b64_tr_b16 a[192:193], v10 offset:8192\n"
"ds_read_b64_tr_b16 a[194:195], v10 offset:10240\n"
"v_mfma_f32_16x16x32_bf16 v[116:119], a[220:223], a[224:227], v[116:119]\n"
"ds_read_b128 a[224:227], v16 offset:0\n"
"v_mfma_f32_16x16x32_bf16 v[72:75], a[208:211], a[228:231], v[72:75]\n"
"ds_read_b64_tr_b16 a[196:197], v11 offset:8192\n"
"ds_read_b64_tr_b16 a[198:199], v11 offset:10240\n"
"v_mfma_f32_16x16x32_bf16 v[76:79], a[212:215], a[228:231], v[76:79]\n"
"v_mfma_f32_16x16x32_bf16 v[120:123], a[216:219], a[228:231], v[120:123]\n"
"ds_read_b64_tr_b16 a[200:201], v12 offset:8192\n"
"ds_read_b64_tr_b16 a[202:203], v12 offset:10240\n"
"v_mfma_f32_16x16x32_bf16 v[124:127], a[220:223], a[228:231], v[124:127]\n"
"ds_read_b128 a[228:231], v16 offset:16384\n"
"v_mfma_f32_16x16x32_bf16 v[80:83], a[208:211], a[232:235], v[80:83]\n"
"ds_read_b64_tr_b16 a[204:205], v13 offset:8192\n"
"ds_read_b64_tr_b16 a[206:207], v13 offset:10240\n"
"v_mfma_f32_16x16x32_bf16 v[84:87], a[212:215], a[232:235], v[84:87]\n"
"v_mfma_f32_16x16x32_bf16 v[128:131], a[216:219], a[232:235], v[128:131]\n"
"s_add_u32 s50, s46, 163840\n"
"s_add_u32 m0, s49, 4096\n"
"v_mfma_f32_16x16x32_bf16 v[132:135], a[220:223], a[232:235], v[132:135]\n"
"ds_read_b128 a[232:235], v16 offset:32768\n"
"buffer_load_dwordx4 v30, s[28:31], s50 offen offset:0 lds\n"
"s_waitcnt lgkmcnt(11)\n"
"v_mfma_f32_16x16x32_bf16 v[88:91], a[208:211], a[236:239], v[88:91]\n"
"buffer_load_dwordx4 v31, s[28:31], s50 offen offset:1024 lds\n"
"buffer_load_dwordx4 v32, s[28:31], s50 offen offset:2048 lds\n"
"v_mfma_f32_16x16x32_bf16 v[92:95], a[212:215], a[236:239], v[92:95]\n"
"buffer_load_dwordx4 v33, s[28:31], s50 offen offset:3072 lds\n"
"v_mfma_f32_16x16x32_bf16 v[136:139], a[216:219], a[236:239], v[136:139]\n"
"v_mfma_f32_16x16x32_bf16 v[140:143], a[220:223], a[236:239], v[140:143]\n"
"ds_read_b128 a[236:239], v16 offset:49152\n"
"v_mfma_f32_16x16x32_bf16 v[96:99], a[208:211], a[240:243], v[96:99]\n"
"v_mfma_f32_16x16x32_bf16 v[100:103], a[212:215], a[240:243], v[100:103]\n"
"v_mfma_f32_16x16x32_bf16 v[144:147], a[216:219], a[240:243], v[144:147]\n"
"v_mfma_f32_16x16x32_bf16 v[148:151], a[220:223], a[240:243], v[148:151]\n"
"ds_read_b128 a[240:243], v20 offset:0\n"
"v_mfma_f32_16x16x32_bf16 v[104:107], a[208:211], a[244:247], v[104:107]\n"
"v_mfma_f32_16x16x32_bf16 v[108:111], a[212:215], a[244:247], v[108:111]\n"
"v_mfma_f32_16x16x32_bf16 v[152:155], a[216:219], a[244:247], v[152:155]\n"
"v_mfma_f32_16x16x32_bf16 v[156:159], a[220:223], a[244:247], v[156:159]\n"
"ds_read_b128 a[244:247], v20 offset:16384\n"
"s_waitcnt lgkmcnt(3)\n"
"v_mfma_f32_16x16x32_bf16 v[64:67], a[192:195], a[224:227], v[64:67]\n"
"v_mfma_f32_16x16x32_bf16 v[68:71], a[196:199], a[224:227], v[68:71]\n"
"v_mfma_f32_16x16x32_bf16 v[112:115], a[200:203], a[224:227], v[112:115]\n"
"ds_read_b64_tr_b16 a[208:209], v10 offset:12288\n"
"ds_read_b64_tr_b16 a[210:211], v10 offset:14336\n"
"v_mfma_f32_16x16x32_bf16 v[116:119], a[204:207], a[224:227], v[116:119]\n"
"ds_read_b128 a[224:227], v17 offset:0\n"
"v_mfma_f32_16x16x32_bf16 v[72:75], a[192:195], a[228:231], v[72:75]\n"
"ds_read_b64_tr_b16 a[212:213], v11 offset:12288\n"
"ds_read_b64_tr_b16 a[214:215], v11 offset:14336\n"
"v_mfma_f32_16x16x32_bf16 v[76:79], a[196:199], a[228:231], v[76:79]\n"
"v_mfma_f32_16x16x32_bf16 v[120:123], a[200:203], a[228:231], v[120:123]\n"
"ds_read_b64_tr_b16 a[216:217], v12 offset:12288\n"
"ds_read_b64_tr_b16 a[218:219], v12 offset:14336\n"
"v_mfma_f32_16x16x32_bf16 v[124:127], a[204:207], a[228:231], v[124:127]\n"
"ds_read_b128 a[228:231], v17 offset:16384\n"
"v_mfma_f32_16x16x32_bf16 v[80:83], a[192:195], a[232:235], v[80:83]\n"
"ds_read_b64_tr_b16 a[220:221], v13 offset:12288\n"
"ds_read_b64_tr_b16 a[222:223], v13 offset:14336\n"
"v_mfma_f32_16x16x32_bf16 v[84:87], a[196:199], a[232:235], v[84:87]\n"
"v_mfma_f32_16x16x32_bf16 v[128:131], a[200:203], a[232:235], v[128:131]\n"
"s_add_u32 s50, s46, 196608\n"
"s_add_u32 m0, s49, 8192\n"
"v_mfma_f32_16x16x32_bf16 v[132:135], a[204:207], a[232:235], v[132:135]\n"
"ds_read_b128 a[232:235], v17 offset:32768\n"
"buffer_load_dwordx4 v30, s[28:31], s50 offen offset:0 lds\n"
"s_waitcnt lgkmcnt(11)\n"
"v_mfma_f32_16x16x32_bf16 v[88:91], a[192:195], a[236:239], v[88:91]\n"
"buffer_load_dwordx4 v31, s[28:31], s50 offen offset:1024 lds\n"
"buffer_load_dwordx4 v32, s[28:31], s50 offen offset:2048 lds\n"
"v_mfma_f32_16x16x32_bf16 v[92:95], a[196:199], a[236:239], v[92:95]\n"
"buffer_load_dwordx4 v33, s[28:31], s50 offen offset:3072 lds\n"
"v_mfma_f32_16x16x32_bf16 v[136:139], a[200:203], a[236:239], v[136:139]\n"
"v_mfma_f32_16x16x32_bf16 v[140:143], a[204:207], a[236:239], v[140:143]\n"
"ds_read_b128 a[236:239], v17 offset:49152\n"
"v_mfma_f32_16x16x32_bf16 v[96:99], a[192:195], a[240:243], v[96:99]\n"
"v_mfma_f32_16x16x32_bf16 v[100:103], a[196:199], a[240:243], v[100:103]\n"
"v_mfma_f32_16x16x32_bf16 v[144:147], a[200:203], a[240:243], v[144:147]\n"
"v_mfma_f32_16x16x32_bf16 v[148:151], a[204:207], a[240:243], v[148:151]\n"
"ds_read_b128 a[240:243], v21 offset:0\n"
"v_mfma_f32_16x16x32_bf16 v[104:107], a[192:195], a[244:247], v[104:107]\n"
"v_mfma_f32_16x16x32_bf16 v[108:111], a[196:199], a[244:247], v[108:111]\n"
"v_mfma_f32_16x16x32_bf16 v[152:155], a[200:203], a[244:247], v[152:155]\n"
"v_mfma_f32_16x16x32_bf16 v[156:159], a[204:207], a[244:247], v[156:159]\n"
"ds_read_b128 a[244:247], v21 offset:16384\n"
"s_waitcnt lgkmcnt(3)\n"
"v_mfma_f32_16x16x32_bf16 v[64:67], a[208:211], a[224:227], v[64:67]\n"
"v_mfma_f32_16x16x32_bf16 v[68:71], a[212:215], a[224:227], v[68:71]\n"
"s_waitcnt vmcnt(8)\n"
"v_mfma_f32_16x16x32_bf16 v[112:115], a[216:219], a[224:227], v[112:115]\n"
"ds_read_b64_tr_b16 a[192:193], v10 offset:0\n"
"ds_read_b64_tr_b16 a[194:195], v10 offset:2048\n"
"v_mfma_f32_16x16x32_bf16 v[116:119], a[220:223], a[224:227], v[116:119]\n"
"ds_read_b128 a[224:227], v14 offset:256\n"
"v_mfma_f32_16x16x32_bf16 v[72:75], a[208:211], a[228:231], v[72:75]\n"
"ds_read_b64_tr_b16 a[196:197], v11 offset:0\n"
"ds_read_b64_tr_b16 a[198:199], v11 offset:2048\n"
"v_mfma_f32_16x16x32_bf16 v[76:79], a[212:215], a[228:231], v[76:79]\n"
"v_mfma_f32_16x16x32_bf16 v[120:123], a[216:219], a[228:231], v[120:123]\n"
"ds_read_b64_tr_b16 a[200:201], v12 offset:0\n"
"ds_read_b64_tr_b16 a[202:203], v12 offset:2048\n"
"v_mfma_f32_16x16x32_bf16 v[124:127], a[220:223], a[228:231], v[124:127]\n"
"ds_read_b128 a[228:231], v14 offset:16640\n"
"v_mfma_f32_16x16x32_bf16 v[80:83], a[208:211], a[232:235], v[80:83]\n"
"ds_read_b64_tr_b16 a[204:205], v13 offset:0\n"
"ds_read_b64_tr_b16 a[206:207], v13 offset:2048\n"
"v_mfma_f32_16x16x32_bf16 v[84:87], a[212:215], a[232:235], v[84:87]\n"
"v_mfma_f32_16x16x32_bf16 v[128:131], a[216:219], a[232:235], v[128:131]\n"
"s_add_u32 s50, s46, 229376\n"
"s_add_u32 m0, s49, 12288\n"
"v_mfma_f32_16x16x32_bf16 v[132:135], a[220:223], a[232:235], v[132:135]\n"
"ds_read_b128 a[232:235], v14 offset:33024\n"
"buffer_load_dwordx4 v30, s[28:31], s50 offen offset:0 lds\n"
"s_waitcnt lgkmcnt(11)\n"
"v_mfma_f32_16x16x32_bf16 v[88:91], a[208:211], a[236:239], v[88:91]\n"
"buffer_load_dwordx4 v31, s[28:31], s50 offen offset:1024 lds\n"
"buffer_load_dwordx4 v32, s[28:31], s50 offen offset:2048 lds\n"
"v_mfma_f32_16x16x32_bf16 v[92:95], a[212:215], a[236:239], v[92:95]\n"
"buffer_load_dwordx4 v33, s[28:31], s50 offen offset:3072 lds\n"
"v_mfma_f32_16x16x32_bf16 v[136:139], a[216:219], a[236:239], v[136:139]\n"
"v_mfma_f32_16x16x32_bf16 v[140:143], a[220:223], a[236:239], v[140:143]\n"
"ds_read_b128 a[236:239], v14 offset:49408\n"
"v_mfma_f32_16x16x32_bf16 v[96:99], a[208:211], a[240:243], v[96:99]\n"
"v_mfma_f32_16x16x32_bf16 v[100:103], a[212:215], a[240:243], v[100:103]\n"
"v_mfma_f32_16x16x32_bf16 v[144:147], a[216:219], a[240:243], v[144:147]\n"
"v_mfma_f32_16x16x32_bf16 v[148:151], a[220:223], a[240:243], v[148:151]\n"
"ds_read_b128 a[240:243], v18 offset:256\n"
"v_mfma_f32_16x16x32_bf16 v[104:107], a[208:211], a[244:247], v[104:107]\n"
"v_mfma_f32_16x16x32_bf16 v[108:111], a[212:215], a[244:247], v[108:111]\n"
"v_mfma_f32_16x16x32_bf16 v[152:155], a[216:219], a[244:247], v[152:155]\n"
"v_mfma_f32_16x16x32_bf16 v[156:159], a[220:223], a[244:247], v[156:159]\n"
"ds_read_b128 a[244:247], v18 offset:16640\n"
"s_waitcnt lgkmcnt(3)\n"
"v_mfma_f32_16x16x32_bf16 v[64:67], a[192:195], a[224:227], v[64:67]\n"
"v_mfma_f32_16x16x32_bf16 v[68:71], a[196:199], a[224:227], v[68:71]\n"
"s_waitcnt vmcnt(8)\n"
"v_mfma_f32_16x16x32_bf16 v[112:115], a[200:203], a[224:227], v[112:115]\n"
"ds_read_b64_tr_b16 a[208:209], v10 offset:4096\n"
"ds_read_b64_tr_b16 a[210:211], v10 offset:6144\n"
"v_mfma_f32_16x16x32_bf16 v[116:119], a[204:207], a[224:227], v[116:119]\n"
"ds_read_b128 a[224:227], v15 offset:256\n"
"v_mfma_f32_16x16x32_bf16 v[72:75], a[192:195], a[228:231], v[72:75]\n"
"ds_read_b64_tr_b16 a[212:213], v11 offset:4096\n"
"ds_read_b64_tr_b16 a[214:215], v11 offset:6144\n"
"v_mfma_f32_16x16x32_bf16 v[76:79], a[196:199], a[228:231], v[76:79]\n"
"v_mfma_f32_16x16x32_bf16 v[120:123], a[200:203], a[228:231], v[120:123]\n"
"ds_read_b64_tr_b16 a[216:217], v12 offset:4096\n"
"ds_read_b64_tr_b16 a[218:219], v12 offset:6144\n"
"v_mfma_f32_16x16x32_bf16 v[124:127], a[204:207], a[228:231], v[124:127]\n"
"ds_read_b128 a[228:231], v15 offset:16640\n"
"v_mfma_f32_16x16x32_bf16 v[80:83], a[192:195], a[232:235], v[80:83]\n"
"ds_read_b64_tr_b16 a[220:221], v13 offset:4096\n"
"ds_read_b64_tr_b16 a[222:223], v13 offset:6144\n"
"v_mfma_f32_16x16x32_bf16 v[84:87], a[196:199], a[232:235], v[84:87]\n"
"v_mfma_f32_16x16x32_bf16 v[128:131], a[200:203], a[232:235], v[128:131]\n"
"s_add_u32 s50, s46, 262144\n"
"s_add_u32 m0, s49, 0\n"
"v_mfma_f32_16x16x32_bf16 v[132:135], a[204:207], a[232:235], v[132:135]\n"
"ds_read_b128 a[232:235], v15 offset:33024\n"
"buffer_load_dwordx4 v30, s[28:31], s50 offen offset:0 lds\n"
"s_waitcnt lgkmcnt(11)\n"
"v_mfma_f32_16x16x32_bf16 v[88:91], a[192:195], a[236:239], v[88:91]\n"
"buffer_load_dwordx4 v31, s[28:31], s50 offen offset:1024 lds\n"
"buffer_load_dwordx4 v32, s[28:31], s50 offen offset:2048 lds\n"
"v_mfma_f32_16x16x32_bf16 v[92:95], a[196:199], a[236:239], v[92:95]\n"
"buffer_load_dwordx4 v33, s[28:31], s50 offen offset:3072 lds\n"
"v_mfma_f32_16x16x32_bf16 v[136:139], a[200:203], a[236:239], v[136:139]\n"
"v_mfma_f32_16x16x32_bf16 v[140:143], a[204:207], a[236:239], v[140:143]\n"
"ds_read_b128 a[236:239], v15 offset:49408\n"
"v_mfma_f32_16x16x32_bf16 v[96:99], a[192:195], a[240:243], v[96:99]\n"
"v_mfma_f32_16x16x32_bf16 v[100:103], a[196:199], a[240:243], v[100:103]\n"
"v_mfma_f32_16x16x32_bf16 v[144:147], a[200:203], a[240:243], v[144:147]\n"
"v_mfma_f32_16x16x32_bf16 v[148:151], a[204:207], a[240:243], v[148:151]\n"
"ds_read_b128 a[240:243], v19 offset:256\n"
"v_mfma_f32_16x16x32_bf16 v[104:107], a[192:195], a[244:247], v[104:107]\n"
"v_mfma_f32_16x16x32_bf16 v[108:111], a[196:199], a[244:247], v[108:111]\n"
"v_mfma_f32_16x16x32_bf16 v[152:155], a[200:203], a[244:247], v[152:155]\n"
"v_mfma_f32_16x16x32_bf16 v[156:159], a[204:207], a[244:247], v[156:159]\n"
"ds_read_b128 a[244:247], v19 offset:16640\n"
"s_waitcnt lgkmcnt(3)\n"
"v_mfma_f32_16x16x32_bf16 v[64:67], a[208:211], a[224:227], v[64:67]\n"
"v_mfma_f32_16x16x32_bf16 v[68:71], a[212:215], a[224:227], v[68:71]\n"
"s_waitcnt vmcnt(8)\n"
"v_mfma_f32_16x16x32_bf16 v[112:115], a[216:219], a[224:227], v[112:115]\n"
"ds_read_b64_tr_b16 a[192:193], v10 offset:8192\n"
"ds_read_b64_tr_b16 a[194:195], v10 offset:10240\n"
"v_mfma_f32_16x16x32_bf16 v[116:119], a[220:223], a[224:227], v[116:119]\n"
"ds_read_b128 a[224:227], v16 offset:256\n"
"v_mfma_f32_16x16x32_bf16 v[72:75], a[208:211], a[228:231], v[72:75]\n"
"ds_read_b64_tr_b16 a[196:197], v11 offset:8192\n"
"ds_read_b64_tr_b16 a[198:199], v11 offset:10240\n"
"v_mfma_f32_16x16x32_bf16 v[76:79], a[212:215], a[228:231], v[76:79]\n"
"v_mfma_f32_16x16x32_bf16 v[120:123], a[216:219], a[228:231], v[120:123]\n"
"ds_read_b64_tr_b16 a[200:201], v12 offset:8192\n"
"ds_read_b64_tr_b16 a[202:203], v12 offset:10240\n"
"v_mfma_f32_16x16x32_bf16 v[124:127], a[220:223], a[228:231], v[124:127]\n"
"ds_read_b128 a[228:231], v16 offset:16640\n"
"v_mfma_f32_16x16x32_bf16 v[80:83], a[208:211], a[232:235], v[80:83]\n"
"ds_read_b64_tr_b16 a[204:205], v13 offset:8192\n"
"ds_read_b64_tr_b16 a[206:207], v13 offset:10240\n"
"v_mfma_f32_16x16x32_bf16 v[84:87], a[212:215], a[232:235], v[84:87]\n"
"v_mfma_f32_16x16x32_bf16 v[128:131], a[216:219], a[232:235], v[128:131]\n"
"s_add_u32 s50, s46, 294912\n"
"s_add_u32 m0, s49, 4096\n"
"v_mfma_f32_16x16x32_bf16 v[132:135], a[220:223], a[232:235], v[132:135]\n"
"ds_read_b128 a[232:235], v16 offset:33024\n"
"buffer_load_dwordx4 v30, s[28:31], s50 offen offset:0 lds\n"
"s_waitcnt lgkmcnt(11)\n"
"v_mfma_f32_16x16x32_bf16 v[88:91], a[208:211], a[236:239], v[88:91]\n"
"buffer_load_dwordx4 v31, s[28:31], s50 offen offset:1024 lds\n"
"buffer_load_dwordx4 v32, s[28:31], s50 offen offset:2048 lds\n"
"v_mfma_f32_16x16x32_bf16 v[92:95], a[212:215], a[236:239], v[92:95]\n"
"buffer_load_dwordx4 v33, s[28:31], s50 offen offset:3072 lds\n"
"v_mfma_f32_16x16x32_bf16 v[136:139], a[216:219], a[236:239], v[136:139]\n"
"v_mfma_f32_16x16x32_bf16 v[140:143], a[220:223], a[236:239], v[140:143]\n"
"ds_read_b128 a[236:239], v16 offset:49408\n"
"v_mfma_f32_16x16x32_bf16 v[96:99], a[208:211], a[240:243], v[96:99]\n"
"v_mfma_f32_16x16x32_bf16 v[100:103], a[212:215], a[240:243], v[100:103]\n"
"v_mfma_f32_16x16x32_bf16 v[144:147], a[216:219], a[240:243], v[144:147]\n"
"v_mfma_f32_16x16x32_bf16 v[148:151], a[220:223], a[240:243], v[148:151]\n"
"ds_read_b128 a[240:243], v20 offset:256\n"
"v_mfma_f32_16x16x32_bf16 v[104:107], a[208:211], a[244:247], v[104:107]\n"
"v_mfma_f32_16x16x32_bf16 v[108:111], a[212:215], a[244:247], v[108:111]\n"
"v_mfma_f32_16x16x32_bf16 v[152:155], a[216:219], a[244:247], v[152:155]\n"
"v_mfma_f32_16x16x32_bf16 v[156:159], a[220:223], a[244:247], v[156:159]\n"
"ds_read_b128 a[244:247], v20 offset:16640\n"
"s_waitcnt lgkmcnt(3)\n"
"v_mfma_f32_16x16x32_bf16 v[64:67], a[192:195], a[224:227], v[64:67]\n"
"v_mfma_f32_16x16x32_bf16 v[68:71], a[196:199], a[224:227], v[68:71]\n"
"s_waitcnt vmcnt(8)\n"
"v_mfma_f32_16x16x32_bf16 v[112:115], a[200:203], a[224:227], v[112:115]\n"
"ds_read_b64_tr_b16 a[208:209], v10 offset:12288\n"
"ds_read_b64_tr_b16 a[210:211], v10 offset:14336\n"
"v_mfma_f32_16x16x32_bf16 v[116:119], a[204:207], a[224:227], v[116:119]\n"
"ds_read_b128 a[224:227], v17 offset:256\n"
"v_mfma_f32_16x16x32_bf16 v[72:75], a[192:195], a[228:231], v[72:75]\n"
"ds_read_b64_tr_b16 a[212:213], v11 offset:12288\n"
"ds_read_b64_tr_b16 a[214:215], v11 offset:14336\n"
"v_mfma_f32_16x16x32_bf16 v[76:79], a[196:199], a[228:231], v[76:79]\n"
"v_mfma_f32_16x16x32_bf16 v[120:123], a[200:203], a[228:231], v[120:123]\n"
"ds_read_b64_tr_b16 a[216:217], v12 offset:12288\n"
"ds_read_b64_tr_b16 a[218:219], v12 offset:14336\n"
"v_mfma_f32_16x16x32_bf16 v[124:127], a[204:207], a[228:231], v[124:127]\n"
"ds_read_b128 a[228:231], v17 offset:16640\n"
"v_mfma_f32_16x16x32_bf16 v[80:83], a[192:195], a[232:235], v[80:83]\n"
"ds_read_b64_tr_b16 a[220:221], v13 offset:12288\n"
"ds_read_b64_tr_b16 a[222:223], v13 offset:14336\n"
"v_mfma_f32_16x16x32_bf16 v[84:87], a[196:199], a[232:235], v[84:87]\n"
"v_mfma_f32_16x16x32_bf16 v[128:131], a[200:203], a[232:235], v[128:131]\n"
"s_add_u32 s50, s46, 327680\n"
"s_add_u32 m0, s49, 8192\n"
"v_mfma_f32_16x16x32_bf16 v[132:135], a[204:207], a[232:235], v[132:135]\n"
"ds_read_b128 a[232:235], v17 offset:33024\n"
"buffer_load_dwordx4 v30, s[28:31], s50 offen offset:0 lds\n"
"s_waitcnt lgkmcnt(11)\n"
"v_mfma_f32_16x16x32_bf16 v[88:91], a[192:195], a[236:239], v[88:91]\n"
"buffer_load_dwordx4 v31, s[28:31], s50 offen offset:1024 lds\n"
"buffer_load_dwordx4 v32, s[28:31], s50 offen offset:2048 lds\n"
"v_mfma_f32_16x16x32_bf16 v[92:95], a[196:199], a[236:239], v[92:95]\n"
"buffer_load_dwordx4 v33, s[28:31], s50 offen offset:3072 lds\n"
"v_mfma_f32_16x16x32_bf16 v[136:139], a[200:203], a[236:239], v[136:139]\n"
"v_mfma_f32_16x16x32_bf16 v[140:143], a[204:207], a[236:239], v[140:143]\n"
"ds_read_b128 a[236:239], v17 offset:49408\n"
"v_mfma_f32_16x16x32_bf16 v[96:99], a[192:195], a[240:243], v[96:99]\n"
"v_mfma_f32_16x16x32_bf16 v[100:103], a[196:199], a[240:243], v[100:103]\n"
"v_mfma_f32_16x16x32_bf16 v[144:147], a[200:203], a[240:243], v[144:147]\n"
"v_mfma_f32_16x16x32_bf16 v[148:151], a[204:207], a[240:243], v[148:151]\n"
"ds_read_b128 a[240:243], v21 offset:256\n"
"v_mfma_f32_16x16x32_bf16 v[104:107], a[192:195], a[244:247], v[104:107]\n"
"v_mfma_f32_16x16x32_bf16 v[108:111], a[196:199], a[244:247], v[108:111]\n"
"v_mfma_f32_16x16x32_bf16 v[152:155], a[200:203], a[244:247], v[152:155]\n"
"v_mfma_f32_16x16x32_bf16 v[156:159], a[204:207], a[244:247], v[156:159]\n"
"ds_read_b128 a[244:247], v21 offset:16640\n"
"s_waitcnt lgkmcnt(3)\n"
"v_mfma_f32_16x16x32_bf16 v[64:67], a[208:211], a[224:227], v[64:67]\n"
"v_mfma_f32_16x16x32_bf16 v[68:71], a[212:215], a[224:227], v[68:71]\n"
"s_waitcnt vmcnt(8)\n"
"v_mfma_f32_16x16x32_bf16 v[112:115], a[216:219], a[224:227], v[112:115]\n"
"ds_read_b64_tr_b16 a[192:193], v10 offset:0\n"
"ds_read_b64_tr_b16 a[194:195], v10 offset:2048\n"
"v_mfma_f32_16x16x32_bf16 v[116:119], a[220:223], a[224:227], v[116:119]\n"
"ds_read_b128 a[224:227], v14 offset:512\n"
"v_mfma_f32_16x16x32_bf16 v[72:75], a[208:211], a[228:231], v[72:75]\n"
"ds_read_b64_tr_b16 a[196:197], v11 offset:0\n"
"ds_read_b64_tr_b16 a[198:199], v11 offset:2048\n"
"v_mfma_f32_16x16x32_bf16 v[76:79], a[212:215], a[228:231], v[76:79]\n"
"v_mfma_f32_16x16x32_bf16 v[120:123], a[216:219], a[228:231], v[120:123]\n"
"ds_read_b64_tr_b16 a[200:201], v12 offset:0\n"
"ds_read_b64_tr_b16 a[202:203], v12 offset:2048\n"
"v_mfma_f32_16x16x32_bf16 v[124:127], a[220:223], a[228:231], v[124:127]\n"
"ds_read_b128 a[228:231], v14 offset:16896\n"
"v_mfma_f32_16x16x32_bf16 v[80:83], a[208:211], a[232:235], v[80:83]\n"
"ds_read_b64_tr_b16 a[204:205], v13 offset:0\n"
"ds_read_b64_tr_b16 a[206:207], v13 offset:2048\n"
"v_mfma_f32_16x16x32_bf16 v[84:87], a[212:215], a[232:235], v[84:87]\n"
"v_mfma_f32_16x16x32_bf16 v[128:131], a[216:219], a[232:235], v[128:131]\n"
"s_add_u32 s50, s46, 360448\n"
"s_add_u32 m0, s49, 12288\n"
"v_mfma_f32_16x16x32_bf16 v[132:135], a[220:223], a[232:235], v[132:135]\n"
"ds_read_b128 a[232:235], v14 offset:33280\n"
"buffer_load_dwordx4 v30, s[28:31], s50 offen offset:0 lds\n"
"s_waitcnt lgkmcnt(11)\n"
"v_mfma_f32_16x16x32_bf16 v[88:91], a[208:211], a[236:239], v[88:91]\n"
"buffer_load_dwordx4 v31, s[28:31], s50 offen offset:1024 lds\n"
"buffer_load_dwordx4 v32, s[28:31], s50 offen offset:2048 lds\n"
"v_mfma_f32_16x16x32_bf16 v[92:95], a[212:215], a[236:239], v[92:95]\n"
"buffer_load_dwordx4 v33, s[28:31], s50 offen offset:3072 lds\n"
"v_mfma_f32_16x16x32_bf16 v[136:139], a[216:219], a[236:239], v[136:139]\n"
"v_mfma_f32_16x16x32_bf16 v[140:143], a[220:223], a[236:239], v[140:143]\n"
"ds_read_b128 a[236:239], v14 offset:49664\n"
"v_mfma_f32_16x16x32_bf16 v[96:99], a[208:211], a[240:243], v[96:99]\n"
"v_mfma_f32_16x16x32_bf16 v[100:103], a[212:215], a[240:243], v[100:103]\n"
"v_mfma_f32_16x16x32_bf16 v[144:147], a[216:219], a[240:243], v[144:147]\n"
"v_mfma_f32_16x16x32_bf16 v[148:151], a[220:223], a[240:243], v[148:151]\n"
"ds_read_b128 a[240:243], v18 offset:512\n"
"v_mfma_f32_16x16x32_bf16 v[104:107], a[208:211], a[244:247], v[104:107]\n"
"v_mfma_f32_16x16x32_bf16 v[108:111], a[212:215], a[244:247], v[108:111]\n"
"v_mfma_f32_16x16x32_bf16 v[152:155], a[216:219], a[244:247], v[152:155]\n"
"v_mfma_f32_16x16x32_bf16 v[156:159], a[220:223], a[244:247], v[156:159]\n"
"ds_read_b128 a[244:247], v18 offset:16896\n"
"s_waitcnt lgkmcnt(3)\n"
"v_mfma_f32_16x16x32_bf16 v[64:67], a[192:195], a[224:227], v[64:67]\n"
"v_mfma_f32_16x16x32_bf16 v[68:71], a[196:199], a[224:227], v[68:71]\n"
"s_waitcnt vmcnt(8)\n"
"v_mfma_f32_16x16x32_bf16 v[112:115], a[200:203], a[224:227], v[112:115]\n"
"ds_read_b64_tr_b16 a[208:209], v10 offset:4096\n"
"ds_read_b64_tr_b16 a[210:211], v10 offset:6144\n"
"v_mfma_f32_16x16x32_bf16 v[116:119], a[204:207], a[224:227], v[116:119]\n"
"ds_read_b128 a[224:227], v15 offset:512\n"
"v_mfma_f32_16x16x32_bf16 v[72:75], a[192:195], a[228:231], v[72:75]\n"
"ds_read_b64_tr_b16 a[212:213], v11 offset:4096\n"
"ds_read_b64_tr_b16 a[214:215], v11 offset:6144\n"
"v_mfma_f32_16x16x32_bf16 v[76:79], a[196:199], a[228:231], v[76:79]\n"
"v_mfma_f32_16x16x32_bf16 v[120:123], a[200:203], a[228:231], v[120:123]\n"
"ds_read_b64_tr_b16 a[216:217], v12 offset:4096\n"
"ds_read_b64_tr_b16 a[218:219], v12 offset:6144\n"
"v_mfma_f32_16x16x32_bf16 v[124:127], a[204:207], a[228:231], v[124:127]\n"
"ds_read_b128 a[228:231], v15 offset:16896\n"
"v_mfma_f32_16x16x32_bf16 v[80:83], a[192:195], a[232:235], v[80:83]\n"
"ds_read_b64_tr_b16 a[220:221], v13 offset:4096\n"
"ds_read_b64_tr_b16 a[222:223], v13 offset:6144\n"
"v_mfma_f32_16x16x32_bf16 v[84:87], a[196:199], a[232:235], v[84:87]\n"
"v_mfma_f32_16x16x32_bf16 v[128:131], a[200:203], a[232:235], v[128:131]\n"
"s_add_u32 s50, s46, 393216\n"
"s_add_u32 m0, s49, 0\n"
"v_mfma_f32_16x16x32_bf16 v[132:135], a[204:207], a[232:235], v[132:135]\n"
"ds_read_b128 a[232:235], v15 offset:33280\n"
"buffer_load_dwordx4 v30, s[28:31], s50 offen offset:0 lds\n"
"s_waitcnt lgkmcnt(11)\n"
"v_mfma_f32_16x16x32_bf16 v[88:91], a[192:195], a[236:239], v[88:91]\n"
"buffer_load_dwordx4 v31, s[28:31], s50 offen offset:1024 lds\n"
"buffer_load_dwordx4 v32, s[28:31], s50 offen offset:2048 lds\n"
"v_mfma_f32_16x16x32_bf16 v[92:95], a[196:199], a[236:239], v[92:95]\n"
"buffer_load_dwordx4 v33, s[28:31], s50 offen offset:3072 lds\n"
"v_mfma_f32_16x16x32_bf16 v[136:139], a[200:203], a[236:239], v[136:139]\n"
"v_mfma_f32_16x16x32_bf16 v[140:143], a[204:207], a[236:239], v[140:143]\n"
"ds_read_b128 a[236:239], v15 offset:49664\n"
"v_mfma_f32_16x16x32_bf16 v[96:99], a[192:195], a[240:243], v[96:99]\n"
"v_mfma_f32_16x16x32_bf16 v[100:103], a[196:199], a[240:243], v[100:103]\n"
"v_mfma_f32_16x16x32_bf16 v[144:147], a[200:203], a[240:243], v[144:147]\n"
"v_mfma_f32_16x16x32_bf16 v[148:151], a[204:207], a[240:243], v[148:151]\n"
"ds_read_b128 a[240:243], v19 offset:512\n"
"v_mfma_f32_16x16x32_bf16 v[104:107], a[192:195], a[244:247], v[104:107]\n"
"v_mfma_f32_16x16x32_bf16 v[108:111], a[196:199], a[244:247], v[108:111]\n"
"v_mfma_f32_16x16x32_bf16 v[152:155], a[200:203], a[244:247], v[152:155]\n"
"v_mfma_f32_16x16x32_bf16 v[156:159], a[204:207], a[244:247], v[156:159]\n"
"ds_read_b128 a[244:247], v19 offset:16896\n"
"s_waitcnt lgkmcnt(3)\n"
"v_mfma_f32_16x16x32_bf16 v[64:67], a[208:211], a[224:227], v[64:67]\n"
"v_mfma_f32_16x16x32_bf16 v[68:71], a[212:215], a[224:227], v[68:71]\n"
"s_waitcnt vmcnt(8)\n"
"v_mfma_f32_16x16x32_bf16 v[112:115], a[216:219], a[224:227], v[112:115]\n"
"ds_read_b64_tr_b16 a[192:193], v10 offset:8192\n"
"ds_read_b64_tr_b16 a[194:195], v10 offset:10240\n"
"v_mfma_f32_16x16x32_bf16 v[116:119], a[220:223], a[224:227], v[116:119]\n"
"ds_read_b128 a[224:227], v16 offset:512\n"
"v_mfma_f32_16x16x32_bf16 v[72:75], a[208:211], a[228:231], v[72:75]\n"
"ds_read_b64_tr_b16 a[196:197], v11 offset:8192\n"
"ds_read_b64_tr_b16 a[198:199], v11 offset:10240\n"
"v_mfma_f32_16x16x32_bf16 v[76:79], a[212:215], a[228:231], v[76:79]\n"
"v_mfma_f32_16x16x32_bf16 v[120:123], a[216:219], a[228:231], v[120:123]\n"
"ds_read_b64_tr_b16 a[200:201], v12 offset:8192\n"
"ds_read_b64_tr_b16 a[202:203], v12 offset:10240\n"
"v_mfma_f32_16x16x32_bf16 v[124:127], a[220:223], a[228:231], v[124:127]\n"
"ds_read_b128 a[228:231], v16 offset:16896\n"
"v_mfma_f32_16x16x32_bf16 v[80:83], a[208:211], a[232:235], v[80:83]\n"
"ds_read_b64_tr_b16 a[204:205], v13 offset:8192\n"
"ds_read_b64_tr_b16 a[206:207], v13 offset:10240\n"
"v_mfma_f32_16x16x32_bf16 v[84:87], a[212:215], a[232:235], v[84:87]\n"
"v_mfma_f32_16x16x32_bf16 v[128:131], a[216:219], a[232:235], v[128:131]\n"
"s_add_u32 s50, s46, 425984\n"
"s_add_u32 m0, s49, 4096\n"
"v_mfma_f32_16x16x32_bf16 v[132:135], a[220:223], a[232:235], v[132:135]\n"
"ds_read_b128 a[232:235], v16 offset:33280\n"
"buffer_load_dwordx4 v30, s[28:31], s50 offen offset:0 lds\n"
"s_waitcnt lgkmcnt(11)\n"
"v_mfma_f32_16x16x32_bf16 v[88:91], a[208:211], a[236:239], v[88:91]\n"
"buffer_load_dwordx4 v31, s[28:31], s50 offen offset:1024 lds\n"
"buffer_load_dwordx4 v32, s[28:31], s50 offen offset:2048 lds\n"
"v_mfma_f32_16x16x32_bf16 v[92:95], a[212:215], a[236:239], v[92:95]\n"
"buffer_load_dwordx4 v33, s[28:31], s50 offen offset:3072 lds\n"
"v_mfma_f32_16x16x32_bf16 v[136:139], a[216:219], a[236:239], v[136:139]\n"
"v_mfma_f32_16x16x32_bf16 v[140:143], a[220:223], a[236:239], v[140:143]\n"
"ds_read_b128 a[236:239], v16 offset:49664\n"
"v_mfma_f32_16x16x32_bf16 v[96:99], a[208:211], a[240:243], v[96:99]\n"
"v_mfma_f32_16x16x32_bf16 v[100:103], a[212:215], a[240:243], v[100:103]\n"
"v_mfma_f32_16x16x32_bf16 v[144:147], a[216:219], a[240:243], v[144:147]\n"
"v_mfma_f32_16x16x32_bf16 v[148:151], a[220:223], a[240:243], v[148:151]\n"
"ds_read_b128 a[240:243], v20 offset:512\n"
"v_mfma_f32_16x16x32_bf16 v[104:107], a[208:211], a[244:247], v[104:107]\n"
"v_mfma_f32_16x16x32_bf16 v[108:111], a[212:215], a[244:247], v[108:111]\n"
"v_mfma_f32_16x16x32_bf16 v[152:155], a[216:219], a[244:247], v[152:155]\n"
"v_mfma_f32_16x16x32_bf16 v[156:159], a[220:223], a[244:247], v[156:159]\n"
"ds_read_b128 a[244:247], v20 offset:16896\n"
"s_waitcnt lgkmcnt(3)\n"
"v_mfma_f32_16x16x32_bf16 v[64:67], a[192:195], a[224:227], v[64:67]\n"
"v_mfma_f32_16x16x32_bf16 v[68:71], a[196:199], a[224:227], v[68:71]\n"
"s_waitcnt vmcnt(8)\n"
"v_mfma_f32_16x16x32_bf16 v[112:115], a[200:203], a[224:227], v[112:115]\n"
"ds_read_b64_tr_b16 a[208:209], v10 offset:12288\n"
"ds_read_b64_tr_b16 a[210:211], v10 offset:14336\n"
"v_mfma_f32_16x16x32_bf16 v[116:119], a[204:207], a[224:227], v[116:119]\n"
"ds_read_b128 a[224:227], v17 offset:512\n"
"v_mfma_f32_16x16x32_bf16 v[72:75], a[192:195], a[228:231], v[72:75]\n"
"ds_read_b64_tr_b16 a[212:213], v11 offset:12288\n"
"ds_read_b64_tr_b16 a[214:215], v11 offset:14336\n"
"v_mfma_f32_16x16x32_bf16 v[76:79], a[196:199], a[228:231], v[76:79]\n"
"v_mfma_f32_16x16x32_bf16 v[120:123], a[200:203], a[228:231], v[120:123]\n"
"ds_read_b64_tr_b16 a[216:217], v12 offset:12288\n"
"ds_read_b64_tr_b16 a[218:219], v12 offset:14336\n"
"v_mfma_f32_16x16x32_bf16 v[124:127], a[204:207], a[228:231], v[124:127]\n"
"ds_read_b128 a[228:231], v17 offset:16896\n"
"v_mfma_f32_16x16x32_bf16 v[80:83], a[192:195], a[232:235], v[80:83]\n"
"ds_read_b64_tr_b16 a[220:221], v13 offset:12288\n"
"ds_read_b64_tr_b16 a[222:223], v13 offset:14336\n"
"v_mfma_f32_16x16x32_bf16 v[84:87], a[196:199], a[232:235], v[84:87]\n"
"v_mfma_f32_16x16x32_bf16 v[128:131], a[200:203], a[232:235], v[128:131]\n"
"s_add_u32 s50, s46, 458752\n"
"s_add_u32 m0, s49, 8192\n"
"v_mfma_f32_16x16x32_bf16 v[132:135], a[204:207], a[232:235], v[132:135]\n"
"ds_read_b128 a[232:235], v17 offset:33280\n"
"buffer_load_dwordx4 v30, s[28:31], s50 offen offset:0 lds\n"
"s_waitcnt lgkmcnt(11)\n"
"v_mfma_f32_16x16x32_bf16 v[88:91], a[192:195], a[236:239], v[88:91]\n"
"buffer_load_dwordx4 v31, s[28:31], s50 offen offset:1024 lds\n"
"buffer_load_dwordx4 v32, s[28:31], s50 offen offset:2048 lds\n"
"v_mfma_f32_16x16x32_bf16 v[92:95], a[196:199], a[236:239], v[92:95]\n"
"buffer_load_dwordx4 v33, s[28:31], s50 offen offset:3072 lds\n"
"v_mfma_f32_16x16x32_bf16 v[136:139], a[200:203], a[236:239], v[136:139]\n"
"v_mfma_f32_16x16x32_bf16 v[140:143], a[204:207], a[236:239], v[140:143]\n"
"ds_read_b128 a[236:239], v17 offset:49664\n"
"v_mfma_f32_16x16x32_bf16 v[96:99], a[192:195], a[240:243], v[96:99]\n"
"v_mfma_f32_16x16x32_bf16 v[100:103], a[196:199], a[240:243], v[100:103]\n"
"v_mfma_f32_16x16x32_bf16 v[144:147], a[200:203], a[240:243], v[144:147]\n"
"v_mfma_f32_16x16x32_bf16 v[148:151], a[204:207], a[240:243], v[148:151]\n"
"ds_read_b128 a[240:243], v21 offset:512\n"
"v_mfma_f32_16x16x32_bf16 v[104:107], a[192:195], a[244:247], v[104:107]\n"
"v_mfma_f32_16x16x32_bf16 v[108:111], a[196:199], a[244:247], v[108:111]\n"
"v_mfma_f32_16x16x32_bf16 v[152:155], a[200:203], a[244:247], v[152:155]\n"
"v_mfma_f32_16x16x32_bf16 v[156:159], a[204:207], a[244:247], v[156:159]\n"
"ds_read_b128 a[244:247], v21 offset:16896\n"
"s_waitcnt lgkmcnt(3)\n"
"v_mfma_f32_16x16x32_bf16 v[64:67], a[208:211], a[224:227], v[64:67]\n"
"v_mfma_f32_16x16x32_bf16 v[68:71], a[212:215], a[224:227], v[68:71]\n"
"s_waitcnt vmcnt(8)\n"
"v_mfma_f32_16x16x32_bf16 v[112:115], a[216:219], a[224:227], v[112:115]\n"
"ds_read_b64_tr_b16 a[192:193], v10 offset:0\n"
"ds_read_b64_tr_b16 a[194:195], v10 offset:2048\n"
"v_mfma_f32_16x16x32_bf16 v[116:119], a[220:223], a[224:227], v[116:119]\n"
"ds_read_b128 a[224:227], v14 offset:768\n"
"v_mfma_f32_16x16x32_bf16 v[72:75], a[208:211], a[228:231], v[72:75]\n"
"ds_read_b64_tr_b16 a[196:197], v11 offset:0\n"
"ds_read_b64_tr_b16 a[198:199], v11 offset:2048\n"
"v_mfma_f32_16x16x32_bf16 v[76:79], a[212:215], a[228:231], v[76:79]\n"
"v_mfma_f32_16x16x32_bf16 v[120:123], a[216:219], a[228:231], v[120:123]\n"
"ds_read_b64_tr_b16 a[200:201], v12 offset:0\n"
"ds_read_b64_tr_b16 a[202:203], v12 offset:2048\n"
"v_mfma_f32_16x16x32_bf16 v[124:127], a[220:223], a[228:231], v[124:127]\n"
"ds_read_b128 a[228:231], v14 offset:17152\n"
"v_mfma_f32_16x16x32_bf16 v[80:83], a[208:211], a[232:235], v[80:83]\n"
"ds_read_b64_tr_b16 a[204:205], v13 offset:0\n"
"ds_read_b64_tr_b16 a[206:207], v13 offset:2048\n"
"v_mfma_f32_16x16x32_bf16 v[84:87], a[212:215], a[232:235], v[84:87]\n"
"v_mfma_f32_16x16x32_bf16 v[128:131], a[216:219], a[232:235], v[128:131]\n"
"s_add_u32 s50, s46, 491520\n"
"s_add_u32 m0, s49, 12288\n"
"v_mfma_f32_16x16x32_bf16 v[132:135], a[220:223], a[232:235], v[132:135]\n"
"ds_read_b128 a[232:235], v14 offset:33536\n"
"buffer_load_dwordx4 v30, s[28:31], s50 offen offset:0 lds\n"
"s_waitcnt lgkmcnt(11)\n"
"v_mfma_f32_16x16x32_bf16 v[88:91], a[208:211], a[236:239], v[88:91]\n"
"buffer_load_dwordx4 v31, s[28:31], s50 offen offset:1024 lds\n"
"buffer_load_dwordx4 v32, s[28:31], s50 offen offset:2048 lds\n"
"v_mfma_f32_16x16x32_bf16 v[92:95], a[212:215], a[236:239], v[92:95]\n"
"buffer_load_dwordx4 v33, s[28:31], s50 offen offset:3072 lds\n"
"v_mfma_f32_16x16x32_bf16 v[136:139], a[216:219], a[236:239], v[136:139]\n"
"v_mfma_f32_16x16x32_bf16 v[140:143], a[220:223], a[236:239], v[140:143]\n"
"ds_read_b128 a[236:239], v14 offset:49920\n"
"v_mfma_f32_16x16x32_bf16 v[96:99], a[208:211], a[240:243], v[96:99]\n"
"v_mfma_f32_16x16x32_bf16 v[100:103], a[212:215], a[240:243], v[100:103]\n"
"v_mfma_f32_16x16x32_bf16 v[144:147], a[216:219], a[240:243], v[144:147]\n"
"v_mfma_f32_16x16x32_bf16 v[148:151], a[220:223], a[240:243], v[148:151]\n"
"ds_read_b128 a[240:243], v18 offset:768\n"
"v_mfma_f32_16x16x32_bf16 v[104:107], a[208:211], a[244:247], v[104:107]\n"
"v_mfma_f32_16x16x32_bf16 v[108:111], a[212:215], a[244:247], v[108:111]\n"
"v_mfma_f32_16x16x32_bf16 v[152:155], a[216:219], a[244:247], v[152:155]\n"
"v_mfma_f32_16x16x32_bf16 v[156:159], a[220:223], a[244:247], v[156:159]\n"
"ds_read_b128 a[244:247], v18 offset:17152\n"
"s_waitcnt lgkmcnt(3)\n"
"v_mfma_f32_16x16x32_bf16 v[64:67], a[192:195], a[224:227], v[64:67]\n"
"v_mfma_f32_16x16x32_bf16 v[68:71], a[196:199], a[224:227], v[68:71]\n"
"s_waitcnt vmcnt(8)\n"
"v_mfma_f32_16x16x32_bf16 v[112:115], a[200:203], a[224:227], v[112:115]\n"
"ds_read_b64_tr_b16 a[208:209], v10 offset:4096\n"
"ds_read_b64_tr_b16 a[210:211], v10 offset:6144\n"
"v_mfma_f32_16x16x32_bf16 v[116:119], a[204:207], a[224:227], v[116:119]\n"
"ds_read_b128 a[224:227], v15 offset:768\n"
"v_mfma_f32_16x16x32_bf16 v[72:75], a[192:195], a[228:231], v[72:75]\n"
"ds_read_b64_tr_b16 a[212:213], v11 offset:4096\n"
"ds_read_b64_tr_b16 a[214:215], v11 offset:6144\n"
"v_mfma_f32_16x16x32_bf16 v[76:79], a[196:199], a[228:231], v[76:79]\n"
"v_mfma_f32_16x16x32_bf16 v[120:123], a[200:203], a[228:231], v[120:123]\n"
"ds_read_b64_tr_b16 a[216:217], v12 offset:4096\n"
"ds_read_b64_tr_b16 a[218:219], v12 offset:6144\n"
"v_mfma_f32_16x16x32_bf16 v[124:127], a[204:207], a[228:231], v[124:127]\n"
"ds_read_b128 a[228:231], v15 offset:17152\n"
"v_mfma_f32_16x16x32_bf16 v[80:83], a[192:195], a[232:235], v[80:83]\n"
"ds_read_b64_tr_b16 a[220:221], v13 offset:4096\n"
"ds_read_b64_tr_b16 a[222:223], v13 offset:6144\n"
"v_mfma_f32_16x16x32_bf16 v[84:87], a[196:199], a[232:235], v[84:87]\n"
"v_mfma_f32_16x16x32_bf16 v[128:131], a[200:203], a[232:235], v[128:131]\n"
"s_add_u32 s50, s46, 0\n"
"s_add_u32 m0, s49, 0\n"
"v_mfma_f32_16x16x32_bf16 v[132:135], a[204:207], a[232:235], v[132:135]\n"
"ds_read_b128 a[232:235], v15 offset:33536\n"
"buffer_load_dwordx4 v34, s[28:31], s50 offen offset:0 lds\n"
"s_waitcnt lgkmcnt(11)\n"
"v_mfma_f32_16x16x32_bf16 v[88:91], a[192:195], a[236:239], v[88:91]\n"
"buffer_load_dwordx4 v35, s[28:31], s50 offen offset:1024 lds\n"
"buffer_load_dwordx4 v36, s[28:31], s50 offen offset:2048 lds\n"
"v_mfma_f32_16x16x32_bf16 v[92:95], a[196:199], a[236:239], v[92:95]\n"
"buffer_load_dwordx4 v37, s[28:31], s50 offen offset:3072 lds\n"
"v_mfma_f32_16x16x32_bf16 v[136:139], a[200:203], a[236:239], v[136:139]\n"
"v_mfma_f32_16x16x32_bf16 v[140:143], a[204:207], a[236:239], v[140:143]\n"
"ds_read_b128 a[236:239], v15 offset:49920\n"
"v_mfma_f32_16x16x32_bf16 v[96:99], a[192:195], a[240:243], v[96:99]\n"
"v_mfma_f32_16x16x32_bf16 v[100:103], a[196:199], a[240:243], v[100:103]\n"
"v_mfma_f32_16x16x32_bf16 v[144:147], a[200:203], a[240:243], v[144:147]\n"
"v_mfma_f32_16x16x32_bf16 v[148:151], a[204:207], a[240:243], v[148:151]\n"
"ds_read_b128 a[240:243], v19 offset:768\n"
"v_mfma_f32_16x16x32_bf16 v[104:107], a[192:195], a[244:247], v[104:107]\n"
"v_mfma_f32_16x16x32_bf16 v[108:111], a[196:199], a[244:247], v[108:111]\n"
"v_mfma_f32_16x16x32_bf16 v[152:155], a[200:203], a[244:247], v[152:155]\n"
"v_mfma_f32_16x16x32_bf16 v[156:159], a[204:207], a[244:247], v[156:159]\n"
"ds_read_b128 a[244:247], v19 offset:17152\n"
"s_waitcnt lgkmcnt(3)\n"
"v_mfma_f32_16x16x32_bf16 v[64:67], a[208:211], a[224:227], v[64:67]\n"
"v_mfma_f32_16x16x32_bf16 v[68:71], a[212:215], a[224:227], v[68:71]\n"
"s_waitcnt vmcnt(8)\n"
"v_mfma_f32_16x16x32_bf16 v[112:115], a[216:219], a[224:227], v[112:115]\n"
"ds_read_b64_tr_b16 a[192:193], v10 offset:8192\n"
"ds_read_b64_tr_b16 a[194:195], v10 offset:10240\n"
"v_mfma_f32_16x16x32_bf16 v[116:119], a[220:223], a[224:227], v[116:119]\n"
"ds_read_b128 a[224:227], v16 offset:768\n"
"v_mfma_f32_16x16x32_bf16 v[72:75], a[208:211], a[228:231], v[72:75]\n"
"ds_read_b64_tr_b16 a[196:197], v11 offset:8192\n"
"ds_read_b64_tr_b16 a[198:199], v11 offset:10240\n"
"v_mfma_f32_16x16x32_bf16 v[76:79], a[212:215], a[228:231], v[76:79]\n"
"v_mfma_f32_16x16x32_bf16 v[120:123], a[216:219], a[228:231], v[120:123]\n"
"ds_read_b64_tr_b16 a[200:201], v12 offset:8192\n"
"ds_read_b64_tr_b16 a[202:203], v12 offset:10240\n"
"v_mfma_f32_16x16x32_bf16 v[124:127], a[220:223], a[228:231], v[124:127]\n"
"ds_read_b128 a[228:231], v16 offset:17152\n"
"v_mfma_f32_16x16x32_bf16 v[80:83], a[208:211], a[232:235], v[80:83]\n"
"ds_read_b64_tr_b16 a[204:205], v13 offset:8192\n"
"ds_read_b64_tr_b16 a[206:207], v13 offset:10240\n"
"v_mfma_f32_16x16x32_bf16 v[84:87], a[212:215], a[232:235], v[84:87]\n"
"v_mfma_f32_16x16x32_bf16 v[128:131], a[216:219], a[232:235], v[128:131]\n"
"s_add_u32 s50, s46, 32768\n"
"s_add_u32 m0, s49, 4096\n"
"v_mfma_f32_16x16x32_bf16 v[132:135], a[220:223], a[232:235], v[132:135]\n"
"ds_read_b128 a[232:235], v16 offset:33536\n"
"buffer_load_dwordx4 v34, s[28:31], s50 offen offset:0 lds\n"
"s_waitcnt lgkmcnt(11)\n"
"v_mfma_f32_16x16x32_bf16 v[88:91], a[208:211], a[236:239], v[88:91]\n"
"buffer_load_dwordx4 v35, s[28:31], s50 offen offset:1024 lds\n"
"buffer_load_dwordx4 v36, s[28:31], s50 offen offset:2048 lds\n"
"v_mfma_f32_16x16x32_bf16 v[92:95], a[212:215], a[236:239], v[92:95]\n"
"buffer_load_dwordx4 v37, s[28:31], s50 offen offset:3072 lds\n"
"v_mfma_f32_16x16x32_bf16 v[136:139], a[216:219], a[236:239], v[136:139]\n"
"v_mfma_f32_16x16x32_bf16 v[140:143], a[220:223], a[236:239], v[140:143]\n"
"ds_read_b128 a[236:239], v16 offset:49920\n"
"v_mfma_f32_16x16x32_bf16 v[96:99], a[208:211], a[240:243], v[96:99]\n"
"v_mfma_f32_16x16x32_bf16 v[100:103], a[212:215], a[240:243], v[100:103]\n"
"v_mfma_f32_16x16x32_bf16 v[144:147], a[216:219], a[240:243], v[144:147]\n"
"v_mfma_f32_16x16x32_bf16 v[148:151], a[220:223], a[240:243], v[148:151]\n"
"ds_read_b128 a[240:243], v20 offset:768\n"
"v_mfma_f32_16x16x32_bf16 v[104:107], a[208:211], a[244:247], v[104:107]\n"
"v_mfma_f32_16x16x32_bf16 v[108:111], a[212:215], a[244:247], v[108:111]\n"
"v_mfma_f32_16x16x32_bf16 v[152:155], a[216:219], a[244:247], v[152:155]\n"
"v_mfma_f32_16x16x32_bf16 v[156:159], a[220:223], a[244:247], v[156:159]\n"
"ds_read_b128 a[244:247], v20 offset:17152\n"
"s_waitcnt lgkmcnt(3)\n"
"v_mfma_f32_16x16x32_bf16 v[64:67], a[192:195], a[224:227], v[64:67]\n"
"v_mfma_f32_16x16x32_bf16 v[68:71], a[196:199], a[224:227], v[68:71]\n"
"s_waitcnt vmcnt(8)\n"
"v_mfma_f32_16x16x32_bf16 v[112:115], a[200:203], a[224:227], v[112:115]\n"
"ds_read_b64_tr_b16 a[208:209], v10 offset:12288\n"
"ds_read_b64_tr_b16 a[210:211], v10 offset:14336\n"
"v_mfma_f32_16x16x32_bf16 v[116:119], a[204:207], a[224:227], v[116:119]\n"
"ds_read_b128 a[224:227], v17 offset:768\n"
"v_mfma_f32_16x16x32_bf16 v[72:75], a[192:195], a[228:231], v[72:75]\n"
"ds_read_b64_tr_b16 a[212:213], v11 offset:12288\n"
"ds_read_b64_tr_b16 a[214:215], v11 offset:14336\n"
"v_mfma_f32_16x16x32_bf16 v[76:79], a[196:199], a[228:231], v[76:79]\n"
"v_mfma_f32_16x16x32_bf16 v[120:123], a[200:203], a[228:231], v[120:123]\n"
"ds_read_b64_tr_b16 a[216:217], v12 offset:12288\n"
"ds_read_b64_tr_b16 a[218:219], v12 offset:14336\n"
"v_mfma_f32_16x16x32_bf16 v[124:127], a[204:207], a[228:231], v[124:127]\n"
"ds_read_b128 a[228:231], v17 offset:17152\n"
"v_mfma_f32_16x16x32_bf16 v[80:83], a[192:195], a[232:235], v[80:83]\n"
"ds_read_b64_tr_b16 a[220:221], v13 offset:12288\n"
"ds_read_b64_tr_b16 a[222:223], v13 offset:14336\n"
"v_mfma_f32_16x16x32_bf16 v[84:87], a[196:199], a[232:235], v[84:87]\n"
"v_mfma_f32_16x16x32_bf16 v[128:131], a[200:203], a[232:235], v[128:131]\n"
"s_add_u32 s50, s46, 65536\n"
"s_add_u32 m0, s49, 8192\n"
"v_mfma_f32_16x16x32_bf16 v[132:135], a[204:207], a[232:235], v[132:135]\n"
"ds_read_b128 a[232:235], v17 offset:33536\n"
"buffer_load_dwordx4 v34, s[28:31], s50 offen offset:0 lds\n"
"s_waitcnt lgkmcnt(11)\n"
"v_mfma_f32_16x16x32_bf16 v[88:91], a[192:195], a[236:239], v[88:91]\n"
"buffer_load_dwordx4 v35, s[28:31], s50 offen offset:1024 lds\n"
"buffer_load_dwordx4 v36, s[28:31], s50 offen offset:2048 lds\n"
"v_mfma_f32_16x16x32_bf16 v[92:95], a[196:199], a[236:239], v[92:95]\n"
"buffer_load_dwordx4 v37, s[28:31], s50 offen offset:3072 lds\n"
"v_mfma_f32_16x16x32_bf16 v[136:139], a[200:203], a[236:239], v[136:139]\n"
"v_mfma_f32_16x16x32_bf16 v[140:143], a[204:207], a[236:239], v[140:143]\n"
"ds_read_b128 a[236:239], v17 offset:49920\n"
"v_mfma_f32_16x16x32_bf16 v[96:99], a[192:195], a[240:243], v[96:99]\n"
"v_mfma_f32_16x16x32_bf16 v[100:103], a[196:199], a[240:243], v[100:103]\n"
"v_mfma_f32_16x16x32_bf16 v[144:147], a[200:203], a[240:243], v[144:147]\n"
"v_mfma_f32_16x16x32_bf16 v[148:151], a[204:207], a[240:243], v[148:151]\n"
"ds_read_b128 a[240:243], v21 offset:768\n"
"v_mfma_f32_16x16x32_bf16 v[104:107], a[192:195], a[244:247], v[104:107]\n"
"v_mfma_f32_16x16x32_bf16 v[108:111], a[196:199], a[244:247], v[108:111]\n"
"v_mfma_f32_16x16x32_bf16 v[152:155], a[200:203], a[244:247], v[152:155]\n"
"v_mfma_f32_16x16x32_bf16 v[156:159], a[204:207], a[244:247], v[156:159]\n"
"ds_read_b128 a[244:247], v21 offset:17152\n"
"s_waitcnt lgkmcnt(3)\n"
"v_mfma_f32_16x16x32_bf16 v[64:67], a[208:211], a[224:227], v[64:67]\n"
"v_mfma_f32_16x16x32_bf16 v[68:71], a[212:215], a[224:227], v[68:71]\n"
"s_waitcnt vmcnt(8)\n"
"v_mfma_f32_16x16x32_bf16 v[112:115], a[216:219], a[224:227], v[112:115]\n"
"ds_read_b64_tr_b16 a[192:193], v10 offset:0\n"
"ds_read_b64_tr_b16 a[194:195], v10 offset:2048\n"
"v_mfma_f32_16x16x32_bf16 v[116:119], a[220:223], a[224:227], v[116:119]\n"
"ds_read_b128 a[224:227], v14 offset:0\n"
"v_mfma_f32_16x16x32_bf16 v[72:75], a[208:211], a[228:231], v[72:75]\n"
"ds_read_b64_tr_b16 a[196:197], v11 offset:0\n"
"ds_read_b64_tr_b16 a[198:199], v11 offset:2048\n"
"v_mfma_f32_16x16x32_bf16 v[76:79], a[212:215], a[228:231], v[76:79]\n"
"v_mfma_f32_16x16x32_bf16 v[120:123], a[216:219], a[228:231], v[120:123]\n"
"ds_read_b64_tr_b16 a[200:201], v12 offset:0\n"
"ds_read_b64_tr_b16 a[202:203], v12 offset:2048\n"
"v_mfma_f32_16x16x32_bf16 v[124:127], a[220:223], a[228:231], v[124:127]\n"
"ds_read_b128 a[228:231], v14 offset:16384\n"
"v_mfma_f32_16x16x32_bf16 v[80:83], a[208:211], a[232:235], v[80:83]\n"
"ds_read_b64_tr_b16 a[204:205], v13 offset:0\n"
"ds_read_b64_tr_b16 a[206:207], v13 offset:2048\n"
"v_mfma_f32_16x16x32_bf16 v[84:87], a[212:215], a[232:235], v[84:87]\n"
"v_mfma_f32_16x16x32_bf16 v[128:131], a[216:219], a[232:235], v[128:131]\n"
"s_add_u32 s50, s46, 98304\n"
"s_add_u32 m0, s49, 12288\n"
"v_mfma_f32_16x16x32_bf16 v[132:135], a[220:223], a[232:235], v[132:135]\n"
"ds_read_b128 a[232:235], v14 offset:32768\n"
"buffer_load_dwordx4 v34, s[28:31], s50 offen offset:0 lds\n"
"s_waitcnt lgkmcnt(11)\n"
"v_mfma_f32_16x16x32_bf16 v[88:91], a[208:211], a[236:239], v[88:91]\n"
"buffer_load_dwordx4 v35, s[28:31], s50 offen offset:1024 lds\n"
"buffer_load_dwordx4 v36, s[28:31], s50 offen offset:2048 lds\n"
"v_mfma_f32_16x16x32_bf16 v[92:95], a[212:215], a[236:239], v[92:95]\n"
"buffer_load_dwordx4 v37, s[28:31], s50 offen offset:3072 lds\n"
"v_mfma_f32_16x16x32_bf16 v[136:139], a[216:219], a[236:239], v[136:139]\n"
"v_mfma_f32_16x16x32_bf16 v[140:143], a[220:223], a[236:239], v[140:143]\n"
"ds_read_b128 a[236:239], v14 offset:49152\n"
"v_mfma_f32_16x16x32_bf16 v[96:99], a[208:211], a[240:243], v[96:99]\n"
"v_mfma_f32_16x16x32_bf16 v[100:103], a[212:215], a[240:243], v[100:103]\n"
"v_mfma_f32_16x16x32_bf16 v[144:147], a[216:219], a[240:243], v[144:147]\n"
"v_mfma_f32_16x16x32_bf16 v[148:151], a[220:223], a[240:243], v[148:151]\n"
"ds_read_b128 a[240:243], v18 offset:0\n"
"v_mfma_f32_16x16x32_bf16 v[104:107], a[208:211], a[244:247], v[104:107]\n"
"v_mfma_f32_16x16x32_bf16 v[108:111], a[212:215], a[244:247], v[108:111]\n"
"v_mfma_f32_16x16x32_bf16 v[152:155], a[216:219], a[244:247], v[152:155]\n"
"v_mfma_f32_16x16x32_bf16 v[156:159], a[220:223], a[244:247], v[156:159]\n"
"ds_read_b128 a[244:247], v18 offset:16384\n"
"s_waitcnt lgkmcnt(3)\n"
"v_mfma_f32_16x16x32_bf16 v[160:163], a[192:195], a[224:227], 0\n"
"v_mfma_f32_16x16x32_bf16 v[164:167], a[196:199], a[224:227], 0\n"
"s_waitcnt vmcnt(8)\n"
"v_mfma_f32_16x16x32_bf16 v[208:211], a[200:203], a[224:227], 0\n"
"ds_read_b64_tr_b16 a[208:209], v10 offset:4096\n"
"ds_read_b64_tr_b16 a[210:211], v10 offset:6144\n"
"v_mfma_f32_16x16x32_bf16 v[212:215], a[204:207], a[224:227], 0\n"
"ds_read_b128 a[224:227], v15 offset:0\n"
"v_mfma_f32_16x16x32_bf16 v[168:171], a[192:195], a[228:231], 0\n"
"ds_read_b64_tr_b16 a[212:213], v11 offset:4096\n"
"ds_read_b64_tr_b16 a[214:215], v11 offset:6144\n"
"v_mfma_f32_16x16x32_bf16 v[172:175], a[196:199], a[228:231], 0\n"
"v_mfma_f32_16x16x32_bf16 v[216:219], a[200:203], a[228:231], 0\n"
"ds_read_b64_tr_b16 a[216:217], v12 offset:4096\n"
"ds_read_b64_tr_b16 a[218:219], v12 offset:6144\n"
"v_mfma_f32_16x16x32_bf16 v[220:223], a[204:207], a[228:231], 0\n"
"ds_read_b128 a[228:231], v15 offset:16384\n"
"v_mfma_f32_16x16x32_bf16 v[176:179], a[192:195], a[232:235], 0\n"
"ds_read_b64_tr_b16 a[220:221], v13 offset:4096\n"
"ds_read_b64_tr_b16 a[222:223], v13 offset:6144\n"
"v_mfma_f32_16x16x32_bf16 v[180:183], a[196:199], a[232:235], 0\n"
"v_mfma_f32_16x16x32_bf16 v[224:227], a[200:203], a[232:235], 0\n"
"s_add_u32 s50, s46, 131072\n"
"s_add_u32 m0, s49, 0\n"
"v_mfma_f32_16x16x32_bf16 v[228:231], a[204:207], a[232:235], 0\n"
"ds_read_b128 a[232:235], v15 offset:32768\n"
"buffer_load_dwordx4 v34, s[28:31], s50 offen offset:0 lds\n"
"s_waitcnt lgkmcnt(11)\n"
"v_mfma_f32_16x16x32_bf16 v[184:187], a[192:195], a[236:239], 0\n"
"buffer_load_dwordx4 v35, s[28:31], s50 offen offset:1024 lds\n"
"buffer_load_dwordx4 v36, s[28:31], s50 offen offset:2048 lds\n"
"v_mfma_f32_16x16x32_bf16 v[188:191], a[196:199], a[236:239], 0\n"
"buffer_load_dwordx4 v37, s[28:31], s50 offen offset:3072 lds\n"
"v_mfma_f32_16x16x32_bf16 v[232:235], a[200:203], a[236:239], 0\n"
"v_mfma_f32_16x16x32_bf16 v[236:239], a[204:207], a[236:239], 0\n"
"ds_read_b128 a[236:239], v15 offset:49152\n"
"v_mfma_f32_16x16x32_bf16 v[192:195], a[192:195], a[240:243], 0\n"
"v_mfma_f32_16x16x32_bf16 v[196:199], a[196:199], a[240:243], 0\n"
"v_mfma_f32_16x16x32_bf16 v[240:243], a[200:203], a[240:243], 0\n"
"v_mfma_f32_16x16x32_bf16 v[244:247], a[204:207], a[240:243], 0\n"
"ds_read_b128 a[240:243], v19 offset:0\n"
"v_mfma_f32_16x16x32_bf16 v[200:203], a[192:195], a[244:247], 0\n"
"v_mfma_f32_16x16x32_bf16 v[204:207], a[196:199], a[244:247], 0\n"
"v_mfma_f32_16x16x32_bf16 v[248:251], a[200:203], a[244:247], 0\n"
"v_mfma_f32_16x16x32_bf16 v[252:255], a[204:207], a[244:247], 0\n"
"ds_read_b128 a[244:247], v19 offset:16384\n"
"s_waitcnt lgkmcnt(3)\n"
"v_mfma_f32_16x16x32_bf16 v[160:163], a[208:211], a[224:227], v[160:163]\n"
"s_mul_i32 s53, s43, 22\n"
"v_mfma_f32_16x16x32_bf16 v[164:167], a[212:215], a[224:227], v[164:167]\n"
"s_waitcnt vmcnt(8)\n"
"v_mfma_f32_16x16x32_bf16 v[208:211], a[216:219], a[224:227], v[208:211]\n"
"ds_read_b64_tr_b16 a[192:193], v10 offset:8192\n"
"ds_read_b64_tr_b16 a[194:195], v10 offset:10240\n"
"v_mfma_f32_16x16x32_bf16 v[212:215], a[220:223], a[224:227], v[212:215]\n"
"ds_read_b128 a[224:227], v16 offset:0\n"
"v_mfma_f32_16x16x32_bf16 v[168:171], a[208:211], a[228:231], v[168:171]\n"
"ds_read_b64_tr_b16 a[196:197], v11 offset:8192\n"
"ds_read_b64_tr_b16 a[198:199], v11 offset:10240\n"
"v_mfma_f32_16x16x32_bf16 v[172:175], a[212:215], a[228:231], v[172:175]\n"
"v_mfma_f32_16x16x32_bf16 v[216:219], a[216:219], a[228:231], v[216:219]\n"
"ds_read_b64_tr_b16 a[200:201], v12 offset:8192\n"
"ds_read_b64_tr_b16 a[202:203], v12 offset:10240\n"
"v_mfma_f32_16x16x32_bf16 v[220:223], a[220:223], a[228:231], v[220:223]\n"
"ds_read_b128 a[228:231], v16 offset:16384\n"
"v_mfma_f32_16x16x32_bf16 v[176:179], a[208:211], a[232:235], v[176:179]\n"
"ds_read_b64_tr_b16 a[204:205], v13 offset:8192\n"
"ds_read_b64_tr_b16 a[206:207], v13 offset:10240\n"
"v_mfma_f32_16x16x32_bf16 v[180:183], a[212:215], a[232:235], v[180:183]\n"
"s_add_u32 s53, s53, s44\n"
"v_mfma_f32_16x16x32_bf16 v[224:227], a[216:219], a[232:235], v[224:227]\n"
"s_add_u32 s50, s46, 163840\n"
"s_add_u32 m0, s49, 4096\n"
"v_mfma_f32_16x16x32_bf16 v[228:231], a[220:223], a[232:235], v[228:231]\n"
"ds_read_b128 a[232:235], v16 offset:32768\n"
"buffer_load_dwordx4 v34, s[28:31], s50 offen offset:0 lds\n"
"s_waitcnt lgkmcnt(11)\n"
"v_mfma_f32_16x16x32_bf16 v[184:187], a[208:211], a[236:239], v[184:187]\n"
"buffer_load_dwordx4 v35, s[28:31], s50 offen offset:1024 lds\n"
"buffer_load_dwordx4 v36, s[28:31], s50 offen offset:2048 lds\n"
"v_mfma_f32_16x16x32_bf16 v[188:191], a[212:215], a[236:239], v[188:191]\n"
"buffer_load_dwordx4 v37, s[28:31], s50 offen offset:3072 lds\n"
"v_mfma_f32_16x16x32_bf16 v[232:235], a[216:219], a[236:239], v[232:235]\n"
"s_lshl_b32 s53, s53, 2\n"
"v_mfma_f32_16x16x32_bf16 v[236:239], a[220:223], a[236:239], v[236:239]\n"
"ds_read_b128 a[236:239], v16 offset:49152\n"
"v_mfma_f32_16x16x32_bf16 v[192:195], a[208:211], a[240:243], v[192:195]\n"
"s_add_u32 s53, s53, s42\n"
"v_mfma_f32_16x16x32_bf16 v[196:199], a[212:215], a[240:243], v[196:199]\n"
"v_mfma_f32_16x16x32_bf16 v[240:243], a[216:219], a[240:243], v[240:243]\n"
"s_mul_i32 s53, s53, 24576\n"
"v_mfma_f32_16x16x32_bf16 v[244:247], a[220:223], a[240:243], v[244:247]\n"
"ds_read_b128 a[240:243], v20 offset:0\n"
"v_mfma_f32_16x16x32_bf16 v[200:203], a[208:211], a[244:247], v[200:203]\n"
"s_add_u32 s40, s80, s53\n"
"v_mfma_f32_16x16x32_bf16 v[204:207], a[212:215], a[244:247], v[204:207]\n"
"v_mfma_f32_16x16x32_bf16 v[248:251], a[216:219], a[244:247], v[248:251]\n"
"s_addc_u32 s41, s81, 0\n"
"v_mfma_f32_16x16x32_bf16 v[252:255], a[220:223], a[244:247], v[252:255]\n"
"ds_read_b128 a[244:247], v20 offset:16384\n"
"s_waitcnt lgkmcnt(3)\n"
"v_mfma_f32_16x16x32_bf16 v[160:163], a[192:195], a[224:227], v[160:163]\n"
"v_lshlrev_b32 v2, 4, v1\n"
"v_mfma_f32_16x16x32_bf16 v[164:167], a[196:199], a[224:227], v[164:167]\n"
"s_waitcnt vmcnt(8)\n"
"v_mfma_f32_16x16x32_bf16 v[208:211], a[200:203], a[224:227], v[208:211]\n"
"ds_read_b64_tr_b16 a[208:209], v10 offset:12288\n"
"ds_read_b64_tr_b16 a[210:211], v10 offset:14336\n"
"v_mfma_f32_16x16x32_bf16 v[212:215], a[204:207], a[224:227], v[212:215]\n"
"ds_read_b128 a[224:227], v17 offset:0\n"
"v_mfma_f32_16x16x32_bf16 v[168:171], a[192:195], a[228:231], v[168:171]\n"
"ds_read_b64_tr_b16 a[212:213], v11 offset:12288\n"
"ds_read_b64_tr_b16 a[214:215], v11 offset:14336\n"
"v_mfma_f32_16x16x32_bf16 v[172:175], a[196:199], a[228:231], v[172:175]\n"
"v_mfma_f32_16x16x32_bf16 v[216:219], a[200:203], a[228:231], v[216:219]\n"
"ds_read_b64_tr_b16 a[216:217], v12 offset:12288\n"
"ds_read_b64_tr_b16 a[218:219], v12 offset:14336\n"
"v_mfma_f32_16x16x32_bf16 v[220:223], a[204:207], a[228:231], v[220:223]\n"
"ds_read_b128 a[228:231], v17 offset:16384\n"
"v_mfma_f32_16x16x32_bf16 v[176:179], a[192:195], a[232:235], v[176:179]\n"
"ds_read_b64_tr_b16 a[220:221], v13 offset:12288\n"
"ds_read_b64_tr_b16 a[222:223], v13 offset:14336\n"
"v_mfma_f32_16x16x32_bf16 v[180:183], a[196:199], a[232:235], v[180:183]\n"
"v_add_u32 v3, 4096, v2\n"
"v_mfma_f32_16x16x32_bf16 v[224:227], a[200:203], a[232:235], v[224:227]\n"
"s_add_u32 s50, s46, 196608\n"
"s_add_u32 m0, s49, 8192\n"
"v_mfma_f32_16x16x32_bf16 v[228:231], a[204:207], a[232:235], v[228:231]\n"
"ds_read_b128 a[232:235], v17 offset:32768\n"
"buffer_load_dwordx4 v34, s[28:31], s50 offen offset:0 lds\n"
"s_waitcnt lgkmcnt(11)\n"
"v_mfma_f32_16x16x32_bf16 v[184:187], a[192:195], a[236:239], v[184:187]\n"
"buffer_load_dwordx4 v35, s[28:31], s50 offen offset:1024 lds\n"
"buffer_load_dwordx4 v36, s[28:31], s50 offen offset:2048 lds\n"
"v_mfma_f32_16x16x32_bf16 v[188:191], a[196:199], a[236:239], v[188:191]\n"
"buffer_load_dwordx4 v37, s[28:31], s50 offen offset:3072 lds\n"
"v_mfma_f32_16x16x32_bf16 v[232:235], a[200:203], a[236:239], v[232:235]\n"
"v_add_u32 v4, 8192, v2\n"
"v_mfma_f32_16x16x32_bf16 v[236:239], a[204:207], a[236:239], v[236:239]\n"
"ds_read_b128 a[236:239], v17 offset:49152\n"
"v_mfma_f32_16x16x32_bf16 v[192:195], a[192:195], a[240:243], v[192:195]\n"
"v_add_u32 v5, 12288, v2\n"
"v_mfma_f32_16x16x32_bf16 v[196:199], a[196:199], a[240:243], v[196:199]\n"
"v_mfma_f32_16x16x32_bf16 v[240:243], a[200:203], a[240:243], v[240:243]\n"
"v_add_u32 v6, 16384, v2\n"
"v_mfma_f32_16x16x32_bf16 v[244:247], a[204:207], a[240:243], v[244:247]\n"
"ds_read_b128 a[240:243], v21 offset:0\n"
"v_mfma_f32_16x16x32_bf16 v[200:203], a[192:195], a[244:247], v[200:203]\n"
"v_add_u32 v7, 20480, v2\n"
"v_mfma_f32_16x16x32_bf16 v[204:207], a[196:199], a[244:247], v[204:207]\n"
"v_mfma_f32_16x16x32_bf16 v[248:251], a[200:203], a[244:247], v[248:251]\n"
"v_cvt_pk_bf16_f32 v64, v64, v65\n"
"v_mfma_f32_16x16x32_bf16 v[252:255], a[204:207], a[244:247], v[252:255]\n"
"ds_read_b128 a[244:247], v21 offset:16384\n"
"s_waitcnt lgkmcnt(3)\n"
"v_mfma_f32_16x16x32_bf16 v[160:163], a[208:211], a[224:227], v[160:163]\n"
"v_cvt_pk_bf16_f32 v65, v66, v67\n"
"v_mfma_f32_16x16x32_bf16 v[164:167], a[212:215], a[224:227], v[164:167]\n"
"s_waitcnt vmcnt(8)\n"
"v_mfma_f32_16x16x32_bf16 v[208:211], a[216:219], a[224:227], v[208:211]\n"
"ds_read_b64_tr_b16 a[192:193], v10 offset:0\n"
"ds_read_b64_tr_b16 a[194:195], v10 offset:2048\n"
"v_mfma_f32_16x16x32_bf16 v[212:215], a[220:223], a[224:227], v[212:215]\n"
"ds_read_b128 a[224:227], v14 offset:256\n"
"v_mfma_f32_16x16x32_bf16 v[168:171], a[208:211], a[228:231], v[168:171]\n"
"ds_read_b64_tr_b16 a[196:197], v11 offset:0\n"
"ds_read_b64_tr_b16 a[198:199], v11 offset:2048\n"
"v_mfma_f32_16x16x32_bf16 v[172:175], a[212:215], a[228:231], v[172:175]\n"
"v_mfma_f32_16x16x32_bf16 v[216:219], a[216:219], a[228:231], v[216:219]\n"
"ds_read_b64_tr_b16 a[200:201], v12 offset:0\n"
"ds_read_b64_tr_b16 a[202:203], v12 offset:2048\n"
"v_mfma_f32_16x16x32_bf16 v[220:223], a[220:223], a[228:231], v[220:223]\n"
"ds_read_b128 a[228:231], v14 offset:16640\n"
"v_mfma_f32_16x16x32_bf16 v[176:179], a[208:211], a[232:235], v[176:179]\n"
"ds_read_b64_tr_b16 a[204:205], v13 offset:0\n"
"ds_read_b64_tr_b16 a[206:207], v13 offset:2048\n"
"v_mfma_f32_16x16x32_bf16 v[180:183], a[212:215], a[232:235], v[180:183]\n"
"v_cvt_pk_bf16_f32 v66, v68, v69\n"
"v_mfma_f32_16x16x32_bf16 v[224:227], a[216:219], a[232:235], v[224:227]\n"
"s_add_u32 s50, s46, 229376\n"
"s_add_u32 m0, s49, 12288\n"
"v_mfma_f32_16x16x32_bf16 v[228:231], a[220:223], a[232:235], v[228:231]\n"
"ds_read_b128 a[232:235], v14 offset:33024\n"
"buffer_load_dwordx4 v34, s[28:31], s50 offen offset:0 lds\n"
"s_waitcnt lgkmcnt(11)\n"
"v_mfma_f32_16x16x32_bf16 v[184:187], a[208:211], a[236:239], v[184:187]\n"
"buffer_load_dwordx4 v35, s[28:31], s50 offen offset:1024 lds\n"
"buffer_load_dwordx4 v36, s[28:31], s50 offen offset:2048 lds\n"
"v_mfma_f32_16x16x32_bf16 v[188:191], a[212:215], a[236:239], v[188:191]\n"
"buffer_load_dwordx4 v37, s[28:31], s50 offen offset:3072 lds\n"
"v_mfma_f32_16x16x32_bf16 v[232:235], a[216:219], a[236:239], v[232:235]\n"
"v_cvt_pk_bf16_f32 v67, v70, v71\n"
"v_mfma_f32_16x16x32_bf16 v[236:239], a[220:223], a[236:239], v[236:239]\n"
"ds_read_b128 a[236:239], v14 offset:49408\n"
"v_mfma_f32_16x16x32_bf16 v[192:195], a[208:211], a[240:243], v[192:195]\n"
"global_store_dwordx4 v2, v[64:67], s[40:41] offset:0 sc1\n"
"v_mfma_f32_16x16x32_bf16 v[196:199], a[212:215], a[240:243], v[196:199]\n"
"v_mfma_f32_16x16x32_bf16 v[240:243], a[216:219], a[240:243], v[240:243]\n"
"v_cvt_pk_bf16_f32 v72, v72, v73\n"
"v_mfma_f32_16x16x32_bf16 v[244:247], a[220:223], a[240:243], v[244:247]\n"
"ds_read_b128 a[240:243], v18 offset:256\n"
"v_mfma_f32_16x16x32_bf16 v[200:203], a[208:211], a[244:247], v[200:203]\n"
"v_cvt_pk_bf16_f32 v73, v74, v75\n"
"v_mfma_f32_16x16x32_bf16 v[204:207], a[212:215], a[244:247], v[204:207]\n"
"v_mfma_f32_16x16x32_bf16 v[248:251], a[216:219], a[244:247], v[248:251]\n"
"v_cvt_pk_bf16_f32 v74, v76, v77\n"
"v_mfma_f32_16x16x32_bf16 v[252:255], a[220:223], a[244:247], v[252:255]\n"
"ds_read_b128 a[244:247], v18 offset:16640\n"
"s_waitcnt lgkmcnt(3)\n"
"v_mfma_f32_16x16x32_bf16 v[160:163], a[192:195], a[224:227], v[160:163]\n"
"v_cvt_pk_bf16_f32 v75, v78, v79\n"
"v_mfma_f32_16x16x32_bf16 v[164:167], a[196:199], a[224:227], v[164:167]\n"
"s_waitcnt vmcnt(9)\n"
"v_mfma_f32_16x16x32_bf16 v[208:211], a[200:203], a[224:227], v[208:211]\n"
"ds_read_b64_tr_b16 a[208:209], v10 offset:4096\n"
"ds_read_b64_tr_b16 a[210:211], v10 offset:6144\n"
"v_mfma_f32_16x16x32_bf16 v[212:215], a[204:207], a[224:227], v[212:215]\n"
"ds_read_b128 a[224:227], v15 offset:256\n"
"v_mfma_f32_16x16x32_bf16 v[168:171], a[192:195], a[228:231], v[168:171]\n"
"ds_read_b64_tr_b16 a[212:213], v11 offset:4096\n"
"ds_read_b64_tr_b16 a[214:215], v11 offset:6144\n"
"v_mfma_f32_16x16x32_bf16 v[172:175], a[196:199], a[228:231], v[172:175]\n"
"v_mfma_f32_16x16x32_bf16 v[216:219], a[200:203], a[228:231], v[216:219]\n"
"ds_read_b64_tr_b16 a[216:217], v12 offset:4096\n"
"ds_read_b64_tr_b16 a[218:219], v12 offset:6144\n"
"v_mfma_f32_16x16x32_bf16 v[220:223], a[204:207], a[228:231], v[220:223]\n"
"ds_read_b128 a[228:231], v15 offset:16640\n"
"v_mfma_f32_16x16x32_bf16 v[176:179], a[192:195], a[232:235], v[176:179]\n"
"ds_read_b64_tr_b16 a[220:221], v13 offset:4096\n"
"ds_read_b64_tr_b16 a[222:223], v13 offset:6144\n"
"v_mfma_f32_16x16x32_bf16 v[180:183], a[196:199], a[232:235], v[180:183]\n"
"global_store_dwordx4 v2, v[72:75], s[40:41] offset:1024 sc1\n"
"v_mfma_f32_16x16x32_bf16 v[224:227], a[200:203], a[232:235], v[224:227]\n"
"s_add_u32 s50, s46, 262144\n"
"s_add_u32 m0, s49, 0\n"
"v_mfma_f32_16x16x32_bf16 v[228:231], a[204:207], a[232:235], v[228:231]\n"
"ds_read_b128 a[232:235], v15 offset:33024\n"
"buffer_load_dwordx4 v34, s[28:31], s50 offen offset:0 lds\n"
"s_waitcnt lgkmcnt(11)\n"
"v_mfma_f32_16x16x32_bf16 v[184:187], a[192:195], a[236:239], v[184:187]\n"
"buffer_load_dwordx4 v35, s[28:31], s50 offen offset:1024 lds\n"
"buffer_load_dwordx4 v36, s[28:31], s50 offen offset:2048 lds\n"
"v_mfma_f32_16x16x32_bf16 v[188:191], a[196:199], a[236:239], v[188:191]\n"
"buffer_load_dwordx4 v37, s[28:31], s50 offen offset:3072 lds\n"
"v_mfma_f32_16x16x32_bf16 v[232:235], a[200:203], a[236:239], v[232:235]\n"
"v_cvt_pk_bf16_f32 v80, v80, v81\n"
"v_mfma_f32_16x16x32_bf16 v[236:239], a[204:207], a[236:239], v[236:239]\n"
"ds_read_b128 a[236:239], v15 offset:49408\n"
"v_mfma_f32_16x16x32_bf16 v[192:195], a[192:195], a[240:243], v[192:195]\n"
"v_cvt_pk_bf16_f32 v81, v82, v83\n"
"v_mfma_f32_16x16x32_bf16 v[196:199], a[196:199], a[240:243], v[196:199]\n"
"v_mfma_f32_16x16x32_bf16 v[240:243], a[200:203], a[240:243], v[240:243]\n"
"v_cvt_pk_bf16_f32 v82, v84, v85\n"
"v_mfma_f32_16x16x32_bf16 v[244:247], a[204:207], a[240:243], v[244:247]\n"
"ds_read_b128 a[240:243], v19 offset:256\n"
"v_mfma_f32_16x16x32_bf16 v[200:203], a[192:195], a[244:247], v[200:203]\n"
"v_cvt_pk_bf16_f32 v83, v86, v87\n"
"v_mfma_f32_16x16x32_bf16 v[204:207], a[196:199], a[244:247], v[204:207]\n"
"v_mfma_f32_16x16x32_bf16 v[248:251], a[200:203], a[244:247], v[248:251]\n"
"global_store_dwordx4 v2, v[80:83], s[40:41] offset:2048 sc1\n"
"v_mfma_f32_16x16x32_bf16 v[252:255], a[204:207], a[244:247], v[252:255]\n"
"ds_read_b128 a[244:247], v19 offset:16640\n"
"s_waitcnt lgkmcnt(3)\n"
"v_mfma_f32_16x16x32_bf16 v[160:163], a[208:211], a[224:227], v[160:163]\n"
"v_cvt_pk_bf16_f32 v88, v88, v89\n"
"v_mfma_f32_16x16x32_bf16 v[164:167], a[212:215], a[224:227], v[164:167]\n"
"s_waitcnt vmcnt(11)\n"
"v_mfma_f32_16x16x32_bf16 v[208:211], a[216:219], a[224:227], v[208:211]\n"
"ds_read_b64_tr_b16 a[192:193], v10 offset:8192\n"
"ds_read_b64_tr_b16 a[194:195], v10 offset:10240\n"
"v_mfma_f32_16x16x32_bf16 v[212:215], a[220:223], a[224:227], v[212:215]\n"
"ds_read_b128 a[224:227], v16 offset:256\n"
"v_mfma_f32_16x16x32_bf16 v[168:171], a[208:211], a[228:231], v[168:171]\n"
"ds_read_b64_tr_b16 a[196:197], v11 offset:8192\n"
"ds_read_b64_tr_b16 a[198:199], v11 offset:10240\n"
"v_mfma_f32_16x16x32_bf16 v[172:175], a[212:215], a[228:231], v[172:175]\n"
"v_mfma_f32_16x16x32_bf16 v[216:219], a[216:219], a[228:231], v[216:219]\n"
"ds_read_b64_tr_b16 a[200:201], v12 offset:8192\n"
"ds_read_b64_tr_b16 a[202:203], v12 offset:10240\n"
"v_mfma_f32_16x16x32_bf16 v[220:223], a[220:223], a[228:231], v[220:223]\n"
"ds_read_b128 a[228:231], v16 offset:16640\n"
"v_mfma_f32_16x16x32_bf16 v[176:179], a[208:211], a[232:235], v[176:179]\n"
"ds_read_b64_tr_b16 a[204:205], v13 offset:8192\n"
"ds_read_b64_tr_b16 a[206:207], v13 offset:10240\n"
"v_mfma_f32_16x16x32_bf16 v[180:183], a[212:215], a[232:235], v[180:183]\n"
"v_cvt_pk_bf16_f32 v89, v90, v91\n"
"v_mfma_f32_16x16x32_bf16 v[224:227], a[216:219], a[232:235], v[224:227]\n"
"s_add_u32 s50, s46, 294912\n"
"s_add_u32 m0, s49, 4096\n"
"v_mfma_f32_16x16x32_bf16 v[228:231], a[220:223], a[232:235], v[228:231]\n"
"ds_read_b128 a[232:235], v16 offset:33024\n"
"buffer_load_dwordx4 v34, s[28:31], s50 offen offset:0 lds\n"
"s_waitcnt lgkmcnt(11)\n"
"v_mfma_f32_16x16x32_bf16 v[184:187], a[208:211], a[236:239], v[184:187]\n"
"buffer_load_dwordx4 v35, s[28:31], s50 offen offset:1024 lds\n"
"buffer_load_dwordx4 v36, s[28:31], s50 offen offset:2048 lds\n"
"v_mfma_f32_16x16x32_bf16 v[188:191], a[212:215], a[236:239], v[188:191]\n"
"buffer_load_dwordx4 v37, s[28:31], s50 offen offset:3072 lds\n"
"v_mfma_f32_16x16x32_bf16 v[232:235], a[216:219], a[236:239], v[232:235]\n"
"v_cvt_pk_bf16_f32 v90, v92, v93\n"
"v_mfma_f32_16x16x32_bf16 v[236:239], a[220:223], a[236:239], v[236:239]\n"
"ds_read_b128 a[236:239], v16 offset:49408\n"
"v_mfma_f32_16x16x32_bf16 v[192:195], a[208:211], a[240:243], v[192:195]\n"
"v_cvt_pk_bf16_f32 v91, v94, v95\n"
"v_mfma_f32_16x16x32_bf16 v[196:199], a[212:215], a[240:243], v[196:199]\n"
"v_mfma_f32_16x16x32_bf16 v[240:243], a[216:219], a[240:243], v[240:243]\n"
"global_store_dwordx4 v2, v[88:91], s[40:41] offset:3072 sc1\n"
"v_mfma_f32_16x16x32_bf16 v[244:247], a[220:223], a[240:243], v[244:247]\n"
"ds_read_b128 a[240:243], v20 offset:256\n"
"v_mfma_f32_16x16x32_bf16 v[200:203], a[208:211], a[244:247], v[200:203]\n"
"v_cvt_pk_bf16_f32 v96, v96, v97\n"
"v_mfma_f32_16x16x32_bf16 v[204:207], a[212:215], a[244:247], v[204:207]\n"
"v_mfma_f32_16x16x32_bf16 v[248:251], a[216:219], a[244:247], v[248:251]\n"
"v_cvt_pk_bf16_f32 v97, v98, v99\n"
"v_mfma_f32_16x16x32_bf16 v[252:255], a[220:223], a[244:247], v[252:255]\n"
"ds_read_b128 a[244:247], v20 offset:16640\n"
"s_waitcnt lgkmcnt(3)\n"
"v_mfma_f32_16x16x32_bf16 v[160:163], a[192:195], a[224:227], v[160:163]\n"
"v_cvt_pk_bf16_f32 v98, v100, v101\n"
"v_mfma_f32_16x16x32_bf16 v[164:167], a[196:199], a[224:227], v[164:167]\n"
"s_waitcnt vmcnt(11)\n"
"v_mfma_f32_16x16x32_bf16 v[208:211], a[200:203], a[224:227], v[208:211]\n"
"ds_read_b64_tr_b16 a[208:209], v10 offset:12288\n"
"ds_read_b64_tr_b16 a[210:211], v10 offset:14336\n"
"v_mfma_f32_16x16x32_bf16 v[212:215], a[204:207], a[224:227], v[212:215]\n"
"ds_read_b128 a[224:227], v17 offset:256\n"
"v_mfma_f32_16x16x32_bf16 v[168:171], a[192:195], a[228:231], v[168:171]\n"
"ds_read_b64_tr_b16 a[212:213], v11 offset:12288\n"
"ds_read_b64_tr_b16 a[214:215], v11 offset:14336\n"
"v_mfma_f32_16x16x32_bf16 v[172:175], a[196:199], a[228:231], v[172:175]\n"
"v_mfma_f32_16x16x32_bf16 v[216:219], a[200:203], a[228:231], v[216:219]\n"
"ds_read_b64_tr_b16 a[216:217], v12 offset:12288\n"
"ds_read_b64_tr_b16 a[218:219], v12 offset:14336\n"
"v_mfma_f32_16x16x32_bf16 v[220:223], a[204:207], a[228:231], v[220:223]\n"
"ds_read_b128 a[228:231], v17 offset:16640\n"
"v_mfma_f32_16x16x32_bf16 v[176:179], a[192:195], a[232:235], v[176:179]\n"
"ds_read_b64_tr_b16 a[220:221], v13 offset:12288\n"
"ds_read_b64_tr_b16 a[222:223], v13 offset:14336\n"
"v_mfma_f32_16x16x32_bf16 v[180:183], a[196:199], a[232:235], v[180:183]\n"
"v_cvt_pk_bf16_f32 v99, v102, v103\n"
"v_mfma_f32_16x16x32_bf16 v[224:227], a[200:203], a[232:235], v[224:227]\n"
"s_add_u32 s50, s46, 327680\n"
"s_add_u32 m0, s49, 8192\n"
"v_mfma_f32_16x16x32_bf16 v[228:231], a[204:207], a[232:235], v[228:231]\n"
"ds_read_b128 a[232:235], v17 offset:33024\n"
"buffer_load_dwordx4 v34, s[28:31], s50 offen offset:0 lds\n"
"s_waitcnt lgkmcnt(11)\n"
"v_mfma_f32_16x16x32_bf16 v[184:187], a[192:195], a[236:239], v[184:187]\n"
"buffer_load_dwordx4 v35, s[28:31], s50 offen offset:1024 lds\n"
"buffer_load_dwordx4 v36, s[28:31], s50 offen offset:2048 lds\n"
"v_mfma_f32_16x16x32_bf16 v[188:191], a[196:199], a[236:239], v[188:191]\n"
"buffer_load_dwordx4 v37, s[28:31], s50 offen offset:3072 lds\n"
"v_mfma_f32_16x16x32_bf16 v[232:235], a[200:203], a[236:239], v[232:235]\n"
"global_store_dwordx4 v3, v[96:99], s[40:41] offset:0 sc1\n"
"v_mfma_f32_16x16x32_bf16 v[236:239], a[204:207], a[236:239], v[236:239]\n"
"ds_read_b128 a[236:239], v17 offset:49408\n"
"v_mfma_f32_16x16x32_bf16 v[192:195], a[192:195], a[240:243], v[192:195]\n"
"v_cvt_pk_bf16_f32 v104, v104, v105\n"
"v_mfma_f32_16x16x32_bf16 v[196:199], a[196:199], a[240:243], v[196:199]\n"
"v_mfma_f32_16x16x32_bf16 v[240:243], a[200:203], a[240:243], v[240:243]\n"
"v_cvt_pk_bf16_f32 v105, v106, v107\n"
"v_mfma_f32_16x16x32_bf16 v[244:247], a[204:207], a[240:243], v[244:247]\n"
"ds_read_b128 a[240:243], v21 offset:256\n"
"v_mfma_f32_16x16x32_bf16 v[200:203], a[192:195], a[244:247], v[200:203]\n"
"v_cvt_pk_bf16_f32 v106, v108, v109\n"
"v_mfma_f32_16x16x32_bf16 v[204:207], a[196:199], a[244:247], v[204:207]\n"
"v_mfma_f32_16x16x32_bf16 v[248:251], a[200:203], a[244:247], v[248:251]\n"
"v_cvt_pk_bf16_f32 v107, v110, v111\n"
"v_mfma_f32_16x16x32_bf16 v[252:255], a[204:207], a[244:247], v[252:255]\n"
"ds_read_b128 a[244:247], v21 offset:16640\n"
"s_waitcnt lgkmcnt(3)\n"
"v_mfma_f32_16x16x32_bf16 v[160:163], a[208:211], a[224:227], v[160:163]\n"
"global_store_dwordx4 v3, v[104:107], s[40:41] offset:1024 sc1\n"
"v_mfma_f32_16x16x32_bf16 v[164:167], a[212:215], a[224:227], v[164:167]\n"
"s_waitcnt vmcnt(10)\n"
"v_mfma_f32_16x16x32_bf16 v[208:211], a[216:219], a[224:227], v[208:211]\n"
"ds_read_b64_tr_b16 a[192:193], v10 offset:0\n"
"ds_read_b64_tr_b16 a[194:195], v10 offset:2048\n"
"v_mfma_f32_16x16x32_bf16 v[212:215], a[220:223], a[224:227], v[212:215]\n"
"ds_read_b128 a[224:227], v14 offset:512\n"
"v_mfma_f32_16x16x32_bf16 v[168:171], a[208:211], a[228:231], v[168:171]\n"
"ds_read_b64_tr_b16 a[196:197], v11 offset:0\n"
"ds_read_b64_tr_b16 a[198:199], v11 offset:2048\n"
"v_mfma_f32_16x16x32_bf16 v[172:175], a[212:215], a[228:231], v[172:175]\n"
"v_mfma_f32_16x16x32_bf16 v[216:219], a[216:219], a[228:231], v[216:219]\n"
"ds_read_b64_tr_b16 a[200:201], v12 offset:0\n"
"ds_read_b64_tr_b16 a[202:203], v12 offset:2048\n"
"v_mfma_f32_16x16x32_bf16 v[220:223], a[220:223], a[228:231], v[220:223]\n"
"ds_read_b128 a[228:231], v14 offset:16896\n"
"v_mfma_f32_16x16x32_bf16 v[176:179], a[208:211], a[232:235], v[176:179]\n"
"ds_read_b64_tr_b16 a[204:205], v13 offset:0\n"
"ds_read_b64_tr_b16 a[206:207], v13 offset:2048\n"
"v_mfma_f32_16x16x32_bf16 v[180:183], a[212:215], a[232:235], v[180:183]\n"
"v_cvt_pk_bf16_f32 v112, v112, v113\n"
"v_mfma_f32_16x16x32_bf16 v[224:227], a[216:219], a[232:235], v[224:227]\n"
"s_add_u32 s50, s46, 360448\n"
"s_add_u32 m0, s49, 12288\n"
"v_mfma_f32_16x16x32_bf16 v[228:231], a[220:223], a[232:235], v[228:231]\n"
"ds_read_b128 a[232:235], v14 offset:33280\n"
"buffer_load_dwordx4 v34, s[28:31], s50 offen offset:0 lds\n"
"s_waitcnt lgkmcnt(11)\n"
"v_mfma_f32_16x16x32_bf16 v[184:187], a[208:211], a[236:239], v[184:187]\n"
"buffer_load_dwordx4 v35, s[28:31], s50 offen offset:1024 lds\n"
"buffer_load_dwordx4 v36, s[28:31], s50 offen offset:2048 lds\n"
"v_mfma_f32_16x16x32_bf16 v[188:191], a[212:215], a[236:239], v[188:191]\n"
"buffer_load_dwordx4 v37, s[28:31], s50 offen offset:3072 lds\n"
"v_mfma_f32_16x16x32_bf16 v[232:235], a[216:219], a[236:239], v[232:235]\n"
"v_cvt_pk_bf16_f32 v113, v114, v115\n"
"v_mfma_f32_16x16x32_bf16 v[236:239], a[220:223], a[236:239], v[236:239]\n"
"ds_read_b128 a[236:239], v14 offset:49664\n"
"v_mfma_f32_16x16x32_bf16 v[192:195], a[208:211], a[240:243], v[192:195]\n"
"v_cvt_pk_bf16_f32 v114, v116, v117\n"
"v_mfma_f32_16x16x32_bf16 v[196:199], a[212:215], a[240:243], v[196:199]\n"
"v_mfma_f32_16x16x32_bf16 v[240:243], a[216:219], a[240:243], v[240:243]\n"
"v_cvt_pk_bf16_f32 v115, v118, v119\n"
"v_mfma_f32_16x16x32_bf16 v[244:247], a[220:223], a[240:243], v[244:247]\n"
"ds_read_b128 a[240:243], v18 offset:512\n"
"v_mfma_f32_16x16x32_bf16 v[200:203], a[208:211], a[244:247], v[200:203]\n"
"global_store_dwordx4 v3, v[112:115], s[40:41] offset:2048 sc1\n"
"v_mfma_f32_16x16x32_bf16 v[204:207], a[212:215], a[244:247], v[204:207]\n"
"v_mfma_f32_16x16x32_bf16 v[248:251], a[216:219], a[244:247], v[248:251]\n"
"v_cvt_pk_bf16_f32 v120, v120, v121\n"
"v_mfma_f32_16x16x32_bf16 v[252:255], a[220:223], a[244:247], v[252:255]\n"
"ds_read_b128 a[244:247], v18 offset:16896\n"
"s_waitcnt lgkmcnt(3)\n"
"v_mfma_f32_16x16x32_bf16 v[160:163], a[192:195], a[224:227], v[160:163]\n"
"v_cvt_pk_bf16_f32 v121, v122, v123\n"
"v_mfma_f32_16x16x32_bf16 v[164:167], a[196:199], a[224:227], v[164:167]\n"
"s_waitcnt vmcnt(11)\n"
"v_mfma_f32_16x16x32_bf16 v[208:211], a[200:203], a[224:227], v[208:211]\n"
"ds_read_b64_tr_b16 a[208:209], v10 offset:4096\n"
"ds_read_b64_tr_b16 a[210:211], v10 offset:6144\n"
"v_mfma_f32_16x16x32_bf16 v[212:215], a[204:207], a[224:227], v[212:215]\n"
"ds_read_b128 a[224:227], v15 offset:512\n"
"v_mfma_f32_16x16x32_bf16 v[168:171], a[192:195], a[228:231], v[168:171]\n"
"ds_read_b64_tr_b16 a[212:213], v11 offset:4096\n"
"ds_read_b64_tr_b16 a[214:215], v11 offset:6144\n"
"v_mfma_f32_16x16x32_bf16 v[172:175], a[196:199], a[228:231], v[172:175]\n"
"v_mfma_f32_16x16x32_bf16 v[216:219], a[200:203], a[228:231], v[216:219]\n"
"ds_read_b64_tr_b16 a[216:217], v12 offset:4096\n"
"ds_read_b64_tr_b16 a[218:219], v12 offset:6144\n"
"v_mfma_f32_16x16x32_bf16 v[220:223], a[204:207], a[228:231], v[220:223]\n"
"ds_read_b128 a[228:231], v15 offset:16896\n"
"v_mfma_f32_16x16x32_bf16 v[176:179], a[192:195], a[232:235], v[176:179]\n"
"ds_read_b64_tr_b16 a[220:221], v13 offset:4096\n"
"ds_read_b64_tr_b16 a[222:223], v13 offset:6144\n"
"v_mfma_f32_16x16x32_bf16 v[180:183], a[196:199], a[232:235], v[180:183]\n"
"v_cvt_pk_bf16_f32 v122, v124, v125\n"
"v_mfma_f32_16x16x32_bf16 v[224:227], a[200:203], a[232:235], v[224:227]\n"
"s_add_u32 s50, s46, 393216\n"
"s_add_u32 m0, s49, 0\n"
"v_mfma_f32_16x16x32_bf16 v[228:231], a[204:207], a[232:235], v[228:231]\n"
"ds_read_b128 a[232:235], v15 offset:33280\n"
"buffer_load_dwordx4 v34, s[28:31], s50 offen offset:0 lds\n"
"s_waitcnt lgkmcnt(11)\n"
"v_mfma_f32_16x16x32_bf16 v[184:187], a[192:195], a[236:239], v[184:187]\n"
"buffer_load_dwordx4 v35, s[28:31], s50 offen offset:1024 lds\n"
"buffer_load_dwordx4 v36, s[28:31], s50 offen offset:2048 lds\n"
"v_mfma_f32_16x16x32_bf16 v[188:191], a[196:199], a[236:239], v[188:191]\n"
"buffer_load_dwordx4 v37, s[28:31], s50 offen offset:3072 lds\n"
"v_mfma_f32_16x16x32_bf16 v[232:235], a[200:203], a[236:239], v[232:235]\n"
"v_cvt_pk_bf16_f32 v123, v126, v127\n"
"v_mfma_f32_16x16x32_bf16 v[236:239], a[204:207], a[236:239], v[236:239]\n"
"ds_read_b128 a[236:239], v15 offset:49664\n"
"v_mfma_f32_16x16x32_bf16 v[192:195], a[192:195], a[240:243], v[192:195]\n"
"global_store_dwordx4 v3, v[120:123], s[40:41] offset:3072 sc1\n"
"v_mfma_f32_16x16x32_bf16 v[196:199], a[196:199], a[240:243], v[196:199]\n"
"v_mfma_f32_16x16x32_bf16 v[240:243], a[200:203], a[240:243], v[240:243]\n"
"v_cvt_pk_bf16_f32 v128, v128, v129\n"
"v_mfma_f32_16x16x32_bf16 v[244:247], a[204:207], a[240:243], v[244:247]\n"
"ds_read_b128 a[240:243], v19 offset:512\n"
"v_mfma_f32_16x16x32_bf16 v[200:203], a[192:195], a[244:247], v[200:203]\n"
"v_cvt_pk_bf16_f32 v129, v130, v131\n"
"v_mfma_f32_16x16x32_bf16 v[204:207], a[196:199], a[244:247], v[204:207]\n"
"v_mfma_f32_16x16x32_bf16 v[248:251], a[200:203], a[244:247], v[248:251]\n"
"v_cvt_pk_bf16_f32 v130, v132, v133\n"
"v_mfma_f32_16x16x32_bf16 v[252:255], a[204:207], a[244:247], v[252:255]\n"
"ds_read_b128 a[244:247], v19 offset:16896\n"
"s_waitcnt lgkmcnt(3)\n"
"v_mfma_f32_16x16x32_bf16 v[160:163], a[208:211], a[224:227], v[160:163]\n"
"v_cvt_pk_bf16_f32 v131, v134, v135\n"
"v_mfma_f32_16x16x32_bf16 v[164:167], a[212:215], a[224:227], v[164:167]\n"
"s_waitcnt vmcnt(11)\n"
"v_mfma_f32_16x16x32_bf16 v[208:211], a[216:219], a[224:227], v[208:211]\n"
"ds_read_b64_tr_b16 a[192:193], v10 offset:8192\n"
"ds_read_b64_tr_b16 a[194:195], v10 offset:10240\n"
"v_mfma_f32_16x16x32_bf16 v[212:215], a[220:223], a[224:227], v[212:215]\n"
"ds_read_b128 a[224:227], v16 offset:512\n"
"v_mfma_f32_16x16x32_bf16 v[168:171], a[208:211], a[228:231], v[168:171]\n"
"ds_read_b64_tr_b16 a[196:197], v11 offset:8192\n"
"ds_read_b64_tr_b16 a[198:199], v11 offset:10240\n"
"v_mfma_f32_16x16x32_bf16 v[172:175], a[212:215], a[228:231], v[172:175]\n"
"v_mfma_f32_16x16x32_bf16 v[216:219], a[216:219], a[228:231], v[216:219]\n"
"ds_read_b64_tr_b16 a[200:201], v12 offset:8192\n"
"ds_read_b64_tr_b16 a[202:203], v12 offset:10240\n"
"v_mfma_f32_16x16x32_bf16 v[220:223], a[220:223], a[228:231], v[220:223]\n"
"ds_read_b128 a[228:231], v16 offset:16896\n"
"v_mfma_f32_16x16x32_bf16 v[176:179], a[208:211], a[232:235], v[176:179]\n"
"ds_read_b64_tr_b16 a[204:205], v13 offset:8192\n"
"ds_read_b64_tr_b16 a[206:207], v13 offset:10240\n"
"v_mfma_f32_16x16x32_bf16 v[180:183], a[212:215], a[232:235], v[180:183]\n"
"global_store_dwordx4 v4, v[128:131], s[40:41] offset:0 sc1\n"
"v_mfma_f32_16x16x32_bf16 v[224:227], a[216:219], a[232:235], v[224:227]\n"
"s_add_u32 s50, s46, 425984\n"
"s_add_u32 m0, s49, 4096\n"
"v_mfma_f32_16x16x32_bf16 v[228:231], a[220:223], a[232:235], v[228:231]\n"
"ds_read_b128 a[232:235], v16 offset:33280\n"
"buffer_load_dwordx4 v34, s[28:31], s50 offen offset:0 lds\n"
"s_waitcnt lgkmcnt(11)\n"
"v_mfma_f32_16x16x32_bf16 v[184:187], a[208:211], a[236:239], v[184:187]\n"
"buffer_load_dwordx4 v35, s[28:31], s50 offen offset:1024 lds\n"
"buffer_load_dwordx4 v36, s[28:31], s50 offen offset:2048 lds\n"
"v_mfma_f32_16x16x32_bf16 v[188:191], a[212:215], a[236:239], v[188:191]\n"
"buffer_load_dwordx4 v37, s[28:31], s50 offen offset:3072 lds\n"
"v_mfma_f32_16x16x32_bf16 v[232:235], a[216:219], a[236:239], v[232:235]\n"
"v_cvt_pk_bf16_f32 v136, v136, v137\n"
"v_mfma_f32_16x16x32_bf16 v[236:239], a[220:223], a[236:239], v[236:239]\n"
"ds_read_b128 a[236:239], v16 offset:49664\n"
"v_mfma_f32_16x16x32_bf16 v[192:195], a[208:211], a[240:243], v[192:195]\n"
"v_cvt_pk_bf16_f32 v137, v138, v139\n"
"v_mfma_f32_16x16x32_bf16 v[196:199], a[212:215], a[240:243], v[196:199]\n"
"v_mfma_f32_16x16x32_bf16 v[240:243], a[216:219], a[240:243], v[240:243]\n"
"v_cvt_pk_bf16_f32 v138, v140, v141\n"
"v_mfma_f32_16x16x32_bf16 v[244:247], a[220:223], a[240:243], v[244:247]\n"
"ds_read_b128 a[240:243], v20 offset:512\n"
"v_mfma_f32_16x16x32_bf16 v[200:203], a[208:211], a[244:247], v[200:203]\n"
"v_cvt_pk_bf16_f32 v139, v142, v143\n"
"v_mfma_f32_16x16x32_bf16 v[204:207], a[212:215], a[244:247], v[204:207]\n"
"v_mfma_f32_16x16x32_bf16 v[248:251], a[216:219], a[244:247], v[248:251]\n"
"global_store_dwordx4 v4, v[136:139], s[40:41] offset:1024 sc1\n"
"v_mfma_f32_16x16x32_bf16 v[252:255], a[220:223], a[244:247], v[252:255]\n"
"ds_read_b128 a[244:247], v20 offset:16896\n"
"s_waitcnt lgkmcnt(3)\n"
"v_mfma_f32_16x16x32_bf16 v[160:163], a[192:195], a[224:227], v[160:163]\n"
"v_cvt_pk_bf16_f32 v144, v144, v145\n"
"v_mfma_f32_16x16x32_bf16 v[164:167], a[196:199], a[224:227], v[164:167]\n"
"s_waitcnt vmcnt(11)\n"
"v_mfma_f32_16x16x32_bf16 v[208:211], a[200:203], a[224:227], v[208:211]\n"
"ds_read_b64_tr_b16 a[208:209], v10 offset:12288\n"
"ds_read_b64_tr_b16 a[210:211], v10 offset:14336\n"
"v_mfma_f32_16x16x32_bf16 v[212:215], a[204:207], a[224:227], v[212:215]\n"
"ds_read_b128 a[224:227], v17 offset:512\n"
"v_mfma_f32_16x16x32_bf16 v[168:171], a[192:195], a[228:231], v[168:171]\n"
"ds_read_b64_tr_b16 a[212:213], v11 offset:12288\n"
"ds_read_b64_tr_b16 a[214:215], v11 offset:14336\n"
"v_mfma_f32_16x16x32_bf16 v[172:175], a[196:199], a[228:231], v[172:175]\n"
"v_mfma_f32_16x16x32_bf16 v[216:219], a[200:203], a[228:231], v[216:219]\n"
"ds_read_b64_tr_b16 a[216:217], v12 offset:12288\n"
"ds_read_b64_tr_b16 a[218:219], v12 offset:14336\n"
"v_mfma_f32_16x16x32_bf16 v[220:223], a[204:207], a[228:231], v[220:223]\n"
"ds_read_b128 a[228:231], v17 offset:16896\n"
"v_mfma_f32_16x16x32_bf16 v[176:179], a[192:195], a[232:235], v[176:179]\n"
"ds_read_b64_tr_b16 a[220:221], v13 offset:12288\n"
"ds_read_b64_tr_b16 a[222:223], v13 offset:14336\n"
"v_mfma_f32_16x16x32_bf16 v[180:183], a[196:199], a[232:235], v[180:183]\n"
"v_cvt_pk_bf16_f32 v145, v146, v147\n"
"v_mfma_f32_16x16x32_bf16 v[224:227], a[200:203], a[232:235], v[224:227]\n"
"s_add_u32 s50, s46, 458752\n"
"s_add_u32 m0, s49, 8192\n"
"v_mfma_f32_16x16x32_bf16 v[228:231], a[204:207], a[232:235], v[228:231]\n"
"ds_read_b128 a[232:235], v17 offset:33280\n"
"buffer_load_dwordx4 v34, s[28:31], s50 offen offset:0 lds\n"
"s_waitcnt lgkmcnt(11)\n"
"v_mfma_f32_16x16x32_bf16 v[184:187], a[192:195], a[236:239], v[184:187]\n"
"buffer_load_dwordx4 v35, s[28:31], s50 offen offset:1024 lds\n"
"buffer_load_dwordx4 v36, s[28:31], s50 offen offset:2048 lds\n"
"v_mfma_f32_16x16x32_bf16 v[188:191], a[196:199], a[236:239], v[188:191]\n"
"buffer_load_dwordx4 v37, s[28:31], s50 offen offset:3072 lds\n"
"v_mfma_f32_16x16x32_bf16 v[232:235], a[200:203], a[236:239], v[232:235]\n"
"v_cvt_pk_bf16_f32 v146, v148, v149\n"
"v_mfma_f32_16x16x32_bf16 v[236:239], a[204:207], a[236:239], v[236:239]\n"
"ds_read_b128 a[236:239], v17 offset:49664\n"
"v_mfma_f32_16x16x32_bf16 v[192:195], a[192:195], a[240:243], v[192:195]\n"
"v_cvt_pk_bf16_f32 v147, v150, v151\n"
"v_mfma_f32_16x16x32_bf16 v[196:199], a[196:199], a[240:243], v[196:199]\n"
"v_mfma_f32_16x16x32_bf16 v[240:243], a[200:203], a[240:243], v[240:243]\n"
"global_store_dwordx4 v4, v[144:147], s[40:41] offset:2048 sc1\n"
"v_mfma_f32_16x16x32_bf16 v[244:247], a[204:207], a[240:243], v[244:247]\n"
"ds_read_b128 a[240:243], v21 offset:512\n"
"v_mfma_f32_16x16x32_bf16 v[200:203], a[192:195], a[244:247], v[200:203]\n"
"v_cvt_pk_bf16_f32 v152, v152, v153\n"
"v_mfma_f32_16x16x32_bf16 v[204:207], a[196:199], a[244:247], v[204:207]\n"
"v_mfma_f32_16x16x32_bf16 v[248:251], a[200:203], a[244:247], v[248:251]\n"
"v_cvt_pk_bf16_f32 v153, v154, v155\n"
"v_mfma_f32_16x16x32_bf16 v[252:255], a[204:207], a[244:247], v[252:255]\n"
"ds_read_b128 a[244:247], v21 offset:16896\n"
"s_waitcnt lgkmcnt(3)\n"
"v_mfma_f32_16x16x32_bf16 v[160:163], a[208:211], a[224:227], v[160:163]\n"
"v_mfma_f32_16x16x32_bf16 v[164:167], a[212:215], a[224:227], v[164:167]\n"
"s_waitcnt vmcnt(11)\n"
"v_mfma_f32_16x16x32_bf16 v[208:211], a[216:219], a[224:227], v[208:211]\n"
"ds_read_b64_tr_b16 a[192:193], v10 offset:0\n"
"ds_read_b64_tr_b16 a[194:195], v10 offset:2048\n"
"v_mfma_f32_16x16x32_bf16 v[212:215], a[220:223], a[224:227], v[212:215]\n"
"ds_read_b128 a[224:227], v14 offset:768\n"
"v_mfma_f32_16x16x32_bf16 v[168:171], a[208:211], a[228:231], v[168:171]\n"
"ds_read_b64_tr_b16 a[196:197], v11 offset:0\n"
"ds_read_b64_tr_b16 a[198:199], v11 offset:2048\n"
"v_mfma_f32_16x16x32_bf16 v[172:175], a[212:215], a[228:231], v[172:175]\n"
"v_cvt_pk_bf16_f32 v154, v156, v157\n"
"v_mfma_f32_16x16x32_bf16 v[216:219], a[216:219], a[228:231], v[216:219]\n"
"ds_read_b64_tr_b16 a[200:201], v12 offset:0\n"
"ds_read_b64_tr_b16 a[202:203], v12 offset:2048\n"
"v_mfma_f32_16x16x32_bf16 v[220:223], a[220:223], a[228:231], v[220:223]\n"
"ds_read_b128 a[228:231], v14 offset:17152\n"
"v_mfma_f32_16x16x32_bf16 v[176:179], a[208:211], a[232:235], v[176:179]\n"
"ds_read_b64_tr_b16 a[204:205], v13 offset:0\n"
"ds_read_b64_tr_b16 a[206:207], v13 offset:2048\n"
"v_mfma_f32_16x16x32_bf16 v[180:183], a[212:215], a[232:235], v[180:183]\n"
"v_mfma_f32_16x16x32_bf16 v[224:227], a[216:219], a[232:235], v[224:227]\n"
"s_add_u32 s50, s46, 491520\n"
"s_add_u32 m0, s49, 12288\n"
"v_mfma_f32_16x16x32_bf16 v[228:231], a[220:223], a[232:235], v[228:231]\n"
"ds_read_b128 a[232:235], v14 offset:33536\n"
"buffer_load_dwordx4 v34, s[28:31], s50 offen offset:0 lds\n"
"s_waitcnt lgkmcnt(11)\n"
"v_mfma_f32_16x16x32_bf16 v[184:187], a[208:211], a[236:239], v[184:187]\n"
"buffer_load_dwordx4 v35, s[28:31], s50 offen offset:1024 lds\n"
"buffer_load_dwordx4 v36, s[28:31], s50 offen offset:2048 lds\n"
"v_mfma_f32_16x16x32_bf16 v[188:191], a[212:215], a[236:239], v[188:191]\n"
"buffer_load_dwordx4 v37, s[28:31], s50 offen offset:3072 lds\n"
"v_mfma_f32_16x16x32_bf16 v[232:235], a[216:219], a[236:239], v[232:235]\n"
"v_mfma_f32_16x16x32_bf16 v[236:239], a[220:223], a[236:239], v[236:239]\n"
"ds_read_b128 a[236:239], v14 offset:49920\n"
"v_mfma_f32_16x16x32_bf16 v[192:195], a[208:211], a[240:243], v[192:195]\n"
"v_cvt_pk_bf16_f32 v155, v158, v159\n"
"v_mfma_f32_16x16x32_bf16 v[196:199], a[212:215], a[240:243], v[196:199]\n"
"v_mfma_f32_16x16x32_bf16 v[240:243], a[216:219], a[240:243], v[240:243]\n"
"v_mfma_f32_16x16x32_bf16 v[244:247], a[220:223], a[240:243], v[244:247]\n"
"ds_read_b128 a[240:243], v18 offset:768\n"
"v_mfma_f32_16x16x32_bf16 v[200:203], a[208:211], a[244:247], v[200:203]\n"
"v_mfma_f32_16x16x32_bf16 v[204:207], a[212:215], a[244:247], v[204:207]\n"
"global_store_dwordx4 v4, v[152:155], s[40:41] offset:3072 sc1\n"
"v_mfma_f32_16x16x32_bf16 v[248:251], a[216:219], a[244:247], v[248:251]\n"
"v_mfma_f32_16x16x32_bf16 v[252:255], a[220:223], a[244:247], v[252:255]\n"
"ds_read_b128 a[244:247], v18 offset:17152\n"
"s_waitcnt lgkmcnt(3)\n"
"v_mfma_f32_16x16x32_bf16 v[160:163], a[192:195], a[224:227], v[160:163]\n"
"v_mfma_f32_16x16x32_bf16 v[164:167], a[196:199], a[224:227], v[164:167]\n"
"s_waitcnt vmcnt(10)\n"
"v_mfma_f32_16x16x32_bf16 v[208:211], a[200:203], a[224:227], v[208:211]\n"
"ds_read_b64_tr_b16 a[208:209], v10 offset:4096\n"
"ds_read_b64_tr_b16 a[210:211], v10 offset:6144\n"
"v_mfma_f32_16x16x32_bf16 v[212:215], a[204:207], a[224:227], v[212:215]\n"
"ds_read_b128 a[224:227], v15 offset:768\n"
"v_mfma_f32_16x16x32_bf16 v[168:171], a[192:195], a[228:231], v[168:171]\n"
"ds_read_b64_tr_b16 a[212:213], v11 offset:4096\n"
"ds_read_b64_tr_b16 a[214:215], v11 offset:6144\n"
"v_mfma_f32_16x16x32_bf16 v[172:175], a[196:199], a[228:231], v[172:175]\n"
"v_mfma_f32_16x16x32_bf16 v[216:219], a[200:203], a[228:231], v[216:219]\n"
"ds_read_b64_tr_b16 a[216:217], v12 offset:4096\n"
"ds_read_b64_tr_b16 a[218:219], v12 offset:6144\n"
"v_mfma_f32_16x16x32_bf16 v[220:223], a[204:207], a[228:231], v[220:223]\n"
"ds_read_b128 a[228:231], v15 offset:17152\n"
"v_mfma_f32_16x16x32_bf16 v[176:179], a[192:195], a[232:235], v[176:179]\n"
"ds_read_b64_tr_b16 a[220:221], v13 offset:4096\n"
"ds_read_b64_tr_b16 a[222:223], v13 offset:6144\n"
"v_mfma_f32_16x16x32_bf16 v[180:183], a[196:199], a[232:235], v[180:183]\n"
"v_mfma_f32_16x16x32_bf16 v[224:227], a[200:203], a[232:235], v[224:227]\n"
"v_mfma_f32_16x16x32_bf16 v[228:231], a[204:207], a[232:235], v[228:231]\n"
"ds_read_b128 a[232:235], v15 offset:33536\n"
"s_waitcnt lgkmcnt(11)\n"
"v_mfma_f32_16x16x32_bf16 v[184:187], a[192:195], a[236:239], v[184:187]\n"
"v_mfma_f32_16x16x32_bf16 v[188:191], a[196:199], a[236:239], v[188:191]\n"
"v_mfma_f32_16x16x32_bf16 v[232:235], a[200:203], a[236:239], v[232:235]\n"
"v_mfma_f32_16x16x32_bf16 v[236:239], a[204:207], a[236:239], v[236:239]\n"
"ds_read_b128 a[236:239], v15 offset:49920\n"
"v_mfma_f32_16x16x32_bf16 v[192:195], a[192:195], a[240:243], v[192:195]\n"
"v_mfma_f32_16x16x32_bf16 v[196:199], a[196:199], a[240:243], v[196:199]\n"
"v_mfma_f32_16x16x32_bf16 v[240:243], a[200:203], a[240:243], v[240:243]\n"
"v_mfma_f32_16x16x32_bf16 v[244:247], a[204:207], a[240:243], v[244:247]\n"
"ds_read_b128 a[240:243], v19 offset:768\n"
"v_mfma_f32_16x16x32_bf16 v[200:203], a[192:195], a[244:247], v[200:203]\n"
"v_mfma_f32_16x16x32_bf16 v[204:207], a[196:199], a[244:247], v[204:207]\n"
"v_mfma_f32_16x16x32_bf16 v[248:251], a[200:203], a[244:247], v[248:251]\n"
"v_mfma_f32_16x16x32_bf16 v[252:255], a[204:207], a[244:247], v[252:255]\n"
"ds_read_b128 a[244:247], v19 offset:17152\n"
"s_waitcnt lgkmcnt(3)\n"
"v_mfma_f32_16x16x32_bf16 v[160:163], a[208:211], a[224:227], v[160:163]\n"
"v_mfma_f32_16x16x32_bf16 v[164:167], a[212:215], a[224:227], v[164:167]\n"
"s_waitcnt vmcnt(5)\n"
"v_mfma_f32_16x16x32_bf16 v[208:211], a[216:219], a[224:227], v[208:211]\n"
"ds_read_b64_tr_b16 a[192:193], v10 offset:8192\n"
"ds_read_b64_tr_b16 a[194:195], v10 offset:10240\n"
"v_mfma_f32_16x16x32_bf16 v[212:215], a[220:223], a[224:227], v[212:215]\n"
"ds_read_b128 a[224:227], v16 offset:768\n"
"v_mfma_f32_16x16x32_bf16 v[168:171], a[208:211], a[228:231], v[168:171]\n"
"ds_read_b64_tr_b16 a[196:197], v11 offset:8192\n"
"ds_read_b64_tr_b16 a[198:199], v11 offset:10240\n"
"v_mfma_f32_16x16x32_bf16 v[172:175], a[212:215], a[228:231], v[172:175]\n"
"v_mfma_f32_16x16x32_bf16 v[216:219], a[216:219], a[228:231], v[216:219]\n"
"ds_read_b64_tr_b16 a[200:201], v12 offset:8192\n"
"ds_read_b64_tr_b16 a[202:203], v12 offset:10240\n"
"v_mfma_f32_16x16x32_bf16 v[220:223], a[220:223], a[228:231], v[220:223]\n"
"ds_read_b128 a[228:231], v16 offset:17152\n"
"v_mfma_f32_16x16x32_bf16 v[176:179], a[208:211], a[232:235], v[176:179]\n"
"ds_read_b64_tr_b16 a[204:205], v13 offset:8192\n"
"ds_read_b64_tr_b16 a[206:207], v13 offset:10240\n"
"v_mfma_f32_16x16x32_bf16 v[180:183], a[212:215], a[232:235], v[180:183]\n"
"v_mfma_f32_16x16x32_bf16 v[224:227], a[216:219], a[232:235], v[224:227]\n"
"v_mfma_f32_16x16x32_bf16 v[228:231], a[220:223], a[232:235], v[228:231]\n"
"ds_read_b128 a[232:235], v16 offset:33536\n"
"s_waitcnt lgkmcnt(11)\n"
"v_mfma_f32_16x16x32_bf16 v[184:187], a[208:211], a[236:239], v[184:187]\n"
"v_mfma_f32_16x16x32_bf16 v[188:191], a[212:215], a[236:239], v[188:191]\n"
"v_mfma_f32_16x16x32_bf16 v[232:235], a[216:219], a[236:239], v[232:235]\n"
"v_mfma_f32_16x16x32_bf16 v[236:239], a[220:223], a[236:239], v[236:239]\n"
"ds_read_b128 a[236:239], v16 offset:49920\n"
"v_mfma_f32_16x16x32_bf16 v[192:195], a[208:211], a[240:243], v[192:195]\n"
"v_mfma_f32_16x16x32_bf16 v[196:199], a[212:215], a[240:243], v[196:199]\n"
"v_mfma_f32_16x16x32_bf16 v[240:243], a[216:219], a[240:243], v[240:243]\n"
"v_mfma_f32_16x16x32_bf16 v[244:247], a[220:223], a[240:243], v[244:247]\n"
"ds_read_b128 a[240:243], v20 offset:768\n"
"v_mfma_f32_16x16x32_bf16 v[200:203], a[208:211], a[244:247], v[200:203]\n"
"v_mfma_f32_16x16x32_bf16 v[204:207], a[212:215], a[244:247], v[204:207]\n"
"v_mfma_f32_16x16x32_bf16 v[248:251], a[216:219], a[244:247], v[248:251]\n"
"v_mfma_f32_16x16x32_bf16 v[252:255], a[220:223], a[244:247], v[252:255]\n"
"ds_read_b128 a[244:247], v20 offset:17152\n"
"s_waitcnt lgkmcnt(3)\n"
"v_mfma_f32_16x16x32_bf16 v[160:163], a[192:195], a[224:227], v[160:163]\n"
"v_mfma_f32_16x16x32_bf16 v[164:167], a[196:199], a[224:227], v[164:167]\n"
"s_waitcnt vmcnt(0)\n"
"v_mfma_f32_16x16x32_bf16 v[208:211], a[200:203], a[224:227], v[208:211]\n"
"ds_read_b64_tr_b16 a[208:209], v10 offset:12288\n"
"ds_read_b64_tr_b16 a[210:211], v10 offset:14336\n"
"v_mfma_f32_16x16x32_bf16 v[212:215], a[204:207], a[224:227], v[212:215]\n"
"ds_read_b128 a[224:227], v17 offset:768\n"
"v_mfma_f32_16x16x32_bf16 v[168:171], a[192:195], a[228:231], v[168:171]\n"
"ds_read_b64_tr_b16 a[212:213], v11 offset:12288\n"
"ds_read_b64_tr_b16 a[214:215], v11 offset:14336\n"
"v_mfma_f32_16x16x32_bf16 v[172:175], a[196:199], a[228:231], v[172:175]\n"
"v_mfma_f32_16x16x32_bf16 v[216:219], a[200:203], a[228:231], v[216:219]\n"
"ds_read_b64_tr_b16 a[216:217], v12 offset:12288\n"
"ds_read_b64_tr_b16 a[218:219], v12 offset:14336\n"
"v_mfma_f32_16x16x32_bf16 v[220:223], a[204:207], a[228:231], v[220:223]\n"
"ds_read_b128 a[228:231], v17 offset:17152\n"
"v_mfma_f32_16x16x32_bf16 v[176:179], a[192:195], a[232:235], v[176:179]\n"
"ds_read_b64_tr_b16 a[220:221], v13 offset:12288\n"
"ds_read_b64_tr_b16 a[222:223], v13 offset:14336\n"
"v_mfma_f32_16x16x32_bf16 v[180:183], a[196:199], a[232:235], v[180:183]\n"
"v_mfma_f32_16x16x32_bf16 v[224:227], a[200:203], a[232:235], v[224:227]\n"
"v_mfma_f32_16x16x32_bf16 v[228:231], a[204:207], a[232:235], v[228:231]\n"
"ds_read_b128 a[232:235], v17 offset:33536\n"
"s_waitcnt lgkmcnt(11)\n"
"v_mfma_f32_16x16x32_bf16 v[184:187], a[192:195], a[236:239], v[184:187]\n"
"v_mfma_f32_16x16x32_bf16 v[188:191], a[196:199], a[236:239], v[188:191]\n"
"v_mfma_f32_16x16x32_bf16 v[232:235], a[200:203], a[236:239], v[232:235]\n"
"v_mfma_f32_16x16x32_bf16 v[236:239], a[204:207], a[236:239], v[236:239]\n"
"ds_read_b128 a[236:239], v17 offset:49920\n"
"v_mfma_f32_16x16x32_bf16 v[192:195], a[192:195], a[240:243], v[192:195]\n"
"v_mfma_f32_16x16x32_bf16 v[196:199], a[196:199], a[240:243], v[196:199]\n"
"v_mfma_f32_16x16x32_bf16 v[240:243], a[200:203], a[240:243], v[240:243]\n"
"v_mfma_f32_16x16x32_bf16 v[244:247], a[204:207], a[240:243], v[244:247]\n"
"ds_read_b128 a[240:243], v21 offset:768\n"
"v_mfma_f32_16x16x32_bf16 v[200:203], a[192:195], a[244:247], v[200:203]\n"
"v_mfma_f32_16x16x32_bf16 v[204:207], a[196:199], a[244:247], v[204:207]\n"
"v_mfma_f32_16x16x32_bf16 v[248:251], a[200:203], a[244:247], v[248:251]\n"
"v_mfma_f32_16x16x32_bf16 v[252:255], a[204:207], a[244:247], v[252:255]\n"
"ds_read_b128 a[244:247], v21 offset:17152\n"
"s_waitcnt lgkmcnt(3)\n"
"v_mfma_f32_16x16x32_bf16 v[160:163], a[208:211], a[224:227], v[160:163]\n"
"v_mfma_f32_16x16x32_bf16 v[164:167], a[212:215], a[224:227], v[164:167]\n"
"v_mfma_f32_16x16x32_bf16 v[208:211], a[216:219], a[224:227], v[208:211]\n"
"v_mfma_f32_16x16x32_bf16 v[212:215], a[220:223], a[224:227], v[212:215]\n"
"v_mfma_f32_16x16x32_bf16 v[168:171], a[208:211], a[228:231], v[168:171]\n"
"v_mfma_f32_16x16x32_bf16 v[172:175], a[212:215], a[228:231], v[172:175]\n"
"v_mfma_f32_16x16x32_bf16 v[216:219], a[216:219], a[228:231], v[216:219]\n"
"v_mfma_f32_16x16x32_bf16 v[220:223], a[220:223], a[228:231], v[220:223]\n"
"v_mfma_f32_16x16x32_bf16 v[176:179], a[208:211], a[232:235], v[176:179]\n"
"v_mfma_f32_16x16x32_bf16 v[180:183], a[212:215], a[232:235], v[180:183]\n"
"v_mfma_f32_16x16x32_bf16 v[224:227], a[216:219], a[232:235], v[224:227]\n"
"v_mfma_f32_16x16x32_bf16 v[228:231], a[220:223], a[232:235], v[228:231]\n"
"s_waitcnt lgkmcnt(0)\n"
"v_mfma_f32_16x16x32_bf16 v[184:187], a[208:211], a[236:239], v[184:187]\n"
"v_mfma_f32_16x16x32_bf16 v[188:191], a[212:215], a[236:239], v[188:191]\n"
"v_mfma_f32_16x16x32_bf16 v[232:235], a[216:219], a[236:239], v[232:235]\n"
"v_mfma_f32_16x16x32_bf16 v[236:239], a[220:223], a[236:239], v[236:239]\n"
"v_mfma_f32_16x16x32_bf16 v[192:195], a[208:211], a[240:243], v[192:195]\n"
"v_mfma_f32_16x16x32_bf16 v[196:199], a[212:215], a[240:243], v[196:199]\n"
"v_mfma_f32_16x16x32_bf16 v[240:243], a[216:219], a[240:243], v[240:243]\n"
"v_mfma_f32_16x16x32_bf16 v[244:247], a[220:223], a[240:243], v[244:247]\n"
"v_mfma_f32_16x16x32_bf16 v[200:203], a[208:211], a[244:247], v[200:203]\n"
"v_mfma_f32_16x16x32_bf16 v[204:207], a[212:215], a[244:247], v[204:207]\n"
"v_mfma_f32_16x16x32_bf16 v[248:251], a[216:219], a[244:247], v[248:251]\n"
"v_mfma_f32_16x16x32_bf16 v[252:255], a[220:223], a[244:247], v[252:255]\n"
"v_cvt_pk_bf16_f32 v160, v160, v161\n"
"v_cvt_pk_bf16_f32 v161, v162, v163\n"
"v_cvt_pk_bf16_f32 v162, v164, v165\n"
"v_cvt_pk_bf16_f32 v163, v166, v167\n"
"global_store_dwordx4 v5, v[160:163], s[40:41] offset:0 sc1\n"
"v_cvt_pk_bf16_f32 v168, v168, v169\n"
"v_cvt_pk_bf16_f32 v169, v170, v171\n"
"v_cvt_pk_bf16_f32 v170, v172, v173\n"
"v_cvt_pk_bf16_f32 v171, v174, v175\n"
"global_store_dwordx4 v5, v[168:171], s[40:41] offset:1024 sc1\n"
"v_cvt_pk_bf16_f32 v176, v176, v177\n"
"v_cvt_pk_bf16_f32 v177, v178, v179\n"
"v_cvt_pk_bf16_f32 v178, v180, v181\n"
"v_cvt_pk_bf16_f32 v179, v182, v183\n"
"global_store_dwordx4 v5, v[176:179], s[40:41] offset:2048 sc1\n"
"v_cvt_pk_bf16_f32 v184, v184, v185\n"
"v_cvt_pk_bf16_f32 v185, v186, v187\n"
"v_cvt_pk_bf16_f32 v186, v188, v189\n"
"v_cvt_pk_bf16_f32 v187, v190, v191\n"
"global_store_dwordx4 v5, v[184:187], s[40:41] offset:3072 sc1\n"
"v_cvt_pk_bf16_f32 v192, v192, v193\n"
"v_cvt_pk_bf16_f32 v193, v194, v195\n"
"v_cvt_pk_bf16_f32 v194, v196, v197\n"
"v_cvt_pk_bf16_f32 v195, v198, v199\n"
"global_store_dwordx4 v6, v[192:195], s[40:41] offset:0 sc1\n"
"v_cvt_pk_bf16_f32 v200, v200, v201\n"
"v_cvt_pk_bf16_f32 v201, v202, v203\n"
"v_cvt_pk_bf16_f32 v202, v204, v205\n"
"v_cvt_pk_bf16_f32 v203, v206, v207\n"
"global_store_dwordx4 v6, v[200:203], s[40:41] offset:1024 sc1\n"
"v_cvt_pk_bf16_f32 v208, v208, v209\n"
"v_cvt_pk_bf16_f32 v209, v210, v211\n"
"v_cvt_pk_bf16_f32 v210, v212, v213\n"
"v_cvt_pk_bf16_f32 v211, v214, v215\n"
"global_store_dwordx4 v6, v[208:211], s[40:41] offset:2048 sc1\n"
"v_cvt_pk_bf16_f32 v216, v216, v217\n"
"v_cvt_pk_bf16_f32 v217, v218, v219\n"
"v_cvt_pk_bf16_f32 v218, v220, v221\n"
"v_cvt_pk_bf16_f32 v219, v222, v223\n"
"global_store_dwordx4 v6, v[216:219], s[40:41] offset:3072 sc1\n"
"v_cvt_pk_bf16_f32 v224, v224, v225\n"
"v_cvt_pk_bf16_f32 v225, v226, v227\n"
"v_cvt_pk_bf16_f32 v226, v228, v229\n"
"v_cvt_pk_bf16_f32 v227, v230, v231\n"
"global_store_dwordx4 v7, v[224:227], s[40:41] offset:0 sc1\n"
"v_cvt_pk_bf16_f32 v232, v232, v233\n"
"v_cvt_pk_bf16_f32 v233, v234, v235\n"
"v_cvt_pk_bf16_f32 v234, v236, v237\n"
"v_cvt_pk_bf16_f32 v235, v238, v239\n"
"global_store_dwordx4 v7, v[232:235], s[40:41] offset:1024 sc1\n"
"v_cvt_pk_bf16_f32 v240, v240, v241\n"
"v_cvt_pk_bf16_f32 v241, v242, v243\n"
"v_cvt_pk_bf16_f32 v242, v244, v245\n"
"v_cvt_pk_bf16_f32 v243, v246, v247\n"
"global_store_dwordx4 v7, v[240:243], s[40:41] offset:2048 sc1\n"
"v_cvt_pk_bf16_f32 v248, v248, v249\n"
"v_cvt_pk_bf16_f32 v249, v250, v251\n"
"v_cvt_pk_bf16_f32 v250, v252, v253\n"
"v_cvt_pk_bf16_f32 v251, v254, v255\n"
"global_store_dwordx4 v7, v[248:251], s[40:41] offset:3072 sc1\n"
"; ---- end of section: oproj\n"
"s_mul_i32 s53, s44, 11\n"
"s_mul_i32 s53, s53, 768\n"
"s_add_u32 s53, s53, 33554432\n"
"s_add_u32 s88, s80, s53\n"
"s_addc_u32 s89, s81, 0\n"
"s_lshl_b32 s54, s43, 2\n"
"s_add_u32 s54, s54, s42\n"
"s_lshl_b32 s55, s54, 3\n"
"v_mul_u32_u24 v2, 768, v1\n"
"v_add_u32 v2, s55, v2\n"
"v_mov_b32 v4, s34\n"
"v_mov_b32 v5, s35\n"
"s_mul_i32 s55, s44, 96\n"
"s_mul_i32 s56, s54, 3\n"
"s_add_u32 s55, s55, s56\n"
"s_lshl_b32 s55, s55, 10\n"
"s_mov_b32 s24, s80\n"
"s_mov_b32 s25, s81\n"
"s_add_u32 s24, s24, s55\n"
"s_addc_u32 s25, s25, 0\n"
"s_and_b32 s25, s25, 0xffff\n"
"s_mov_b32 s26, 23789568\n"
"s_mov_b32 s27, 0x20000\n"
"s_sub_u32 s26, s26, s55\n"
"s_mov_b32 s28, s82\n"
"s_mov_b32 s29, s83\n"
"s_and_b32 s29, s29, 0xffff\n"
"s_mov_b32 s30, 2097152\n"
"s_mov_b32 s31, 0x20000\n"
"v_lshlrev_b32 v7, 4, v1\n"
"v_mov_b32 v8, 0x3f80\n"
"v_mov_b32 v9, 0x3f800000\n"
"s_lshl_b32 s90, 1, s43\n"
"s_andn2_b32 s90, 0xff, s90\n"
"s_or_b32 s91, s90, 0x700\n"
"s_mov_b64 exec, 0x7ff\n"
"global_load_dwordx2 v[10:11], v2, s[88:89] offset:256 sc1\n"
"s_mov_b64 exec, -1\n"
"s_waitcnt vmcnt(0)\n"
"s_barrier\n"
"s_cmp_lg_u32 s42, 0\n"
"s_cbranch_scc1 LHPA_%=\n"
"s_mov_b32 exec_hi, 0\n"
"global_store_dwordx2 v60, v[58:59], s[86:87] offset:256 sc1\n"
"s_mov_b64 exec, -1\n"
"LHPA_%=:\n"
"; ---- end of section: pub\n"
"s_mov_b64 exec, 0x7ff\n"
"v_cmp_ne_u64 vcc, v[10:11], v[4:5]\n"
"s_nop 1\n"
"s_and_b32 s36, vcc_lo, s91\n"
"s_cmp_eq_u32 s36, 0\n"
"s_cbranch_scc1 LPB_%=\n"
"s_mov_b32 s47, 131072\n"
"LP0_%=:\n"
"global_load_dwordx2 v[10:11], v2, s[88:89] offset:256 sc1\n"
"s_waitcnt vmcnt(0)\n"
"v_cmp_ne_u64 vcc, v[10:11], v[4:5]\n"
"s_nop 1\n"
"s_and_b32 s36, vcc_lo, s91\n"
"s_cmp_eq_u32 s36, 0\n"
"s_cbranch_scc1 LPB_%=\n"
"s_and_b32 s36, vcc_lo, 0x700\n"
"s_cmp_eq_u32 s36, 0\n"
"s_cbranch_scc1 LPF_%=\n"
"s_and_b32 s36, vcc_lo, s90\n"
"s_cmp_eq_u32 s36, 0\n"
"s_cbranch_scc1 LPA_%=\n"
"s_sleep 1\n"
"s_sub_u32 s47, s47, 1\n"
"s_cmp_lg_u32 s47, 0\n"
"s_cbranch_scc1 LP0_%=\n"
"LPF_%=:\n"
"s_mov_b64 exec, 0x700\n"
"global_store_dwordx2 v2, v[4:5], s[88:89] offset:512 sc1\n"
"s_mov_b64 exec, -1\n"
"s_mov_b32 s50, 17301504\n"
"buffer_load_dwordx4 v[96:99], v7, s[24:27], s50 offen offset:0 sc1\n"
"s_mov_b32 s50, 19464192\n"
"buffer_load_dwordx4 v[100:103], v7, s[24:27], s50 offen offset:0 sc1\n"
"s_mov_b32 s50, 21626880\n"
"buffer_load_dwordx4 v[104:107], v7, s[24:27], s50 offen offset:0 sc1\n"
"s_mov_b32 s50, 17301504\n"
"buffer_load_dwordx4 v[144:147], v7, s[24:27], s50 offen offset:1024 sc1\n"
"s_mov_b32 s50, 19464192\n"
"buffer_load_dwordx4 v[148:151], v7, s[24:27], s50 offen offset:1024 sc1\n"
"s_mov_b32 s50, 21626880\n"
"buffer_load_dwordx4 v[152:155], v7, s[24:27], s50 offen offset:1024 sc1\n"
"s_mov_b32 s50, 17301504\n"
"buffer_load_dwordx4 v[192:195], v7, s[24:27], s50 offen offset:2048 sc1\n"
"s_mov_b32 s50, 19464192\n"
"buffer_load_dwordx4 v[196:199], v7, s[24:27], s50 offen offset:2048 sc1\n"
"s_mov_b32 s50, 21626880\n"
"buffer_load_dwordx4 v[200:203], v7, s[24:27], s50 offen offset:2048 sc1\n"
"s_waitcnt vmcnt(0)\n"
"v_dot2_f32_bf16 v18, v96, v8, 0\n"
"v_dot2_f32_bf16 v19, v96, v9, 0\n"
"v_dot2_f32_bf16 v20, v97, v8, 0\n"
"v_dot2_f32_bf16 v21, v97, v9, 0\n"
"v_dot2_f32_bf16 v22, v98, v8, 0\n"
"v_dot2_f32_bf16 v23, v98, v9, 0\n"
"v_dot2_f32_bf16 v24, v99, v8, 0\n"
"v_dot2_f32_bf16 v25, v99, v9, 0\n"
"v_dot2_f32_bf16 v18, v100, v8, v18\n"
"v_dot2_f32_bf16 v19, v100, v9, v19\n"
"v_dot2_f32_bf16 v20, v101, v8, v20\n"
"v_dot2_f32_bf16 v21, v101, v9, v21\n"
"v_dot2_f32_bf16 v22, v102, v8, v22\n"
"v_dot2_f32_bf16 v23, v102, v9, v23\n"
"v_dot2_f32_bf16 v24, v103, v8, v24\n"
"v_dot2_f32_bf16 v25, v103, v9, v25\n"
"v_dot2_f32_bf16 v18, v104, v8, v18\n"
"v_dot2_f32_bf16 v19, v104, v9, v19\n"
"v_dot2_f32_bf16 v20, v105, v8, v20\n"
"v_dot2_f32_bf16 v21, v105, v9, v21\n"
"v_dot2_f32_bf16 v22, v106, v8, v22\n"
"v_dot2_f32_bf16 v23, v106, v9, v23\n"
"v_dot2_f32_bf16 v24, v107, v8, v24\n"
"v_dot2_f32_bf16 v25, v107, v9, v25\n"
"v_mul_f32 v26, 0xbfb8aa3b, v18\n"
"v_mul_f32 v27, 0xbfb8aa3b, v19\n"
"v_mul_f32 v28, 0xbfb8aa3b, v20\n"
"v_mul_f32 v29, 0xbfb8aa3b, v21\n"
"v_mul_f32 v30, 0xbfb8aa3b, v22\n"
"v_mul_f32 v31, 0xbfb8aa3b, v23\n"
"v_mul_f32 v32, 0xbfb8aa3b, v24\n"
"v_mul_f32 v33, 0xbfb8aa3b, v25\n"
"v_exp_f32 v26, v26\n"
"v_exp_f32 v27, v27\n"
"v_exp_f32 v28, v28\n"
"v_exp_f32 v29, v29\n"
"v_exp_f32 v30, v30\n"
"v_exp_f32 v31, v31\n"
"v_exp_f32 v32, v32\n"
"v_exp_f32 v33, v33\n"
"s_nop 0\n"
"v_add_f32 v26, 1.0, v26\n"
"v_add_f32 v27, 1.0, v27\n"
"v_add_f32 v28, 1.0, v28\n"
"v_add_f32 v29, 1.0, v29\n"
"v_add_f32 v30, 1.0, v30\n"
"v_add_f32 v31, 1.0, v31\n"
"v_add_f32 v32, 1.0, v32\n"
"v_add_f32 v33, 1.0, v33\n"
"v_rcp_f32 v26, v26\n"
"v_rcp_f32 v27, v27\n"
"v_rcp_f32 v28, v28\n"
"v_rcp_f32 v29, v29\n"
"v_rcp_f32 v30, v30\n"
"v_rcp_f32 v31, v31\n"
"v_rcp_f32 v32, v32\n"
"v_rcp_f32 v33, v33\n"
"s_nop 0\n"
"v_pk_mul_f32 v[96:97], v[18:19], v[26:27]\n"
"v_pk_mul_f32 v[98:99], v[20:21], v[28:29]\n"
"v_pk_mul_f32 v[100:101], v[22:23], v[30:31]\n"
"v_pk_mul_f32 v[102:103], v[24:25], v[32:33]\n"
"v_dot2_f32_bf16 v18, v144, v8, 0\n"
"v_dot2_f32_bf16 v19, v144, v9, 0\n"
"v_dot2_f32_bf16 v20, v145, v8, 0\n"
"v_dot2_f32_bf16 v21, v145, v9, 0\n"
"v_dot2_f32_bf16 v22, v146, v8, 0\n"
"v_dot2_f32_bf16 v23, v146, v9, 0\n"
"v_dot2_f32_bf16 v24, v147, v8, 0\n"
"v_dot2_f32_bf16 v25, v147, v9, 0\n"
"v_dot2_f32_bf16 v18, v148, v8, v18\n"
"v_dot2_f32_bf16 v19, v148, v9, v19\n"
"v_dot2_f32_bf16 v20, v149, v8, v20\n"
"v_dot2_f32_bf16 v21, v149, v9, v21\n"
"v_dot2_f32_bf16 v22, v150, v8, v22\n"
"v_dot2_f32_bf16 v23, v150, v9, v23\n"
"v_dot2_f32_bf16 v24, v151, v8, v24\n"
"v_dot2_f32_bf16 v25, v151, v9, v25\n"
"v_dot2_f32_bf16 v18, v152, v8, v18\n"
"v_dot2_f32_bf16 v19, v152, v9, v19\n"
"v_dot2_f32_bf16 v20, v153, v8, v20\n"
"v_dot2_f32_bf16 v21, v153, v9, v21\n"
"v_dot2_f32_bf16 v22, v154, v8, v22\n"
"v_dot2_f32_bf16 v23, v154, v9, v23\n"
"v_dot2_f32_bf16 v24, v155, v8, v24\n"
"v_dot2_f32_bf16 v25, v155, v9, v25\n"
"v_mul_f32 v26, 0xbfb8aa3b, v18\n"
"v_mul_f32 v27, 0xbfb8aa3b, v19\n"
"v_mul_f32 v28, 0xbfb8aa3b, v20\n"
"v_mul_f32 v29, 0xbfb8aa3b, v21\n"
"v_mul_f32 v30, 0xbfb8aa3b, v22\n"
"v_mul_f32 v31, 0xbfb8aa3b, v23\n"
"v_mul_f32 v32, 0xbfb8aa3b, v24\n"
"v_mul_f32 v33, 0xbfb8aa3b, v25\n"
"v_exp_f32 v26, v26\n"
"v_exp_f32 v27, v27\n"
"v_exp_f32 v28, v28\n"
"v_exp_f32 v29, v29\n"
"v_exp_f32 v30, v30\n"
"v_exp_f32 v31, v31\n"
"v_exp_f32 v32, v32\n"
"v_exp_f32 v33, v33\n"
"s_nop 0\n"
"v_add_f32 v26, 1.0, v26\n"
"v_add_f32 v27, 1.0, v27\n"
"v_add_f32 v28, 1.0, v28\n"
"v_add_f32 v29, 1.0, v29\n"
"v_add_f32 v30, 1.0, v30\n"
"v_add_f32 v31, 1.0, v31\n"
"v_add_f32 v32, 1.0, v32\n"
"v_add_f32 v33, 1.0, v33\n"
"v_rcp_f32 v26, v26\n"
"v_rcp_f32 v27, v27\n"
"v_rcp_f32 v28, v28\n"
"v_rcp_f32 v29, v29\n"
"v_rcp_f32 v30, v30\n"
"v_rcp_f32 v31, v31\n"
"v_rcp_f32 v32, v32\n"
"v_rcp_f32 v33, v33\n"
"s_nop 0\n"
"v_pk_mul_f32 v[144:145], v[18:19], v[26:27]\n"
"v_pk_mul_f32 v[146:147], v[20:21], v[28:29]\n"
"v_pk_mul_f32 v[148:149], v[22:23], v[30:31]\n"
"v_pk_mul_f32 v[150:151], v[24:25], v[32:33]\n"
"v_dot2_f32_bf16 v18, v192, v8, 0\n"
"v_dot2_f32_bf16 v19, v192, v9, 0\n"
"v_dot2_f32_bf16 v20, v193, v8, 0\n"
"v_dot2_f32_bf16 v21, v193, v9, 0\n"
"v_dot2_f32_bf16 v22, v194, v8, 0\n"
"v_dot2_f32_bf16 v23, v194, v9, 0\n"
"v_dot2_f32_bf16 v24, v195, v8, 0\n"
"v_dot2_f32_bf16 v25, v195, v9, 0\n"
"v_dot2_f32_bf16 v18, v196, v8, v18\n"
"v_dot2_f32_bf16 v19, v196, v9, v19\n"
"v_dot2_f32_bf16 v20, v197, v8, v20\n"
"v_dot2_f32_bf16 v21, v197, v9, v21\n"
"v_dot2_f32_bf16 v22, v198, v8, v22\n"
"v_dot2_f32_bf16 v23, v198, v9, v23\n"
"v_dot2_f32_bf16 v24, v199, v8, v24\n"
"v_dot2_f32_bf16 v25, v199, v9, v25\n"
"v_dot2_f32_bf16 v18, v200, v8, v18\n"
"v_dot2_f32_bf16 v19, v200, v9, v19\n"
"v_dot2_f32_bf16 v20, v201, v8, v20\n"
"v_dot2_f32_bf16 v21, v201, v9, v21\n"
"v_dot2_f32_bf16 v22, v202, v8, v22\n"
"v_dot2_f32_bf16 v23, v202, v9, v23\n"
"v_dot2_f32_bf16 v24, v203, v8, v24\n"
"v_dot2_f32_bf16 v25, v203, v9, v25\n"
"v_mul_f32 v26, 0xbfb8aa3b, v18\n"
"v_mul_f32 v27, 0xbfb8aa3b, v19\n"
"v_mul_f32 v28, 0xbfb8aa3b, v20\n"
"v_mul_f32 v29, 0xbfb8aa3b, v21\n"
"v_mul_f32 v30, 0xbfb8aa3b, v22\n"
"v_mul_f32 v31, 0xbfb8aa3b, v23\n"
"v_mul_f32 v32, 0xbfb8aa3b, v24\n"
"v_mul_f32 v33, 0xbfb8aa3b, v25\n"
"v_exp_f32 v26, v26\n"
"v_exp_f32 v27, v27\n"
"v_exp_f32 v28, v28\n"
"v_exp_f32 v29, v29\n"
"v_exp_f32 v30, v30\n"
"v_exp_f32 v31, v31\n"
"v_exp_f32 v32, v32\n"
"v_exp_f32 v33, v33\n"
"s_nop 0\n"
"v_add_f32 v26, 1.0, v26\n"
"v_add_f32 v27, 1.0, v27\n"
"v_add_f32 v28, 1.0, v28\n"
"v_add_f32 v29, 1.0, v29\n"
"v_add_f32 v30, 1.0, v30\n"
"v_add_f32 v31, 1.0, v31\n"
"v_add_f32 v32, 1.0, v32\n"
"v_add_f32 v33, 1.0, v33\n"
"v_rcp_f32 v26, v26\n"
"v_rcp_f32 v27, v27\n"
"v_rcp_f32 v28, v28\n"
"v_rcp_f32 v29, v29\n"
"v_rcp_f32 v30, v30\n"
"v_rcp_f32 v31, v31\n"
"v_rcp_f32 v32, v32\n"
"v_rcp_f32 v33, v33\n"
"s_nop 0\n"
"v_pk_mul_f32 v[192:193], v[18:19], v[26:27]\n"
"v_pk_mul_f32 v[194:195], v[20:21], v[28:29]\n"
"v_pk_mul_f32 v[196:197], v[22:23], v[30:31]\n"
"v_pk_mul_f32 v[198:199], v[24:25], v[32:33]\n"
"s_mov_b32 s47, 131072\n"
"s_mov_b32 exec_lo, s90\n"
"s_mov_b32 exec_hi, 0\n"
"LPOLLA_%=:\n"
"global_load_dwordx2 v[10:11], v2, s[88:89] offset:256 sc1\n"
"s_waitcnt vmcnt(0)\n"
"v_cmp_ne_u64 vcc, v[10:11], v[4:5]\n"
"s_nop 4\n"
"s_cbranch_vccz LPOKA_%=\n"
"s_sleep 1\n"
"s_sub_u32 s47, s47, 1\n"
"s_cmp_lg_u32 s47, 0\n"
"s_cbranch_scc1 LPOLLA_%=\n"
"LPOKA_%=:\n"
"s_mov_b64 exec, 0xff\n"
"global_store_dwordx2 v2, v[4:5], s[88:89] offset:512 sc1\n"
"s_mov_b64 exec, -1\n"
"s_mov_b32 s50, 0\n"
"buffer_load_dwordx4 v[64:67], v7, s[24:27], s50 offen offset:0 sc1\n"
"s_mov_b32 s50, 2162688\n"
"buffer_load_dwordx4 v[68:71], v7, s[24:27], s50 offen offset:0 sc1\n"
"s_mov_b32 s50, 4325376\n"
"buffer_load_dwordx4 v[72:75], v7, s[24:27], s50 offen offset:0 sc1\n"
"s_mov_b32 s50, 6488064\n"
"buffer_load_dwordx4 v[76:79], v7, s[24:27], s50 offen offset:0 sc1\n"
"s_mov_b32 s50, 8650752\n"
"buffer_load_dwordx4 v[80:83], v7, s[24:27], s50 offen offset:0 sc1\n"
"s_mov_b32 s50, 10813440\n"
"buffer_load_dwordx4 v[84:87], v7, s[24:27], s50 offen offset:0 sc1\n"
"s_mov_b32 s50, 12976128\n"
"buffer_load_dwordx4 v[88:91], v7, s[24:27], s50 offen offset:0 sc1\n"
"s_mov_b32 s50, 15138816\n"
"buffer_load_dwordx4 v[92:95], v7, s[24:27], s50 offen offset:0 sc1\n"
"s_mov_b32 s50, 0\n"
"buffer_load_dwordx4 v[112:115], v7, s[24:27], s50 offen offset:1024 sc1\n"
"s_mov_b32 s50, 2162688\n"
"buffer_load_dwordx4 v[116:119], v7, s[24:27], s50 offen offset:1024 sc1\n"
"s_mov_b32 s50, 4325376\n"
"buffer_load_dwordx4 v[120:123], v7, s[24:27], s50 offen offset:1024 sc1\n"
"s_mov_b32 s50, 6488064\n"
"buffer_load_dwordx4 v[124:127], v7, s[24:27], s50 offen offset:1024 sc1\n"
"s_mov_b32 s50, 8650752\n"
"buffer_load_dwordx4 v[128:131], v7, s[24:27], s50 offen offset:1024 sc1\n"
"s_mov_b32 s50, 10813440\n"
"buffer_load_dwordx4 v[132:135], v7, s[24:27], s50 offen offset:1024 sc1\n"
"s_mov_b32 s50, 12976128\n"
"buffer_load_dwordx4 v[136:139], v7, s[24:27], s50 offen offset:1024 sc1\n"
"s_mov_b32 s50, 15138816\n"
"buffer_load_dwordx4 v[140:143], v7, s[24:27], s50 offen offset:1024 sc1\n"
"s_mov_b32 s50, 0\n"
"buffer_load_dwordx4 v[160:163], v7, s[24:27], s50 offen offset:2048 sc1\n"
"s_mov_b32 s50, 2162688\n"
"buffer_load_dwordx4 v[164:167], v7, s[24:27], s50 offen offset:2048 sc1\n"
"s_mov_b32 s50, 4325376\n"
"buffer_load_dwordx4 v[168:171], v7, s[24:27], s50 offen offset:2048 sc1\n"
"s_mov_b32 s50, 6488064\n"
"buffer_load_dwordx4 v[172:175], v7, s[24:27], s50 offen offset:2048 sc1\n"
"s_mov_b32 s50, 8650752\n"
"buffer_load_dwordx4 v[176:179], v7, s[24:27], s50 offen offset:2048 sc1\n"
"s_mov_b32 s50, 10813440\n"
"buffer_load_dwordx4 v[180:183], v7, s[24:27], s50 offen offset:2048 sc1\n"
"s_mov_b32 s50, 12976128\n"
"buffer_load_dwordx4 v[184:187], v7, s[24:27], s50 offen offset:2048 sc1\n"
"s_mov_b32 s50, 15138816\n"
"buffer_load_dwordx4 v[188:191], v7, s[24:27], s50 offen offset:2048 sc1\n"
"s_waitcnt vmcnt(16)\n"
"v_dot2_f32_bf16 v10, v64, v8, 0\n"
"v_dot2_f32_bf16 v11, v64, v9, 0\n"
"v_dot2_f32_bf16 v12, v65, v8, 0\n"
"v_dot2_f32_bf16 v13, v65, v9, 0\n"
"v_dot2_f32_bf16 v14, v66, v8, 0\n"
"v_dot2_f32_bf16 v15, v66, v9, 0\n"
"v_dot2_f32_bf16 v16, v67, v8, 0\n"
"v_dot2_f32_bf16 v17, v67, v9, 0\n"
"v_dot2_f32_bf16 v10, v68, v8, v10\n"
"v_dot2_f32_bf16 v11, v68, v9, v11\n"
"v_dot2_f32_bf16 v12, v69, v8, v12\n"
"v_dot2_f32_bf16 v13, v69, v9, v13\n"
"v_dot2_f32_bf16 v14, v70, v8, v14\n"
"v_dot2_f32_bf16 v15, v70, v9, v15\n"
"v_dot2_f32_bf16 v16, v71, v8, v16\n"
"v_dot2_f32_bf16 v17, v71, v9, v17\n"
"v_dot2_f32_bf16 v10, v72, v8, v10\n"
"v_dot2_f32_bf16 v11, v72, v9, v11\n"
"v_dot2_f32_bf16 v12, v73, v8, v12\n"
"v_dot2_f32_bf16 v13, v73, v9, v13\n"
"v_dot2_f32_bf16 v14, v74, v8, v14\n"
"v_dot2_f32_bf16 v15, v74, v9, v15\n"
"v_dot2_f32_bf16 v16, v75, v8, v16\n"
"v_dot2_f32_bf16 v17, v75, v9, v17\n"
"v_dot2_f32_bf16 v10, v76, v8, v10\n"
"v_dot2_f32_bf16 v11, v76, v9, v11\n"
"v_dot2_f32_bf16 v12, v77, v8, v12\n"
"v_dot2_f32_bf16 v13, v77, v9, v13\n"
"v_dot2_f32_bf16 v14, v78, v8, v14\n"
"v_dot2_f32_bf16 v15, v78, v9, v15\n"
"v_dot2_f32_bf16 v16, v79, v8, v16\n"
"v_dot2_f32_bf16 v17, v79, v9, v17\n"
"v_dot2_f32_bf16 v10, v80, v8, v10\n"
"v_dot2_f32_bf16 v11, v80, v9, v11\n"
"v_dot2_f32_bf16 v12, v81, v8, v12\n"
"v_dot2_f32_bf16 v13, v81, v9, v13\n"
"v_dot2_f32_bf16 v14, v82, v8, v14\n"
"v_dot2_f32_bf16 v15, v82, v9, v15\n"
"v_dot2_f32_bf16 v16, v83, v8, v16\n"
"v_dot2_f32_bf16 v17, v83, v9, v17\n"
"v_dot2_f32_bf16 v10, v84, v8, v10\n"
"v_dot2_f32_bf16 v11, v84, v9, v11\n"
"v_dot2_f32_bf16 v12, v85, v8, v12\n"
"v_dot2_f32_bf16 v13, v85, v9, v13\n"
"v_dot2_f32_bf16 v14, v86, v8, v14\n"
"v_dot2_f32_bf16 v15, v86, v9, v15\n"
"v_dot2_f32_bf16 v16, v87, v8, v16\n"
"v_dot2_f32_bf16 v17, v87, v9, v17\n"
"v_dot2_f32_bf16 v10, v88, v8, v10\n"
"v_dot2_f32_bf16 v11, v88, v9, v11\n"
"v_dot2_f32_bf16 v12, v89, v8, v12\n"
"v_dot2_f32_bf16 v13, v89, v9, v13\n"
"v_dot2_f32_bf16 v14, v90, v8, v14\n"
"v_dot2_f32_bf16 v15, v90, v9, v15\n"
"v_dot2_f32_bf16 v16, v91, v8, v16\n"
"v_dot2_f32_bf16 v17, v91, v9, v17\n"
"v_dot2_f32_bf16 v10, v92, v8, v10\n"
"v_dot2_f32_bf16 v11, v92, v9, v11\n"
"v_dot2_f32_bf16 v12, v93, v8, v12\n"
"v_dot2_f32_bf16 v13, v93, v9, v13\n"
"v_dot2_f32_bf16 v14, v94, v8, v14\n"
"v_dot2_f32_bf16 v15, v94, v9, v15\n"
"v_dot2_f32_bf16 v16, v95, v8, v16\n"
"v_dot2_f32_bf16 v17, v95, v9, v17\n"
"v_lshlrev_b32 v232, 16, v40\n"
"v_and_b32 v233, 0xffff0000, v40\n"
"v_lshlrev_b32 v234, 16, v41\n"
"v_and_b32 v235, 0xffff0000, v41\n"
"v_lshlrev_b32 v236, 16, v42\n"
"v_and_b32 v237, 0xffff0000, v42\n"
"v_lshlrev_b32 v238, 16, v43\n"
"v_and_b32 v239, 0xffff0000, v43\n"
"v_pk_fma_f32 v[232:233], v[10:11], v[96:97], v[232:233]\n"
"v_pk_fma_f32 v[234:235], v[12:13], v[98:99], v[234:235]\n"
"v_pk_fma_f32 v[236:237], v[14:15], v[100:101], v[236:237]\n"
"v_pk_fma_f32 v[238:239], v[16:17], v[102:103], v[238:239]\n"
"v_cvt_pk_bf16_f32 v240, v232, v233\n"
"v_cvt_pk_bf16_f32 v241, v234, v235\n"
"v_cvt_pk_bf16_f32 v242, v236, v237\n"
"v_cvt_pk_bf16_f32 v243, v238, v239\n"
"s_add_u32 s50, s48, 0\n"
"buffer_store_dwordx2 v[240:241], v52, s[28:31], s50 offen\n"
"buffer_store_dwordx2 v[242:243], v52, s[28:31], s50 offen offset:32\n"
"s_waitcnt vmcnt(10)\n"
"v_dot2_f32_bf16 v10, v112, v8, 0\n"
"v_dot2_f32_bf16 v11, v112, v9, 0\n"
"v_dot2_f32_bf16 v12, v113, v8, 0\n"
"v_dot2_f32_bf16 v13, v113, v9, 0\n"
"v_dot2_f32_bf16 v14, v114, v8, 0\n"
"v_dot2_f32_bf16 v15, v114, v9, 0\n"
"v_dot2_f32_bf16 v16, v115, v8, 0\n"
"v_dot2_f32_bf16 v17, v115, v9, 0\n"
"v_dot2_f32_bf16 v10, v116, v8, v10\n"
"v_dot2_f32_bf16 v11, v116, v9, v11\n"
"v_dot2_f32_bf16 v12, v117, v8, v12\n"
"v_dot2_f32_bf16 v13, v117, v9, v13\n"
"v_dot2_f32_bf16 v14, v118, v8, v14\n"
"v_dot2_f32_bf16 v15, v118, v9, v15\n"
"v_dot2_f32_bf16 v16, v119, v8, v16\n"
"v_dot2_f32_bf16 v17, v119, v9, v17\n"
"v_dot2_f32_bf16 v10, v120, v8, v10\n"
"v_dot2_f32_bf16 v11, v120, v9, v11\n"
"v_dot2_f32_bf16 v12, v121, v8, v12\n"
"v_dot2_f32_bf16 v13, v121, v9, v13\n"
"v_dot2_f32_bf16 v14, v122, v8, v14\n"
"v_dot2_f32_bf16 v15, v122, v9, v15\n"
"v_dot2_f32_bf16 v16, v123, v8, v16\n"
"v_dot2_f32_bf16 v17, v123, v9, v17\n"
"v_dot2_f32_bf16 v10, v124, v8, v10\n"
"v_dot2_f32_bf16 v11, v124, v9, v11\n"
"v_dot2_f32_bf16 v12, v125, v8, v12\n"
"v_dot2_f32_bf16 v13, v125, v9, v13\n"
"v_dot2_f32_bf16 v14, v126, v8, v14\n"
"v_dot2_f32_bf16 v15, v126, v9, v15\n"
"v_dot2_f32_bf16 v16, v127, v8, v16\n"
"v_dot2_f32_bf16 v17, v127, v9, v17\n"
"v_dot2_f32_bf16 v10, v128, v8, v10\n"
"v_dot2_f32_bf16 v11, v128, v9, v11\n"
"v_dot2_f32_bf16 v12, v129, v8, v12\n"
"v_dot2_f32_bf16 v13, v129, v9, v13\n"
"v_dot2_f32_bf16 v14, v130, v8, v14\n"
"v_dot2_f32_bf16 v15, v130, v9, v15\n"
"v_dot2_f32_bf16 v16, v131, v8, v16\n"
"v_dot2_f32_bf16 v17, v131, v9, v17\n"
"v_dot2_f32_bf16 v10, v132, v8, v10\n"
"v_dot2_f32_bf16 v11, v132, v9, v11\n"
"v_dot2_f32_bf16 v12, v133, v8, v12\n"
"v_dot2_f32_bf16 v13, v133, v9, v13\n"
"v_dot2_f32_bf16 v14, v134, v8, v14\n"
"v_dot2_f32_bf16 v15, v134, v9, v15\n"
"v_dot2_f32_bf16 v16, v135, v8, v16\n"
"v_dot2_f32_bf16 v17, v135, v9, v17\n"
"v_dot2_f32_bf16 v10, v136, v8, v10\n"
"v_dot2_f32_bf16 v11, v136, v9, v11\n"
"v_dot2_f32_bf16 v12, v137, v8, v12\n"
"v_dot2_f32_bf16 v13, v137, v9, v13\n"
"v_dot2_f32_bf16 v14, v138, v8, v14\n"
"v_dot2_f32_bf16 v15, v138, v9, v15\n"
"v_dot2_f32_bf16 v16, v139, v8, v16\n"
"v_dot2_f32_bf16 v17, v139, v9, v17\n"
"v_dot2_f32_bf16 v10, v140, v8, v10\n"
"v_dot2_f32_bf16 v11, v140, v9, v11\n"
"v_dot2_f32_bf16 v12, v141, v8, v12\n"
"v_dot2_f32_bf16 v13, v141, v9, v13\n"
"v_dot2_f32_bf16 v14, v142, v8, v14\n"
"v_dot2_f32_bf16 v15, v142, v9, v15\n"
"v_dot2_f32_bf16 v16, v143, v8, v16\n"
"v_dot2_f32_bf16 v17, v143, v9, v17\n"
"v_lshlrev_b32 v232, 16, v44\n"
"v_and_b32 v233, 0xffff0000, v44\n"
"v_lshlrev_b32 v234, 16, v45\n"
"v_and_b32 v235, 0xffff0000, v45\n"
"v_lshlrev_b32 v236, 16, v46\n"
"v_and_b32 v237, 0xffff0000, v46\n"
"v_lshlrev_b32 v238, 16, v47\n"
"v_and_b32 v239, 0xffff0000, v47\n"
"v_pk_fma_f32 v[232:233], v[10:11], v[144:145], v[232:233]\n"
"v_pk_fma_f32 v[234:235], v[12:13], v[146:147], v[234:235]\n"
"v_pk_fma_f32 v[236:237], v[14:15], v[148:149], v[236:237]\n"
"v_pk_fma_f32 v[238:239], v[16:17], v[150:151], v[238:239]\n"
"v_cvt_pk_bf16_f32 v244, v232, v233\n"
"v_cvt_pk_bf16_f32 v245, v234, v235\n"
"v_cvt_pk_bf16_f32 v246, v236, v237\n"
"v_cvt_pk_bf16_f32 v247, v238, v239\n"
"s_add_u32 s50, s48, 16384\n"
"buffer_store_dwordx2 v[244:245], v52, s[28:31], s50 offen\n"
"buffer_store_dwordx2 v[246:247], v52, s[28:31], s50 offen offset:32\n"
"s_waitcnt vmcnt(4)\n"
"v_dot2_f32_bf16 v10, v160, v8, 0\n"
"v_dot2_f32_bf16 v11, v160, v9, 0\n"
"v_dot2_f32_bf16 v12, v161, v8, 0\n"
"v_dot2_f32_bf16 v13, v161, v9, 0\n"
"v_dot2_f32_bf16 v14, v162, v8, 0\n"
"v_dot2_f32_bf16 v15, v162, v9, 0\n"
"v_dot2_f32_bf16 v16, v163, v8, 0\n"
"v_dot2_f32_bf16 v17, v163, v9, 0\n"
"v_dot2_f32_bf16 v10, v164, v8, v10\n"
"v_dot2_f32_bf16 v11, v164, v9, v11\n"
"v_dot2_f32_bf16 v12, v165, v8, v12\n"
"v_dot2_f32_bf16 v13, v165, v9, v13\n"
"v_dot2_f32_bf16 v14, v166, v8, v14\n"
"v_dot2_f32_bf16 v15, v166, v9, v15\n"
"v_dot2_f32_bf16 v16, v167, v8, v16\n"
"v_dot2_f32_bf16 v17, v167, v9, v17\n"
"v_dot2_f32_bf16 v10, v168, v8, v10\n"
"v_dot2_f32_bf16 v11, v168, v9, v11\n"
"v_dot2_f32_bf16 v12, v169, v8, v12\n"
"v_dot2_f32_bf16 v13, v169, v9, v13\n"
"v_dot2_f32_bf16 v14, v170, v8, v14\n"
"v_dot2_f32_bf16 v15, v170, v9, v15\n"
"v_dot2_f32_bf16 v16, v171, v8, v16\n"
"v_dot2_f32_bf16 v17, v171, v9, v17\n"
"v_dot2_f32_bf16 v10, v172, v8, v10\n"
"v_dot2_f32_bf16 v11, v172, v9, v11\n"
"v_dot2_f32_bf16 v12, v173, v8, v12\n"
"v_dot2_f32_bf16 v13, v173, v9, v13\n"
"v_dot2_f32_bf16 v14, v174, v8, v14\n"
"v_dot2_f32_bf16 v15, v174, v9, v15\n"
"v_dot2_f32_bf16 v16, v175, v8, v16\n"
"v_dot2_f32_bf16 v17, v175, v9, v17\n"
"v_dot2_f32_bf16 v10, v176, v8, v10\n"
"v_dot2_f32_bf16 v11, v176, v9, v11\n"
"v_dot2_f32_bf16 v12, v177, v8, v12\n"
"v_dot2_f32_bf16 v13, v177, v9, v13\n"
"v_dot2_f32_bf16 v14, v178, v8, v14\n"
"v_dot2_f32_bf16 v15, v178, v9, v15\n"
"v_dot2_f32_bf16 v16, v179, v8, v16\n"
"v_dot2_f32_bf16 v17, v179, v9, v17\n"
"v_dot2_f32_bf16 v10, v180, v8, v10\n"
"v_dot2_f32_bf16 v11, v180, v9, v11\n"
"v_dot2_f32_bf16 v12, v181, v8, v12\n"
"v_dot2_f32_bf16 v13, v181, v9, v13\n"
"v_dot2_f32_bf16 v14, v182, v8, v14\n"
"v_dot2_f32_bf16 v15, v182, v9, v15\n"
"v_dot2_f32_bf16 v16, v183, v8, v16\n"
"v_dot2_f32_bf16 v17, v183, v9, v17\n"
"v_dot2_f32_bf16 v10, v184, v8, v10\n"
"v_dot2_f32_bf16 v11, v184, v9, v11\n"
"v_dot2_f32_bf16 v12, v185, v8, v12\n"
"v_dot2_f32_bf16 v13, v185, v9, v13\n"
"v_dot2_f32_bf16 v14, v186, v8, v14\n"
"v_dot2_f32_bf16 v15, v186, v9, v15\n"
"v_dot2_f32_bf16 v16, v187, v8, v16\n"
"v_dot2_f32_bf16 v17, v187, v9, v17\n"
"v_dot2_f32_bf16 v10, v188, v8, v10\n"
"v_dot2_f32_bf16 v11, v188, v9, v11\n"
"v_dot2_f32_bf16 v12, v189, v8, v12\n"
"v_dot2_f32_bf16 v13, v189, v9, v13\n"
"v_dot2_f32_bf16 v14, v190, v8, v14\n"
"v_dot2_f32_bf16 v15, v190, v9, v15\n"
"v_dot2_f32_bf16 v16, v191, v8, v16\n"
"v_dot2_f32_bf16 v17, v191, v9, v17\n"
"v_lshlrev_b32 v232, 16, v48\n"
"v_and_b32 v233, 0xffff0000, v48\n"
"v_lshlrev_b32 v234, 16, v49\n"
"v_and_b32 v235, 0xffff0000, v49\n"
"v_lshlrev_b32 v236, 16, v50\n"
"v_and_b32 v237, 0xffff0000, v50\n"
"v_lshlrev_b32 v238, 16, v51\n"
"v_and_b32 v239, 0xffff0000, v51\n"
"v_pk_fma_f32 v[232:233], v[10:11], v[192:193], v[232:233]\n"
"v_pk_fma_f32 v[234:235], v[12:13], v[194:195], v[234:235]\n"
"v_pk_fma_f32 v[236:237], v[14:15], v[196:197], v[236:237]\n"
"v_pk_fma_f32 v[238:239], v[16:17], v[198:199], v[238:239]\n"
"v_cvt_pk_bf16_f32 v248, v232, v233\n"
"v_cvt_pk_bf16_f32 v249, v234, v235\n"
"v_cvt_pk_bf16_f32 v250, v236, v237\n"
"v_cvt_pk_bf16_f32 v251, v238, v239\n"
"s_add_u32 s50, s48, 32768\n"
"buffer_store_dwordx2 v[248:249], v52, s[28:31], s50 offen\n"
"buffer_store_dwordx2 v[250:251], v52, s[28:31], s50 offen offset:32\n"
"s_branch LPX_%=\n"
"LPA_%=:\n"
"s_mov_b64 exec, 0xff\n"
"global_store_dwordx2 v2, v[4:5], s[88:89] offset:512 sc1\n"
"s_mov_b64 exec, -1\n"
"s_mov_b32 s50, 0\n"
"buffer_load_dwordx4 v[64:67], v7, s[24:27], s50 offen offset:0 sc1\n"
"s_mov_b32 s50, 2162688\n"
"buffer_load_dwordx4 v[68:71], v7, s[24:27], s50 offen offset:0 sc1\n"
"s_mov_b32 s50, 4325376\n"
"buffer_load_dwordx4 v[72:75], v7, s[24:27], s50 offen offset:0 sc1\n"
"s_mov_b32 s50, 6488064\n"
"buffer_load_dwordx4 v[76:79], v7, s[24:27], s50 offen offset:0 sc1\n"
"s_mov_b32 s50, 8650752\n"
"buffer_load_dwordx4 v[80:83], v7, s[24:27], s50 offen offset:0 sc1\n"
"s_mov_b32 s50, 10813440\n"
"buffer_load_dwordx4 v[84:87], v7, s[24:27], s50 offen offset:0 sc1\n"
"s_mov_b32 s50, 12976128\n"
"buffer_load_dwordx4 v[88:91], v7, s[24:27], s50 offen offset:0 sc1\n"
"s_mov_b32 s50, 15138816\n"
"buffer_load_dwordx4 v[92:95], v7, s[24:27], s50 offen offset:0 sc1\n"
"s_mov_b32 s50, 0\n"
"buffer_load_dwordx4 v[112:115], v7, s[24:27], s50 offen offset:1024 sc1\n"
"s_mov_b32 s50, 2162688\n"
"buffer_load_dwordx4 v[116:119], v7, s[24:27], s50 offen offset:1024 sc1\n"
"s_mov_b32 s50, 4325376\n"
"buffer_load_dwordx4 v[120:123], v7, s[24:27], s50 offen offset:1024 sc1\n"
"s_mov_b32 s50, 6488064\n"
"buffer_load_dwordx4 v[124:127], v7, s[24:27], s50 offen offset:1024 sc1\n"
"s_mov_b32 s50, 8650752\n"
"buffer_load_dwordx4 v[128:131], v7, s[24:27], s50 offen offset:1024 sc1\n"
"s_mov_b32 s50, 10813440\n"
"buffer_load_dwordx4 v[132:135], v7, s[24:27], s50 offen offset:1024 sc1\n"
"s_mov_b32 s50, 12976128\n"
"buffer_load_dwordx4 v[136:139], v7, s[24:27], s50 offen offset:1024 sc1\n"
"s_mov_b32 s50, 15138816\n"
"buffer_load_dwordx4 v[140:143], v7, s[24:27], s50 offen offset:1024 sc1\n"
"s_mov_b32 s50, 0\n"
"buffer_load_dwordx4 v[160:163], v7, s[24:27], s50 offen offset:2048 sc1\n"
"s_mov_b32 s50, 2162688\n"
"buffer_load_dwordx4 v[164:167], v7, s[24:27], s50 offen offset:2048 sc1\n"
"s_mov_b32 s50, 4325376\n"
"buffer_load_dwordx4 v[168:171], v7, s[24:27], s50 offen offset:2048 sc1\n"
"s_mov_b32 s50, 6488064\n"
"buffer_load_dwordx4 v[172:175], v7, s[24:27], s50 offen offset:2048 sc1\n"
"s_mov_b32 s50, 8650752\n"
"buffer_load_dwordx4 v[176:179], v7, s[24:27], s50 offen offset:2048 sc1\n"
"s_mov_b32 s50, 10813440\n"
"buffer_load_dwordx4 v[180:183], v7, s[24:27], s50 offen offset:2048 sc1\n"
"s_mov_b32 s50, 12976128\n"
"buffer_load_dwordx4 v[184:187], v7, s[24:27], s50 offen offset:2048 sc1\n"
"s_mov_b32 s50, 15138816\n"
"buffer_load_dwordx4 v[188:191], v7, s[24:27], s50 offen offset:2048 sc1\n"
"s_mov_b32 s47, 131072\n"
"s_mov_b64 exec, 0x700\n"
"LPOLLF_%=:\n"
"global_load_dwordx2 v[10:11], v2, s[88:89] offset:256 sc1\n"
"s_waitcnt vmcnt(0)\n"
"v_cmp_ne_u64 vcc, v[10:11], v[4:5]\n"
"s_nop 4\n"
"s_cbranch_vccz LPOKF_%=\n"
"s_sleep 1\n"
"s_sub_u32 s47, s47, 1\n"
"s_cmp_lg_u32 s47, 0\n"
"s_cbranch_scc1 LPOLLF_%=\n"
"LPOKF_%=:\n"
"global_store_dwordx2 v2, v[4:5], s[88:89] offset:512 sc1\n"
"s_mov_b64 exec, -1\n"
"s_mov_b32 s50, 17301504\n"
"buffer_load_dwordx4 v[96:99], v7, s[24:27], s50 offen offset:0 sc1\n"
"s_mov_b32 s50, 19464192\n"
"buffer_load_dwordx4 v[100:103], v7, s[24:27], s50 offen offset:0 sc1\n"
"s_mov_b32 s50, 21626880\n"
"buffer_load_dwordx4 v[104:107], v7, s[24:27], s50 offen offset:0 sc1\n"
"s_mov_b32 s50, 17301504\n"
"buffer_load_dwordx4 v[144:147], v7, s[24:27], s50 offen offset:1024 sc1\n"
"s_mov_b32 s50, 19464192\n"
"buffer_load_dwordx4 v[148:151], v7, s[24:27], s50 offen offset:1024 sc1\n"
"s_mov_b32 s50, 21626880\n"
"buffer_load_dwordx4 v[152:155], v7, s[24:27], s50 offen offset:1024 sc1\n"
"s_mov_b32 s50, 17301504\n"
"buffer_load_dwordx4 v[192:195], v7, s[24:27], s50 offen offset:2048 sc1\n"
"s_mov_b32 s50, 19464192\n"
"buffer_load_dwordx4 v[196:199], v7, s[24:27], s50 offen offset:2048 sc1\n"
"s_mov_b32 s50, 21626880\n"
"buffer_load_dwordx4 v[200:203], v7, s[24:27], s50 offen offset:2048 sc1\n"
"v_dot2_f32_bf16 v208, v64, v8, 0\n"
"v_dot2_f32_bf16 v209, v64, v9, 0\n"
"v_dot2_f32_bf16 v210, v65, v8, 0\n"
"v_dot2_f32_bf16 v211, v65, v9, 0\n"
"v_dot2_f32_bf16 v212, v66, v8, 0\n"
"v_dot2_f32_bf16 v213, v66, v9, 0\n"
"v_dot2_f32_bf16 v214, v67, v8, 0\n"
"v_dot2_f32_bf16 v215, v67, v9, 0\n"
"v_dot2_f32_bf16 v208, v68, v8, v208\n"
"v_dot2_f32_bf16 v209, v68, v9, v209\n"
"v_dot2_f32_bf16 v210, v69, v8, v210\n"
"v_dot2_f32_bf16 v211, v69, v9, v211\n"
"v_dot2_f32_bf16 v212, v70, v8, v212\n"
"v_dot2_f32_bf16 v213, v70, v9, v213\n"
"v_dot2_f32_bf16 v214, v71, v8, v214\n"
"v_dot2_f32_bf16 v215, v71, v9, v215\n"
"v_dot2_f32_bf16 v208, v72, v8, v208\n"
"v_dot2_f32_bf16 v209, v72, v9, v209\n"
"v_dot2_f32_bf16 v210, v73, v8, v210\n"
"v_dot2_f32_bf16 v211, v73, v9, v211\n"
"v_dot2_f32_bf16 v212, v74, v8, v212\n"
"v_dot2_f32_bf16 v213, v74, v9, v213\n"
"v_dot2_f32_bf16 v214, v75, v8, v214\n"
"v_dot2_f32_bf16 v215, v75, v9, v215\n"
"v_dot2_f32_bf16 v208, v76, v8, v208\n"
"v_dot2_f32_bf16 v209, v76, v9, v209\n"
"v_dot2_f32_bf16 v210, v77, v8, v210\n"
"v_dot2_f32_bf16 v211, v77, v9, v211\n"
"v_dot2_f32_bf16 v212, v78, v8, v212\n"
"v_dot2_f32_bf16 v213, v78, v9, v213\n"
"v_dot2_f32_bf16 v214, v79, v8, v214\n"
"v_dot2_f32_bf16 v215, v79, v9, v215\n"
"v_dot2_f32_bf16 v208, v80, v8, v208\n"
"v_dot2_f32_bf16 v209, v80, v9, v209\n"
"v_dot2_f32_bf16 v210, v81, v8, v210\n"
"v_dot2_f32_bf16 v211, v81, v9, v211\n"
"v_dot2_f32_bf16 v212, v82, v8, v212\n"
"v_dot2_f32_bf16 v213, v82, v9, v213\n"
"v_dot2_f32_bf16 v214, v83, v8, v214\n"
"v_dot2_f32_bf16 v215, v83, v9, v215\n"
"v_dot2_f32_bf16 v208, v84, v8, v208\n"
"v_dot2_f32_bf16 v209, v84, v9, v209\n"
"v_dot2_f32_bf16 v210, v85, v8, v210\n"
"v_dot2_f32_bf16 v211, v85, v9, v211\n"
"v_dot2_f32_bf16 v212, v86, v8, v212\n"
"v_dot2_f32_bf16 v213, v86, v9, v213\n"
"v_dot2_f32_bf16 v214, v87, v8, v214\n"
"v_dot2_f32_bf16 v215, v87, v9, v215\n"
"v_dot2_f32_bf16 v208, v88, v8, v208\n"
"v_dot2_f32_bf16 v209, v88, v9, v209\n"
"v_dot2_f32_bf16 v210, v89, v8, v210\n"
"v_dot2_f32_bf16 v211, v89, v9, v211\n"
"v_dot2_f32_bf16 v212, v90, v8, v212\n"
"v_dot2_f32_bf16 v213, v90, v9, v213\n"
"v_dot2_f32_bf16 v214, v91, v8, v214\n"
"v_dot2_f32_bf16 v215, v91, v9, v215\n"
"v_dot2_f32_bf16 v208, v92, v8, v208\n"
"v_dot2_f32_bf16 v209, v92, v9, v209\n"
"v_dot2_f32_bf16 v210, v93, v8, v210\n"
"v_dot2_f32_bf16 v211, v93, v9, v211\n"
"v_dot2_f32_bf16 v212, v94, v8, v212\n"
"v_dot2_f32_bf16 v213, v94, v9, v213\n"
"v_dot2_f32_bf16 v214, v95, v8, v214\n"
"v_dot2_f32_bf16 v215, v95, v9, v215\n"
"v_dot2_f32_bf16 v216, v112, v8, 0\n"
"v_dot2_f32_bf16 v217, v112, v9, 0\n"
"v_dot2_f32_bf16 v218, v113, v8, 0\n"
"v_dot2_f32_bf16 v219, v113, v9, 0\n"
"v_dot2_f32_bf16 v220, v114, v8, 0\n"
"v_dot2_f32_bf16 v221, v114, v9, 0\n"
"v_dot2_f32_bf16 v222, v115, v8, 0\n"
"v_dot2_f32_bf16 v223, v115, v9, 0\n"
"v_dot2_f32_bf16 v216, v116, v8, v216\n"
"v_dot2_f32_bf16 v217, v116, v9, v217\n"
"v_dot2_f32_bf16 v218, v117, v8, v218\n"
"v_dot2_f32_bf16 v219, v117, v9, v219\n"
"v_dot2_f32_bf16 v220, v118, v8, v220\n"
"v_dot2_f32_bf16 v221, v118, v9, v221\n"
"v_dot2_f32_bf16 v222, v119, v8, v222\n"
"v_dot2_f32_bf16 v223, v119, v9, v223\n"
"v_dot2_f32_bf16 v216, v120, v8, v216\n"
"v_dot2_f32_bf16 v217, v120, v9, v217\n"
"v_dot2_f32_bf16 v218, v121, v8, v218\n"
"v_dot2_f32_bf16 v219, v121, v9, v219\n"
"v_dot2_f32_bf16 v220, v122, v8, v220\n"
"v_dot2_f32_bf16 v221, v122, v9, v221\n"
"v_dot2_f32_bf16 v222, v123, v8, v222\n"
"v_dot2_f32_bf16 v223, v123, v9, v223\n"
"v_dot2_f32_bf16 v216, v124, v8, v216\n"
"v_dot2_f32_bf16 v217, v124, v9, v217\n"
"v_dot2_f32_bf16 v218, v125, v8, v218\n"
"v_dot2_f32_bf16 v219, v125, v9, v219\n"
"v_dot2_f32_bf16 v220, v126, v8, v220\n"
"v_dot2_f32_bf16 v221, v126, v9, v221\n"
"v_dot2_f32_bf16 v222, v127, v8, v222\n"
"v_dot2_f32_bf16 v223, v127, v9, v223\n"
"v_dot2_f32_bf16 v216, v128, v8, v216\n"
"v_dot2_f32_bf16 v217, v128, v9, v217\n"
"v_dot2_f32_bf16 v218, v129, v8, v218\n"
"v_dot2_f32_bf16 v219, v129, v9, v219\n"
"v_dot2_f32_bf16 v220, v130, v8, v220\n"
"v_dot2_f32_bf16 v221, v130, v9, v221\n"
"v_dot2_f32_bf16 v222, v131, v8, v222\n"
"v_dot2_f32_bf16 v223, v131, v9, v223\n"
"v_dot2_f32_bf16 v216, v132, v8, v216\n"
"v_dot2_f32_bf16 v217, v132, v9, v217\n"
"v_dot2_f32_bf16 v218, v133, v8, v218\n"
"v_dot2_f32_bf16 v219, v133, v9, v219\n"
"v_dot2_f32_bf16 v220, v134, v8, v220\n"
"v_dot2_f32_bf16 v221, v134, v9, v221\n"
"v_dot2_f32_bf16 v222, v135, v8, v222\n"
"v_dot2_f32_bf16 v223, v135, v9, v223\n"
"v_dot2_f32_bf16 v216, v136, v8, v216\n"
"v_dot2_f32_bf16 v217, v136, v9, v217\n"
"v_dot2_f32_bf16 v218, v137, v8, v218\n"
"v_dot2_f32_bf16 v219, v137, v9, v219\n"
"v_dot2_f32_bf16 v220, v138, v8, v220\n"
"v_dot2_f32_bf16 v221, v138, v9, v221\n"
"v_dot2_f32_bf16 v222, v139, v8, v222\n"
"v_dot2_f32_bf16 v223, v139, v9, v223\n"
"v_dot2_f32_bf16 v216, v140, v8, v216\n"
"v_dot2_f32_bf16 v217, v140, v9, v217\n"
"v_dot2_f32_bf16 v218, v141, v8, v218\n"
"v_dot2_f32_bf16 v219, v141, v9, v219\n"
"v_dot2_f32_bf16 v220, v142, v8, v220\n"
"v_dot2_f32_bf16 v221, v142, v9, v221\n"
"v_dot2_f32_bf16 v222, v143, v8, v222\n"
"v_dot2_f32_bf16 v223, v143, v9, v223\n"
"v_dot2_f32_bf16 v224, v160, v8, 0\n"
"v_dot2_f32_bf16 v225, v160, v9, 0\n"
"v_dot2_f32_bf16 v226, v161, v8, 0\n"
"v_dot2_f32_bf16 v227, v161, v9, 0\n"
"v_dot2_f32_bf16 v228, v162, v8, 0\n"
"v_dot2_f32_bf16 v229, v162, v9, 0\n"
"v_dot2_f32_bf16 v230, v163, v8, 0\n"
"v_dot2_f32_bf16 v231, v163, v9, 0\n"
"v_dot2_f32_bf16 v224, v164, v8, v224\n"
"v_dot2_f32_bf16 v225, v164, v9, v225\n"
"v_dot2_f32_bf16 v226, v165, v8, v226\n"
"v_dot2_f32_bf16 v227, v165, v9, v227\n"
"v_dot2_f32_bf16 v228, v166, v8, v228\n"
"v_dot2_f32_bf16 v229, v166, v9, v229\n"
"v_dot2_f32_bf16 v230, v167, v8, v230\n"
"v_dot2_f32_bf16 v231, v167, v9, v231\n"
"v_dot2_f32_bf16 v224, v168, v8, v224\n"
"v_dot2_f32_bf16 v225, v168, v9, v225\n"
"v_dot2_f32_bf16 v226, v169, v8, v226\n"
"v_dot2_f32_bf16 v227, v169, v9, v227\n"
"v_dot2_f32_bf16 v228, v170, v8, v228\n"
"v_dot2_f32_bf16 v229, v170, v9, v229\n"
"v_dot2_f32_bf16 v230, v171, v8, v230\n"
"v_dot2_f32_bf16 v231, v171, v9, v231\n"
"v_dot2_f32_bf16 v224, v172, v8, v224\n"
"v_dot2_f32_bf16 v225, v172, v9, v225\n"
"v_dot2_f32_bf16 v226, v173, v8, v226\n"
"v_dot2_f32_bf16 v227, v173, v9, v227\n"
"v_dot2_f32_bf16 v228, v174, v8, v228\n"
"v_dot2_f32_bf16 v229, v174, v9, v229\n"
"v_dot2_f32_bf16 v230, v175, v8, v230\n"
"v_dot2_f32_bf16 v231, v175, v9, v231\n"
"v_dot2_f32_bf16 v224, v176, v8, v224\n"
"v_dot2_f32_bf16 v225, v176, v9, v225\n"
"v_dot2_f32_bf16 v226, v177, v8, v226\n"
"v_dot2_f32_bf16 v227, v177, v9, v227\n"
"v_dot2_f32_bf16 v228, v178, v8, v228\n"
"v_dot2_f32_bf16 v229, v178, v9, v229\n"
"v_dot2_f32_bf16 v230, v179, v8, v230\n"
"v_dot2_f32_bf16 v231, v179, v9, v231\n"
"v_dot2_f32_bf16 v224, v180, v8, v224\n"
"v_dot2_f32_bf16 v225, v180, v9, v225\n"
"v_dot2_f32_bf16 v226, v181, v8, v226\n"
"v_dot2_f32_bf16 v227, v181, v9, v227\n"
"v_dot2_f32_bf16 v228, v182, v8, v228\n"
"v_dot2_f32_bf16 v229, v182, v9, v229\n"
"v_dot2_f32_bf16 v230, v183, v8, v230\n"
"v_dot2_f32_bf16 v231, v183, v9, v231\n"
"v_dot2_f32_bf16 v224, v184, v8, v224\n"
"v_dot2_f32_bf16 v225, v184, v9, v225\n"
"v_dot2_f32_bf16 v226, v185, v8, v226\n"
"v_dot2_f32_bf16 v227, v185, v9, v227\n"
"v_dot2_f32_bf16 v228, v186, v8, v228\n"
"v_dot2_f32_bf16 v229, v186, v9, v229\n"
"v_dot2_f32_bf16 v230, v187, v8, v230\n"
"v_dot2_f32_bf16 v231, v187, v9, v231\n"
"v_dot2_f32_bf16 v224, v188, v8, v224\n"
"v_dot2_f32_bf16 v225, v188, v9, v225\n"
"v_dot2_f32_bf16 v226, v189, v8, v226\n"
"v_dot2_f32_bf16 v227, v189, v9, v227\n"
"v_dot2_f32_bf16 v228, v190, v8, v228\n"
"v_dot2_f32_bf16 v229, v190, v9, v229\n"
"v_dot2_f32_bf16 v230, v191, v8, v230\n"
"v_dot2_f32_bf16 v231, v191, v9, v231\n"
"s_waitcnt vmcnt(0)\n"
"v_dot2_f32_bf16 v18, v96, v8, 0\n"
"v_dot2_f32_bf16 v19, v96, v9, 0\n"
"v_dot2_f32_bf16 v20, v97, v8, 0\n"
"v_dot2_f32_bf16 v21, v97, v9, 0\n"
"v_dot2_f32_bf16 v22, v98, v8, 0\n"
"v_dot2_f32_bf16 v23, v98, v9, 0\n"
"v_dot2_f32_bf16 v24, v99, v8, 0\n"
"v_dot2_f32_bf16 v25, v99, v9, 0\n"
"v_dot2_f32_bf16 v18, v100, v8, v18\n"
"v_dot2_f32_bf16 v19, v100, v9, v19\n"
"v_dot2_f32_bf16 v20, v101, v8, v20\n"
"v_dot2_f32_bf16 v21, v101, v9, v21\n"
"v_dot2_f32_bf16 v22, v102, v8, v22\n"
"v_dot2_f32_bf16 v23, v102, v9, v23\n"
"v_dot2_f32_bf16 v24, v103, v8, v24\n"
"v_dot2_f32_bf16 v25, v103, v9, v25\n"
"v_dot2_f32_bf16 v18, v104, v8, v18\n"
"v_dot2_f32_bf16 v19, v104, v9, v19\n"
"v_dot2_f32_bf16 v20, v105, v8, v20\n"
"v_dot2_f32_bf16 v21, v105, v9, v21\n"
"v_dot2_f32_bf16 v22, v106, v8, v22\n"
"v_dot2_f32_bf16 v23, v106, v9, v23\n"
"v_dot2_f32_bf16 v24, v107, v8, v24\n"
"v_dot2_f32_bf16 v25, v107, v9, v25\n"
"v_mul_f32 v26, 0xbfb8aa3b, v18\n"
"v_mul_f32 v27, 0xbfb8aa3b, v19\n"
"v_mul_f32 v28, 0xbfb8aa3b, v20\n"
"v_mul_f32 v29, 0xbfb8aa3b, v21\n"
"v_mul_f32 v30, 0xbfb8aa3b, v22\n"
"v_mul_f32 v31, 0xbfb8aa3b, v23\n"
"v_mul_f32 v32, 0xbfb8aa3b, v24\n"
"v_mul_f32 v33, 0xbfb8aa3b, v25\n"
"v_exp_f32 v26, v26\n"
"v_exp_f32 v27, v27\n"
"v_exp_f32 v28, v28\n"
"v_exp_f32 v29, v29\n"
"v_exp_f32 v30, v30\n"
"v_exp_f32 v31, v31\n"
"v_exp_f32 v32, v32\n"
"v_exp_f32 v33, v33\n"
"s_nop 0\n"
"v_add_f32 v26, 1.0, v26\n"
"v_add_f32 v27, 1.0, v27\n"
"v_add_f32 v28, 1.0, v28\n"
"v_add_f32 v29, 1.0, v29\n"
"v_add_f32 v30, 1.0, v30\n"
"v_add_f32 v31, 1.0, v31\n"
"v_add_f32 v32, 1.0, v32\n"
"v_add_f32 v33, 1.0, v33\n"
"v_rcp_f32 v26, v26\n"
"v_rcp_f32 v27, v27\n"
"v_rcp_f32 v28, v28\n"
"v_rcp_f32 v29, v29\n"
"v_rcp_f32 v30, v30\n"
"v_rcp_f32 v31, v31\n"
"v_rcp_f32 v32, v32\n"
"v_rcp_f32 v33, v33\n"
"s_nop 0\n"
"v_pk_mul_f32 v[96:97], v[18:19], v[26:27]\n"
"v_pk_mul_f32 v[98:99], v[20:21], v[28:29]\n"
"v_pk_mul_f32 v[100:101], v[22:23], v[30:31]\n"
"v_pk_mul_f32 v[102:103], v[24:25], v[32:33]\n"
"v_dot2_f32_bf16 v18, v144, v8, 0\n"
"v_dot2_f32_bf16 v19, v144, v9, 0\n"
"v_dot2_f32_bf16 v20, v145, v8, 0\n"
"v_dot2_f32_bf16 v21, v145, v9, 0\n"
"v_dot2_f32_bf16 v22, v146, v8, 0\n"
"v_dot2_f32_bf16 v23, v146, v9, 0\n"
"v_dot2_f32_bf16 v24, v147, v8, 0\n"
"v_dot2_f32_bf16 v25, v147, v9, 0\n"
"v_dot2_f32_bf16 v18, v148, v8, v18\n"
"v_dot2_f32_bf16 v19, v148, v9, v19\n"
"v_dot2_f32_bf16 v20, v149, v8, v20\n"
"v_dot2_f32_bf16 v21, v149, v9, v21\n"
"v_dot2_f32_bf16 v22, v150, v8, v22\n"
"v_dot2_f32_bf16 v23, v150, v9, v23\n"
"v_dot2_f32_bf16 v24, v151, v8, v24\n"
"v_dot2_f32_bf16 v25, v151, v9, v25\n"
"v_dot2_f32_bf16 v18, v152, v8, v18\n"
"v_dot2_f32_bf16 v19, v152, v9, v19\n"
"v_dot2_f32_bf16 v20, v153, v8, v20\n"
"v_dot2_f32_bf16 v21, v153, v9, v21\n"
"v_dot2_f32_bf16 v22, v154, v8, v22\n"
"v_dot2_f32_bf16 v23, v154, v9, v23\n"
"v_dot2_f32_bf16 v24, v155, v8, v24\n"
"v_dot2_f32_bf16 v25, v155, v9, v25\n"
"v_mul_f32 v26, 0xbfb8aa3b, v18\n"
"v_mul_f32 v27, 0xbfb8aa3b, v19\n"
"v_mul_f32 v28, 0xbfb8aa3b, v20\n"
"v_mul_f32 v29, 0xbfb8aa3b, v21\n"
"v_mul_f32 v30, 0xbfb8aa3b, v22\n"
"v_mul_f32 v31, 0xbfb8aa3b, v23\n"
"v_mul_f32 v32, 0xbfb8aa3b, v24\n"
"v_mul_f32 v33, 0xbfb8aa3b, v25\n"
"v_exp_f32 v26, v26\n"
"v_exp_f32 v27, v27\n"
"v_exp_f32 v28, v28\n"
"v_exp_f32 v29, v29\n"
"v_exp_f32 v30, v30\n"
"v_exp_f32 v31, v31\n"
"v_exp_f32 v32, v32\n"
"v_exp_f32 v33, v33\n"
"s_nop 0\n"
"v_add_f32 v26, 1.0, v26\n"
"v_add_f32 v27, 1.0, v27\n"
"v_add_f32 v28, 1.0, v28\n"
"v_add_f32 v29, 1.0, v29\n"
"v_add_f32 v30, 1.0, v30\n"
"v_add_f32 v31, 1.0, v31\n"
"v_add_f32 v32, 1.0, v32\n"
"v_add_f32 v33, 1.0, v33\n"
"v_rcp_f32 v26, v26\n"
"v_rcp_f32 v27, v27\n"
"v_rcp_f32 v28, v28\n"
"v_rcp_f32 v29, v29\n"
"v_rcp_f32 v30, v30\n"
"v_rcp_f32 v31, v31\n"
"v_rcp_f32 v32, v32\n"
"v_rcp_f32 v33, v33\n"
"s_nop 0\n"
"v_pk_mul_f32 v[144:145], v[18:19], v[26:27]\n"
"v_pk_mul_f32 v[146:147], v[20:21], v[28:29]\n"
"v_pk_mul_f32 v[148:149], v[22:23], v[30:31]\n"
"v_pk_mul_f32 v[150:151], v[24:25], v[32:33]\n"
"v_dot2_f32_bf16 v18, v192, v8, 0\n"
"v_dot2_f32_bf16 v19, v192, v9, 0\n"
"v_dot2_f32_bf16 v20, v193, v8, 0\n"
"v_dot2_f32_bf16 v21, v193, v9, 0\n"
"v_dot2_f32_bf16 v22, v194, v8, 0\n"
"v_dot2_f32_bf16 v23, v194, v9, 0\n"
"v_dot2_f32_bf16 v24, v195, v8, 0\n"
"v_dot2_f32_bf16 v25, v195, v9, 0\n"
"v_dot2_f32_bf16 v18, v196, v8, v18\n"
"v_dot2_f32_bf16 v19, v196, v9, v19\n"
"v_dot2_f32_bf16 v20, v197, v8, v20\n"
"v_dot2_f32_bf16 v21, v197, v9, v21\n"
"v_dot2_f32_bf16 v22, v198, v8, v22\n"
"v_dot2_f32_bf16 v23, v198, v9, v23\n"
"v_dot2_f32_bf16 v24, v199, v8, v24\n"
"v_dot2_f32_bf16 v25, v199, v9, v25\n"
"v_dot2_f32_bf16 v18, v200, v8, v18\n"
"v_dot2_f32_bf16 v19, v200, v9, v19\n"
"v_dot2_f32_bf16 v20, v201, v8, v20\n"
"v_dot2_f32_bf16 v21, v201, v9, v21\n"
"v_dot2_f32_bf16 v22, v202, v8, v22\n"
"v_dot2_f32_bf16 v23, v202, v9, v23\n"
"v_dot2_f32_bf16 v24, v203, v8, v24\n"
"v_dot2_f32_bf16 v25, v203, v9, v25\n"
"v_mul_f32 v26, 0xbfb8aa3b, v18\n"
"v_mul_f32 v27, 0xbfb8aa3b, v19\n"
"v_mul_f32 v28, 0xbfb8aa3b, v20\n"
"v_mul_f32 v29, 0xbfb8aa3b, v21\n"
"v_mul_f32 v30, 0xbfb8aa3b, v22\n"
"v_mul_f32 v31, 0xbfb8aa3b, v23\n"
"v_mul_f32 v32, 0xbfb8aa3b, v24\n"
"v_mul_f32 v33, 0xbfb8aa3b, v25\n"
"v_exp_f32 v26, v26\n"
"v_exp_f32 v27, v27\n"
"v_exp_f32 v28, v28\n"
"v_exp_f32 v29, v29\n"
"v_exp_f32 v30, v30\n"
"v_exp_f32 v31, v31\n"
"v_exp_f32 v32, v32\n"
"v_exp_f32 v33, v33\n"
"s_nop 0\n"
"v_add_f32 v26, 1.0, v26\n"
"v_add_f32 v27, 1.0, v27\n"
"v_add_f32 v28, 1.0, v28\n"
"v_add_f32 v29, 1.0, v29\n"
"v_add_f32 v30, 1.0, v30\n"
"v_add_f32 v31, 1.0, v31\n"
"v_add_f32 v32, 1.0, v32\n"
"v_add_f32 v33, 1.0, v33\n"
"v_rcp_f32 v26, v26\n"
"v_rcp_f32 v27, v27\n"
"v_rcp_f32 v28, v28\n"
"v_rcp_f32 v29, v29\n"
"v_rcp_f32 v30, v30\n"
"v_rcp_f32 v31, v31\n"
"v_rcp_f32 v32, v32\n"
"v_rcp_f32 v33, v33\n"
"s_nop 0\n"
"v_pk_mul_f32 v[192:193], v[18:19], v[26:27]\n"
"v_pk_mul_f32 v[194:195], v[20:21], v[28:29]\n"
"v_pk_mul_f32 v[196:197], v[22:23], v[30:31]\n"
"v_pk_mul_f32 v[198:199], v[24:25], v[32:33]\n"
"v_lshlrev_b32 v232, 16, v40\n"
"v_and_b32 v233, 0xffff0000, v40\n"
"v_lshlrev_b32 v234, 16, v41\n"
"v_and_b32 v235, 0xffff0000, v41\n"
"v_lshlrev_b32 v236, 16, v42\n"
"v_and_b32 v237, 0xffff0000, v42\n"
"v_lshlrev_b32 v238, 16, v43\n"
"v_and_b32 v239, 0xffff0000, v43\n"
"v_pk_fma_f32 v[232:233], v[208:209], v[96:97], v[232:233]\n"
"v_pk_fma_f32 v[234:235], v[210:211], v[98:99], v[234:235]\n"
"v_pk_fma_f32 v[236:237], v[212:213], v[100:101], v[236:237]\n"
"v_pk_fma_f32 v[238:239], v[214:215], v[102:103], v[238:239]\n"
"v_cvt_pk_bf16_f32 v240, v232, v233\n"
"v_cvt_pk_bf16_f32 v241, v234, v235\n"
"v_cvt_pk_bf16_f32 v242, v236, v237\n"
"v_cvt_pk_bf16_f32 v243, v238, v239\n"
"s_add_u32 s50, s48, 0\n"
"buffer_store_dwordx2 v[240:241], v52, s[28:31], s50 offen\n"
"buffer_store_dwordx2 v[242:243], v52, s[28:31], s50 offen offset:32\n"
"v_lshlrev_b32 v232, 16, v44\n"
"v_and_b32 v233, 0xffff0000, v44\n"
"v_lshlrev_b32 v234, 16, v45\n"
"v_and_b32 v235, 0xffff0000, v45\n"
"v_lshlrev_b32 v236, 16, v46\n"
"v_and_b32 v237, 0xffff0000, v46\n"
"v_lshlrev_b32 v238, 16, v47\n"
"v_and_b32 v239, 0xffff0000, v47\n"
"v_pk_fma_f32 v[232:233], v[216:217], v[144:145], v[232:233]\n"
"v_pk_fma_f32 v[234:235], v[218:219], v[146:147], v[234:235]\n"
"v_pk_fma_f32 v[236:237], v[220:221], v[148:149], v[236:237]\n"
"v_pk_fma_f32 v[238:239], v[222:223], v[150:151], v[238:239]\n"
"v_cvt_pk_bf16_f32 v244, v232, v233\n"
"v_cvt_pk_bf16_f32 v245, v234, v235\n"
"v_cvt_pk_bf16_f32 v246, v236, v237\n"
"v_cvt_pk_bf16_f32 v247, v238, v239\n"
"s_add_u32 s50, s48, 16384\n"
"buffer_store_dwordx2 v[244:245], v52, s[28:31], s50 offen\n"
"buffer_store_dwordx2 v[246:247], v52, s[28:31], s50 offen offset:32\n"
"v_lshlrev_b32 v232, 16, v48\n"
"v_and_b32 v233, 0xffff0000, v48\n"
"v_lshlrev_b32 v234, 16, v49\n"
"v_and_b32 v235, 0xffff0000, v49\n"
"v_lshlrev_b32 v236, 16, v50\n"
"v_and_b32 v237, 0xffff0000, v50\n"
"v_lshlrev_b32 v238, 16, v51\n"
"v_and_b32 v239, 0xffff0000, v51\n"
"v_pk_fma_f32 v[232:233], v[224:225], v[192:193], v[232:233]\n"
"v_pk_fma_f32 v[234:235], v[226:227], v[194:195], v[234:235]\n"
"v_pk_fma_f32 v[236:237], v[228:229], v[196:197], v[236:237]\n"
"v_pk_fma_f32 v[238:239], v[230:231], v[198:199], v[238:239]\n"
"v_cvt_pk_bf16_f32 v248, v232, v233\n"
"v_cvt_pk_bf16_f32 v249, v234, v235\n"
"v_cvt_pk_bf16_f32 v250, v236, v237\n"
"v_cvt_pk_bf16_f32 v251, v238, v239\n"
"s_add_u32 s50, s48, 32768\n"
"buffer_store_dwordx2 v[248:249], v52, s[28:31], s50 offen\n"
"buffer_store_dwordx2 v[250:251], v52, s[28:31], s50 offen offset:32\n"
"s_branch LPX_%=\n"
"LPB_%=:\n"
"s_mov_b64 exec, 0x7ff\n"
"global_store_dwordx2 v2, v[4:5], s[88:89] offset:512 sc1\n"
"s_mov_b64 exec, -1\n"
"s_mov_b32 s50, 17301504\n"
"buffer_load_dwordx4 v[96:99], v7, s[24:27], s50 offen offset:0 sc1\n"
"s_mov_b32 s50, 19464192\n"
"buffer_load_dwordx4 v[100:103], v7, s[24:27], s50 offen offset:0 sc1\n"
"s_mov_b32 s50, 21626880\n"
"buffer_load_dwordx4 v[104:107], v7, s[24:27], s50 offen offset:0 sc1\n"
"s_mov_b32 s50, 17301504\n"
"buffer_load_dwordx4 v[144:147], v7, s[24:27], s50 offen offset:1024 sc1\n"
"s_mov_b32 s50, 19464192\n"
"buffer_load_dwordx4 v[148:151], v7, s[24:27], s50 offen offset:1024 sc1\n"
"s_mov_b32 s50, 21626880\n"
"buffer_load_dwordx4 v[152:155], v7, s[24:27], s50 offen offset:1024 sc1\n"
"s_mov_b32 s50, 17301504\n"
"buffer_load_dwordx4 v[192:195], v7, s[24:27], s50 offen offset:2048 sc1\n"
"s_mov_b32 s50, 19464192\n"
"buffer_load_dwordx4 v[196:199], v7, s[24:27], s50 offen offset:2048 sc1\n"
"s_mov_b32 s50, 21626880\n"
"buffer_load_dwordx4 v[200:203], v7, s[24:27], s50 offen offset:2048 sc1\n"
"s_mov_b32 s50, 0\n"
"buffer_load_dwordx4 v[64:67], v7, s[24:27], s50 offen offset:0 sc1\n"
"s_mov_b32 s50, 2162688\n"
"buffer_load_dwordx4 v[68:71], v7, s[24:27], s50 offen offset:0 sc1\n"
"s_mov_b32 s50, 4325376\n"
"buffer_load_dwordx4 v[72:75], v7, s[24:27], s50 offen offset:0 sc1\n"
"s_mov_b32 s50, 6488064\n"
"buffer_load_dwordx4 v[76:79], v7, s[24:27], s50 offen offset:0 sc1\n"
"s_mov_b32 s50, 8650752\n"
"buffer_load_dwordx4 v[80:83], v7, s[24:27], s50 offen offset:0 sc1\n"
"s_mov_b32 s50, 10813440\n"
"buffer_load_dwordx4 v[84:87], v7, s[24:27], s50 offen offset:0 sc1\n"
"s_mov_b32 s50, 12976128\n"
"buffer_load_dwordx4 v[88:91], v7, s[24:27], s50 offen offset:0 sc1\n"
"s_mov_b32 s50, 15138816\n"
"buffer_load_dwordx4 v[92:95], v7, s[24:27], s50 offen offset:0 sc1\n"
"s_mov_b32 s50, 0\n"
"buffer_load_dwordx4 v[112:115], v7, s[24:27], s50 offen offset:1024 sc1\n"
"s_mov_b32 s50, 2162688\n"
"buffer_load_dwordx4 v[116:119], v7, s[24:27], s50 offen offset:1024 sc1\n"
"s_mov_b32 s50, 4325376\n"
"buffer_load_dwordx4 v[120:123], v7, s[24:27], s50 offen offset:1024 sc1\n"
"s_mov_b32 s50, 6488064\n"
"buffer_load_dwordx4 v[124:127], v7, s[24:27], s50 offen offset:1024 sc1\n"
"s_mov_b32 s50, 8650752\n"
"buffer_load_dwordx4 v[128:131], v7, s[24:27], s50 offen offset:1024 sc1\n"
"s_mov_b32 s50, 10813440\n"
"buffer_load_dwordx4 v[132:135], v7, s[24:27], s50 offen offset:1024 sc1\n"
"s_mov_b32 s50, 12976128\n"
"buffer_load_dwordx4 v[136:139], v7, s[24:27], s50 offen offset:1024 sc1\n"
"s_mov_b32 s50, 15138816\n"
"buffer_load_dwordx4 v[140:143], v7, s[24:27], s50 offen offset:1024 sc1\n"
"s_mov_b32 s50, 0\n"
"buffer_load_dwordx4 v[160:163], v7, s[24:27], s50 offen offset:2048 sc1\n"
"s_mov_b32 s50, 2162688\n"
"buffer_load_dwordx4 v[164:167], v7, s[24:27], s50 offen offset:2048 sc1\n"
"s_mov_b32 s50, 4325376\n"
"buffer_load_dwordx4 v[168:171], v7, s[24:27], s50 offen offset:2048 sc1\n"
"s_mov_b32 s50, 6488064\n"
"buffer_load_dwordx4 v[172:175], v7, s[24:27], s50 offen offset:2048 sc1\n"
"s_mov_b32 s50, 8650752\n"
"buffer_load_dwordx4 v[176:179], v7, s[24:27], s50 offen offset:2048 sc1\n"
"s_mov_b32 s50, 10813440\n"
"buffer_load_dwordx4 v[180:183], v7, s[24:27], s50 offen offset:2048 sc1\n"
"s_mov_b32 s50, 12976128\n"
"buffer_load_dwordx4 v[184:187], v7, s[24:27], s50 offen offset:2048 sc1\n"
"s_mov_b32 s50, 15138816\n"
"buffer_load_dwordx4 v[188:191], v7, s[24:27], s50 offen offset:2048 sc1\n"
"s_waitcnt vmcnt(24)\n"
"v_dot2_f32_bf16 v18, v96, v8, 0\n"
"v_dot2_f32_bf16 v19, v96, v9, 0\n"
"v_dot2_f32_bf16 v20, v97, v8, 0\n"
"v_dot2_f32_bf16 v21, v97, v9, 0\n"
"v_dot2_f32_bf16 v22, v98, v8, 0\n"
"v_dot2_f32_bf16 v23, v98, v9, 0\n"
"v_dot2_f32_bf16 v24, v99, v8, 0\n"
"v_dot2_f32_bf16 v25, v99, v9, 0\n"
"v_dot2_f32_bf16 v18, v100, v8, v18\n"
"v_dot2_f32_bf16 v19, v100, v9, v19\n"
"v_dot2_f32_bf16 v20, v101, v8, v20\n"
"v_dot2_f32_bf16 v21, v101, v9, v21\n"
"v_dot2_f32_bf16 v22, v102, v8, v22\n"
"v_dot2_f32_bf16 v23, v102, v9, v23\n"
"v_dot2_f32_bf16 v24, v103, v8, v24\n"
"v_dot2_f32_bf16 v25, v103, v9, v25\n"
"v_dot2_f32_bf16 v18, v104, v8, v18\n"
"v_dot2_f32_bf16 v19, v104, v9, v19\n"
"v_dot2_f32_bf16 v20, v105, v8, v20\n"
"v_dot2_f32_bf16 v21, v105, v9, v21\n"
"v_dot2_f32_bf16 v22, v106, v8, v22\n"
"v_dot2_f32_bf16 v23, v106, v9, v23\n"
"v_dot2_f32_bf16 v24, v107, v8, v24\n"
"v_dot2_f32_bf16 v25, v107, v9, v25\n"
"v_mul_f32 v26, 0xbfb8aa3b, v18\n"
"v_mul_f32 v27, 0xbfb8aa3b, v19\n"
"v_mul_f32 v28, 0xbfb8aa3b, v20\n"
"v_mul_f32 v29, 0xbfb8aa3b, v21\n"
"v_mul_f32 v30, 0xbfb8aa3b, v22\n"
"v_mul_f32 v31, 0xbfb8aa3b, v23\n"
"v_mul_f32 v32, 0xbfb8aa3b, v24\n"
"v_mul_f32 v33, 0xbfb8aa3b, v25\n"
"v_exp_f32 v26, v26\n"
"v_exp_f32 v27, v27\n"
"v_exp_f32 v28, v28\n"
"v_exp_f32 v29, v29\n"
"v_exp_f32 v30, v30\n"
"v_exp_f32 v31, v31\n"
"v_exp_f32 v32, v32\n"
"v_exp_f32 v33, v33\n"
"s_nop 0\n"
"v_add_f32 v26, 1.0, v26\n"
"v_add_f32 v27, 1.0, v27\n"
"v_add_f32 v28, 1.0, v28\n"
"v_add_f32 v29, 1.0, v29\n"
"v_add_f32 v30, 1.0, v30\n"
"v_add_f32 v31, 1.0, v31\n"
"v_add_f32 v32, 1.0, v32\n"
"v_add_f32 v33, 1.0, v33\n"
"v_rcp_f32 v26, v26\n"
"v_rcp_f32 v27, v27\n"
"v_rcp_f32 v28, v28\n"
"v_rcp_f32 v29, v29\n"
"v_rcp_f32 v30, v30\n"
"v_rcp_f32 v31, v31\n"
"v_rcp_f32 v32, v32\n"
"v_rcp_f32 v33, v33\n"
"s_nop 0\n"
"v_pk_mul_f32 v[96:97], v[18:19], v[26:27]\n"
"v_pk_mul_f32 v[98:99], v[20:21], v[28:29]\n"
"v_pk_mul_f32 v[100:101], v[22:23], v[30:31]\n"
"v_pk_mul_f32 v[102:103], v[24:25], v[32:33]\n"
"v_dot2_f32_bf16 v18, v144, v8, 0\n"
"v_dot2_f32_bf16 v19, v144, v9, 0\n"
"v_dot2_f32_bf16 v20, v145, v8, 0\n"
"v_dot2_f32_bf16 v21, v145, v9, 0\n"
"v_dot2_f32_bf16 v22, v146, v8, 0\n"
"v_dot2_f32_bf16 v23, v146, v9, 0\n"
"v_dot2_f32_bf16 v24, v147, v8, 0\n"
"v_dot2_f32_bf16 v25, v147, v9, 0\n"
"v_dot2_f32_bf16 v18, v148, v8, v18\n"
"v_dot2_f32_bf16 v19, v148, v9, v19\n"
"v_dot2_f32_bf16 v20, v149, v8, v20\n"
"v_dot2_f32_bf16 v21, v149, v9, v21\n"
"v_dot2_f32_bf16 v22, v150, v8, v22\n"
"v_dot2_f32_bf16 v23, v150, v9, v23\n"
"v_dot2_f32_bf16 v24, v151, v8, v24\n"
"v_dot2_f32_bf16 v25, v151, v9, v25\n"
"v_dot2_f32_bf16 v18, v152, v8, v18\n"
"v_dot2_f32_bf16 v19, v152, v9, v19\n"
"v_dot2_f32_bf16 v20, v153, v8, v20\n"
"v_dot2_f32_bf16 v21, v153, v9, v21\n"
"v_dot2_f32_bf16 v22, v154, v8, v22\n"
"v_dot2_f32_bf16 v23, v154, v9, v23\n"
"v_dot2_f32_bf16 v24, v155, v8, v24\n"
"v_dot2_f32_bf16 v25, v155, v9, v25\n"
"v_mul_f32 v26, 0xbfb8aa3b, v18\n"
"v_mul_f32 v27, 0xbfb8aa3b, v19\n"
"v_mul_f32 v28, 0xbfb8aa3b, v20\n"
"v_mul_f32 v29, 0xbfb8aa3b, v21\n"
"v_mul_f32 v30, 0xbfb8aa3b, v22\n"
"v_mul_f32 v31, 0xbfb8aa3b, v23\n"
"v_mul_f32 v32, 0xbfb8aa3b, v24\n"
"v_mul_f32 v33, 0xbfb8aa3b, v25\n"
"v_exp_f32 v26, v26\n"
"v_exp_f32 v27, v27\n"
"v_exp_f32 v28, v28\n"
"v_exp_f32 v29, v29\n"
"v_exp_f32 v30, v30\n"
"v_exp_f32 v31, v31\n"
"v_exp_f32 v32, v32\n"
"v_exp_f32 v33, v33\n"
"s_nop 0\n"
"v_add_f32 v26, 1.0, v26\n"
"v_add_f32 v27, 1.0, v27\n"
"v_add_f32 v28, 1.0, v28\n"
"v_add_f32 v29, 1.0, v29\n"
"v_add_f32 v30, 1.0, v30\n"
"v_add_f32 v31, 1.0, v31\n"
"v_add_f32 v32, 1.0, v32\n"
"v_add_f32 v33, 1.0, v33\n"
"v_rcp_f32 v26, v26\n"
"v_rcp_f32 v27, v27\n"
"v_rcp_f32 v28, v28\n"
"v_rcp_f32 v29, v29\n"
"v_rcp_f32 v30, v30\n"
"v_rcp_f32 v31, v31\n"
"v_rcp_f32 v32, v32\n"
"v_rcp_f32 v33, v33\n"
"s_nop 0\n"
"v_pk_mul_f32 v[144:145], v[18:19], v[26:27]\n"
"v_pk_mul_f32 v[146:147], v[20:21], v[28:29]\n"
"v_pk_mul_f32 v[148:149], v[22:23], v[30:31]\n"
"v_pk_mul_f32 v[150:151], v[24:25], v[32:33]\n"
"v_dot2_f32_bf16 v18, v192, v8, 0\n"
"v_dot2_f32_bf16 v19, v192, v9, 0\n"
"v_dot2_f32_bf16 v20, v193, v8, 0\n"
"v_dot2_f32_bf16 v21, v193, v9, 0\n"
"v_dot2_f32_bf16 v22, v194, v8, 0\n"
"v_dot2_f32_bf16 v23, v194, v9, 0\n"
"v_dot2_f32_bf16 v24, v195, v8, 0\n"
"v_dot2_f32_bf16 v25, v195, v9, 0\n"
"v_dot2_f32_bf16 v18, v196, v8, v18\n"
"v_dot2_f32_bf16 v19, v196, v9, v19\n"
"v_dot2_f32_bf16 v20, v197, v8, v20\n"
"v_dot2_f32_bf16 v21, v197, v9, v21\n"
"v_dot2_f32_bf16 v22, v198, v8, v22\n"
"v_dot2_f32_bf16 v23, v198, v9, v23\n"
"v_dot2_f32_bf16 v24, v199, v8, v24\n"
"v_dot2_f32_bf16 v25, v199, v9, v25\n"
"v_dot2_f32_bf16 v18, v200, v8, v18\n"
"v_dot2_f32_bf16 v19, v200, v9, v19\n"
"v_dot2_f32_bf16 v20, v201, v8, v20\n"
"v_dot2_f32_bf16 v21, v201, v9, v21\n"
"v_dot2_f32_bf16 v22, v202, v8, v22\n"
"v_dot2_f32_bf16 v23, v202, v9, v23\n"
"v_dot2_f32_bf16 v24, v203, v8, v24\n"
"v_dot2_f32_bf16 v25, v203, v9, v25\n"
"v_mul_f32 v26, 0xbfb8aa3b, v18\n"
"v_mul_f32 v27, 0xbfb8aa3b, v19\n"
"v_mul_f32 v28, 0xbfb8aa3b, v20\n"
"v_mul_f32 v29, 0xbfb8aa3b, v21\n"
"v_mul_f32 v30, 0xbfb8aa3b, v22\n"
"v_mul_f32 v31, 0xbfb8aa3b, v23\n"
"v_mul_f32 v32, 0xbfb8aa3b, v24\n"
"v_mul_f32 v33, 0xbfb8aa3b, v25\n"
"v_exp_f32 v26, v26\n"
"v_exp_f32 v27, v27\n"
"v_exp_f32 v28, v28\n"
"v_exp_f32 v29, v29\n"
"v_exp_f32 v30, v30\n"
"v_exp_f32 v31, v31\n"
"v_exp_f32 v32, v32\n"
"v_exp_f32 v33, v33\n"
"s_nop 0\n"
"v_add_f32 v26, 1.0, v26\n"
"v_add_f32 v27, 1.0, v27\n"
"v_add_f32 v28, 1.0, v28\n"
"v_add_f32 v29, 1.0, v29\n"
"v_add_f32 v30, 1.0, v30\n"
"v_add_f32 v31, 1.0, v31\n"
"v_add_f32 v32, 1.0, v32\n"
"v_add_f32 v33, 1.0, v33\n"
"v_rcp_f32 v26, v26\n"
"v_rcp_f32 v27, v27\n"
"v_rcp_f32 v28, v28\n"
"v_rcp_f32 v29, v29\n"
"v_rcp_f32 v30, v30\n"
"v_rcp_f32 v31, v31\n"
"v_rcp_f32 v32, v32\n"
"v_rcp_f32 v33, v33\n"
"s_nop 0\n"
"v_pk_mul_f32 v[192:193], v[18:19], v[26:27]\n"
"v_pk_mul_f32 v[194:195], v[20:21], v[28:29]\n"
"v_pk_mul_f32 v[196:197], v[22:23], v[30:31]\n"
"v_pk_mul_f32 v[198:199], v[24:25], v[32:33]\n"
"s_waitcnt vmcnt(16)\n"
"v_dot2_f32_bf16 v10, v64, v8, 0\n"
"v_dot2_f32_bf16 v11, v64, v9, 0\n"
"v_dot2_f32_bf16 v12, v65, v8, 0\n"
"v_dot2_f32_bf16 v13, v65, v9, 0\n"
"v_dot2_f32_bf16 v14, v66, v8, 0\n"
"v_dot2_f32_bf16 v15, v66, v9, 0\n"
"v_dot2_f32_bf16 v16, v67, v8, 0\n"
"v_dot2_f32_bf16 v17, v67, v9, 0\n"
"v_dot2_f32_bf16 v10, v68, v8, v10\n"
"v_dot2_f32_bf16 v11, v68, v9, v11\n"
"v_dot2_f32_bf16 v12, v69, v8, v12\n"
"v_dot2_f32_bf16 v13, v69, v9, v13\n"
"v_dot2_f32_bf16 v14, v70, v8, v14\n"
"v_dot2_f32_bf16 v15, v70, v9, v15\n"
"v_dot2_f32_bf16 v16, v71, v8, v16\n"
"v_dot2_f32_bf16 v17, v71, v9, v17\n"
"v_dot2_f32_bf16 v10, v72, v8, v10\n"
"v_dot2_f32_bf16 v11, v72, v9, v11\n"
"v_dot2_f32_bf16 v12, v73, v8, v12\n"
"v_dot2_f32_bf16 v13, v73, v9, v13\n"
"v_dot2_f32_bf16 v14, v74, v8, v14\n"
"v_dot2_f32_bf16 v15, v74, v9, v15\n"
"v_dot2_f32_bf16 v16, v75, v8, v16\n"
"v_dot2_f32_bf16 v17, v75, v9, v17\n"
"v_dot2_f32_bf16 v10, v76, v8, v10\n"
"v_dot2_f32_bf16 v11, v76, v9, v11\n"
"v_dot2_f32_bf16 v12, v77, v8, v12\n"
"v_dot2_f32_bf16 v13, v77, v9, v13\n"
"v_dot2_f32_bf16 v14, v78, v8, v14\n"
"v_dot2_f32_bf16 v15, v78, v9, v15\n"
"v_dot2_f32_bf16 v16, v79, v8, v16\n"
"v_dot2_f32_bf16 v17, v79, v9, v17\n"
"v_dot2_f32_bf16 v10, v80, v8, v10\n"
"v_dot2_f32_bf16 v11, v80, v9, v11\n"
"v_dot2_f32_bf16 v12, v81, v8, v12\n"
"v_dot2_f32_bf16 v13, v81, v9, v13\n"
"v_dot2_f32_bf16 v14, v82, v8, v14\n"
"v_dot2_f32_bf16 v15, v82, v9, v15\n"
"v_dot2_f32_bf16 v16, v83, v8, v16\n"
"v_dot2_f32_bf16 v17, v83, v9, v17\n"
"v_dot2_f32_bf16 v10, v84, v8, v10\n"
"v_dot2_f32_bf16 v11, v84, v9, v11\n"
"v_dot2_f32_bf16 v12, v85, v8, v12\n"
"v_dot2_f32_bf16 v13, v85, v9, v13\n"
"v_dot2_f32_bf16 v14, v86, v8, v14\n"
"v_dot2_f32_bf16 v15, v86, v9, v15\n"
"v_dot2_f32_bf16 v16, v87, v8, v16\n"
"v_dot2_f32_bf16 v17, v87, v9, v17\n"
"v_dot2_f32_bf16 v10, v88, v8, v10\n"
"v_dot2_f32_bf16 v11, v88, v9, v11\n"
"v_dot2_f32_bf16 v12, v89, v8, v12\n"
"v_dot2_f32_bf16 v13, v89, v9, v13\n"
"v_dot2_f32_bf16 v14, v90, v8, v14\n"
"v_dot2_f32_bf16 v15, v90, v9, v15\n"
"v_dot2_f32_bf16 v16, v91, v8, v16\n"
"v_dot2_f32_bf16 v17, v91, v9, v17\n"
"v_dot2_f32_bf16 v10, v92, v8, v10\n"
"v_dot2_f32_bf16 v11, v92, v9, v11\n"
"v_dot2_f32_bf16 v12, v93, v8, v12\n"
"v_dot2_f32_bf16 v13, v93, v9, v13\n"
"v_dot2_f32_bf16 v14, v94, v8, v14\n"
"v_dot2_f32_bf16 v15, v94, v9, v15\n"
"v_dot2_f32_bf16 v16, v95, v8, v16\n"
"v_dot2_f32_bf16 v17, v95, v9, v17\n"
"v_lshlrev_b32 v232, 16, v40\n"
"v_and_b32 v233, 0xffff0000, v40\n"
"v_lshlrev_b32 v234, 16, v41\n"
"v_and_b32 v235, 0xffff0000, v41\n"
"v_lshlrev_b32 v236, 16, v42\n"
"v_and_b32 v237, 0xffff0000, v42\n"
"v_lshlrev_b32 v238, 16, v43\n"
"v_and_b32 v239, 0xffff0000, v43\n"
"v_pk_fma_f32 v[232:233], v[10:11], v[96:97], v[232:233]\n"
"v_pk_fma_f32 v[234:235], v[12:13], v[98:99], v[234:235]\n"
"v_pk_fma_f32 v[236:237], v[14:15], v[100:101], v[236:237]\n"
"v_pk_fma_f32 v[238:239], v[16:17], v[102:103], v[238:239]\n"
"v_cvt_pk_bf16_f32 v240, v232, v233\n"
"v_cvt_pk_bf16_f32 v241, v234, v235\n"
"v_cvt_pk_bf16_f32 v242, v236, v237\n"
"v_cvt_pk_bf16_f32 v243, v238, v239\n"
"s_add_u32 s50, s48, 0\n"
"buffer_store_dwordx2 v[240:241], v52, s[28:31], s50 offen\n"
"buffer_store_dwordx2 v[242:243], v52, s[28:31], s50 offen offset:32\n"
"s_waitcnt vmcnt(10)\n"
"v_dot2_f32_bf16 v10, v112, v8, 0\n"
"v_dot2_f32_bf16 v11, v112, v9, 0\n"
"v_dot2_f32_bf16 v12, v113, v8, 0\n"
"v_dot2_f32_bf16 v13, v113, v9, 0\n"
"v_dot2_f32_bf16 v14, v114, v8, 0\n"
"v_dot2_f32_bf16 v15, v114, v9, 0\n"
"v_dot2_f32_bf16 v16, v115, v8, 0\n"
"v_dot2_f32_bf16 v17, v115, v9, 0\n"
"v_dot2_f32_bf16 v10, v116, v8, v10\n"
"v_dot2_f32_bf16 v11, v116, v9, v11\n"
"v_dot2_f32_bf16 v12, v117, v8, v12\n"
"v_dot2_f32_bf16 v13, v117, v9, v13\n"
"v_dot2_f32_bf16 v14, v118, v8, v14\n"
"v_dot2_f32_bf16 v15, v118, v9, v15\n"
"v_dot2_f32_bf16 v16, v119, v8, v16\n"
"v_dot2_f32_bf16 v17, v119, v9, v17\n"
"v_dot2_f32_bf16 v10, v120, v8, v10\n"
"v_dot2_f32_bf16 v11, v120, v9, v11\n"
"v_dot2_f32_bf16 v12, v121, v8, v12\n"
"v_dot2_f32_bf16 v13, v121, v9, v13\n"
"v_dot2_f32_bf16 v14, v122, v8, v14\n"
"v_dot2_f32_bf16 v15, v122, v9, v15\n"
"v_dot2_f32_bf16 v16, v123, v8, v16\n"
"v_dot2_f32_bf16 v17, v123, v9, v17\n"
"v_dot2_f32_bf16 v10, v124, v8, v10\n"
"v_dot2_f32_bf16 v11, v124, v9, v11\n"
"v_dot2_f32_bf16 v12, v125, v8, v12\n"
"v_dot2_f32_bf16 v13, v125, v9, v13\n"
"v_dot2_f32_bf16 v14, v126, v8, v14\n"
"v_dot2_f32_bf16 v15, v126, v9, v15\n"
"v_dot2_f32_bf16 v16, v127, v8, v16\n"
"v_dot2_f32_bf16 v17, v127, v9, v17\n"
"v_dot2_f32_bf16 v10, v128, v8, v10\n"
"v_dot2_f32_bf16 v11, v128, v9, v11\n"
"v_dot2_f32_bf16 v12, v129, v8, v12\n"
"v_dot2_f32_bf16 v13, v129, v9, v13\n"
"v_dot2_f32_bf16 v14, v130, v8, v14\n"
"v_dot2_f32_bf16 v15, v130, v9, v15\n"
"v_dot2_f32_bf16 v16, v131, v8, v16\n"
"v_dot2_f32_bf16 v17, v131, v9, v17\n"
"v_dot2_f32_bf16 v10, v132, v8, v10\n"
"v_dot2_f32_bf16 v11, v132, v9, v11\n"
"v_dot2_f32_bf16 v12, v133, v8, v12\n"
"v_dot2_f32_bf16 v13, v133, v9, v13\n"
"v_dot2_f32_bf16 v14, v134, v8, v14\n"
"v_dot2_f32_bf16 v15, v134, v9, v15\n"
"v_dot2_f32_bf16 v16, v135, v8, v16\n"
"v_dot2_f32_bf16 v17, v135, v9, v17\n"
"v_dot2_f32_bf16 v10, v136, v8, v10\n"
"v_dot2_f32_bf16 v11, v136, v9, v11\n"
"v_dot2_f32_bf16 v12, v137, v8, v12\n"
"v_dot2_f32_bf16 v13, v137, v9, v13\n"
"v_dot2_f32_bf16 v14, v138, v8, v14\n"
"v_dot2_f32_bf16 v15, v138, v9, v15\n"
"v_dot2_f32_bf16 v16, v139, v8, v16\n"
"v_dot2_f32_bf16 v17, v139, v9, v17\n"
"v_dot2_f32_bf16 v10, v140, v8, v10\n"
"v_dot2_f32_bf16 v11, v140, v9, v11\n"
"v_dot2_f32_bf16 v12, v141, v8, v12\n"
"v_dot2_f32_bf16 v13, v141, v9, v13\n"
"v_dot2_f32_bf16 v14, v142, v8, v14\n"
"v_dot2_f32_bf16 v15, v142, v9, v15\n"
"v_dot2_f32_bf16 v16, v143, v8, v16\n"
"v_dot2_f32_bf16 v17, v143, v9, v17\n"
"v_lshlrev_b32 v232, 16, v44\n"
"v_and_b32 v233, 0xffff0000, v44\n"
"v_lshlrev_b32 v234, 16, v45\n"
"v_and_b32 v235, 0xffff0000, v45\n"
"v_lshlrev_b32 v236, 16, v46\n"
"v_and_b32 v237, 0xffff0000, v46\n"
"v_lshlrev_b32 v238, 16, v47\n"
"v_and_b32 v239, 0xffff0000, v47\n"
"v_pk_fma_f32 v[232:233], v[10:11], v[144:145], v[232:233]\n"
"v_pk_fma_f32 v[234:235], v[12:13], v[146:147], v[234:235]\n"
"v_pk_fma_f32 v[236:237], v[14:15], v[148:149], v[236:237]\n"
"v_pk_fma_f32 v[238:239], v[16:17], v[150:151], v[238:239]\n"
"v_cvt_pk_bf16_f32 v244, v232, v233\n"
"v_cvt_pk_bf16_f32 v245, v234, v235\n"
"v_cvt_pk_bf16_f32 v246, v236, v237\n"
"v_cvt_pk_bf16_f32 v247, v238, v239\n"
"s_add_u32 s50, s48, 16384\n"
"buffer_store_dwordx2 v[244:245], v52, s[28:31], s50 offen\n"
"buffer_store_dwordx2 v[246:247], v52, s[28:31], s50 offen offset:32\n"
"s_waitcnt vmcnt(4)\n"
"v_dot2_f32_bf16 v10, v160, v8, 0\n"
"v_dot2_f32_bf16 v11, v160, v9, 0\n"
"v_dot2_f32_bf16 v12, v161, v8, 0\n"
"v_dot2_f32_bf16 v13, v161, v9, 0\n"
"v_dot2_f32_bf16 v14, v162, v8, 0\n"
"v_dot2_f32_bf16 v15, v162, v9, 0\n"
"v_dot2_f32_bf16 v16, v163, v8, 0\n"
"v_dot2_f32_bf16 v17, v163, v9, 0\n"
"v_dot2_f32_bf16 v10, v164, v8, v10\n"
"v_dot2_f32_bf16 v11, v164, v9, v11\n"
"v_dot2_f32_bf16 v12, v165, v8, v12\n"
"v_dot2_f32_bf16 v13, v165, v9, v13\n"
"v_dot2_f32_bf16 v14, v166, v8, v14\n"
"v_dot2_f32_bf16 v15, v166, v9, v15\n"
"v_dot2_f32_bf16 v16, v167, v8, v16\n"
"v_dot2_f32_bf16 v17, v167, v9, v17\n"
"v_dot2_f32_bf16 v10, v168, v8, v10\n"
"v_dot2_f32_bf16 v11, v168, v9, v11\n"
"v_dot2_f32_bf16 v12, v169, v8, v12\n"
"v_dot2_f32_bf16 v13, v169, v9, v13\n"
"v_dot2_f32_bf16 v14, v170, v8, v14\n"
"v_dot2_f32_bf16 v15, v170, v9, v15\n"
"v_dot2_f32_bf16 v16, v171, v8, v16\n"
"v_dot2_f32_bf16 v17, v171, v9, v17\n"
"v_dot2_f32_bf16 v10, v172, v8, v10\n"
"v_dot2_f32_bf16 v11, v172, v9, v11\n"
"v_dot2_f32_bf16 v12, v173, v8, v12\n"
"v_dot2_f32_bf16 v13, v173, v9, v13\n"
"v_dot2_f32_bf16 v14, v174, v8, v14\n"
"v_dot2_f32_bf16 v15, v174, v9, v15\n"
"v_dot2_f32_bf16 v16, v175, v8, v16\n"
"v_dot2_f32_bf16 v17, v175, v9, v17\n"
"v_dot2_f32_bf16 v10, v176, v8, v10\n"
"v_dot2_f32_bf16 v11, v176, v9, v11\n"
"v_dot2_f32_bf16 v12, v177, v8, v12\n"
"v_dot2_f32_bf16 v13, v177, v9, v13\n"
"v_dot2_f32_bf16 v14, v178, v8, v14\n"
"v_dot2_f32_bf16 v15, v178, v9, v15\n"
"v_dot2_f32_bf16 v16, v179, v8, v16\n"
"v_dot2_f32_bf16 v17, v179, v9, v17\n"
"v_dot2_f32_bf16 v10, v180, v8, v10\n"
"v_dot2_f32_bf16 v11, v180, v9, v11\n"
"v_dot2_f32_bf16 v12, v181, v8, v12\n"
"v_dot2_f32_bf16 v13, v181, v9, v13\n"
"v_dot2_f32_bf16 v14, v182, v8, v14\n"
"v_dot2_f32_bf16 v15, v182, v9, v15\n"
"v_dot2_f32_bf16 v16, v183, v8, v16\n"
"v_dot2_f32_bf16 v17, v183, v9, v17\n"
"v_dot2_f32_bf16 v10, v184, v8, v10\n"
"v_dot2_f32_bf16 v11, v184, v9, v11\n"
"v_dot2_f32_bf16 v12, v185, v8, v12\n"
"v_dot2_f32_bf16 v13, v185, v9, v13\n"
"v_dot2_f32_bf16 v14, v186, v8, v14\n"
"v_dot2_f32_bf16 v15, v186, v9, v15\n"
"v_dot2_f32_bf16 v16, v187, v8, v16\n"
"v_dot2_f32_bf16 v17, v187, v9, v17\n"
"v_dot2_f32_bf16 v10, v188, v8, v10\n"
"v_dot2_f32_bf16 v11, v188, v9, v11\n"
"v_dot2_f32_bf16 v12, v189, v8, v12\n"
"v_dot2_f32_bf16 v13, v189, v9, v13\n"
"v_dot2_f32_bf16 v14, v190, v8, v14\n"
"v_dot2_f32_bf16 v15, v190, v9, v15\n"
"v_dot2_f32_bf16 v16, v191, v8, v16\n"
"v_dot2_f32_bf16 v17, v191, v9, v17\n"
"v_lshlrev_b32 v232, 16, v48\n"
"v_and_b32 v233, 0xffff0000, v48\n"
"v_lshlrev_b32 v234, 16, v49\n"
"v_and_b32 v235, 0xffff0000, v49\n"
"v_lshlrev_b32 v236, 16, v50\n"
"v_and_b32 v237, 0xffff0000, v50\n"
"v_lshlrev_b32 v238, 16, v51\n"
"v_and_b32 v239, 0xffff0000, v51\n"
"v_pk_fma_f32 v[232:233], v[10:11], v[192:193], v[232:233]\n"
"v_pk_fma_f32 v[234:235], v[12:13], v[194:195], v[234:235]\n"
"v_pk_fma_f32 v[236:237], v[14:15], v[196:197], v[236:237]\n"
"v_pk_fma_f32 v[238:239], v[16:17], v[198:199], v[238:239]\n"
"v_cvt_pk_bf16_f32 v248, v232, v233\n"
"v_cvt_pk_bf16_f32 v249, v234, v235\n"
"v_cvt_pk_bf16_f32 v250, v236, v237\n"
"v_cvt_pk_bf16_f32 v251, v238, v239\n"
"s_add_u32 s50, s48, 32768\n"
"buffer_store_dwordx2 v[248:249], v52, s[28:31], s50 offen\n"
"buffer_store_dwordx2 v[250:251], v52, s[28:31], s50 offen offset:32\n"
"LPX_%=:\n"
"; ---- end of section: red\n"
"s_cmp_lg_u32 s42, 0\n"
"s_cbranch_scc1 LEND_%=\n"
"s_mov_b32 s47, 131072\n"
"s_mov_b32 exec_hi, 0\n"
"LHFA_%=:\n"
"global_load_dwordx2 v[56:57], v60, s[86:87] offset:512 sc1\n"
"global_load_dwordx2 v[62:63], v60, s[86:87] sc1\n"
"s_waitcnt vmcnt(0)\n"
"v_cmp_ne_u64 vcc, v[56:57], v[58:59]\n"
"s_nop 4\n"
"s_cbranch_vccz LEND_%=\n"
"v_mov_b32 v58, v62\n"
"v_mov_b32 v59, v63\n"
"global_store_dwordx2 v60, v[58:59], s[86:87] offset:256 sc1\n"
"s_sleep 8\n"
"s_sub_u32 s47, s47, 1\n"
"s_cmp_lg_u32 s47, 0\n"
"s_cbranch_scc1 LHFA_%=\n"
"s_branch LEND_%=\n"
"LSLOW_%=:\n"
"s_nop 7\n"
"s_nop 7\n"
"s_waitcnt vmcnt(0) lgkmcnt(0)\n"
"s_barrier\n"
"v_and_b32 v2, 15, v1\n"
"v_lshrrev_b32 v3, 4, v1\n"
"v_xor_b32 v3, v3, v2\n"
"v_lshlrev_b32 v3, 4, v3\n"
"v_lshl_add_u32 v3, v2, 8, v3\n"
"v_xor_b32 v38, 0, v3\n"
"v_xor_b32 v39, 64, v3\n"
"v_xor_b32 v40, 128, v3\n"
"v_xor_b32 v41, 192, v3\n"
"v_bfe_u32 v2, v1, 2, 3\n"
"v_lshrrev_b32 v3, 5, v1\n"
"v_and_b32 v4, 3, v1\n"
"v_lshlrev_b32 v3, 11, v3\n"
"v_lshl_add_u32 v3, v2, 7, v3\n"
"v_lshl_add_u32 v3, v4, 3, v3\n"
"v_add_u32 v3, 90112, v3\n"
"v_bfe_u32 v2, v2, 1, 2\n"
"v_xor_b32 v4, 0, v2\n"
"v_lshl_add_u32 v42, v4, 5, v3\n"
"v_xor_b32 v4, 1, v2\n"
"v_lshl_add_u32 v43, v4, 5, v3\n"
"v_xor_b32 v4, 2, v2\n"
"v_lshl_add_u32 v44, v4, 5, v3\n"
"v_xor_b32 v4, 3, v2\n"
"v_lshl_add_u32 v45, v4, 5, v3\n"
"v_lshrrev_b32 v2, 4, v1\n"
"s_lshl_b32 s53, s42, 3\n"
"v_add_u32 v2, s53, v2\n"
"v_and_b32 v3, 15, v1\n"
"v_add_u32 v4, 0, v2\n"
"v_and_b32 v5, 15, v4\n"
"v_xor_b32 v5, v5, v3\n"
"v_lshlrev_b32 v5, 4, v5\n"
"v_lshl_add_u32 v5, v4, 8, v5\n"
"v_subrev_u32 v46, 0, v5\n"
"v_add_u32 v4, 4, v2\n"
"v_and_b32 v5, 15, v4\n"
"v_xor_b32 v5, v5, v3\n"
"v_lshlrev_b32 v5, 4, v5\n"
"v_lshl_add_u32 v5, v4, 8, v5\n"
"v_subrev_u32 v47, 1024, v5\n"
"v_lshrrev_b32 v2, 3, v1\n"
"v_bfe_u32 v3, v1, 1, 2\n"
"v_bfe_u32 v4, v2, 1, 2\n"
"v_xor_b32 v3, v3, v4\n"
"v_lshlrev_b32 v3, 5, v3\n"
"v_and_b32 v4, 1, v1\n"
"v_lshl_add_u32 v3, v4, 4, v3\n"
"s_mov_b32 s53, 256\n"
"v_mul_lo_u32 v2, v2, s53\n"
"v_add_u32 v2, v2, v3\n"
"s_lshl_b32 s53, s42, 11\n"
"v_add_u32 v2, s53, v2\n"
"v_add_u32 v48, 4096, v2\n"
"v_add_u32 v49, 3200, v2\n"
"s_lshl_b32 s57, s42, 11\n"
"s_mov_b32 s60, 0\n"
"s_mov_b32 s61, 0\n"
"s_mov_b32 s55, 4294885376\n"
"s_mov_b32 s56, 16384\n"
"s_mov_b32 s62, 0\n"
"s_mov_b32 s63, 5\n"
"v_mov_b32 v50, 0x3f803f80\n"
"v_mov_b32 v51, 0x3f803f80\n"
"v_mov_b32 v52, 0x3f803f80\n"
"v_mov_b32 v53, 0x3f803f80\n"
"v_subrev_u32 v42, 81920, v42\n"
"v_subrev_u32 v43, 81920, v43\n"
"v_subrev_u32 v44, 81920, v44\n"
"v_subrev_u32 v45, 81920, v45\n"
"v_mov_b32 v58, 0xff800000\n"
"v_mov_b32 v59, 0xff800000\n"
"v_mov_b32 v60, 0xff800000\n"
"v_mov_b32 v61, 0xff800000\n"
"v_mov_b32 v62, 0xff800000\n"
"v_mov_b32 v63, 0xff800000\n"
"v_accvgpr_write_b32 a0, 0\n"
"v_accvgpr_write_b32 a1, 0\n"
"v_accvgpr_write_b32 a2, 0\n"
"v_accvgpr_write_b32 a3, 0\n"
"v_accvgpr_write_b32 a4, 0\n"
"v_accvgpr_write_b32 a5, 0\n"
"v_accvgpr_write_b32 a6, 0\n"
"v_accvgpr_write_b32 a7, 0\n"
"v_accvgpr_write_b32 a8, 0\n"
"v_accvgpr_write_b32 a9, 0\n"
"v_accvgpr_write_b32 a10, 0\n"
"v_accvgpr_write_b32 a11, 0\n"
"v_accvgpr_write_b32 a12, 0\n"
"v_accvgpr_write_b32 a13, 0\n"
"v_accvgpr_write_b32 a14, 0\n"
"v_accvgpr_write_b32 a15, 0\n"
"v_accvgpr_write_b32 a16, 0\n"
"v_accvgpr_write_b32 a17, 0\n"
"v_accvgpr_write_b32 a18, 0\n"
"v_accvgpr_write_b32 a19, 0\n"
"v_accvgpr_write_b32 a20, 0\n"
"v_accvgpr_write_b32 a21, 0\n"
"v_accvgpr_write_b32 a22, 0\n"
"v_accvgpr_write_b32 a23, 0\n"
"v_accvgpr_write_b32 a24, 0\n"
"v_accvgpr_write_b32 a25, 0\n"
"v_accvgpr_write_b32 a26, 0\n"
"v_accvgpr_write_b32 a27, 0\n"
"v_accvgpr_write_b32 a28, 0\n"
"v_accvgpr_write_b32 a29, 0\n"
"v_accvgpr_write_b32 a30, 0\n"
"v_accvgpr_write_b32 a31, 0\n"
"v_accvgpr_write_b32 a32, 0\n"
"v_accvgpr_write_b32 a33, 0\n"
"v_accvgpr_write_b32 a34, 0\n"
"v_accvgpr_write_b32 a35, 0\n"
"v_accvgpr_write_b32 a36, 0\n"
"v_accvgpr_write_b32 a37, 0\n"
"v_accvgpr_write_b32 a38, 0\n"
"v_accvgpr_write_b32 a39, 0\n"
"v_accvgpr_write_b32 a40, 0\n"
"v_accvgpr_write_b32 a41, 0\n"
"v_accvgpr_write_b32 a42, 0\n"
"v_accvgpr_write_b32 a43, 0\n"
"v_accvgpr_write_b32 a44, 0\n"
"v_accvgpr_write_b32 a45, 0\n"
"v_accvgpr_write_b32 a46, 0\n"
"v_accvgpr_write_b32 a47, 0\n"
"v_accvgpr_write_b32 a48, 0\n"
"v_accvgpr_write_b32 a49, 0\n"
"v_accvgpr_write_b32 a50, 0\n"
"v_accvgpr_write_b32 a51, 0\n"
"v_accvgpr_write_b32 a52, 0\n"
"v_accvgpr_write_b32 a53, 0\n"
"v_accvgpr_write_b32 a54, 0\n"
"v_accvgpr_write_b32 a55, 0\n"
"v_accvgpr_write_b32 a56, 0\n"
"v_accvgpr_write_b32 a57, 0\n"
"v_accvgpr_write_b32 a58, 0\n"
"v_accvgpr_write_b32 a59, 0\n"
"v_accvgpr_write_b32 a60, 0\n"
"v_accvgpr_write_b32 a61, 0\n"
"v_accvgpr_write_b32 a62, 0\n"
"v_accvgpr_write_b32 a63, 0\n"
"v_accvgpr_write_b32 a64, 0\n"
"v_accvgpr_write_b32 a65, 0\n"
"v_accvgpr_write_b32 a66, 0\n"
"v_accvgpr_write_b32 a67, 0\n"
"v_accvgpr_write_b32 a68, 0\n"
"v_accvgpr_write_b32 a69, 0\n"
"v_accvgpr_write_b32 a70, 0\n"
"v_accvgpr_write_b32 a71, 0\n"
"v_accvgpr_write_b32 a72, 0\n"
"v_accvgpr_write_b32 a73, 0\n"
"v_accvgpr_write_b32 a74, 0\n"
"v_accvgpr_write_b32 a75, 0\n"
"v_accvgpr_write_b32 a76, 0\n"
"v_accvgpr_write_b32 a77, 0\n"
"v_accvgpr_write_b32 a78, 0\n"
"v_accvgpr_write_b32 a79, 0\n"
"v_accvgpr_write_b32 a80, 0\n"
"v_accvgpr_write_b32 a81, 0\n"
"v_accvgpr_write_b32 a82, 0\n"
"v_accvgpr_write_b32 a83, 0\n"
"v_accvgpr_write_b32 a84, 0\n"
"v_accvgpr_write_b32 a85, 0\n"
"v_accvgpr_write_b32 a86, 0\n"
"v_accvgpr_write_b32 a87, 0\n"
"v_accvgpr_write_b32 a88, 0\n"
"v_accvgpr_write_b32 a89, 0\n"
"v_accvgpr_write_b32 a90, 0\n"
"v_accvgpr_write_b32 a91, 0\n"
"v_accvgpr_write_b32 a92, 0\n"
"v_accvgpr_write_b32 a93, 0\n"
"v_accvgpr_write_b32 a94, 0\n"
"v_accvgpr_write_b32 a95, 0\n"
"v_accvgpr_write_b32 a96, 0\n"
"v_accvgpr_write_b32 a97, 0\n"
"v_accvgpr_write_b32 a98, 0\n"
"v_accvgpr_write_b32 a99, 0\n"
"v_accvgpr_write_b32 a100, 0\n"
"v_accvgpr_write_b32 a101, 0\n"
"v_accvgpr_write_b32 a102, 0\n"
"v_accvgpr_write_b32 a103, 0\n"
"v_accvgpr_write_b32 a104, 0\n"
"v_accvgpr_write_b32 a105, 0\n"
"v_accvgpr_write_b32 a106, 0\n"
"v_accvgpr_write_b32 a107, 0\n"
"v_accvgpr_write_b32 a108, 0\n"
"v_accvgpr_write_b32 a109, 0\n"
"v_accvgpr_write_b32 a110, 0\n"
"v_accvgpr_write_b32 a111, 0\n"
"v_accvgpr_write_b32 a112, 0\n"
"v_accvgpr_write_b32 a113, 0\n"
"v_accvgpr_write_b32 a114, 0\n"
"v_accvgpr_write_b32 a115, 0\n"
"v_accvgpr_write_b32 a116, 0\n"
"v_accvgpr_write_b32 a117, 0\n"
"v_accvgpr_write_b32 a118, 0\n"
"v_accvgpr_write_b32 a119, 0\n"
"v_accvgpr_write_b32 a120, 0\n"
"v_accvgpr_write_b32 a121, 0\n"
"v_accvgpr_write_b32 a122, 0\n"
"v_accvgpr_write_b32 a123, 0\n"
"v_accvgpr_write_b32 a124, 0\n"
"v_accvgpr_write_b32 a125, 0\n"
"v_accvgpr_write_b32 a126, 0\n"
"v_accvgpr_write_b32 a127, 0\n"
"v_accvgpr_write_b32 a128, 0\n"
"v_accvgpr_write_b32 a129, 0\n"
"v_accvgpr_write_b32 a130, 0\n"
"v_accvgpr_write_b32 a131, 0\n"
"v_accvgpr_write_b32 a132, 0\n"
"v_accvgpr_write_b32 a133, 0\n"
"v_accvgpr_write_b32 a134, 0\n"
"v_accvgpr_write_b32 a135, 0\n"
"v_accvgpr_write_b32 a136, 0\n"
"v_accvgpr_write_b32 a137, 0\n"
"v_accvgpr_write_b32 a138, 0\n"
"v_accvgpr_write_b32 a139, 0\n"
"v_accvgpr_write_b32 a140, 0\n"
"v_accvgpr_write_b32 a141, 0\n"
"v_accvgpr_write_b32 a142, 0\n"
"v_accvgpr_write_b32 a143, 0\n"
"v_accvgpr_write_b32 a144, 0\n"
"v_accvgpr_write_b32 a145, 0\n"
"v_accvgpr_write_b32 a146, 0\n"
"v_accvgpr_write_b32 a147, 0\n"
"v_accvgpr_write_b32 a148, 0\n"
"v_accvgpr_write_b32 a149, 0\n"
"v_accvgpr_write_b32 a150, 0\n"
"v_accvgpr_write_b32 a151, 0\n"
"v_accvgpr_write_b32 a152, 0\n"
"v_accvgpr_write_b32 a153, 0\n"
"v_accvgpr_write_b32 a154, 0\n"
"v_accvgpr_write_b32 a155, 0\n"
"v_accvgpr_write_b32 a156, 0\n"
"v_accvgpr_write_b32 a157, 0\n"
"v_accvgpr_write_b32 a158, 0\n"
"v_accvgpr_write_b32 a159, 0\n"
"v_accvgpr_write_b32 a160, 0\n"
"v_accvgpr_write_b32 a161, 0\n"
"v_accvgpr_write_b32 a162, 0\n"
"v_accvgpr_write_b32 a163, 0\n"
"v_accvgpr_write_b32 a164, 0\n"
"v_accvgpr_write_b32 a165, 0\n"
"v_accvgpr_write_b32 a166, 0\n"
"v_accvgpr_write_b32 a167, 0\n"
"v_accvgpr_write_b32 a168, 0\n"
"v_accvgpr_write_b32 a169, 0\n"
"v_accvgpr_write_b32 a170, 0\n"
"v_accvgpr_write_b32 a171, 0\n"
"v_accvgpr_write_b32 a172, 0\n"
"v_accvgpr_write_b32 a173, 0\n"
"v_accvgpr_write_b32 a174, 0\n"
"v_accvgpr_write_b32 a175, 0\n"
"v_accvgpr_write_b32 a176, 0\n"
"v_accvgpr_write_b32 a177, 0\n"
"v_accvgpr_write_b32 a178, 0\n"
"v_accvgpr_write_b32 a179, 0\n"
"v_accvgpr_write_b32 a180, 0\n"
"v_accvgpr_write_b32 a181, 0\n"
"v_accvgpr_write_b32 a182, 0\n"
"v_accvgpr_write_b32 a183, 0\n"
"v_accvgpr_write_b32 a184, 0\n"
"v_accvgpr_write_b32 a185, 0\n"
"v_accvgpr_write_b32 a186, 0\n"
"v_accvgpr_write_b32 a187, 0\n"
"v_accvgpr_write_b32 a188, 0\n"
"v_accvgpr_write_b32 a189, 0\n"
"v_accvgpr_write_b32 a190, 0\n"
"v_accvgpr_write_b32 a191, 0\n"
"v_mov_b32 v10, 0\n"
"v_mov_b32 v11, 0\n"
"v_mov_b32 v12, 0\n"
"v_mov_b32 v13, 0\n"
"v_mov_b32 v14, 0\n"
"v_mov_b32 v15, 0\n"
"v_mov_b32 v16, 0\n"
"v_mov_b32 v17, 0\n"
"v_mov_b32 v18, 0\n"
"v_mov_b32 v19, 0\n"
"v_mov_b32 v20, 0\n"
"v_mov_b32 v21, 0\n"
"v_mov_b32 v22, 0\n"
"v_mov_b32 v23, 0\n"
"v_mov_b32 v24, 0\n"
"v_mov_b32 v25, 0\n"
"v_mov_b32 v26, 0\n"
"v_mov_b32 v27, 0\n"
"v_mov_b32 v28, 0\n"
"v_mov_b32 v29, 0\n"
"v_mov_b32 v54, 0\n"
"v_mov_b32 v55, 0\n"
"v_mov_b32 v56, 0\n"
"v_mov_b32 v57, 0\n"
"s_mov_b32 s47, 16\n"
"LS1_%=:\n"
"s_barrier\n"
"s_add_u32 m0, s61, s57\n"
"s_nop 0\n"
"buffer_load_dwordx4 v46, s[84:87], s60 offen lds\n"
"buffer_load_dwordx4 v47, s[84:87], s60 offen offset:1024 lds\n"
"s_add_u32 m0, m0, 8192\n"
"s_nop 0\n"
"buffer_load_dwordx4 v48, s[88:91], s60 offen lds\n"
"buffer_load_dwordx4 v49, s[88:91], s60 offen offset:1024 lds\n"
"s_add_u32 s60, s60, 8192\n"
"s_waitcnt vmcnt(0)\n"
"s_barrier\n"
"ds_read_b128 a[192:195], v38 offset:0\n"
"ds_read_b128 a[196:199], v39 offset:0\n"
"ds_read_b128 a[200:203], v40 offset:0\n"
"ds_read_b128 a[204:207], v41 offset:0\n"
"ds_read_b128 a[208:211], v38 offset:4096\n"
"ds_read_b128 a[212:215], v39 offset:4096\n"
"ds_read_b128 a[216:219], v40 offset:4096\n"
"ds_read_b128 a[220:223], v41 offset:4096\n"
"s_waitcnt lgkmcnt(7)\n"
"v_mfma_f32_16x16x32_bf16 v[68:71], a[192:195], v[64:67], 0\n"
"s_waitcnt lgkmcnt(6)\n"
"v_mfma_f32_16x16x32_bf16 v[68:71], a[196:199], v[112:115], v[68:71]\n"
"s_waitcnt lgkmcnt(5)\n"
"v_mfma_f32_16x16x32_bf16 v[68:71], a[200:203], v[160:163], v[68:71]\n"
"s_waitcnt lgkmcnt(4)\n"
"v_mfma_f32_16x16x32_bf16 v[68:71], a[204:207], v[208:211], v[68:71]\n"
"v_mfma_f32_16x16x32_bf16 v[76:79], a[192:195], v[72:75], 0\n"
"v_mfma_f32_16x16x32_bf16 v[76:79], a[196:199], v[120:123], v[76:79]\n"
"v_mfma_f32_16x16x32_bf16 v[76:79], a[200:203], v[168:171], v[76:79]\n"
"v_mfma_f32_16x16x32_bf16 v[76:79], a[204:207], v[216:219], v[76:79]\n"
"v_mfma_f32_16x16x32_bf16 v[84:87], a[192:195], v[80:83], 0\n"
"v_mfma_f32_16x16x32_bf16 v[84:87], a[196:199], v[128:131], v[84:87]\n"
"v_mfma_f32_16x16x32_bf16 v[84:87], a[200:203], v[176:179], v[84:87]\n"
"v_mfma_f32_16x16x32_bf16 v[84:87], a[204:207], v[224:227], v[84:87]\n"
"v_mfma_f32_16x16x32_bf16 v[92:95], a[192:195], v[88:91], 0\n"
"v_mfma_f32_16x16x32_bf16 v[92:95], a[196:199], v[136:139], v[92:95]\n"
"v_mfma_f32_16x16x32_bf16 v[92:95], a[200:203], v[184:187], v[92:95]\n"
"v_mfma_f32_16x16x32_bf16 v[92:95], a[204:207], v[232:235], v[92:95]\n"
"v_mfma_f32_16x16x32_bf16 v[100:103], a[192:195], v[96:99], 0\n"
"v_mfma_f32_16x16x32_bf16 v[100:103], a[196:199], v[144:147], v[100:103]\n"
"v_mfma_f32_16x16x32_bf16 v[100:103], a[200:203], v[192:195], v[100:103]\n"
"v_mfma_f32_16x16x32_bf16 v[100:103], a[204:207], v[240:243], v[100:103]\n"
"v_mfma_f32_16x16x32_bf16 v[108:111], a[192:195], v[104:107], 0\n"
"v_mfma_f32_16x16x32_bf16 v[108:111], a[196:199], v[152:155], v[108:111]\n"
"v_mfma_f32_16x16x32_bf16 v[108:111], a[200:203], v[200:203], v[108:111]\n"
"v_mfma_f32_16x16x32_bf16 v[108:111], a[204:207], v[248:251], v[108:111]\n"
"s_waitcnt lgkmcnt(0)\n"
"v_mfma_f32_16x16x32_bf16 v[116:119], a[208:211], v[64:67], 0\n"
"v_mfma_f32_16x16x32_bf16 v[116:119], a[212:215], v[112:115], v[116:119]\n"
"v_mfma_f32_16x16x32_bf16 v[116:119], a[216:219], v[160:163], v[116:119]\n"
"v_mfma_f32_16x16x32_bf16 v[116:119], a[220:223], v[208:211], v[116:119]\n"
"v_mfma_f32_16x16x32_bf16 v[124:127], a[208:211], v[72:75], 0\n"
"v_mfma_f32_16x16x32_bf16 v[124:127], a[212:215], v[120:123], v[124:127]\n"
"v_mfma_f32_16x16x32_bf16 v[124:127], a[216:219], v[168:171], v[124:127]\n"
"v_mfma_f32_16x16x32_bf16 v[124:127], a[220:223], v[216:219], v[124:127]\n"
"v_mfma_f32_16x16x32_bf16 v[132:135], a[208:211], v[80:83], 0\n"
"v_mfma_f32_16x16x32_bf16 v[132:135], a[212:215], v[128:131], v[132:135]\n"
"v_mfma_f32_16x16x32_bf16 v[132:135], a[216:219], v[176:179], v[132:135]\n"
"v_mfma_f32_16x16x32_bf16 v[132:135], a[220:223], v[224:227], v[132:135]\n"
"v_mfma_f32_16x16x32_bf16 v[140:143], a[208:211], v[88:91], 0\n"
"v_mfma_f32_16x16x32_bf16 v[140:143], a[212:215], v[136:139], v[140:143]\n"
"v_mfma_f32_16x16x32_bf16 v[140:143], a[216:219], v[184:187], v[140:143]\n"
"v_mfma_f32_16x16x32_bf16 v[140:143], a[220:223], v[232:235], v[140:143]\n"
"v_mfma_f32_16x16x32_bf16 v[148:151], a[208:211], v[96:99], 0\n"
"v_mfma_f32_16x16x32_bf16 v[148:151], a[212:215], v[144:147], v[148:151]\n"
"v_mfma_f32_16x16x32_bf16 v[148:151], a[216:219], v[192:195], v[148:151]\n"
"v_mfma_f32_16x16x32_bf16 v[148:151], a[220:223], v[240:243], v[148:151]\n"
"v_mfma_f32_16x16x32_bf16 v[156:159], a[208:211], v[104:107], 0\n"
"v_mfma_f32_16x16x32_bf16 v[156:159], a[212:215], v[152:155], v[156:159]\n"
"v_mfma_f32_16x16x32_bf16 v[156:159], a[216:219], v[200:203], v[156:159]\n"
"v_mfma_f32_16x16x32_bf16 v[156:159], a[220:223], v[248:251], v[156:159]\n"
"v_max3_f32 v58, v68, v69, v58\n"
"v_max3_f32 v58, v70, v71, v58\n"
"v_max3_f32 v58, v116, v117, v58\n"
"v_max3_f32 v58, v118, v119, v58\n"
"v_max3_f32 v59, v76, v77, v59\n"
"v_max3_f32 v59, v78, v79, v59\n"
"v_max3_f32 v59, v124, v125, v59\n"
"v_max3_f32 v59, v126, v127, v59\n"
"v_max3_f32 v60, v84, v85, v60\n"
"v_max3_f32 v60, v86, v87, v60\n"
"v_max3_f32 v60, v132, v133, v60\n"
"v_max3_f32 v60, v134, v135, v60\n"
"v_max3_f32 v61, v92, v93, v61\n"
"v_max3_f32 v61, v94, v95, v61\n"
"v_max3_f32 v61, v140, v141, v61\n"
"v_max3_f32 v61, v142, v143, v61\n"
"v_max3_f32 v62, v100, v101, v62\n"
"v_max3_f32 v62, v102, v103, v62\n"
"v_max3_f32 v62, v148, v149, v62\n"
"v_max3_f32 v62, v150, v151, v62\n"
"v_max3_f32 v63, v108, v109, v63\n"
"v_max3_f32 v63, v110, v111, v63\n"
"v_max3_f32 v63, v156, v157, v63\n"
"v_max3_f32 v63, v158, v159, v63\n"
"s_waitcnt lgkmcnt(0)\n"
"s_sub_u32 s47, s47, 1\n"
"s_cmp_lg_u32 s47, 0\n"
"s_cbranch_scc1 LS1_%=\n"
"v_xor_b32 v2, 16, v1\n"
"v_lshlrev_b32 v2, 2, v2\n"
"v_xor_b32 v3, 32, v1\n"
"v_lshlrev_b32 v3, 2, v3\n"
"ds_bpermute_b32 v4, v2, v58\n"
"s_waitcnt lgkmcnt(0)\n"
"v_max_f32 v58, v58, v4\n"
"ds_bpermute_b32 v4, v3, v58\n"
"s_waitcnt lgkmcnt(0)\n"
"v_max_f32 v58, v58, v4\n"
"ds_bpermute_b32 v4, v2, v59\n"
"s_waitcnt lgkmcnt(0)\n"
"v_max_f32 v59, v59, v4\n"
"ds_bpermute_b32 v4, v3, v59\n"
"s_waitcnt lgkmcnt(0)\n"
"v_max_f32 v59, v59, v4\n"
"ds_bpermute_b32 v4, v2, v60\n"
"s_waitcnt lgkmcnt(0)\n"
"v_max_f32 v60, v60, v4\n"
"ds_bpermute_b32 v4, v3, v60\n"
"s_waitcnt lgkmcnt(0)\n"
"v_max_f32 v60, v60, v4\n"
"ds_bpermute_b32 v4, v2, v61\n"
"s_waitcnt lgkmcnt(0)\n"
"v_max_f32 v61, v61, v4\n"
"ds_bpermute_b32 v4, v3, v61\n"
"s_waitcnt lgkmcnt(0)\n"
"v_max_f32 v61, v61, v4\n"
"ds_bpermute_b32 v4, v2, v62\n"
"s_waitcnt lgkmcnt(0)\n"
"v_max_f32 v62, v62, v4\n"
"ds_bpermute_b32 v4, v3, v62\n"
"s_waitcnt lgkmcnt(0)\n"
"v_max_f32 v62, v62, v4\n"
"ds_bpermute_b32 v4, v2, v63\n"
"s_waitcnt lgkmcnt(0)\n"
"v_max_f32 v63, v63, v4\n"
"ds_bpermute_b32 v4, v3, v63\n"
"s_waitcnt lgkmcnt(0)\n"
"v_max_f32 v63, v63, v4\n"
"s_mov_b32 s60, 0\n"
"s_mov_b32 s47, 16\n"
"LS2_%=:\n"
"s_barrier\n"
"s_add_u32 m0, s61, s57\n"
"s_nop 0\n"
"buffer_load_dwordx4 v46, s[84:87], s60 offen lds\n"
"buffer_load_dwordx4 v47, s[84:87], s60 offen offset:1024 lds\n"
"s_add_u32 m0, m0, 8192\n"
"s_nop 0\n"
"buffer_load_dwordx4 v48, s[88:91], s60 offen lds\n"
"buffer_load_dwordx4 v49, s[88:91], s60 offen offset:1024 lds\n"
"s_add_u32 s60, s60, 8192\n"
"s_waitcnt vmcnt(0)\n"
"s_barrier\n"
"ds_read_b128 a[192:195], v38 offset:0\n"
"ds_read_b128 a[196:199], v39 offset:0\n"
"ds_read_b128 a[200:203], v40 offset:0\n"
"ds_read_b128 a[204:207], v41 offset:0\n"
"ds_read_b128 a[208:211], v38 offset:4096\n"
"ds_read_b128 a[212:215], v39 offset:4096\n"
"ds_read_b128 a[216:219], v40 offset:4096\n"
"ds_read_b128 a[220:223], v41 offset:4096\n"
"s_waitcnt lgkmcnt(7)\n"
"v_mfma_f32_16x16x32_bf16 v[68:71], a[192:195], v[64:67], 0\n"
"s_waitcnt lgkmcnt(6)\n"
"v_mfma_f32_16x16x32_bf16 v[68:71], a[196:199], v[112:115], v[68:71]\n"
"s_waitcnt lgkmcnt(5)\n"
"v_mfma_f32_16x16x32_bf16 v[68:71], a[200:203], v[160:163], v[68:71]\n"
"s_waitcnt lgkmcnt(4)\n"
"v_mfma_f32_16x16x32_bf16 v[68:71], a[204:207], v[208:211], v[68:71]\n"
"v_mfma_f32_16x16x32_bf16 v[76:79], a[192:195], v[72:75], 0\n"
"v_mfma_f32_16x16x32_bf16 v[76:79], a[196:199], v[120:123], v[76:79]\n"
"v_mfma_f32_16x16x32_bf16 v[76:79], a[200:203], v[168:171], v[76:79]\n"
"v_mfma_f32_16x16x32_bf16 v[76:79], a[204:207], v[216:219], v[76:79]\n"
"v_mfma_f32_16x16x32_bf16 v[84:87], a[192:195], v[80:83], 0\n"
"v_mfma_f32_16x16x32_bf16 v[84:87], a[196:199], v[128:131], v[84:87]\n"
"v_mfma_f32_16x16x32_bf16 v[84:87], a[200:203], v[176:179], v[84:87]\n"
"v_mfma_f32_16x16x32_bf16 v[84:87], a[204:207], v[224:227], v[84:87]\n"
"v_mfma_f32_16x16x32_bf16 v[92:95], a[192:195], v[88:91], 0\n"
"v_mfma_f32_16x16x32_bf16 v[92:95], a[196:199], v[136:139], v[92:95]\n"
"v_mfma_f32_16x16x32_bf16 v[92:95], a[200:203], v[184:187], v[92:95]\n"
"v_mfma_f32_16x16x32_bf16 v[92:95], a[204:207], v[232:235], v[92:95]\n"
"v_mfma_f32_16x16x32_bf16 v[100:103], a[192:195], v[96:99], 0\n"
"v_mfma_f32_16x16x32_bf16 v[100:103], a[196:199], v[144:147], v[100:103]\n"
"v_mfma_f32_16x16x32_bf16 v[100:103], a[200:203], v[192:195], v[100:103]\n"
"v_mfma_f32_16x16x32_bf16 v[100:103], a[204:207], v[240:243], v[100:103]\n"
"v_mfma_f32_16x16x32_bf16 v[108:111], a[192:195], v[104:107], 0\n"
"v_mfma_f32_16x16x32_bf16 v[108:111], a[196:199], v[152:155], v[108:111]\n"
"v_mfma_f32_16x16x32_bf16 v[108:111], a[200:203], v[200:203], v[108:111]\n"
"v_mfma_f32_16x16x32_bf16 v[108:111], a[204:207], v[248:251], v[108:111]\n"
"s_waitcnt lgkmcnt(0)\n"
"v_mfma_f32_16x16x32_bf16 v[116:119], a[208:211], v[64:67], 0\n"
"v_mfma_f32_16x16x32_bf16 v[116:119], a[212:215], v[112:115], v[116:119]\n"
"v_mfma_f32_16x16x32_bf16 v[116:119], a[216:219], v[160:163], v[116:119]\n"
"v_mfma_f32_16x16x32_bf16 v[116:119], a[220:223], v[208:211], v[116:119]\n"
"v_mfma_f32_16x16x32_bf16 v[124:127], a[208:211], v[72:75], 0\n"
"v_mfma_f32_16x16x32_bf16 v[124:127], a[212:215], v[120:123], v[124:127]\n"
"v_mfma_f32_16x16x32_bf16 v[124:127], a[216:219], v[168:171], v[124:127]\n"
"v_mfma_f32_16x16x32_bf16 v[124:127], a[220:223], v[216:219], v[124:127]\n"
"v_mfma_f32_16x16x32_bf16 v[132:135], a[208:211], v[80:83], 0\n"
"v_mfma_f32_16x16x32_bf16 v[132:135], a[212:215], v[128:131], v[132:135]\n"
"v_mfma_f32_16x16x32_bf16 v[132:135], a[216:219], v[176:179], v[132:135]\n"
"v_mfma_f32_16x16x32_bf16 v[132:135], a[220:223], v[224:227], v[132:135]\n"
"v_mfma_f32_16x16x32_bf16 v[140:143], a[208:211], v[88:91], 0\n"
"v_mfma_f32_16x16x32_bf16 v[140:143], a[212:215], v[136:139], v[140:143]\n"
"v_mfma_f32_16x16x32_bf16 v[140:143], a[216:219], v[184:187], v[140:143]\n"
"v_mfma_f32_16x16x32_bf16 v[140:143], a[220:223], v[232:235], v[140:143]\n"
"v_mfma_f32_16x16x32_bf16 v[148:151], a[208:211], v[96:99], 0\n"
"v_mfma_f32_16x16x32_bf16 v[148:151], a[212:215], v[144:147], v[148:151]\n"
"v_mfma_f32_16x16x32_bf16 v[148:151], a[216:219], v[192:195], v[148:151]\n"
"v_mfma_f32_16x16x32_bf16 v[148:151], a[220:223], v[240:243], v[148:151]\n"
"v_mfma_f32_16x16x32_bf16 v[156:159], a[208:211], v[104:107], 0\n"
"v_mfma_f32_16x16x32_bf16 v[156:159], a[212:215], v[152:155], v[156:159]\n"
"v_mfma_f32_16x16x32_bf16 v[156:159], a[216:219], v[200:203], v[156:159]\n"
"v_mfma_f32_16x16x32_bf16 v[156:159], a[220:223], v[248:251], v[156:159]\n"
"v_sub_f32 v68, v68, v58\n"
"v_sub_f32 v69, v69, v58\n"
"v_sub_f32 v70, v70, v58\n"
"v_sub_f32 v71, v71, v58\n"
"v_exp_f32 v68, v68\n"
"v_exp_f32 v69, v69\n"
"v_exp_f32 v70, v70\n"
"v_exp_f32 v71, v71\n"
"s_nop 0\n"
"v_cvt_pk_bf16_f32 v164, v68, v69\n"
"v_cvt_pk_bf16_f32 v165, v70, v71\n"
"v_sub_f32 v76, v76, v59\n"
"v_sub_f32 v77, v77, v59\n"
"v_sub_f32 v78, v78, v59\n"
"v_sub_f32 v79, v79, v59\n"
"v_exp_f32 v76, v76\n"
"v_exp_f32 v77, v77\n"
"v_exp_f32 v78, v78\n"
"v_exp_f32 v79, v79\n"
"s_nop 0\n"
"v_cvt_pk_bf16_f32 v172, v76, v77\n"
"v_cvt_pk_bf16_f32 v173, v78, v79\n"
"v_sub_f32 v84, v84, v60\n"
"v_sub_f32 v85, v85, v60\n"
"v_sub_f32 v86, v86, v60\n"
"v_sub_f32 v87, v87, v60\n"
"v_exp_f32 v84, v84\n"
"v_exp_f32 v85, v85\n"
"v_exp_f32 v86, v86\n"
"v_exp_f32 v87, v87\n"
"s_nop 0\n"
"v_cvt_pk_bf16_f32 v180, v84, v85\n"
"v_cvt_pk_bf16_f32 v181, v86, v87\n"
"v_sub_f32 v92, v92, v61\n"
"v_sub_f32 v93, v93, v61\n"
"v_sub_f32 v94, v94, v61\n"
"v_sub_f32 v95, v95, v61\n"
"v_exp_f32 v92, v92\n"
"v_exp_f32 v93, v93\n"
"v_exp_f32 v94, v94\n"
"v_exp_f32 v95, v95\n"
"s_nop 0\n"
"v_cvt_pk_bf16_f32 v188, v92, v93\n"
"v_cvt_pk_bf16_f32 v189, v94, v95\n"
"v_sub_f32 v100, v100, v62\n"
"v_sub_f32 v101, v101, v62\n"
"v_sub_f32 v102, v102, v62\n"
"v_sub_f32 v103, v103, v62\n"
"v_exp_f32 v100, v100\n"
"v_exp_f32 v101, v101\n"
"v_exp_f32 v102, v102\n"
"v_exp_f32 v103, v103\n"
"s_nop 0\n"
"v_cvt_pk_bf16_f32 v196, v100, v101\n"
"v_cvt_pk_bf16_f32 v197, v102, v103\n"
"v_sub_f32 v108, v108, v63\n"
"v_sub_f32 v109, v109, v63\n"
"v_sub_f32 v110, v110, v63\n"
"v_sub_f32 v111, v111, v63\n"
"v_exp_f32 v108, v108\n"
"v_exp_f32 v109, v109\n"
"v_exp_f32 v110, v110\n"
"v_exp_f32 v111, v111\n"
"s_nop 0\n"
"v_cvt_pk_bf16_f32 v204, v108, v109\n"
"v_cvt_pk_bf16_f32 v205, v110, v111\n"
"v_sub_f32 v116, v116, v58\n"
"v_sub_f32 v117, v117, v58\n"
"v_sub_f32 v118, v118, v58\n"
"v_sub_f32 v119, v119, v58\n"
"v_exp_f32 v116, v116\n"
"v_exp_f32 v117, v117\n"
"v_exp_f32 v118, v118\n"
"v_exp_f32 v119, v119\n"
"s_nop 0\n"
"v_cvt_pk_bf16_f32 v166, v116, v117\n"
"v_cvt_pk_bf16_f32 v167, v118, v119\n"
"v_sub_f32 v124, v124, v59\n"
"v_sub_f32 v125, v125, v59\n"
"v_sub_f32 v126, v126, v59\n"
"v_sub_f32 v127, v127, v59\n"
"v_exp_f32 v124, v124\n"
"v_exp_f32 v125, v125\n"
"v_exp_f32 v126, v126\n"
"v_exp_f32 v127, v127\n"
"s_nop 0\n"
"v_cvt_pk_bf16_f32 v174, v124, v125\n"
"v_cvt_pk_bf16_f32 v175, v126, v127\n"
"v_sub_f32 v132, v132, v60\n"
"v_sub_f32 v133, v133, v60\n"
"v_sub_f32 v134, v134, v60\n"
"v_sub_f32 v135, v135, v60\n"
"v_exp_f32 v132, v132\n"
"v_exp_f32 v133, v133\n"
"v_exp_f32 v134, v134\n"
"v_exp_f32 v135, v135\n"
"s_nop 0\n"
"v_cvt_pk_bf16_f32 v182, v132, v133\n"
"v_cvt_pk_bf16_f32 v183, v134, v135\n"
"v_sub_f32 v140, v140, v61\n"
"v_sub_f32 v141, v141, v61\n"
"v_sub_f32 v142, v142, v61\n"
"v_sub_f32 v143, v143, v61\n"
"v_exp_f32 v140, v140\n"
"v_exp_f32 v141, v141\n"
"v_exp_f32 v142, v142\n"
"v_exp_f32 v143, v143\n"
"s_nop 0\n"
"v_cvt_pk_bf16_f32 v190, v140, v141\n"
"v_cvt_pk_bf16_f32 v191, v142, v143\n"
"v_sub_f32 v148, v148, v62\n"
"v_sub_f32 v149, v149, v62\n"
"v_sub_f32 v150, v150, v62\n"
"v_sub_f32 v151, v151, v62\n"
"v_exp_f32 v148, v148\n"
"v_exp_f32 v149, v149\n"
"v_exp_f32 v150, v150\n"
"v_exp_f32 v151, v151\n"
"s_nop 0\n"
"v_cvt_pk_bf16_f32 v198, v148, v149\n"
"v_cvt_pk_bf16_f32 v199, v150, v151\n"
"v_sub_f32 v156, v156, v63\n"
"v_sub_f32 v157, v157, v63\n"
"v_sub_f32 v158, v158, v63\n"
"v_sub_f32 v159, v159, v63\n"
"v_exp_f32 v156, v156\n"
"v_exp_f32 v157, v157\n"
"v_exp_f32 v158, v158\n"
"v_exp_f32 v159, v159\n"
"s_nop 0\n"
"v_cvt_pk_bf16_f32 v206, v156, v157\n"
"v_cvt_pk_bf16_f32 v207, v158, v159\n"
"ds_read_b64_tr_b16 a[224:225], v42 offset:0\n"
"ds_read_b64_tr_b16 a[226:227], v42 offset:4096\n"
"ds_read_b64_tr_b16 a[228:229], v43 offset:0\n"
"ds_read_b64_tr_b16 a[230:231], v43 offset:4096\n"
"ds_read_b64_tr_b16 a[232:233], v44 offset:0\n"
"ds_read_b64_tr_b16 a[234:235], v44 offset:4096\n"
"ds_read_b64_tr_b16 a[236:237], v45 offset:0\n"
"ds_read_b64_tr_b16 a[238:239], v45 offset:4096\n"
"ds_read_b64_tr_b16 a[240:241], v42 offset:1024\n"
"ds_read_b64_tr_b16 a[242:243], v42 offset:5120\n"
"ds_read_b64_tr_b16 a[244:245], v43 offset:1024\n"
"ds_read_b64_tr_b16 a[246:247], v43 offset:5120\n"
"ds_read_b64_tr_b16 a[248:249], v44 offset:1024\n"
"ds_read_b64_tr_b16 a[250:251], v44 offset:5120\n"
"ds_read_b64_tr_b16 a[252:253], v45 offset:1024\n"
"ds_read_b64_tr_b16 a[254:255], v45 offset:5120\n"
"v_mfma_f32_16x16x32_bf16 v[10:13], v[50:53], v[164:167], v[10:13]\n"
"v_mfma_f32_16x16x32_bf16 v[14:17], v[50:53], v[172:175], v[14:17]\n"
"v_mfma_f32_16x16x32_bf16 v[18:21], v[50:53], v[180:183], v[18:21]\n"
"v_mfma_f32_16x16x32_bf16 v[22:25], v[50:53], v[188:191], v[22:25]\n"
"v_mfma_f32_16x16x32_bf16 v[26:29], v[50:53], v[196:199], v[26:29]\n"
"v_mfma_f32_16x16x32_bf16 v[54:57], v[50:53], v[204:207], v[54:57]\n"
"s_waitcnt lgkmcnt(14)\n"
"v_mfma_f32_16x16x32_bf16 a[0:3], a[224:227], v[164:167], a[0:3]\n"
"v_mfma_f32_16x16x32_bf16 a[4:7], a[224:227], v[172:175], a[4:7]\n"
"v_mfma_f32_16x16x32_bf16 a[8:11], a[224:227], v[180:183], a[8:11]\n"
"v_mfma_f32_16x16x32_bf16 a[12:15], a[224:227], v[188:191], a[12:15]\n"
"v_mfma_f32_16x16x32_bf16 a[16:19], a[224:227], v[196:199], a[16:19]\n"
"v_mfma_f32_16x16x32_bf16 a[20:23], a[224:227], v[204:207], a[20:23]\n"
"s_waitcnt lgkmcnt(0)\n"
"v_mfma_f32_16x16x32_bf16 a[24:27], a[228:231], v[164:167], a[24:27]\n"
"v_mfma_f32_16x16x32_bf16 a[28:31], a[228:231], v[172:175], a[28:31]\n"
"v_mfma_f32_16x16x32_bf16 a[32:35], a[228:231], v[180:183], a[32:35]\n"
"v_mfma_f32_16x16x32_bf16 a[36:39], a[228:231], v[188:191], a[36:39]\n"
"v_mfma_f32_16x16x32_bf16 a[40:43], a[228:231], v[196:199], a[40:43]\n"
"v_mfma_f32_16x16x32_bf16 a[44:47], a[228:231], v[204:207], a[44:47]\n"
"v_mfma_f32_16x16x32_bf16 a[48:51], a[232:235], v[164:167], a[48:51]\n"
"v_mfma_f32_16x16x32_bf16 a[52:55], a[232:235], v[172:175], a[52:55]\n"
"v_mfma_f32_16x16x32_bf16 a[56:59], a[232:235], v[180:183], a[56:59]\n"
"v_mfma_f32_16x16x32_bf16 a[60:63], a[232:235], v[188:191], a[60:63]\n"
"v_mfma_f32_16x16x32_bf16 a[64:67], a[232:235], v[196:199], a[64:67]\n"
"v_mfma_f32_16x16x32_bf16 a[68:71], a[232:235], v[204:207], a[68:71]\n"
"v_mfma_f32_16x16x32_bf16 a[72:75], a[236:239], v[164:167], a[72:75]\n"
"v_mfma_f32_16x16x32_bf16 a[76:79], a[236:239], v[172:175], a[76:79]\n"
"v_mfma_f32_16x16x32_bf16 a[80:83], a[236:239], v[180:183], a[80:83]\n"
"v_mfma_f32_16x16x32_bf16 a[84:87], a[236:239], v[188:191], a[84:87]\n"
"v_mfma_f32_16x16x32_bf16 a[88:91], a[236:239], v[196:199], a[88:91]\n"
"v_mfma_f32_16x16x32_bf16 a[92:95], a[236:239], v[204:207], a[92:95]\n"
"v_mfma_f32_16x16x32_bf16 a[96:99], a[240:243], v[164:167], a[96:99]\n"
"v_mfma_f32_16x16x32_bf16 a[100:103], a[240:243], v[172:175], a[100:103]\n"
"v_mfma_f32_16x16x32_bf16 a[104:107], a[240:243], v[180:183], a[104:107]\n"
"v_mfma_f32_16x16x32_bf16 a[108:111], a[240:243], v[188:191], a[108:111]\n"
"v_mfma_f32_16x16x32_bf16 a[112:115], a[240:243], v[196:199], a[112:115]\n"
"v_mfma_f32_16x16x32_bf16 a[116:119], a[240:243], v[204:207], a[116:119]\n"
"v_mfma_f32_16x16x32_bf16 a[120:123], a[244:247], v[164:167], a[120:123]\n"
"v_mfma_f32_16x16x32_bf16 a[124:127], a[244:247], v[172:175], a[124:127]\n"
"v_mfma_f32_16x16x32_bf16 a[128:131], a[244:247], v[180:183], a[128:131]\n"
"v_mfma_f32_16x16x32_bf16 a[132:135], a[244:247], v[188:191], a[132:135]\n"
"v_mfma_f32_16x16x32_bf16 a[136:139], a[244:247], v[196:199], a[136:139]\n"
"v_mfma_f32_16x16x32_bf16 a[140:143], a[244:247], v[204:207], a[140:143]\n"
"v_mfma_f32_16x16x32_bf16 a[144:147], a[248:251], v[164:167], a[144:147]\n"
"v_mfma_f32_16x16x32_bf16 a[148:151], a[248:251], v[172:175], a[148:151]\n"
"v_mfma_f32_16x16x32_bf16 a[152:155], a[248:251], v[180:183], a[152:155]\n"
"v_mfma_f32_16x16x32_bf16 a[156:159], a[248:251], v[188:191], a[156:159]\n"
"v_mfma_f32_16x16x32_bf16 a[160:163], a[248:251], v[196:199], a[160:163]\n"
"v_mfma_f32_16x16x32_bf16 a[164:167], a[248:251], v[204:207], a[164:167]\n"
"v_mfma_f32_16x16x32_bf16 a[168:171], a[252:255], v[164:167], a[168:171]\n"
"v_mfma_f32_16x16x32_bf16 a[172:175], a[252:255], v[172:175], a[172:175]\n"
"v_mfma_f32_16x16x32_bf16 a[176:179], a[252:255], v[180:183], a[176:179]\n"
"v_mfma_f32_16x16x32_bf16 a[180:183], a[252:255], v[188:191], a[180:183]\n"
"v_mfma_f32_16x16x32_bf16 a[184:187], a[252:255], v[196:199], a[184:187]\n"
"v_mfma_f32_16x16x32_bf16 a[188:191], a[252:255], v[204:207], a[188:191]\n"
"s_waitcnt lgkmcnt(0)\n"
"s_sub_u32 s47, s47, 1\n"
"s_cmp_lg_u32 s47, 0\n"
"s_cbranch_scc1 LS2_%=\n"
"s_nop 7\n"
"s_nop 7\n"
"s_barrier\n"
"v_rcp_f32 v2, v10\n"
"v_rcp_f32 v3, v14\n"
"v_rcp_f32 v4, v18\n"
"v_rcp_f32 v5, v22\n"
"v_rcp_f32 v6, v26\n"
"v_rcp_f32 v7, v54\n"
"v_and_b32 v9, 15, v1\n"
"v_lshrrev_b32 v8, 4, v1\n"
"v_xor_b32 v8, v8, v9\n"
"v_lshlrev_b32 v8, 4, v8\n"
"v_lshl_add_u32 v8, v9, 10, v8\n"
"s_lshl_b32 s53, s42, 8\n"
"v_add_u32 v8, s53, v8\n"
"v_xor_b32 v62, 0, v8\n"
"v_add_u32 v63, 0x10000, v62\n"
"v_accvgpr_read_b32 v38, a0\n"
"v_accvgpr_read_b32 v39, a1\n"
"v_accvgpr_read_b32 v40, a2\n"
"v_accvgpr_read_b32 v41, a3\n"
"v_accvgpr_read_b32 v42, a24\n"
"v_accvgpr_read_b32 v43, a25\n"
"v_accvgpr_read_b32 v44, a26\n"
"v_accvgpr_read_b32 v45, a27\n"
"v_pk_mul_f32 v[38:39], v[38:39], v[2:3] op_sel:[0,0] op_sel_hi:[1,0]\n"
"v_pk_mul_f32 v[40:41], v[40:41], v[2:3] op_sel:[0,0] op_sel_hi:[1,0]\n"
"v_pk_mul_f32 v[42:43], v[42:43], v[2:3] op_sel:[0,0] op_sel_hi:[1,0]\n"
"v_pk_mul_f32 v[44:45], v[44:45], v[2:3] op_sel:[0,0] op_sel_hi:[1,0]\n"
"v_cvt_pk_bf16_f32 v46, v38, v39\n"
"v_cvt_pk_bf16_f32 v47, v40, v41\n"
"v_cvt_pk_bf16_f32 v48, v42, v43\n"
"v_cvt_pk_bf16_f32 v49, v44, v45\n"
"ds_write_b128 v62, v[46:49] offset:0\n"
"v_accvgpr_read_b32 v50, a4\n"
"v_accvgpr_read_b32 v51, a5\n"
"v_accvgpr_read_b32 v52, a6\n"
"v_accvgpr_read_b32 v53, a7\n"
"v_accvgpr_read_b32 v54, a28\n"
"v_accvgpr_read_b32 v55, a29\n"
"v_accvgpr_read_b32 v56, a30\n"
"v_accvgpr_read_b32 v57, a31\n"
"v_pk_mul_f32 v[50:51], v[50:51], v[2:3] op_sel:[0,1] op_sel_hi:[1,1]\n"
"v_pk_mul_f32 v[52:53], v[52:53], v[2:3] op_sel:[0,1] op_sel_hi:[1,1]\n"
"v_pk_mul_f32 v[54:55], v[54:55], v[2:3] op_sel:[0,1] op_sel_hi:[1,1]\n"
"v_pk_mul_f32 v[56:57], v[56:57], v[2:3] op_sel:[0,1] op_sel_hi:[1,1]\n"
"v_cvt_pk_bf16_f32 v58, v50, v51\n"
"v_cvt_pk_bf16_f32 v59, v52, v53\n"
"v_cvt_pk_bf16_f32 v60, v54, v55\n"
"v_cvt_pk_bf16_f32 v61, v56, v57\n"
"ds_write_b128 v62, v[58:61] offset:16384\n"
"v_accvgpr_read_b32 v38, a8\n"
"v_accvgpr_read_b32 v39, a9\n"
"v_accvgpr_read_b32 v40, a10\n"
"v_accvgpr_read_b32 v41, a11\n"
"v_accvgpr_read_b32 v42, a32\n"
"v_accvgpr_read_b32 v43, a33\n"
"v_accvgpr_read_b32 v44, a34\n"
"v_accvgpr_read_b32 v45, a35\n"
"v_pk_mul_f32 v[38:39], v[38:39], v[4:5] op_sel:[0,0] op_sel_hi:[1,0]\n"
"v_pk_mul_f32 v[40:41], v[40:41], v[4:5] op_sel:[0,0] op_sel_hi:[1,0]\n"
"v_pk_mul_f32 v[42:43], v[42:43], v[4:5] op_sel:[0,0] op_sel_hi:[1,0]\n"
"v_pk_mul_f32 v[44:45], v[44:45], v[4:5] op_sel:[0,0] op_sel_hi:[1,0]\n"
"v_cvt_pk_bf16_f32 v46, v38, v39\n"
"v_cvt_pk_bf16_f32 v47, v40, v41\n"
"v_cvt_pk_bf16_f32 v48, v42, v43\n"
"v_cvt_pk_bf16_f32 v49, v44, v45\n"
"ds_write_b128 v62, v[46:49] offset:32768\n"
"s_waitcnt lgkmcnt(0)\n"
"s_branch LJOIN_%=\n"
"LFPROG_%=:\n"
"s_nop 7\n"
"s_nop 7\n"
"; ---- end of section: start\n"
"s_sub_u32 s53, s58, 176\n"
"s_lshr_b32 s54, s53, 3\n"
"s_and_b32 s55, s53, 3\n"
"s_bfe_u32 s56, s53, 0x10002\n"
"s_min_u32 s53, s55, 2\n"
"s_cmp_eq_u32 s56, 0\n"
"s_cselect_b32 s53, s53, 0\n"
"s_lshl_b32 s55, s55, 3\n"
"s_add_u32 s53, s53, s55\n"
"s_add_u32 s53, s53, s54\n"
"s_cmp_eq_u32 s53, 33\n"
"s_cselect_b32 s56, 1, s56\n"
"s_cselect_b32 s53, 32, s53\n"
"s_mov_b32 s43, 0\n"
"s_cmp_ge_u32 s53, 11\n"
"s_cselect_b32 s43, 1, s43\n"
"s_cmp_ge_u32 s53, 22\n"
"s_cselect_b32 s43, 2, s43\n"
"s_mul_i32 s54, s43, 11\n"
"s_sub_u32 s53, s53, s54\n"
"s_mul_i32 s56, s56, 11\n"
"s_add_u32 s44, s53, s56\n"
"s_mov_b32 s20, s64\n"
"s_mov_b32 s21, s65\n"
"s_and_b32 s21, s21, 0xffff\n"
"s_mov_b32 s22, 2097152\n"
"s_mov_b32 s23, 0x20000\n"
"v_lshrrev_b32 v2, 4, v1\n"
"v_and_b32 v3, 15, v1\n"
"v_lshlrev_b32 v2, 10, v2\n"
"v_lshl_add_u32 v9, v3, 4, v2\n"
"s_mul_i32 s53, s44, 98304\n"
"s_mul_i32 s54, s42, 24576\n"
"s_add_u32 s40, s53, s54\n"
"s_add_u32 s50, s40, 0\n"
"buffer_load_dwordx4 v[64:67], v9, s[20:23], s50 offen offset:0\n"
"buffer_load_dwordx4 v[68:71], v9, s[20:23], s50 offen offset:256\n"
"buffer_load_dwordx4 v[72:75], v9, s[20:23], s50 offen offset:512\n"
"buffer_load_dwordx4 v[76:79], v9, s[20:23], s50 offen offset:768\n"
"s_add_u32 s50, s40, 4096\n"
"buffer_load_dwordx4 v[80:83], v9, s[20:23], s50 offen offset:0\n"
"buffer_load_dwordx4 v[84:87], v9, s[20:23], s50 offen offset:256\n"
"buffer_load_dwordx4 v[88:91], v9, s[20:23], s50 offen offset:512\n"
"buffer_load_dwordx4 v[92:95], v9, s[20:23], s50 offen offset:768\n"
"s_mov_b32 s24, s74\n"
"s_mov_b32 s25, s75\n"
"s_and_b32 s25, s25, 0xffff\n"
"s_mov_b32 s26, 1835008\n"
"s_mov_b32 s27, 0x20000\n"
"s_mov_b32 s84, s76\n"
"s_mov_b32 s85, s77\n"
"s_and_b32 s85, s85, 0xffff\n"
"s_mov_b32 s86, 1835008\n"
"s_mov_b32 s87, 0x20000\n"
"s_lshl_b32 s49, s42, 14\n"
"s_add_u32 s49, s49, 98304\n"
"v_lshrrev_b32 v2, 2, v1\n"
"v_and_b32 v3, 3, v1\n"
"v_bfe_u32 v4, v2, 3, 1\n"
"v_lshlrev_b32 v4, 1, v4\n"
"v_xor_b32 v3, v3, v4\n"
"v_lshlrev_b32 v3, 4, v3\n"
"s_mov_b32 s53, 3584\n"
"v_mul_lo_u32 v2, v2, s53\n"
"v_add_u32 v22, v2, v3\n"
"v_add_u32 v23, 56320, v22\n"
"s_mul_i32 s62, s43, 1280\n"
"s_lshl_b32 s53, s42, 6\n"
"s_add_u32 s62, s62, s53\n"
"s_mov_b32 s28, s78\n"
"s_mov_b32 s29, s79\n"
"s_sub_u32 s28, s28, 4096\n"
"s_subb_u32 s29, s29, 0\n"
"s_and_b32 s29, s29, 0xffff\n"
"s_mov_b32 s30, 1839104\n"
"s_mov_b32 s31, 0x20000\n"
"s_mov_b32 s59, 1.0\n"
"s_mov_b32 s61, 0xbdd2d3e8\n"
"v_mov_b32 v39, 0xc0135761\n"
"v_lshrrev_b32 v2, 3, v1\n"
"v_bfe_u32 v3, v1, 1, 2\n"
"v_bfe_u32 v4, v2, 1, 2\n"
"v_xor_b32 v3, v3, v4\n"
"v_lshlrev_b32 v3, 5, v3\n"
"s_add_u32 s50, s40, 8192\n"
"buffer_load_dwordx4 v[96:99], v9, s[20:23], s50 offen offset:0\n"
"buffer_load_dwordx4 v[100:103], v9, s[20:23], s50 offen offset:256\n"
"buffer_load_dwordx4 v[104:107], v9, s[20:23], s50 offen offset:512\n"
"buffer_load_dwordx4 v[108:111], v9, s[20:23], s50 offen offset:768\n"
"v_and_b32 v4, 1, v1\n"
"v_lshl_add_u32 v3, v4, 4, v3\n"
"s_mov_b32 s53, 1024\n"
"v_mul_lo_u32 v2, v2, s53\n"
"v_add_u32 v2, v2, v3\n"
"s_lshl_b32 s54, s42, 8\n"
"v_add_u32 v2, s54, v2\n"
"v_add_u32 v30, 4096, v2\n"
"v_add_u32 v31, 11264, v2\n"
"v_add_u32 v32, 18432, v2\n"
"v_add_u32 v33, 25600, v2\n"
"v_add_u32 v34, 4224, v2\n"
"v_add_u32 v35, 11392, v2\n"
"v_add_u32 v36, 18560, v2\n"
"v_add_u32 v37, 25728, v2\n"
"s_mul_i32 s46, s43, 655360\n"
"v_lshrrev_b32 v2, 5, v1\n"
"v_bfe_u32 v3, v1, 4, 1\n"
"v_bfe_u32 v4, v1, 2, 2\n"
"v_and_b32 v5, 3, v1\n"
"v_lshlrev_b32 v2, 10, v2\n"
"v_lshl_add_u32 v2, v3, 9, v2\n"
"v_lshl_add_u32 v2, v4, 6, v2\n"
"v_lshl_add_u32 v2, v5, 3, v2\n"
"v_add_u32 v2, s49, v2\n"
"v_lshl_add_u32 v10, v3, 5, v2\n"
"v_xor_b32 v3, 1, v3\n"
"v_lshl_add_u32 v11, v3, 5, v2\n"
"v_and_b32 v2, 15, v1\n"
"v_lshrrev_b32 v3, 4, v1\n"
"v_xor_b32 v3, v3, v2\n"
"v_lshlrev_b32 v3, 4, v3\n"
"v_lshl_add_u32 v3, v2, 10, v3\n"
"v_xor_b32 v14, 0, v3\n"
"v_add_u32 v18, 0x10000, v14\n"
"v_xor_b32 v15, 64, v3\n"
"v_add_u32 v19, 0x10000, v15\n"
"v_xor_b32 v16, 128, v3\n"
"v_add_u32 v20, 0x10000, v16\n"
"v_xor_b32 v17, 192, v3\n"
"s_add_u32 s50, s40, 12288\n"
"buffer_load_dwordx4 v[112:115], v9, s[20:23], s50 offen offset:0\n"
"buffer_load_dwordx4 v[116:119], v9, s[20:23], s50 offen offset:256\n"
"buffer_load_dwordx4 v[120:123], v9, s[20:23], s50 offen offset:512\n"
"buffer_load_dwordx4 v[124:127], v9, s[20:23], s50 offen offset:768\n"
"v_add_u32 v21, 0x10000, v17\n"
"s_memrealtime s[34:35]\n"
"s_waitcnt lgkmcnt(0)\n"
"s_cmp_lg_u32 s42, 0\n"
"s_cbranch_scc1 LHEF_%=\n"
"s_mul_i32 s53, s44, 11\n"
"s_add_u32 s53, s53, s43\n"
"s_add_u32 s53, s53, 8\n"
"s_mul_i32 s53, s53, 768\n"
"s_add_u32 s53, s53, 33554432\n"
"s_add_u32 s36, s80, s53\n"
"s_addc_u32 s37, s81, 0\n"
"v_lshlrev_b32 v56, 3, v1\n"
"s_not_b32 s38, s34\n"
"s_not_b32 s39, s35\n"
"v_mov_b32 v58, s38\n"
"v_mov_b32 v59, s39\n"
"s_mov_b32 exec_hi, 0\n"
"global_store_dwordx2 v56, v[58:59], s[36:37] offset:512 sc1\n"
"s_mov_b64 exec, -1\n"
"LHEF_%=:\n"
"; ---- end of section: loads\n"
"v_mov_b32 v6, 0x3f803f80\n"
"v_lshrrev_b32 v7, 4, v1\n"
"s_mul_i32 s54, s42, 24\n"
"v_add_u32 v7, s54, v7\n"
"v_and_b32 v8, 15, v1\n"
"s_waitcnt vmcnt(8)\n"
"v_dot2_f32_bf16 v160, v64, v6, 0\n"
"v_dot2_f32_bf16 v161, v64, v64, 0\n"
"v_dot2_f32_bf16 v162, v80, v6, 0\n"
"v_dot2_f32_bf16 v163, v80, v80, 0\n"
"v_dot2_f32_bf16 v160, v65, v6, v160\n"
"v_dot2_f32_bf16 v161, v65, v65, v161\n"
"v_dot2_f32_bf16 v162, v81, v6, v162\n"
"v_dot2_f32_bf16 v163, v81, v81, v163\n"
"v_dot2_f32_bf16 v160, v66, v6, v160\n"
"v_dot2_f32_bf16 v161, v66, v66, v161\n"
"v_dot2_f32_bf16 v162, v82, v6, v162\n"
"v_dot2_f32_bf16 v163, v82, v82, v163\n"
"v_dot2_f32_bf16 v160, v67, v6, v160\n"
"v_dot2_f32_bf16 v161, v67, v67, v161\n"
"v_dot2_f32_bf16 v162, v83, v6, v162\n"
"v_dot2_f32_bf16 v163, v83, v83, v163\n"
"v_dot2_f32_bf16 v160, v68, v6, v160\n"
"v_dot2_f32_bf16 v161, v68, v68, v161\n"
"v_dot2_f32_bf16 v162, v84, v6, v162\n"
"v_dot2_f32_bf16 v163, v84, v84, v163\n"
"v_dot2_f32_bf16 v160, v69, v6, v160\n"
"v_dot2_f32_bf16 v161, v69, v69, v161\n"
"v_dot2_f32_bf16 v162, v85, v6, v162\n"
"v_dot2_f32_bf16 v163, v85, v85, v163\n"
"v_dot2_f32_bf16 v160, v70, v6, v160\n"
"s_add_u32 s50, s40, 16384\n"
"buffer_load_dwordx4 v[128:131], v9, s[20:23], s50 offen offset:0\n"
"v_dot2_f32_bf16 v161, v70, v70, v161\n"
"v_dot2_f32_bf16 v162, v86, v6, v162\n"
"v_dot2_f32_bf16 v163, v86, v86, v163\n"
"v_dot2_f32_bf16 v160, v71, v6, v160\n"
"v_dot2_f32_bf16 v161, v71, v71, v161\n"
"v_dot2_f32_bf16 v162, v87, v6, v162\n"
"v_dot2_f32_bf16 v163, v87, v87, v163\n"
"v_dot2_f32_bf16 v160, v72, v6, v160\n"
"v_dot2_f32_bf16 v161, v72, v72, v161\n"
"v_dot2_f32_bf16 v162, v88, v6, v162\n"
"v_dot2_f32_bf16 v163, v88, v88, v163\n"
"v_dot2_f32_bf16 v160, v73, v6, v160\n"
"v_dot2_f32_bf16 v161, v73, v73, v161\n"
"v_dot2_f32_bf16 v162, v89, v6, v162\n"
"v_dot2_f32_bf16 v163, v89, v89, v163\n"
"v_dot2_f32_bf16 v160, v74, v6, v160\n"
"v_dot2_f32_bf16 v161, v74, v74, v161\n"
"v_dot2_f32_bf16 v162, v90, v6, v162\n"
"v_dot2_f32_bf16 v163, v90, v90, v163\n"
"v_dot2_f32_bf16 v160, v75, v6, v160\n"
"v_dot2_f32_bf16 v161, v75, v75, v161\n"
"v_dot2_f32_bf16 v162, v91, v6, v162\n"
"v_dot2_f32_bf16 v163, v91, v91, v163\n"
"v_dot2_f32_bf16 v160, v76, v6, v160\n"
"v_dot2_f32_bf16 v161, v76, v76, v161\n"
"buffer_load_dwordx4 v[132:135], v9, s[20:23], s50 offen offset:256\n"
"v_dot2_f32_bf16 v162, v92, v6, v162\n"
"v_dot2_f32_bf16 v163, v92, v92, v163\n"
"v_dot2_f32_bf16 v160, v77, v6, v160\n"
"v_dot2_f32_bf16 v161, v77, v77, v161\n"
"v_dot2_f32_bf16 v162, v93, v6, v162\n"
"v_dot2_f32_bf16 v163, v93, v93, v163\n"
"v_dot2_f32_bf16 v160, v78, v6, v160\n"
"v_dot2_f32_bf16 v161, v78, v78, v161\n"
"v_dot2_f32_bf16 v162, v94, v6, v162\n"
"v_dot2_f32_bf16 v163, v94, v94, v163\n"
"v_dot2_f32_bf16 v160, v79, v6, v160\n"
"v_dot2_f32_bf16 v161, v79, v79, v161\n"
"v_dot2_f32_bf16 v162, v95, v6, v162\n"
"v_dot2_f32_bf16 v163, v95, v95, v163\n"
"v_add_u32 v5, 0, v7\n"
"v_and_b32 v168, 15, v5\n"
"v_xor_b32 v168, v168, v8\n"
"v_lshlrev_b32 v168, 4, v168\n"
"v_lshl_add_u32 v168, v5, 10, v168\n"
"v_add_u32 v5, 4, v7\n"
"v_and_b32 v169, 15, v5\n"
"v_xor_b32 v169, v169, v8\n"
"v_lshlrev_b32 v169, 4, v169\n"
"v_lshl_add_u32 v169, v5, 10, v169\n"
"v_add_f32_dpp v160, v160, v160 row_ror:8 row_mask:0xf bank_mask:0xf\n"
"buffer_load_dwordx4 v[136:139], v9, s[20:23], s50 offen offset:512\n"
"v_add_f32_dpp v161, v161, v161 row_ror:8 row_mask:0xf bank_mask:0xf\n"
"v_add_f32_dpp v162, v162, v162 row_ror:8 row_mask:0xf bank_mask:0xf\n"
"v_add_f32_dpp v163, v163, v163 row_ror:8 row_mask:0xf bank_mask:0xf\n"
"v_add_f32_dpp v160, v160, v160 row_ror:4 row_mask:0xf bank_mask:0xf\n"
"v_add_f32_dpp v161, v161, v161 row_ror:4 row_mask:0xf bank_mask:0xf\n"
"v_add_f32_dpp v162, v162, v162 row_ror:4 row_mask:0xf bank_mask:0xf\n"
"v_add_f32_dpp v163, v163, v163 row_ror:4 row_mask:0xf bank_mask:0xf\n"
"v_add_f32_dpp v160, v160, v160 row_ror:2 row_mask:0xf bank_mask:0xf\n"
"v_add_f32_dpp v161, v161, v161 row_ror:2 row_mask:0xf bank_mask:0xf\n"
"v_add_f32_dpp v162, v162, v162 row_ror:2 row_mask:0xf bank_mask:0xf\n"
"v_add_f32_dpp v163, v163, v163 row_ror:2 row_mask:0xf bank_mask:0xf\n"
"v_add_f32_dpp v160, v160, v160 row_ror:1 row_mask:0xf bank_mask:0xf\n"
"v_add_f32_dpp v161, v161, v161 row_ror:1 row_mask:0xf bank_mask:0xf\n"
"v_add_f32_dpp v162, v162, v162 row_ror:1 row_mask:0xf bank_mask:0xf\n"
"v_add_f32_dpp v163, v163, v163 row_ror:1 row_mask:0xf bank_mask:0xf\n"
"v_mul_f32 v160, 0x3b000000, v160\n"
"v_mul_f32 v161, 0x3b000000, v161\n"
"v_mul_f32 v162, 0x3b000000, v162\n"
"v_mul_f32 v163, 0x3b000000, v163\n"
"v_fma_f32 v161, -v160, v160, v161\n"
"v_fma_f32 v163, -v162, v162, v163\n"
"v_max_f32 v161, 0, v161\n"
"v_max_f32 v163, 0, v163\n"
"v_add_f32 v161, 0x3727c5ac, v161\n"
"v_add_f32 v163, 0x3727c5ac, v163\n"
"buffer_load_dwordx4 v[140:143], v9, s[20:23], s50 offen offset:768\n"
"v_rsq_f32 v164, v161\n"
"v_rsq_f32 v166, v163\n"
"s_nop 0\n"
"v_mul_f32 v164, s59, v164\n"
"v_mul_f32 v166, s59, v166\n"
"v_mul_f32 v165, -v160, v164\n"
"v_mul_f32 v167, -v162, v166\n"
"v_lshlrev_b32 v172, 16, v64\n"
"v_and_b32 v173, 0xffff0000, v64\n"
"v_pk_fma_f32 v[172:173], v[172:173], v[164:165], v[164:165] op_sel:[0,0,1] op_sel_hi:[1,0,1]\n"
"v_cvt_pk_bf16_f32 v64, v172, v173\n"
"v_lshlrev_b32 v174, 16, v65\n"
"v_and_b32 v175, 0xffff0000, v65\n"
"v_pk_fma_f32 v[174:175], v[174:175], v[164:165], v[164:165] op_sel:[0,0,1] op_sel_hi:[1,0,1]\n"
"v_cvt_pk_bf16_f32 v65, v174, v175\n"
"v_lshlrev_b32 v176, 16, v66\n"
"v_and_b32 v177, 0xffff0000, v66\n"
"v_pk_fma_f32 v[176:177], v[176:177], v[164:165], v[164:165] op_sel:[0,0,1] op_sel_hi:[1,0,1]\n"
"v_cvt_pk_bf16_f32 v66, v176, v177\n"
"v_lshlrev_b32 v178, 16, v67\n"
"v_and_b32 v179, 0xffff0000, v67\n"
"v_pk_fma_f32 v[178:179], v[178:179], v[164:165], v[164:165] op_sel:[0,0,1] op_sel_hi:[1,0,1]\n"
"v_cvt_pk_bf16_f32 v67, v178, v179\n"
"ds_write_b128 v168, v[64:67] offset:0\n"
"v_lshlrev_b32 v172, 16, v68\n"
"s_add_u32 s50, s40, 20480\n"
"buffer_load_dwordx4 v[144:147], v9, s[20:23], s50 offen offset:0\n"
"v_and_b32 v173, 0xffff0000, v68\n"
"v_pk_fma_f32 v[172:173], v[172:173], v[164:165], v[164:165] op_sel:[0,0,1] op_sel_hi:[1,0,1]\n"
"v_cvt_pk_bf16_f32 v68, v172, v173\n"
"v_lshlrev_b32 v174, 16, v69\n"
"v_and_b32 v175, 0xffff0000, v69\n"
"v_pk_fma_f32 v[174:175], v[174:175], v[164:165], v[164:165] op_sel:[0,0,1] op_sel_hi:[1,0,1]\n"
"v_cvt_pk_bf16_f32 v69, v174, v175\n"
"v_lshlrev_b32 v176, 16, v70\n"
"v_and_b32 v177, 0xffff0000, v70\n"
"v_pk_fma_f32 v[176:177], v[176:177], v[164:165], v[164:165] op_sel:[0,0,1] op_sel_hi:[1,0,1]\n"
"v_cvt_pk_bf16_f32 v70, v176, v177\n"
"v_lshlrev_b32 v178, 16, v71\n"
"v_and_b32 v179, 0xffff0000, v71\n"
"v_pk_fma_f32 v[178:179], v[178:179], v[164:165], v[164:165] op_sel:[0,0,1] op_sel_hi:[1,0,1]\n"
"v_cvt_pk_bf16_f32 v71, v178, v179\n"
"ds_write_b128 v168, v[68:71] offset:256\n"
"v_lshlrev_b32 v172, 16, v72\n"
"v_and_b32 v173, 0xffff0000, v72\n"
"v_pk_fma_f32 v[172:173], v[172:173], v[164:165], v[164:165] op_sel:[0,0,1] op_sel_hi:[1,0,1]\n"
"v_cvt_pk_bf16_f32 v72, v172, v173\n"
"v_lshlrev_b32 v174, 16, v73\n"
"v_and_b32 v175, 0xffff0000, v73\n"
"v_pk_fma_f32 v[174:175], v[174:175], v[164:165], v[164:165] op_sel:[0,0,1] op_sel_hi:[1,0,1]\n"
"v_cvt_pk_bf16_f32 v73, v174, v175\n"
"v_lshlrev_b32 v176, 16, v74\n"
"buffer_load_dwordx4 v[148:151], v9, s[20:23], s50 offen offset:256\n"
"v_and_b32 v177, 0xffff0000, v74\n"
"v_pk_fma_f32 v[176:177], v[176:177], v[164:165], v[164:165] op_sel:[0,0,1] op_sel_hi:[1,0,1]\n"
"v_cvt_pk_bf16_f32 v74, v176, v177\n"
"v_lshlrev_b32 v178, 16, v75\n"
"v_and_b32 v179, 0xffff0000, v75\n"
"v_pk_fma_f32 v[178:179], v[178:179], v[164:165], v[164:165] op_sel:[0,0,1] op_sel_hi:[1,0,1]\n"
"v_cvt_pk_bf16_f32 v75, v178, v179\n"
"ds_write_b128 v168, v[72:75] offset:512\n"
"v_lshlrev_b32 v172, 16, v76\n"
"v_and_b32 v173, 0xffff0000, v76\n"
"v_pk_fma_f32 v[172:173], v[172:173], v[164:165], v[164:165] op_sel:[0,0,1] op_sel_hi:[1,0,1]\n"
"v_cvt_pk_bf16_f32 v76, v172, v173\n"
"v_lshlrev_b32 v174, 16, v77\n"
"v_and_b32 v175, 0xffff0000, v77\n"
"v_pk_fma_f32 v[174:175], v[174:175], v[164:165], v[164:165] op_sel:[0,0,1] op_sel_hi:[1,0,1]\n"
"v_cvt_pk_bf16_f32 v77, v174, v175\n"
"v_lshlrev_b32 v176, 16, v78\n"
"v_and_b32 v177, 0xffff0000, v78\n"
"v_pk_fma_f32 v[176:177], v[176:177], v[164:165], v[164:165] op_sel:[0,0,1] op_sel_hi:[1,0,1]\n"
"v_cvt_pk_bf16_f32 v78, v176, v177\n"
"v_lshlrev_b32 v178, 16, v79\n"
"v_and_b32 v179, 0xffff0000, v79\n"
"v_pk_fma_f32 v[178:179], v[178:179], v[164:165], v[164:165] op_sel:[0,0,1] op_sel_hi:[1,0,1]\n"
"v_cvt_pk_bf16_f32 v79, v178, v179\n"
"ds_write_b128 v168, v[76:79] offset:768\n"
"buffer_load_dwordx4 v[152:155], v9, s[20:23], s50 offen offset:512\n"
"v_lshlrev_b32 v172, 16, v80\n"
"v_and_b32 v173, 0xffff0000, v80\n"
"v_pk_fma_f32 v[172:173], v[172:173], v[166:167], v[166:167] op_sel:[0,0,1] op_sel_hi:[1,0,1]\n"
"v_cvt_pk_bf16_f32 v80, v172, v173\n"
"v_lshlrev_b32 v174, 16, v81\n"
"v_and_b32 v175, 0xffff0000, v81\n"
"v_pk_fma_f32 v[174:175], v[174:175], v[166:167], v[166:167] op_sel:[0,0,1] op_sel_hi:[1,0,1]\n"
"v_cvt_pk_bf16_f32 v81, v174, v175\n"
"v_lshlrev_b32 v176, 16, v82\n"
"v_and_b32 v177, 0xffff0000, v82\n"
"v_pk_fma_f32 v[176:177], v[176:177], v[166:167], v[166:167] op_sel:[0,0,1] op_sel_hi:[1,0,1]\n"
"v_cvt_pk_bf16_f32 v82, v176, v177\n"
"v_lshlrev_b32 v178, 16, v83\n"
"v_and_b32 v179, 0xffff0000, v83\n"
"v_pk_fma_f32 v[178:179], v[178:179], v[166:167], v[166:167] op_sel:[0,0,1] op_sel_hi:[1,0,1]\n"
"v_cvt_pk_bf16_f32 v83, v178, v179\n"
"ds_write_b128 v169, v[80:83] offset:0\n"
"v_lshlrev_b32 v172, 16, v84\n"
"v_and_b32 v173, 0xffff0000, v84\n"
"v_pk_fma_f32 v[172:173], v[172:173], v[166:167], v[166:167] op_sel:[0,0,1] op_sel_hi:[1,0,1]\n"
"v_cvt_pk_bf16_f32 v84, v172, v173\n"
"v_lshlrev_b32 v174, 16, v85\n"
"v_and_b32 v175, 0xffff0000, v85\n"
"v_pk_fma_f32 v[174:175], v[174:175], v[166:167], v[166:167] op_sel:[0,0,1] op_sel_hi:[1,0,1]\n"
"v_cvt_pk_bf16_f32 v85, v174, v175\n"
"buffer_load_dwordx4 v[156:159], v9, s[20:23], s50 offen offset:768\n"
"v_lshlrev_b32 v176, 16, v86\n"
"v_and_b32 v177, 0xffff0000, v86\n"
"v_pk_fma_f32 v[176:177], v[176:177], v[166:167], v[166:167] op_sel:[0,0,1] op_sel_hi:[1,0,1]\n"
"v_cvt_pk_bf16_f32 v86, v176, v177\n"
"v_lshlrev_b32 v178, 16, v87\n"
"v_and_b32 v179, 0xffff0000, v87\n"
"v_pk_fma_f32 v[178:179], v[178:179], v[166:167], v[166:167] op_sel:[0,0,1] op_sel_hi:[1,0,1]\n"
"v_cvt_pk_bf16_f32 v87, v178, v179\n"
"ds_write_b128 v169, v[84:87] offset:256\n"
"v_lshlrev_b32 v172, 16, v88\n"
"v_and_b32 v173, 0xffff0000, v88\n"
"v_pk_fma_f32 v[172:173], v[172:173], v[166:167], v[166:167] op_sel:[0,0,1] op_sel_hi:[1,0,1]\n"
"v_cvt_pk_bf16_f32 v88, v172, v173\n"
"v_lshlrev_b32 v174, 16, v89\n"
"v_and_b32 v175, 0xffff0000, v89\n"
"v_pk_fma_f32 v[174:175], v[174:175], v[166:167], v[166:167] op_sel:[0,0,1] op_sel_hi:[1,0,1]\n"
"v_cvt_pk_bf16_f32 v89, v174, v175\n"
"v_lshlrev_b32 v176, 16, v90\n"
"v_and_b32 v177, 0xffff0000, v90\n"
"v_pk_fma_f32 v[176:177], v[176:177], v[166:167], v[166:167] op_sel:[0,0,1] op_sel_hi:[1,0,1]\n"
"v_cvt_pk_bf16_f32 v90, v176, v177\n"
"v_lshlrev_b32 v178, 16, v91\n"
"v_and_b32 v179, 0xffff0000, v91\n"
"v_pk_fma_f32 v[178:179], v[178:179], v[166:167], v[166:167] op_sel:[0,0,1] op_sel_hi:[1,0,1]\n"
"v_cvt_pk_bf16_f32 v91, v178, v179\n"
"ds_write_b128 v169, v[88:91] offset:512\n"
"v_lshlrev_b32 v172, 16, v92\n"
"v_and_b32 v173, 0xffff0000, v92\n"
"v_pk_fma_f32 v[172:173], v[172:173], v[166:167], v[166:167] op_sel:[0,0,1] op_sel_hi:[1,0,1]\n"
"v_cvt_pk_bf16_f32 v92, v172, v173\n"
"v_lshlrev_b32 v174, 16, v93\n"
"v_and_b32 v175, 0xffff0000, v93\n"
"v_pk_fma_f32 v[174:175], v[174:175], v[166:167], v[166:167] op_sel:[0,0,1] op_sel_hi:[1,0,1]\n"
"v_cvt_pk_bf16_f32 v93, v174, v175\n"
"v_lshlrev_b32 v176, 16, v94\n"
"v_and_b32 v177, 0xffff0000, v94\n"
"v_pk_fma_f32 v[176:177], v[176:177], v[166:167], v[166:167] op_sel:[0,0,1] op_sel_hi:[1,0,1]\n"
"v_cvt_pk_bf16_f32 v94, v176, v177\n"
"v_lshlrev_b32 v178, 16, v95\n"
"v_and_b32 v179, 0xffff0000, v95\n"
"v_pk_fma_f32 v[178:179], v[178:179], v[166:167], v[166:167] op_sel:[0,0,1] op_sel_hi:[1,0,1]\n"
"v_cvt_pk_bf16_f32 v95, v178, v179\n"
"ds_write_b128 v169, v[92:95] offset:768\n"
"s_waitcnt vmcnt(8)\n"
"v_dot2_f32_bf16 v160, v96, v6, 0\n"
"v_dot2_f32_bf16 v161, v96, v96, 0\n"
"v_dot2_f32_bf16 v162, v112, v6, 0\n"
"v_dot2_f32_bf16 v163, v112, v112, 0\n"
"v_dot2_f32_bf16 v160, v97, v6, v160\n"
"v_dot2_f32_bf16 v161, v97, v97, v161\n"
"v_dot2_f32_bf16 v162, v113, v6, v162\n"
"v_dot2_f32_bf16 v163, v113, v113, v163\n"
"v_dot2_f32_bf16 v160, v98, v6, v160\n"
"v_dot2_f32_bf16 v161, v98, v98, v161\n"
"v_dot2_f32_bf16 v162, v114, v6, v162\n"
"v_dot2_f32_bf16 v163, v114, v114, v163\n"
"s_add_u32 s50, s62, 0\n"
"s_add_u32 m0, s49, 0\n"
"s_nop 0\n"
"buffer_load_dwordx4 v22, s[24:27], s50 offen lds\n"
"v_dot2_f32_bf16 v160, v99, v6, v160\n"
"v_dot2_f32_bf16 v161, v99, v99, v161\n"
"v_dot2_f32_bf16 v162, v115, v6, v162\n"
"v_dot2_f32_bf16 v163, v115, v115, v163\n"
"v_dot2_f32_bf16 v160, v100, v6, v160\n"
"v_dot2_f32_bf16 v161, v100, v100, v161\n"
"v_dot2_f32_bf16 v162, v116, v6, v162\n"
"v_dot2_f32_bf16 v163, v116, v116, v163\n"
"v_dot2_f32_bf16 v160, v101, v6, v160\n"
"v_dot2_f32_bf16 v161, v101, v101, v161\n"
"v_dot2_f32_bf16 v162, v117, v6, v162\n"
"v_dot2_f32_bf16 v163, v117, v117, v163\n"
"buffer_load_dwordx4 v23, s[24:27], s50 offen offset:1024 lds\n"
"v_dot2_f32_bf16 v160, v102, v6, v160\n"
"v_dot2_f32_bf16 v161, v102, v102, v161\n"
"v_dot2_f32_bf16 v162, v118, v6, v162\n"
"v_dot2_f32_bf16 v163, v118, v118, v163\n"
"v_dot2_f32_bf16 v160, v103, v6, v160\n"
"v_dot2_f32_bf16 v161, v103, v103, v161\n"
"v_dot2_f32_bf16 v162, v119, v6, v162\n"
"v_dot2_f32_bf16 v163, v119, v119, v163\n"
"v_dot2_f32_bf16 v160, v104, v6, v160\n"
"v_dot2_f32_bf16 v161, v104, v104, v161\n"
"v_dot2_f32_bf16 v162, v120, v6, v162\n"
"v_dot2_f32_bf16 v163, v120, v120, v163\n"
"s_add_u32 m0, s49, 2048\n"
"s_nop 0\n"
"buffer_load_dwordx4 v22, s[84:87], s50 offen lds\n"
"v_dot2_f32_bf16 v160, v105, v6, v160\n"
"v_dot2_f32_bf16 v161, v105, v105, v161\n"
"v_dot2_f32_bf16 v162, v121, v6, v162\n"
"v_dot2_f32_bf16 v163, v121, v121, v163\n"
"v_dot2_f32_bf16 v160, v106, v6, v160\n"
"v_dot2_f32_bf16 v161, v106, v106, v161\n"
"v_dot2_f32_bf16 v162, v122, v6, v162\n"
"v_dot2_f32_bf16 v163, v122, v122, v163\n"
"v_dot2_f32_bf16 v160, v107, v6, v160\n"
"v_dot2_f32_bf16 v161, v107, v107, v161\n"
"v_dot2_f32_bf16 v162, v123, v6, v162\n"
"v_dot2_f32_bf16 v163, v123, v123, v163\n"
"buffer_load_dwordx4 v23, s[84:87], s50 offen offset:1024 lds\n"
"v_dot2_f32_bf16 v160, v108, v6, v160\n"
"v_dot2_f32_bf16 v161, v108, v108, v161\n"
"v_dot2_f32_bf16 v162, v124, v6, v162\n"
"v_dot2_f32_bf16 v163, v124, v124, v163\n"
"v_dot2_f32_bf16 v160, v109, v6, v160\n"
"v_dot2_f32_bf16 v161, v109, v109, v161\n"
"v_dot2_f32_bf16 v162, v125, v6, v162\n"
"v_dot2_f32_bf16 v163, v125, v125, v163\n"
"v_dot2_f32_bf16 v160, v110, v6, v160\n"
"v_dot2_f32_bf16 v161, v110, v110, v161\n"
"v_dot2_f32_bf16 v162, v126, v6, v162\n"
"v_dot2_f32_bf16 v163, v126, v126, v163\n"
"s_add_u32 s50, s62, 114688\n"
"s_add_u32 m0, s49, 4096\n"
"s_nop 0\n"
"buffer_load_dwordx4 v22, s[24:27], s50 offen lds\n"
"v_dot2_f32_bf16 v160, v111, v6, v160\n"
"v_dot2_f32_bf16 v161, v111, v111, v161\n"
"v_dot2_f32_bf16 v162, v127, v6, v162\n"
"v_dot2_f32_bf16 v163, v127, v127, v163\n"
"v_add_u32 v5, 8, v7\n"
"v_and_b32 v168, 15, v5\n"
"v_xor_b32 v168, v168, v8\n"
"v_lshlrev_b32 v168, 4, v168\n"
"v_lshl_add_u32 v168, v5, 10, v168\n"
"v_add_u32 v5, 12, v7\n"
"v_and_b32 v169, 15, v5\n"
"v_xor_b32 v169, v169, v8\n"
"buffer_load_dwordx4 v23, s[24:27], s50 offen offset:1024 lds\n"
"v_lshlrev_b32 v169, 4, v169\n"
"v_lshl_add_u32 v169, v5, 10, v169\n"
"v_add_f32_dpp v160, v160, v160 row_ror:8 row_mask:0xf bank_mask:0xf\n"
"v_add_f32_dpp v161, v161, v161 row_ror:8 row_mask:0xf bank_mask:0xf\n"
"v_add_f32_dpp v162, v162, v162 row_ror:8 row_mask:0xf bank_mask:0xf\n"
"v_add_f32_dpp v163, v163, v163 row_ror:8 row_mask:0xf bank_mask:0xf\n"
"v_add_f32_dpp v160, v160, v160 row_ror:4 row_mask:0xf bank_mask:0xf\n"
"v_add_f32_dpp v161, v161, v161 row_ror:4 row_mask:0xf bank_mask:0xf\n"
"v_add_f32_dpp v162, v162, v162 row_ror:4 row_mask:0xf bank_mask:0xf\n"
"v_add_f32_dpp v163, v163, v163 row_ror:4 row_mask:0xf bank_mask:0xf\n"
"v_add_f32_dpp v160, v160, v160 row_ror:2 row_mask:0xf bank_mask:0xf\n"
"v_add_f32_dpp v161, v161, v161 row_ror:2 row_mask:0xf bank_mask:0xf\n"
"s_add_u32 m0, s49, 6144\n"
"s_nop 0\n"
"buffer_load_dwordx4 v22, s[84:87], s50 offen lds\n"
"v_add_f32_dpp v162, v162, v162 row_ror:2 row_mask:0xf bank_mask:0xf\n"
"v_add_f32_dpp v163, v163, v163 row_ror:2 row_mask:0xf bank_mask:0xf\n"
"v_add_f32_dpp v160, v160, v160 row_ror:1 row_mask:0xf bank_mask:0xf\n"
"v_add_f32_dpp v161, v161, v161 row_ror:1 row_mask:0xf bank_mask:0xf\n"
"v_add_f32_dpp v162, v162, v162 row_ror:1 row_mask:0xf bank_mask:0xf\n"
"v_add_f32_dpp v163, v163, v163 row_ror:1 row_mask:0xf bank_mask:0xf\n"
"v_mul_f32 v160, 0x3b000000, v160\n"
"v_mul_f32 v161, 0x3b000000, v161\n"
"v_mul_f32 v162, 0x3b000000, v162\n"
"v_mul_f32 v163, 0x3b000000, v163\n"
"v_fma_f32 v161, -v160, v160, v161\n"
"v_fma_f32 v163, -v162, v162, v163\n"
"buffer_load_dwordx4 v23, s[84:87], s50 offen offset:1024 lds\n"
"v_max_f32 v161, 0, v161\n"
"v_max_f32 v163, 0, v163\n"
"v_add_f32 v161, 0x3727c5ac, v161\n"
"v_add_f32 v163, 0x3727c5ac, v163\n"
"v_rsq_f32 v164, v161\n"
"v_rsq_f32 v166, v163\n"
"s_nop 0\n"
"v_mul_f32 v164, s59, v164\n"
"v_mul_f32 v166, s59, v166\n"
"v_mul_f32 v165, -v160, v164\n"
"v_mul_f32 v167, -v162, v166\n"
"v_lshlrev_b32 v172, 16, v96\n"
"s_add_u32 s50, s62, 229376\n"
"s_add_u32 m0, s49, 8192\n"
"s_nop 0\n"
"buffer_load_dwordx4 v22, s[24:27], s50 offen lds\n"
"v_and_b32 v173, 0xffff0000, v96\n"
"v_pk_fma_f32 v[172:173], v[172:173], v[164:165], v[164:165] op_sel:[0,0,1] op_sel_hi:[1,0,1]\n"
"v_cvt_pk_bf16_f32 v96, v172, v173\n"
"v_lshlrev_b32 v174, 16, v97\n"
"v_and_b32 v175, 0xffff0000, v97\n"
"v_pk_fma_f32 v[174:175], v[174:175], v[164:165], v[164:165] op_sel:[0,0,1] op_sel_hi:[1,0,1]\n"
"v_cvt_pk_bf16_f32 v97, v174, v175\n"
"v_lshlrev_b32 v176, 16, v98\n"
"v_and_b32 v177, 0xffff0000, v98\n"
"v_pk_fma_f32 v[176:177], v[176:177], v[164:165], v[164:165] op_sel:[0,0,1] op_sel_hi:[1,0,1]\n"
"v_cvt_pk_bf16_f32 v98, v176, v177\n"
"v_lshlrev_b32 v178, 16, v99\n"
"buffer_load_dwordx4 v23, s[24:27], s50 offen offset:1024 lds\n"
"v_and_b32 v179, 0xffff0000, v99\n"
"v_pk_fma_f32 v[178:179], v[178:179], v[164:165], v[164:165] op_sel:[0,0,1] op_sel_hi:[1,0,1]\n"
"v_cvt_pk_bf16_f32 v99, v178, v179\n"
"ds_write_b128 v168, v[96:99] offset:0\n"
"v_lshlrev_b32 v172, 16, v100\n"
"v_and_b32 v173, 0xffff0000, v100\n"
"v_pk_fma_f32 v[172:173], v[172:173], v[164:165], v[164:165] op_sel:[0,0,1] op_sel_hi:[1,0,1]\n"
"v_cvt_pk_bf16_f32 v100, v172, v173\n"
"v_lshlrev_b32 v174, 16, v101\n"
"v_and_b32 v175, 0xffff0000, v101\n"
"v_pk_fma_f32 v[174:175], v[174:175], v[164:165], v[164:165] op_sel:[0,0,1] op_sel_hi:[1,0,1]\n"
"v_cvt_pk_bf16_f32 v101, v174, v175\n"
"s_add_u32 m0, s49, 10240\n"
"s_nop 0\n"
"buffer_load_dwordx4 v22, s[84:87], s50 offen lds\n"
"v_lshlrev_b32 v176, 16, v102\n"
"v_and_b32 v177, 0xffff0000, v102\n"
"v_pk_fma_f32 v[176:177], v[176:177], v[164:165], v[164:165] op_sel:[0,0,1] op_sel_hi:[1,0,1]\n"
"v_cvt_pk_bf16_f32 v102, v176, v177\n"
"v_lshlrev_b32 v178, 16, v103\n"
"v_and_b32 v179, 0xffff0000, v103\n"
"v_pk_fma_f32 v[178:179], v[178:179], v[164:165], v[164:165] op_sel:[0,0,1] op_sel_hi:[1,0,1]\n"
"v_cvt_pk_bf16_f32 v103, v178, v179\n"
"ds_write_b128 v168, v[100:103] offset:256\n"
"v_lshlrev_b32 v172, 16, v104\n"
"v_and_b32 v173, 0xffff0000, v104\n"
"v_pk_fma_f32 v[172:173], v[172:173], v[164:165], v[164:165] op_sel:[0,0,1] op_sel_hi:[1,0,1]\n"
"buffer_load_dwordx4 v23, s[84:87], s50 offen offset:1024 lds\n"
"v_cvt_pk_bf16_f32 v104, v172, v173\n"
"v_lshlrev_b32 v174, 16, v105\n"
"v_and_b32 v175, 0xffff0000, v105\n"
"v_pk_fma_f32 v[174:175], v[174:175], v[164:165], v[164:165] op_sel:[0,0,1] op_sel_hi:[1,0,1]\n"
"v_cvt_pk_bf16_f32 v105, v174, v175\n"
"v_lshlrev_b32 v176, 16, v106\n"
"v_and_b32 v177, 0xffff0000, v106\n"
"v_pk_fma_f32 v[176:177], v[176:177], v[164:165], v[164:165] op_sel:[0,0,1] op_sel_hi:[1,0,1]\n"
"v_cvt_pk_bf16_f32 v106, v176, v177\n"
"v_lshlrev_b32 v178, 16, v107\n"
"v_and_b32 v179, 0xffff0000, v107\n"
"v_pk_fma_f32 v[178:179], v[178:179], v[164:165], v[164:165] op_sel:[0,0,1] op_sel_hi:[1,0,1]\n"
"s_add_u32 s50, s62, 344064\n"
"s_add_u32 m0, s49, 12288\n"
"s_nop 0\n"
"buffer_load_dwordx4 v22, s[24:27], s50 offen lds\n"
"v_cvt_pk_bf16_f32 v107, v178, v179\n"
"ds_write_b128 v168, v[104:107] offset:512\n"
"v_lshlrev_b32 v172, 16, v108\n"
"v_and_b32 v173, 0xffff0000, v108\n"
"v_pk_fma_f32 v[172:173], v[172:173], v[164:165], v[164:165] op_sel:[0,0,1] op_sel_hi:[1,0,1]\n"
"v_cvt_pk_bf16_f32 v108, v172, v173\n"
"v_lshlrev_b32 v174, 16, v109\n"
"v_and_b32 v175, 0xffff0000, v109\n"
"v_pk_fma_f32 v[174:175], v[174:175], v[164:165], v[164:165] op_sel:[0,0,1] op_sel_hi:[1,0,1]\n"
"v_cvt_pk_bf16_f32 v109, v174, v175\n"
"v_lshlrev_b32 v176, 16, v110\n"
"v_and_b32 v177, 0xffff0000, v110\n"
"buffer_load_dwordx4 v23, s[24:27], s50 offen offset:1024 lds\n"
"v_pk_fma_f32 v[176:177], v[176:177], v[164:165], v[164:165] op_sel:[0,0,1] op_sel_hi:[1,0,1]\n"
"v_cvt_pk_bf16_f32 v110, v176, v177\n"
"v_lshlrev_b32 v178, 16, v111\n"
"v_and_b32 v179, 0xffff0000, v111\n"
"v_pk_fma_f32 v[178:179], v[178:179], v[164:165], v[164:165] op_sel:[0,0,1] op_sel_hi:[1,0,1]\n"
"v_cvt_pk_bf16_f32 v111, v178, v179\n"
"ds_write_b128 v168, v[108:111] offset:768\n"
"v_lshlrev_b32 v172, 16, v112\n"
"v_and_b32 v173, 0xffff0000, v112\n"
"v_pk_fma_f32 v[172:173], v[172:173], v[166:167], v[166:167] op_sel:[0,0,1] op_sel_hi:[1,0,1]\n"
"v_cvt_pk_bf16_f32 v112, v172, v173\n"
"v_lshlrev_b32 v174, 16, v113\n"
"s_add_u32 m0, s49, 14336\n"
"s_nop 0\n"
"buffer_load_dwordx4 v22, s[84:87], s50 offen lds\n"
"v_and_b32 v175, 0xffff0000, v113\n"
"v_pk_fma_f32 v[174:175], v[174:175], v[166:167], v[166:167] op_sel:[0,0,1] op_sel_hi:[1,0,1]\n"
"v_cvt_pk_bf16_f32 v113, v174, v175\n"
"v_lshlrev_b32 v176, 16, v114\n"
"v_and_b32 v177, 0xffff0000, v114\n"
"v_pk_fma_f32 v[176:177], v[176:177], v[166:167], v[166:167] op_sel:[0,0,1] op_sel_hi:[1,0,1]\n"
"v_cvt_pk_bf16_f32 v114, v176, v177\n"
"v_lshlrev_b32 v178, 16, v115\n"
"v_and_b32 v179, 0xffff0000, v115\n"
"v_pk_fma_f32 v[178:179], v[178:179], v[166:167], v[166:167] op_sel:[0,0,1] op_sel_hi:[1,0,1]\n"
"v_cvt_pk_bf16_f32 v115, v178, v179\n"
"ds_write_b128 v169, v[112:115] offset:0\n"
"buffer_load_dwordx4 v23, s[84:87], s50 offen offset:1024 lds\n"
"v_lshlrev_b32 v172, 16, v116\n"
"v_and_b32 v173, 0xffff0000, v116\n"
"v_pk_fma_f32 v[172:173], v[172:173], v[166:167], v[166:167] op_sel:[0,0,1] op_sel_hi:[1,0,1]\n"
"v_cvt_pk_bf16_f32 v116, v172, v173\n"
"v_lshlrev_b32 v174, 16, v117\n"
"v_and_b32 v175, 0xffff0000, v117\n"
"v_pk_fma_f32 v[174:175], v[174:175], v[166:167], v[166:167] op_sel:[0,0,1] op_sel_hi:[1,0,1]\n"
"v_cvt_pk_bf16_f32 v117, v174, v175\n"
"v_lshlrev_b32 v176, 16, v118\n"
"v_and_b32 v177, 0xffff0000, v118\n"
"v_pk_fma_f32 v[176:177], v[176:177], v[166:167], v[166:167] op_sel:[0,0,1] op_sel_hi:[1,0,1]\n"
"v_cvt_pk_bf16_f32 v118, v176, v177\n"
"v_lshlrev_b32 v178, 16, v119\n"
"v_and_b32 v179, 0xffff0000, v119\n"
"v_pk_fma_f32 v[178:179], v[178:179], v[166:167], v[166:167] op_sel:[0,0,1] op_sel_hi:[1,0,1]\n"
"v_cvt_pk_bf16_f32 v119, v178, v179\n"
"ds_write_b128 v169, v[116:119] offset:256\n"
"v_lshlrev_b32 v172, 16, v120\n"
"v_and_b32 v173, 0xffff0000, v120\n"
"v_pk_fma_f32 v[172:173], v[172:173], v[166:167], v[166:167] op_sel:[0,0,1] op_sel_hi:[1,0,1]\n"
"v_cvt_pk_bf16_f32 v120, v172, v173\n"
"v_lshlrev_b32 v174, 16, v121\n"
"v_and_b32 v175, 0xffff0000, v121\n"
"v_pk_fma_f32 v[174:175], v[174:175], v[166:167], v[166:167] op_sel:[0,0,1] op_sel_hi:[1,0,1]\n"
"v_cvt_pk_bf16_f32 v121, v174, v175\n"
"v_lshlrev_b32 v176, 16, v122\n"
"v_and_b32 v177, 0xffff0000, v122\n"
"v_pk_fma_f32 v[176:177], v[176:177], v[166:167], v[166:167] op_sel:[0,0,1] op_sel_hi:[1,0,1]\n"
"v_cvt_pk_bf16_f32 v122, v176, v177\n"
"v_lshlrev_b32 v178, 16, v123\n"
"v_and_b32 v179, 0xffff0000, v123\n"
"v_pk_fma_f32 v[178:179], v[178:179], v[166:167], v[166:167] op_sel:[0,0,1] op_sel_hi:[1,0,1]\n"
"v_cvt_pk_bf16_f32 v123, v178, v179\n"
"ds_write_b128 v169, v[120:123] offset:512\n"
"v_lshlrev_b32 v172, 16, v124\n"
"v_and_b32 v173, 0xffff0000, v124\n"
"v_pk_fma_f32 v[172:173], v[172:173], v[166:167], v[166:167] op_sel:[0,0,1] op_sel_hi:[1,0,1]\n"
"v_cvt_pk_bf16_f32 v124, v172, v173\n"
"v_lshlrev_b32 v174, 16, v125\n"
"v_and_b32 v175, 0xffff0000, v125\n"
"v_pk_fma_f32 v[174:175], v[174:175], v[166:167], v[166:167] op_sel:[0,0,1] op_sel_hi:[1,0,1]\n"
"v_cvt_pk_bf16_f32 v125, v174, v175\n"
"v_lshlrev_b32 v176, 16, v126\n"
"v_and_b32 v177, 0xffff0000, v126\n"
"v_pk_fma_f32 v[176:177], v[176:177], v[166:167], v[166:167] op_sel:[0,0,1] op_sel_hi:[1,0,1]\n"
"v_cvt_pk_bf16_f32 v126, v176, v177\n"
"v_lshlrev_b32 v178, 16, v127\n"
"v_and_b32 v179, 0xffff0000, v127\n"
"v_pk_fma_f32 v[178:179], v[178:179], v[166:167], v[166:167] op_sel:[0,0,1] op_sel_hi:[1,0,1]\n"
"v_cvt_pk_bf16_f32 v127, v178, v179\n"
"ds_write_b128 v169, v[124:127] offset:768\n"
"s_waitcnt vmcnt(16)\n"
"v_dot2_f32_bf16 v160, v128, v6, 0\n"
"v_dot2_f32_bf16 v161, v128, v128, 0\n"
"v_dot2_f32_bf16 v162, v144, v6, 0\n"
"v_dot2_f32_bf16 v163, v144, v144, 0\n"
"v_dot2_f32_bf16 v160, v129, v6, v160\n"
"v_dot2_f32_bf16 v161, v129, v129, v161\n"
"v_dot2_f32_bf16 v162, v145, v6, v162\n"
"v_dot2_f32_bf16 v163, v145, v145, v163\n"
"v_dot2_f32_bf16 v160, v130, v6, v160\n"
"v_dot2_f32_bf16 v161, v130, v130, v161\n"
"v_dot2_f32_bf16 v162, v146, v6, v162\n"
"v_dot2_f32_bf16 v163, v146, v146, v163\n"
"v_dot2_f32_bf16 v160, v131, v6, v160\n"
"v_dot2_f32_bf16 v161, v131, v131, v161\n"
"v_dot2_f32_bf16 v162, v147, v6, v162\n"
"v_dot2_f32_bf16 v163, v147, v147, v163\n"
"v_dot2_f32_bf16 v160, v132, v6, v160\n"
"v_dot2_f32_bf16 v161, v132, v132, v161\n"
"v_dot2_f32_bf16 v162, v148, v6, v162\n"
"v_dot2_f32_bf16 v163, v148, v148, v163\n"
"v_dot2_f32_bf16 v160, v133, v6, v160\n"
"v_dot2_f32_bf16 v161, v133, v133, v161\n"
"v_dot2_f32_bf16 v162, v149, v6, v162\n"
"v_dot2_f32_bf16 v163, v149, v149, v163\n"
"v_dot2_f32_bf16 v160, v134, v6, v160\n"
"v_dot2_f32_bf16 v161, v134, v134, v161\n"
"v_dot2_f32_bf16 v162, v150, v6, v162\n"
"v_dot2_f32_bf16 v163, v150, v150, v163\n"
"v_dot2_f32_bf16 v160, v135, v6, v160\n"
"v_dot2_f32_bf16 v161, v135, v135, v161\n"
"v_dot2_f32_bf16 v162, v151, v6, v162\n"
"v_dot2_f32_bf16 v163, v151, v151, v163\n"
"v_dot2_f32_bf16 v160, v136, v6, v160\n"
"v_dot2_f32_bf16 v161, v136, v136, v161\n"
"v_dot2_f32_bf16 v162, v152, v6, v162\n"
"v_dot2_f32_bf16 v163, v152, v152, v163\n"
"v_dot2_f32_bf16 v160, v137, v6, v160\n"
"v_dot2_f32_bf16 v161, v137, v137, v161\n"
"v_dot2_f32_bf16 v162, v153, v6, v162\n"
"v_dot2_f32_bf16 v163, v153, v153, v163\n"
"v_dot2_f32_bf16 v160, v138, v6, v160\n"
"v_dot2_f32_bf16 v161, v138, v138, v161\n"
"v_dot2_f32_bf16 v162, v154, v6, v162\n"
"v_dot2_f32_bf16 v163, v154, v154, v163\n"
"v_dot2_f32_bf16 v160, v139, v6, v160\n"
"v_dot2_f32_bf16 v161, v139, v139, v161\n"
"v_dot2_f32_bf16 v162, v155, v6, v162\n"
"v_dot2_f32_bf16 v163, v155, v155, v163\n"
"v_dot2_f32_bf16 v160, v140, v6, v160\n"
"v_dot2_f32_bf16 v161, v140, v140, v161\n"
"v_dot2_f32_bf16 v162, v156, v6, v162\n"
"v_dot2_f32_bf16 v163, v156, v156, v163\n"
"v_dot2_f32_bf16 v160, v141, v6, v160\n"
"v_dot2_f32_bf16 v161, v141, v141, v161\n"
"v_dot2_f32_bf16 v162, v157, v6, v162\n"
"v_dot2_f32_bf16 v163, v157, v157, v163\n"
"v_dot2_f32_bf16 v160, v142, v6, v160\n"
"v_dot2_f32_bf16 v161, v142, v142, v161\n"
"v_dot2_f32_bf16 v162, v158, v6, v162\n"
"v_dot2_f32_bf16 v163, v158, v158, v163\n"
"v_dot2_f32_bf16 v160, v143, v6, v160\n"
"v_dot2_f32_bf16 v161, v143, v143, v161\n"
"v_dot2_f32_bf16 v162, v159, v6, v162\n"
"v_dot2_f32_bf16 v163, v159, v159, v163\n"
"v_add_u32 v5, 16, v7\n"
"v_and_b32 v168, 15, v5\n"
"v_xor_b32 v168, v168, v8\n"
"v_lshlrev_b32 v168, 4, v168\n"
"v_lshl_add_u32 v168, v5, 10, v168\n"
"v_add_u32 v5, 20, v7\n"
"v_and_b32 v169, 15, v5\n"
"v_xor_b32 v169, v169, v8\n"
"v_lshlrev_b32 v169, 4, v169\n"
"v_lshl_add_u32 v169, v5, 10, v169\n"
"v_add_f32_dpp v160, v160, v160 row_ror:8 row_mask:0xf bank_mask:0xf\n"
"v_add_f32_dpp v161, v161, v161 row_ror:8 row_mask:0xf bank_mask:0xf\n"
"v_add_f32_dpp v162, v162, v162 row_ror:8 row_mask:0xf bank_mask:0xf\n"
"v_add_f32_dpp v163, v163, v163 row_ror:8 row_mask:0xf bank_mask:0xf\n"
"v_add_f32_dpp v160, v160, v160 row_ror:4 row_mask:0xf bank_mask:0xf\n"
"v_add_f32_dpp v161, v161, v161 row_ror:4 row_mask:0xf bank_mask:0xf\n"
"v_add_f32_dpp v162, v162, v162 row_ror:4 row_mask:0xf bank_mask:0xf\n"
"v_add_f32_dpp v163, v163, v163 row_ror:4 row_mask:0xf bank_mask:0xf\n"
"v_add_f32_dpp v160, v160, v160 row_ror:2 row_mask:0xf bank_mask:0xf\n"
"v_add_f32_dpp v161, v161, v161 row_ror:2 row_mask:0xf bank_mask:0xf\n"
"v_add_f32_dpp v162, v162, v162 row_ror:2 row_mask:0xf bank_mask:0xf\n"
"v_add_f32_dpp v163, v163, v163 row_ror:2 row_mask:0xf bank_mask:0xf\n"
"v_add_f32_dpp v160, v160, v160 row_ror:1 row_mask:0xf bank_mask:0xf\n"
"v_add_f32_dpp v161, v161, v161 row_ror:1 row_mask:0xf bank_mask:0xf\n"
"v_add_f32_dpp v162, v162, v162 row_ror:1 row_mask:0xf bank_mask:0xf\n"
"v_add_f32_dpp v163, v163, v163 row_ror:1 row_mask:0xf bank_mask:0xf\n"
"v_mul_f32 v160, 0x3b000000, v160\n"
"v_mul_f32 v161, 0x3b000000, v161\n"
"v_mul_f32 v162, 0x3b000000, v162\n"
"v_mul_f32 v163, 0x3b000000, v163\n"
"v_fma_f32 v161, -v160, v160, v161\n"
"v_fma_f32 v163, -v162, v162, v163\n"
"v_max_f32 v161, 0, v161\n"
"v_max_f32 v163, 0, v163\n"
"v_add_f32 v161, 0x3727c5ac, v161\n"
"v_add_f32 v163, 0x3727c5ac, v163\n"
"v_rsq_f32 v164, v161\n"
"v_rsq_f32 v166, v163\n"
"s_nop 0\n"
"v_mul_f32 v164, s59, v164\n"
"v_mul_f32 v166, s59, v166\n"
"v_mul_f32 v165, -v160, v164\n"
"v_mul_f32 v167, -v162, v166\n"
"v_lshlrev_b32 v172, 16, v128\n"
"v_and_b32 v173, 0xffff0000, v128\n"
"v_pk_fma_f32 v[172:173], v[172:173], v[164:165], v[164:165] op_sel:[0,0,1] op_sel_hi:[1,0,1]\n"
"v_cvt_pk_bf16_f32 v128, v172, v173\n"
"v_lshlrev_b32 v174, 16, v129\n"
"v_and_b32 v175, 0xffff0000, v129\n"
"v_pk_fma_f32 v[174:175], v[174:175], v[164:165], v[164:165] op_sel:[0,0,1] op_sel_hi:[1,0,1]\n"
"v_cvt_pk_bf16_f32 v129, v174, v175\n"
"v_lshlrev_b32 v176, 16, v130\n"
"v_and_b32 v177, 0xffff0000, v130\n"
"v_pk_fma_f32 v[176:177], v[176:177], v[164:165], v[164:165] op_sel:[0,0,1] op_sel_hi:[1,0,1]\n"
"v_cvt_pk_bf16_f32 v130, v176, v177\n"
"v_lshlrev_b32 v178, 16, v131\n"
"v_and_b32 v179, 0xffff0000, v131\n"
"v_pk_fma_f32 v[178:179], v[178:179], v[164:165], v[164:165] op_sel:[0,0,1] op_sel_hi:[1,0,1]\n"
"v_cvt_pk_bf16_f32 v131, v178, v179\n"
"ds_write_b128 v168, v[128:131] offset:0\n"
"v_lshlrev_b32 v172, 16, v132\n"
"v_and_b32 v173, 0xffff0000, v132\n"
"v_pk_fma_f32 v[172:173], v[172:173], v[164:165], v[164:165] op_sel:[0,0,1] op_sel_hi:[1,0,1]\n"
"v_cvt_pk_bf16_f32 v132, v172, v173\n"
"v_lshlrev_b32 v174, 16, v133\n"
"v_and_b32 v175, 0xffff0000, v133\n"
"v_pk_fma_f32 v[174:175], v[174:175], v[164:165], v[164:165] op_sel:[0,0,1] op_sel_hi:[1,0,1]\n"
"v_cvt_pk_bf16_f32 v133, v174, v175\n"
"v_lshlrev_b32 v176, 16, v134\n"
"v_and_b32 v177, 0xffff0000, v134\n"
"v_pk_fma_f32 v[176:177], v[176:177], v[164:165], v[164:165] op_sel:[0,0,1] op_sel_hi:[1,0,1]\n"
"v_cvt_pk_bf16_f32 v134, v176, v177\n"
"v_lshlrev_b32 v178, 16, v135\n"
"v_and_b32 v179, 0xffff0000, v135\n"
"v_pk_fma_f32 v[178:179], v[178:179], v[164:165], v[164:165] op_sel:[0,0,1] op_sel_hi:[1,0,1]\n"
"v_cvt_pk_bf16_f32 v135, v178, v179\n"
"ds_write_b128 v168, v[132:135] offset:256\n"
"v_lshlrev_b32 v172, 16, v136\n"
"v_and_b32 v173, 0xffff0000, v136\n"
"v_pk_fma_f32 v[172:173], v[172:173], v[164:165], v[164:165] op_sel:[0,0,1] op_sel_hi:[1,0,1]\n"
"v_cvt_pk_bf16_f32 v136, v172, v173\n"
"v_lshlrev_b32 v174, 16, v137\n"
"v_and_b32 v175, 0xffff0000, v137\n"
"v_pk_fma_f32 v[174:175], v[174:175], v[164:165], v[164:165] op_sel:[0,0,1] op_sel_hi:[1,0,1]\n"
"v_cvt_pk_bf16_f32 v137, v174, v175\n"
"v_lshlrev_b32 v176, 16, v138\n"
"v_and_b32 v177, 0xffff0000, v138\n"
"v_pk_fma_f32 v[176:177], v[176:177], v[164:165], v[164:165] op_sel:[0,0,1] op_sel_hi:[1,0,1]\n"
"v_cvt_pk_bf16_f32 v138, v176, v177\n"
"v_lshlrev_b32 v178, 16, v139\n"
"v_and_b32 v179, 0xffff0000, v139\n"
"v_pk_fma_f32 v[178:179], v[178:179], v[164:165], v[164:165] op_sel:[0,0,1] op_sel_hi:[1,0,1]\n"
"v_cvt_pk_bf16_f32 v139, v178, v179\n"
"ds_write_b128 v168, v[136:139] offset:512\n"
"v_lshlrev_b32 v172, 16, v140\n"
"v_and_b32 v173, 0xffff0000, v140\n"
"v_pk_fma_f32 v[172:173], v[172:173], v[164:165], v[164:165] op_sel:[0,0,1] op_sel_hi:[1,0,1]\n"
"v_cvt_pk_bf16_f32 v140, v172, v173\n"
"v_lshlrev_b32 v174, 16, v141\n"
"v_and_b32 v175, 0xffff0000, v141\n"
"v_pk_fma_f32 v[174:175], v[174:175], v[164:165], v[164:165] op_sel:[0,0,1] op_sel_hi:[1,0,1]\n"
"v_cvt_pk_bf16_f32 v141, v174, v175\n"
"v_lshlrev_b32 v176, 16, v142\n"
"v_and_b32 v177, 0xffff0000, v142\n"
"v_pk_fma_f32 v[176:177], v[176:177], v[164:165], v[164:165] op_sel:[0,0,1] op_sel_hi:[1,0,1]\n"
"v_cvt_pk_bf16_f32 v142, v176, v177\n"
"v_lshlrev_b32 v178, 16, v143\n"
"v_and_b32 v179, 0xffff0000, v143\n"
"v_pk_fma_f32 v[178:179], v[178:179], v[164:165], v[164:165] op_sel:[0,0,1] op_sel_hi:[1,0,1]\n"
"v_cvt_pk_bf16_f32 v143, v178, v179\n"
"ds_write_b128 v168, v[140:143] offset:768\n"
"v_lshlrev_b32 v172, 16, v144\n"
"v_and_b32 v173, 0xffff0000, v144\n"
"v_pk_fma_f32 v[172:173], v[172:173], v[166:167], v[166:167] op_sel:[0,0,1] op_sel_hi:[1,0,1]\n"
"v_cvt_pk_bf16_f32 v144, v172, v173\n"
"v_lshlrev_b32 v174, 16, v145\n"
"v_and_b32 v175, 0xffff0000, v145\n"
"v_pk_fma_f32 v[174:175], v[174:175], v[166:167], v[166:167] op_sel:[0,0,1] op_sel_hi:[1,0,1]\n"
"v_cvt_pk_bf16_f32 v145, v174, v175\n"
"v_lshlrev_b32 v176, 16, v146\n"
"v_and_b32 v177, 0xffff0000, v146\n"
"v_pk_fma_f32 v[176:177], v[176:177], v[166:167], v[166:167] op_sel:[0,0,1] op_sel_hi:[1,0,1]\n"
"v_cvt_pk_bf16_f32 v146, v176, v177\n"
"v_lshlrev_b32 v178, 16, v147\n"
"v_and_b32 v179, 0xffff0000, v147\n"
"v_pk_fma_f32 v[178:179], v[178:179], v[166:167], v[166:167] op_sel:[0,0,1] op_sel_hi:[1,0,1]\n"
"v_cvt_pk_bf16_f32 v147, v178, v179\n"
"ds_write_b128 v169, v[144:147] offset:0\n"
"v_lshlrev_b32 v172, 16, v148\n"
"v_and_b32 v173, 0xffff0000, v148\n"
"v_pk_fma_f32 v[172:173], v[172:173], v[166:167], v[166:167] op_sel:[0,0,1] op_sel_hi:[1,0,1]\n"
"v_cvt_pk_bf16_f32 v148, v172, v173\n"
"v_lshlrev_b32 v174, 16, v149\n"
"v_and_b32 v175, 0xffff0000, v149\n"
"v_pk_fma_f32 v[174:175], v[174:175], v[166:167], v[166:167] op_sel:[0,0,1] op_sel_hi:[1,0,1]\n"
"v_cvt_pk_bf16_f32 v149, v174, v175\n"
"v_lshlrev_b32 v176, 16, v150\n"
"v_and_b32 v177, 0xffff0000, v150\n"
"v_pk_fma_f32 v[176:177], v[176:177], v[166:167], v[166:167] op_sel:[0,0,1] op_sel_hi:[1,0,1]\n"
"s_mov_b32 s39, 0x20000\n"
"s_lshl_b32 s53, s58, 2\n"
"s_add_u32 s53, s53, s42\n"
"s_cmp_ge_u32 s53, 448\n"
"s_waitcnt lgkmcnt(0)\n"
"s_cbranch_scc1 LPSF%=\n"
"s_add_u32 s53, s53, 512\n"
"s_cmp_lt_u32 s53, 512\n"
"s_cbranch_scc1 LPQFa%=\n"
"s_cmp_lt_u32 s53, 768\n"
"s_cbranch_scc1 LPKFa%=\n"
"s_cmp_lt_u32 s53, 1248\n"
"s_cbranch_scc1 LPGFa%=\n"
"s_cmp_lt_u32 s53, 1472\n"
"s_cbranch_scc1 LPDFa%=\n"
"s_sub_u32 s53, s53, 1472\n"
"s_mov_b32 s36, s72\n"
"s_and_b32 s37, s73, 0xffff\n"
"s_mov_b32 s38, 4194304\n"
"s_lshr_b32 s54, s53, 5\n"
"s_lshl_b32 s54, s54, 15\n"
"s_bfe_u32 s55, s53, 0x30002\n"
"s_lshl_b32 s55, s55, 19\n"
"s_add_u32 s54, s54, s55\n"
"s_and_b32 s53, s53, 3\n"
"s_lshl_b32 s53, s53, 13\n"
"s_add_u32 s54, s54, s53\n"
"v_lshlrev_b32 v56, 7, v1\n"
"s_branch LPLFa%=\n"
"LPQFa%=:\n"
"s_mov_b32 s36, s70\n"
"s_and_b32 s37, s71, 0xffff\n"
"s_mov_b32 s38, 4194304\n"
"s_lshl_b32 s54, s53, 13\n"
"v_lshlrev_b32 v56, 7, v1\n"
"s_branch LPLFa%=\n"
"LPKFa%=:\n"
"s_sub_u32 s53, s53, 512\n"
"s_bitcmp0_b32 s53, 0\n"
"s_cselect_b32 s36, s66, s68\n"
"s_cselect_b32 s37, s67, s69\n"
"s_and_b32 s37, s37, 0xffff\n"
"s_mov_b32 s38, 4194304\n"
"s_lshr_b32 s54, s53, 4\n"
"s_lshl_b32 s54, s54, 13\n"
"s_bfe_u32 s55, s53, 0x30001\n"
"s_lshl_b32 s55, s55, 19\n"
"s_add_u32 s54, s54, s55\n"
"s_add_u32 s54, s54, 393216\n"
"v_lshlrev_b32 v56, 7, v1\n"
"s_branch LPLFa%=\n"
"LPGFa%=:\n"
"s_sub_u32 s53, s53, 768\n"
"s_bitcmp0_b32 s53, 4\n"
"s_cselect_b32 s36, s74, s76\n"
"s_cselect_b32 s37, s75, s77\n"
"s_and_b32 s37, s37, 0xffff\n"
"s_mov_b32 s38, 1835008\n"
"s_and_b32 s54, s53, 15\n"
"s_mul_i32 s54, s54, 114688\n"
"s_lshr_b32 s53, s53, 5\n"
"s_mul_i32 s55, s53, 22\n"
"s_lshr_b32 s55, s55, 6\n"
"s_mul_i32 s56, s55, 3\n"
"s_sub_u32 s53, s53, s56\n"
"s_mul_i32 s53, s53, 1280\n"
"s_lshl_b32 s55, s55, 8\n"
"s_add_u32 s54, s54, s53\n"
"s_add_u32 s54, s54, s55\n"
"v_lshrrev_b32 v56, 1, v1\n"
"v_and_b32 v57, 1, v1\n"
"v_mul_u32_u24 v56, 3584, v56\n"
"v_lshl_add_u32 v56, v57, 7, v56\n"
"s_branch LPLFa%=\n"
"LPDFa%=:\n"
"s_sub_u32 s53, s53, 1248\n"
"s_mov_b32 s36, s78\n"
"s_and_b32 s37, s79, 0xffff\n"
"s_mov_b32 s38, 1835008\n"
"s_lshl_b32 s54, s53, 13\n"
"v_lshlrev_b32 v56, 7, v1\n"
"LPLFa%=:\n"
"buffer_load_dword v63, v56, s[36:39], s54 offen\n"
"LPSF%=:\n"
"v_cvt_pk_bf16_f32 v150, v176, v177\n"
"v_lshlrev_b32 v178, 16, v151\n"
"v_and_b32 v179, 0xffff0000, v151\n"
"v_pk_fma_f32 v[178:179], v[178:179], v[166:167], v[166:167] op_sel:[0,0,1] op_sel_hi:[1,0,1]\n"
"v_cvt_pk_bf16_f32 v151, v178, v179\n"
"ds_write_b128 v169, v[148:151] offset:256\n"
"v_lshlrev_b32 v172, 16, v152\n"
"v_and_b32 v173, 0xffff0000, v152\n"
"v_pk_fma_f32 v[172:173], v[172:173], v[166:167], v[166:167] op_sel:[0,0,1] op_sel_hi:[1,0,1]\n"
"v_cvt_pk_bf16_f32 v152, v172, v173\n"
"v_lshlrev_b32 v174, 16, v153\n"
"v_and_b32 v175, 0xffff0000, v153\n"
"v_pk_fma_f32 v[174:175], v[174:175], v[166:167], v[166:167] op_sel:[0,0,1] op_sel_hi:[1,0,1]\n"
"v_cvt_pk_bf16_f32 v153, v174, v175\n"
"v_lshlrev_b32 v176, 16, v154\n"
"v_and_b32 v177, 0xffff0000, v154\n"
"v_pk_fma_f32 v[176:177], v[176:177], v[166:167], v[166:167] op_sel:[0,0,1] op_sel_hi:[1,0,1]\n"
"v_cvt_pk_bf16_f32 v154, v176, v177\n"
"v_lshlrev_b32 v178, 16, v155\n"
"v_and_b32 v179, 0xffff0000, v155\n"
"v_pk_fma_f32 v[178:179], v[178:179], v[166:167], v[166:167] op_sel:[0,0,1] op_sel_hi:[1,0,1]\n"
"v_cvt_pk_bf16_f32 v155, v178, v179\n"
"ds_write_b128 v169, v[152:155] offset:512\n"
"v_lshlrev_b32 v172, 16, v156\n"
"v_and_b32 v173, 0xffff0000, v156\n"
"v_pk_fma_f32 v[172:173], v[172:173], v[166:167], v[166:167] op_sel:[0,0,1] op_sel_hi:[1,0,1]\n"
"v_cvt_pk_bf16_f32 v156, v172, v173\n"
"v_lshlrev_b32 v174, 16, v157\n"
"v_and_b32 v175, 0xffff0000, v157\n"
"v_pk_fma_f32 v[174:175], v[174:175], v[166:167], v[166:167] op_sel:[0,0,1] op_sel_hi:[1,0,1]\n"
"v_cvt_pk_bf16_f32 v157, v174, v175\n"
"v_lshlrev_b32 v176, 16, v158\n"
"v_and_b32 v177, 0xffff0000, v158\n"
"v_pk_fma_f32 v[176:177], v[176:177], v[166:167], v[166:167] op_sel:[0,0,1] op_sel_hi:[1,0,1]\n"
"v_cvt_pk_bf16_f32 v158, v176, v177\n"
"v_lshlrev_b32 v178, 16, v159\n"
"v_and_b32 v179, 0xffff0000, v159\n"
"v_pk_fma_f32 v[178:179], v[178:179], v[166:167], v[166:167] op_sel:[0,0,1] op_sel_hi:[1,0,1]\n"
"v_cvt_pk_bf16_f32 v159, v178, v179\n"
"ds_write_b128 v169, v[156:159] offset:768\n"
"s_waitcnt vmcnt(12)\n"
"s_waitcnt lgkmcnt(0)\n"
"s_barrier\n"
"ds_read_b64_tr_b16 a[192:193], v10 offset:0\n"
"ds_read_b64_tr_b16 a[194:195], v10 offset:256\n"
"ds_read_b64_tr_b16 a[196:197], v11 offset:0\n"
"ds_read_b64_tr_b16 a[198:199], v11 offset:256\n"
"ds_read_b64_tr_b16 a[200:201], v10 offset:2048\n"
"ds_read_b64_tr_b16 a[202:203], v10 offset:2304\n"
"ds_read_b64_tr_b16 a[204:205], v11 offset:2048\n"
"ds_read_b64_tr_b16 a[206:207], v11 offset:2304\n"
"ds_read_b128 a[224:227], v14 offset:0\n"
"ds_read_b128 a[228:231], v14 offset:16384\n"
"ds_read_b128 a[232:235], v14 offset:32768\n"
"ds_read_b128 a[236:239], v14 offset:49152\n"
"ds_read_b128 a[240:243], v18 offset:0\n"
"ds_read_b128 a[244:247], v18 offset:16384\n"
"; ---- end of section: ln\n"
"s_waitcnt lgkmcnt(5)\n"
"v_mfma_f32_16x16x32_bf16 v[64:67], a[192:195], a[224:227], 0\n"
"v_mfma_f32_16x16x32_bf16 v[88:91], a[196:199], a[224:227], 0\n"
"s_waitcnt vmcnt(8)\n"
"v_mfma_f32_16x16x32_bf16 v[112:115], a[200:203], a[224:227], 0\n"
"ds_read_b64_tr_b16 a[208:209], v10 offset:4096\n"
"ds_read_b64_tr_b16 a[210:211], v10 offset:4352\n"
"v_mfma_f32_16x16x32_bf16 v[136:139], a[204:207], a[224:227], 0\n"
"ds_read_b128 a[224:227], v15 offset:0\n"
"s_waitcnt lgkmcnt(7)\n"
"v_mfma_f32_16x16x32_bf16 v[68:71], a[192:195], a[228:231], 0\n"
"ds_read_b64_tr_b16 a[212:213], v11 offset:4096\n"
"ds_read_b64_tr_b16 a[214:215], v11 offset:4352\n"
"v_mfma_f32_16x16x32_bf16 v[92:95], a[196:199], a[228:231], 0\n"
"v_mfma_f32_16x16x32_bf16 v[116:119], a[200:203], a[228:231], 0\n"
"ds_read_b64_tr_b16 a[216:217], v10 offset:6144\n"
"ds_read_b64_tr_b16 a[218:219], v10 offset:6400\n"
"v_mfma_f32_16x16x32_bf16 v[140:143], a[204:207], a[228:231], 0\n"
"ds_read_b128 a[228:231], v15 offset:16384\n"
"s_waitcnt lgkmcnt(11)\n"
"v_mfma_f32_16x16x32_bf16 v[72:75], a[192:195], a[232:235], 0\n"
"ds_read_b64_tr_b16 a[220:221], v11 offset:6144\n"
"ds_read_b64_tr_b16 a[222:223], v11 offset:6400\n"
"v_mfma_f32_16x16x32_bf16 v[96:99], a[196:199], a[232:235], 0\n"
"v_mfma_f32_16x16x32_bf16 v[120:123], a[200:203], a[232:235], 0\n"
"s_add_u32 s50, s62, 458752\n"
"s_add_u32 m0, s49, 0\n"
"v_mfma_f32_16x16x32_bf16 v[144:147], a[204:207], a[232:235], 0\n"
"buffer_load_dwordx4 v22, s[24:27], s50 offen lds\n"
"buffer_load_dwordx4 v23, s[24:27], s50 offen offset:1024 lds\n"
"ds_read_b128 a[232:235], v15 offset:32768\n"
"s_waitcnt lgkmcnt(11)\n"
"v_mfma_f32_16x16x32_bf16 v[76:79], a[192:195], a[236:239], 0\n"
"s_add_u32 m0, s49, 2048\n"
"s_nop 0\n"
"buffer_load_dwordx4 v22, s[84:87], s50 offen lds\n"
"v_mfma_f32_16x16x32_bf16 v[100:103], a[196:199], a[236:239], 0\n"
"buffer_load_dwordx4 v23, s[84:87], s50 offen offset:1024 lds\n"
"v_mfma_f32_16x16x32_bf16 v[124:127], a[200:203], a[236:239], 0\n"
"v_mfma_f32_16x16x32_bf16 v[148:151], a[204:207], a[236:239], 0\n"
"ds_read_b128 a[236:239], v15 offset:49152\n"
"v_mfma_f32_16x16x32_bf16 v[80:83], a[192:195], a[240:243], 0\n"
"v_mfma_f32_16x16x32_bf16 v[104:107], a[196:199], a[240:243], 0\n"
"v_mfma_f32_16x16x32_bf16 v[128:131], a[200:203], a[240:243], 0\n"
"v_mfma_f32_16x16x32_bf16 v[152:155], a[204:207], a[240:243], 0\n"
"ds_read_b128 a[240:243], v19 offset:0\n"
"v_mfma_f32_16x16x32_bf16 v[84:87], a[192:195], a[244:247], 0\n"
"v_mfma_f32_16x16x32_bf16 v[108:111], a[196:199], a[244:247], 0\n"
"v_mfma_f32_16x16x32_bf16 v[132:135], a[200:203], a[244:247], 0\n"
"v_mfma_f32_16x16x32_bf16 v[156:159], a[204:207], a[244:247], 0\n"
"ds_read_b128 a[244:247], v19 offset:16384\n"
"s_waitcnt lgkmcnt(3)\n"
"v_mfma_f32_16x16x32_bf16 v[64:67], a[208:211], a[224:227], v[64:67]\n"
"v_mfma_f32_16x16x32_bf16 v[88:91], a[212:215], a[224:227], v[88:91]\n"
"s_waitcnt vmcnt(8)\n"
"v_mfma_f32_16x16x32_bf16 v[112:115], a[216:219], a[224:227], v[112:115]\n"
"ds_read_b64_tr_b16 a[192:193], v10 offset:8192\n"
"ds_read_b64_tr_b16 a[194:195], v10 offset:8448\n"
"v_mfma_f32_16x16x32_bf16 v[136:139], a[220:223], a[224:227], v[136:139]\n"
"ds_read_b128 a[224:227], v16 offset:0\n"
"v_mfma_f32_16x16x32_bf16 v[68:71], a[208:211], a[228:231], v[68:71]\n"
"ds_read_b64_tr_b16 a[196:197], v11 offset:8192\n"
"ds_read_b64_tr_b16 a[198:199], v11 offset:8448\n"
"v_mfma_f32_16x16x32_bf16 v[92:95], a[212:215], a[228:231], v[92:95]\n"
"v_mfma_f32_16x16x32_bf16 v[116:119], a[216:219], a[228:231], v[116:119]\n"
"ds_read_b64_tr_b16 a[200:201], v10 offset:10240\n"
"ds_read_b64_tr_b16 a[202:203], v10 offset:10496\n"
"v_mfma_f32_16x16x32_bf16 v[140:143], a[220:223], a[228:231], v[140:143]\n"
"ds_read_b128 a[228:231], v16 offset:16384\n"
"v_mfma_f32_16x16x32_bf16 v[72:75], a[208:211], a[232:235], v[72:75]\n"
"ds_read_b64_tr_b16 a[204:205], v11 offset:10240\n"
"ds_read_b64_tr_b16 a[206:207], v11 offset:10496\n"
"v_mfma_f32_16x16x32_bf16 v[96:99], a[212:215], a[232:235], v[96:99]\n"
"v_mfma_f32_16x16x32_bf16 v[120:123], a[216:219], a[232:235], v[120:123]\n"
"s_add_u32 s50, s62, 573440\n"
"s_add_u32 m0, s49, 4096\n"
"v_mfma_f32_16x16x32_bf16 v[144:147], a[220:223], a[232:235], v[144:147]\n"
"buffer_load_dwordx4 v22, s[24:27], s50 offen lds\n"
"buffer_load_dwordx4 v23, s[24:27], s50 offen offset:1024 lds\n"
"ds_read_b128 a[232:235], v16 offset:32768\n"
"s_waitcnt lgkmcnt(11)\n"
"v_mfma_f32_16x16x32_bf16 v[76:79], a[208:211], a[236:239], v[76:79]\n"
"s_add_u32 m0, s49, 6144\n"
"s_nop 0\n"
"buffer_load_dwordx4 v22, s[84:87], s50 offen lds\n"
"v_mfma_f32_16x16x32_bf16 v[100:103], a[212:215], a[236:239], v[100:103]\n"
"buffer_load_dwordx4 v23, s[84:87], s50 offen offset:1024 lds\n"
"v_mfma_f32_16x16x32_bf16 v[124:127], a[216:219], a[236:239], v[124:127]\n"
"v_mfma_f32_16x16x32_bf16 v[148:151], a[220:223], a[236:239], v[148:151]\n"
"ds_read_b128 a[236:239], v16 offset:49152\n"
"v_mfma_f32_16x16x32_bf16 v[80:83], a[208:211], a[240:243], v[80:83]\n"
"v_mfma_f32_16x16x32_bf16 v[104:107], a[212:215], a[240:243], v[104:107]\n"
"v_mfma_f32_16x16x32_bf16 v[128:131], a[216:219], a[240:243], v[128:131]\n"
"v_mfma_f32_16x16x32_bf16 v[152:155], a[220:223], a[240:243], v[152:155]\n"
"ds_read_b128 a[240:243], v20 offset:0\n"
"v_mfma_f32_16x16x32_bf16 v[84:87], a[208:211], a[244:247], v[84:87]\n"
"v_mfma_f32_16x16x32_bf16 v[108:111], a[212:215], a[244:247], v[108:111]\n"
"v_mfma_f32_16x16x32_bf16 v[132:135], a[216:219], a[244:247], v[132:135]\n"
"v_mfma_f32_16x16x32_bf16 v[156:159], a[220:223], a[244:247], v[156:159]\n"
"ds_read_b128 a[244:247], v20 offset:16384\n"
"s_waitcnt lgkmcnt(3)\n"
"v_mfma_f32_16x16x32_bf16 v[64:67], a[192:195], a[224:227], v[64:67]\n"
"v_mfma_f32_16x16x32_bf16 v[88:91], a[196:199], a[224:227], v[88:91]\n"
"s_waitcnt vmcnt(8)\n"
"v_mfma_f32_16x16x32_bf16 v[112:115], a[200:203], a[224:227], v[112:115]\n"
"ds_read_b64_tr_b16 a[208:209], v10 offset:12288\n"
"ds_read_b64_tr_b16 a[210:211], v10 offset:12544\n"
"v_mfma_f32_16x16x32_bf16 v[136:139], a[204:207], a[224:227], v[136:139]\n"
"ds_read_b128 a[224:227], v17 offset:0\n"
"v_mfma_f32_16x16x32_bf16 v[68:71], a[192:195], a[228:231], v[68:71]\n"
"ds_read_b64_tr_b16 a[212:213], v11 offset:12288\n"
"ds_read_b64_tr_b16 a[214:215], v11 offset:12544\n"
"v_mfma_f32_16x16x32_bf16 v[92:95], a[196:199], a[228:231], v[92:95]\n"
"v_mfma_f32_16x16x32_bf16 v[116:119], a[200:203], a[228:231], v[116:119]\n"
"ds_read_b64_tr_b16 a[216:217], v10 offset:14336\n"
"ds_read_b64_tr_b16 a[218:219], v10 offset:14592\n"
"v_mfma_f32_16x16x32_bf16 v[140:143], a[204:207], a[228:231], v[140:143]\n"
"ds_read_b128 a[228:231], v17 offset:16384\n"
"v_mfma_f32_16x16x32_bf16 v[72:75], a[192:195], a[232:235], v[72:75]\n"
"ds_read_b64_tr_b16 a[220:221], v11 offset:14336\n"
"ds_read_b64_tr_b16 a[222:223], v11 offset:14592\n"
"v_mfma_f32_16x16x32_bf16 v[96:99], a[196:199], a[232:235], v[96:99]\n"
"v_mfma_f32_16x16x32_bf16 v[120:123], a[200:203], a[232:235], v[120:123]\n"
"s_add_u32 s50, s62, 688128\n"
"s_add_u32 m0, s49, 8192\n"
"v_mfma_f32_16x16x32_bf16 v[144:147], a[204:207], a[232:235], v[144:147]\n"
"buffer_load_dwordx4 v22, s[24:27], s50 offen lds\n"
"buffer_load_dwordx4 v23, s[24:27], s50 offen offset:1024 lds\n"
"ds_read_b128 a[232:235], v17 offset:32768\n"
"s_waitcnt lgkmcnt(11)\n"
"v_mfma_f32_16x16x32_bf16 v[76:79], a[192:195], a[236:239], v[76:79]\n"
"s_add_u32 m0, s49, 10240\n"
"s_nop 0\n"
"buffer_load_dwordx4 v22, s[84:87], s50 offen lds\n"
"v_mfma_f32_16x16x32_bf16 v[100:103], a[196:199], a[236:239], v[100:103]\n"
"buffer_load_dwordx4 v23, s[84:87], s50 offen offset:1024 lds\n"
"v_mfma_f32_16x16x32_bf16 v[124:127], a[200:203], a[236:239], v[124:127]\n"
"v_mfma_f32_16x16x32_bf16 v[148:151], a[204:207], a[236:239], v[148:151]\n"
"ds_read_b128 a[236:239], v17 offset:49152\n"
"v_mfma_f32_16x16x32_bf16 v[80:83], a[192:195], a[240:243], v[80:83]\n"
"v_mfma_f32_16x16x32_bf16 v[104:107], a[196:199], a[240:243], v[104:107]\n"
"v_mfma_f32_16x16x32_bf16 v[128:131], a[200:203], a[240:243], v[128:131]\n"
"v_mfma_f32_16x16x32_bf16 v[152:155], a[204:207], a[240:243], v[152:155]\n"
"ds_read_b128 a[240:243], v21 offset:0\n"
"v_mfma_f32_16x16x32_bf16 v[84:87], a[192:195], a[244:247], v[84:87]\n"
"v_mfma_f32_16x16x32_bf16 v[108:111], a[196:199], a[244:247], v[108:111]\n"
"v_mfma_f32_16x16x32_bf16 v[132:135], a[200:203], a[244:247], v[132:135]\n"
"v_mfma_f32_16x16x32_bf16 v[156:159], a[204:207], a[244:247], v[156:159]\n"
"ds_read_b128 a[244:247], v21 offset:16384\n"
"s_waitcnt lgkmcnt(3)\n"
"v_mfma_f32_16x16x32_bf16 v[64:67], a[208:211], a[224:227], v[64:67]\n"
"v_mfma_f32_16x16x32_bf16 v[88:91], a[212:215], a[224:227], v[88:91]\n"
"s_waitcnt vmcnt(8)\n"
"v_mfma_f32_16x16x32_bf16 v[112:115], a[216:219], a[224:227], v[112:115]\n"
"ds_read_b64_tr_b16 a[192:193], v10 offset:0\n"
"ds_read_b64_tr_b16 a[194:195], v10 offset:256\n"
"v_mfma_f32_16x16x32_bf16 v[136:139], a[220:223], a[224:227], v[136:139]\n"
"ds_read_b128 a[224:227], v14 offset:256\n"
"v_mfma_f32_16x16x32_bf16 v[68:71], a[208:211], a[228:231], v[68:71]\n"
"ds_read_b64_tr_b16 a[196:197], v11 offset:0\n"
"ds_read_b64_tr_b16 a[198:199], v11 offset:256\n"
"v_mfma_f32_16x16x32_bf16 v[92:95], a[212:215], a[228:231], v[92:95]\n"
"v_mfma_f32_16x16x32_bf16 v[116:119], a[216:219], a[228:231], v[116:119]\n"
"ds_read_b64_tr_b16 a[200:201], v10 offset:2048\n"
"ds_read_b64_tr_b16 a[202:203], v10 offset:2304\n"
"v_mfma_f32_16x16x32_bf16 v[140:143], a[220:223], a[228:231], v[140:143]\n"
"ds_read_b128 a[228:231], v14 offset:16640\n"
"v_mfma_f32_16x16x32_bf16 v[72:75], a[208:211], a[232:235], v[72:75]\n"
"ds_read_b64_tr_b16 a[204:205], v11 offset:2048\n"
"ds_read_b64_tr_b16 a[206:207], v11 offset:2304\n"
"v_mfma_f32_16x16x32_bf16 v[96:99], a[212:215], a[232:235], v[96:99]\n"
"v_mfma_f32_16x16x32_bf16 v[120:123], a[216:219], a[232:235], v[120:123]\n"
"s_add_u32 s50, s62, 802816\n"
"s_add_u32 m0, s49, 12288\n"
"v_mfma_f32_16x16x32_bf16 v[144:147], a[220:223], a[232:235], v[144:147]\n"
"buffer_load_dwordx4 v22, s[24:27], s50 offen lds\n"
"buffer_load_dwordx4 v23, s[24:27], s50 offen offset:1024 lds\n"
"ds_read_b128 a[232:235], v14 offset:33024\n"
"s_waitcnt lgkmcnt(11)\n"
"v_mfma_f32_16x16x32_bf16 v[76:79], a[208:211], a[236:239], v[76:79]\n"
"s_add_u32 m0, s49, 14336\n"
"s_nop 0\n"
"buffer_load_dwordx4 v22, s[84:87], s50 offen lds\n"
"v_mfma_f32_16x16x32_bf16 v[100:103], a[212:215], a[236:239], v[100:103]\n"
"buffer_load_dwordx4 v23, s[84:87], s50 offen offset:1024 lds\n"
"v_mfma_f32_16x16x32_bf16 v[124:127], a[216:219], a[236:239], v[124:127]\n"
"v_mfma_f32_16x16x32_bf16 v[148:151], a[220:223], a[236:239], v[148:151]\n"
"ds_read_b128 a[236:239], v14 offset:49408\n"
"v_mfma_f32_16x16x32_bf16 v[80:83], a[208:211], a[240:243], v[80:83]\n"
"v_mfma_f32_16x16x32_bf16 v[104:107], a[212:215], a[240:243], v[104:107]\n"
"v_mfma_f32_16x16x32_bf16 v[128:131], a[216:219], a[240:243], v[128:131]\n"
"v_mfma_f32_16x16x32_bf16 v[152:155], a[220:223], a[240:243], v[152:155]\n"
"ds_read_b128 a[240:243], v18 offset:256\n"
"v_mfma_f32_16x16x32_bf16 v[84:87], a[208:211], a[244:247], v[84:87]\n"
"v_mfma_f32_16x16x32_bf16 v[108:111], a[212:215], a[244:247], v[108:111]\n"
"v_mfma_f32_16x16x32_bf16 v[132:135], a[216:219], a[244:247], v[132:135]\n"
"v_mfma_f32_16x16x32_bf16 v[156:159], a[220:223], a[244:247], v[156:159]\n"
"ds_read_b128 a[244:247], v18 offset:16640\n"
"s_waitcnt lgkmcnt(3)\n"
"v_mfma_f32_16x16x32_bf16 v[64:67], a[192:195], a[224:227], v[64:67]\n"
"v_mfma_f32_16x16x32_bf16 v[88:91], a[196:199], a[224:227], v[88:91]\n"
"s_waitcnt vmcnt(8)\n"
"v_mfma_f32_16x16x32_bf16 v[112:115], a[200:203], a[224:227], v[112:115]\n"
"ds_read_b64_tr_b16 a[208:209], v10 offset:4096\n"
"ds_read_b64_tr_b16 a[210:211], v10 offset:4352\n"
"v_mfma_f32_16x16x32_bf16 v[136:139], a[204:207], a[224:227], v[136:139]\n"
"ds_read_b128 a[224:227], v15 offset:256\n"
"v_mfma_f32_16x16x32_bf16 v[68:71], a[192:195], a[228:231], v[68:71]\n"
"ds_read_b64_tr_b16 a[212:213], v11 offset:4096\n"
"ds_read_b64_tr_b16 a[214:215], v11 offset:4352\n"
"v_mfma_f32_16x16x32_bf16 v[92:95], a[196:199], a[228:231], v[92:95]\n"
"v_mfma_f32_16x16x32_bf16 v[116:119], a[200:203], a[228:231], v[116:119]\n"
"ds_read_b64_tr_b16 a[216:217], v10 offset:6144\n"
"ds_read_b64_tr_b16 a[218:219], v10 offset:6400\n"
"v_mfma_f32_16x16x32_bf16 v[140:143], a[204:207], a[228:231], v[140:143]\n"
"ds_read_b128 a[228:231], v15 offset:16640\n"
"v_mfma_f32_16x16x32_bf16 v[72:75], a[192:195], a[232:235], v[72:75]\n"
"ds_read_b64_tr_b16 a[220:221], v11 offset:6144\n"
"ds_read_b64_tr_b16 a[222:223], v11 offset:6400\n"
"v_mfma_f32_16x16x32_bf16 v[96:99], a[196:199], a[232:235], v[96:99]\n"
"v_mfma_f32_16x16x32_bf16 v[120:123], a[200:203], a[232:235], v[120:123]\n"
"s_add_u32 s50, s62, 917504\n"
"s_add_u32 m0, s49, 0\n"
"v_mfma_f32_16x16x32_bf16 v[144:147], a[204:207], a[232:235], v[144:147]\n"
"buffer_load_dwordx4 v22, s[24:27], s50 offen lds\n"
"buffer_load_dwordx4 v23, s[24:27], s50 offen offset:1024 lds\n"
"ds_read_b128 a[232:235], v15 offset:33024\n"
"s_waitcnt lgkmcnt(11)\n"
"v_mfma_f32_16x16x32_bf16 v[76:79], a[192:195], a[236:239], v[76:79]\n"
"s_add_u32 m0, s49, 2048\n"
"s_nop 0\n"
"buffer_load_dwordx4 v22, s[84:87], s50 offen lds\n"
"v_mfma_f32_16x16x32_bf16 v[100:103], a[196:199], a[236:239], v[100:103]\n"
"buffer_load_dwordx4 v23, s[84:87], s50 offen offset:1024 lds\n"
"v_mfma_f32_16x16x32_bf16 v[124:127], a[200:203], a[236:239], v[124:127]\n"
"v_mfma_f32_16x16x32_bf16 v[148:151], a[204:207], a[236:239], v[148:151]\n"
"ds_read_b128 a[236:239], v15 offset:49408\n"
"v_mfma_f32_16x16x32_bf16 v[80:83], a[192:195], a[240:243], v[80:83]\n"
"v_mfma_f32_16x16x32_bf16 v[104:107], a[196:199], a[240:243], v[104:107]\n"
"v_mfma_f32_16x16x32_bf16 v[128:131], a[200:203], a[240:243], v[128:131]\n"
"v_mfma_f32_16x16x32_bf16 v[152:155], a[204:207], a[240:243], v[152:155]\n"
"ds_read_b128 a[240:243], v19 offset:256\n"
"v_mfma_f32_16x16x32_bf16 v[84:87], a[192:195], a[244:247], v[84:87]\n"
"v_mfma_f32_16x16x32_bf16 v[108:111], a[196:199], a[244:247], v[108:111]\n"
"v_mfma_f32_16x16x32_bf16 v[132:135], a[200:203], a[244:247], v[132:135]\n"
"v_mfma_f32_16x16x32_bf16 v[156:159], a[204:207], a[244:247], v[156:159]\n"
"ds_read_b128 a[244:247], v19 offset:16640\n"
"s_waitcnt lgkmcnt(3)\n"
"v_mfma_f32_16x16x32_bf16 v[64:67], a[208:211], a[224:227], v[64:67]\n"
"v_mfma_f32_16x16x32_bf16 v[88:91], a[212:215], a[224:227], v[88:91]\n"
"s_waitcnt vmcnt(8)\n"
"v_mfma_f32_16x16x32_bf16 v[112:115], a[216:219], a[224:227], v[112:115]\n"
"ds_read_b64_tr_b16 a[192:193], v10 offset:8192\n"
"ds_read_b64_tr_b16 a[194:195], v10 offset:8448\n"
"v_mfma_f32_16x16x32_bf16 v[136:139], a[220:223], a[224:227], v[136:139]\n"
"ds_read_b128 a[224:227], v16 offset:256\n"
"v_mfma_f32_16x16x32_bf16 v[68:71], a[208:211], a[228:231], v[68:71]\n"
"ds_read_b64_tr_b16 a[196:197], v11 offset:8192\n"
"ds_read_b64_tr_b16 a[198:199], v11 offset:8448\n"
"v_mfma_f32_16x16x32_bf16 v[92:95], a[212:215], a[228:231], v[92:95]\n"
"v_mfma_f32_16x16x32_bf16 v[116:119], a[216:219], a[228:231], v[116:119]\n"
"ds_read_b64_tr_b16 a[200:201], v10 offset:10240\n"
"ds_read_b64_tr_b16 a[202:203], v10 offset:10496\n"
"v_mfma_f32_16x16x32_bf16 v[140:143], a[220:223], a[228:231], v[140:143]\n"
"ds_read_b128 a[228:231], v16 offset:16640\n"
"v_mfma_f32_16x16x32_bf16 v[72:75], a[208:211], a[232:235], v[72:75]\n"
"ds_read_b64_tr_b16 a[204:205], v11 offset:10240\n"
"ds_read_b64_tr_b16 a[206:207], v11 offset:10496\n"
"v_mfma_f32_16x16x32_bf16 v[96:99], a[212:215], a[232:235], v[96:99]\n"
"v_mfma_f32_16x16x32_bf16 v[120:123], a[216:219], a[232:235], v[120:123]\n"
"s_add_u32 s50, s62, 1032192\n"
"s_add_u32 m0, s49, 4096\n"
"v_mfma_f32_16x16x32_bf16 v[144:147], a[220:223], a[232:235], v[144:147]\n"
"buffer_load_dwordx4 v22, s[24:27], s50 offen lds\n"
"buffer_load_dwordx4 v23, s[24:27], s50 offen offset:1024 lds\n"
"ds_read_b128 a[232:235], v16 offset:33024\n"
"s_waitcnt lgkmcnt(11)\n"
"v_mfma_f32_16x16x32_bf16 v[76:79], a[208:211], a[236:239], v[76:79]\n"
"s_add_u32 m0, s49, 6144\n"
"s_nop 0\n"
"buffer_load_dwordx4 v22, s[84:87], s50 offen lds\n"
"v_mfma_f32_16x16x32_bf16 v[100:103], a[212:215], a[236:239], v[100:103]\n"
"buffer_load_dwordx4 v23, s[84:87], s50 offen offset:1024 lds\n"
"v_mfma_f32_16x16x32_bf16 v[124:127], a[216:219], a[236:239], v[124:127]\n"
"v_mfma_f32_16x16x32_bf16 v[148:151], a[220:223], a[236:239], v[148:151]\n"
"ds_read_b128 a[236:239], v16 offset:49408\n"
"v_mfma_f32_16x16x32_bf16 v[80:83], a[208:211], a[240:243], v[80:83]\n"
"v_mfma_f32_16x16x32_bf16 v[104:107], a[212:215], a[240:243], v[104:107]\n"
"v_mfma_f32_16x16x32_bf16 v[128:131], a[216:219], a[240:243], v[128:131]\n"
"v_mfma_f32_16x16x32_bf16 v[152:155], a[220:223], a[240:243], v[152:155]\n"
"ds_read_b128 a[240:243], v20 offset:256\n"
"v_mfma_f32_16x16x32_bf16 v[84:87], a[208:211], a[244:247], v[84:87]\n"
"v_mfma_f32_16x16x32_bf16 v[108:111], a[212:215], a[244:247], v[108:111]\n"
"v_mfma_f32_16x16x32_bf16 v[132:135], a[216:219], a[244:247], v[132:135]\n"
"v_mfma_f32_16x16x32_bf16 v[156:159], a[220:223], a[244:247], v[156:159]\n"
"ds_read_b128 a[244:247], v20 offset:16640\n"
"s_waitcnt lgkmcnt(3)\n"
"v_mfma_f32_16x16x32_bf16 v[64:67], a[192:195], a[224:227], v[64:67]\n"
"v_mfma_f32_16x16x32_bf16 v[88:91], a[196:199], a[224:227], v[88:91]\n"
"s_waitcnt vmcnt(8)\n"
"v_mfma_f32_16x16x32_bf16 v[112:115], a[200:203], a[224:227], v[112:115]\n"
"ds_read_b64_tr_b16 a[208:209], v10 offset:12288\n"
"ds_read_b64_tr_b16 a[210:211], v10 offset:12544\n"
"v_mfma_f32_16x16x32_bf16 v[136:139], a[204:207], a[224:227], v[136:139]\n"
"ds_read_b128 a[224:227], v17 offset:256\n"
"v_mfma_f32_16x16x32_bf16 v[68:71], a[192:195], a[228:231], v[68:71]\n"
"ds_read_b64_tr_b16 a[212:213], v11 offset:12288\n"
"ds_read_b64_tr_b16 a[214:215], v11 offset:12544\n"
"v_mfma_f32_16x16x32_bf16 v[92:95], a[196:199], a[228:231], v[92:95]\n"
"v_mfma_f32_16x16x32_bf16 v[116:119], a[200:203], a[228:231], v[116:119]\n"
"ds_read_b64_tr_b16 a[216:217], v10 offset:14336\n"
"ds_read_b64_tr_b16 a[218:219], v10 offset:14592\n"
"v_mfma_f32_16x16x32_bf16 v[140:143], a[204:207], a[228:231], v[140:143]\n"
"ds_read_b128 a[228:231], v17 offset:16640\n"
"v_mfma_f32_16x16x32_bf16 v[72:75], a[192:195], a[232:235], v[72:75]\n"
"ds_read_b64_tr_b16 a[220:221], v11 offset:14336\n"
"ds_read_b64_tr_b16 a[222:223], v11 offset:14592\n"
"v_mfma_f32_16x16x32_bf16 v[96:99], a[196:199], a[232:235], v[96:99]\n"
"v_mfma_f32_16x16x32_bf16 v[120:123], a[200:203], a[232:235], v[120:123]\n"
"s_add_u32 s50, s62, 1146880\n"
"s_add_u32 m0, s49, 8192\n"
"v_mfma_f32_16x16x32_bf16 v[144:147], a[204:207], a[232:235], v[144:147]\n"
"buffer_load_dwordx4 v22, s[24:27], s50 offen lds\n"
"buffer_load_dwordx4 v23, s[24:27], s50 offen offset:1024 lds\n"
"ds_read_b128 a[232:235], v17 offset:33024\n"
"s_waitcnt lgkmcnt(11)\n"
"v_mfma_f32_16x16x32_bf16 v[76:79], a[192:195], a[236:239], v[76:79]\n"
"s_add_u32 m0, s49, 10240\n"
"s_nop 0\n"
"buffer_load_dwordx4 v22, s[84:87], s50 offen lds\n"
"v_mfma_f32_16x16x32_bf16 v[100:103], a[196:199], a[236:239], v[100:103]\n"
"buffer_load_dwordx4 v23, s[84:87], s50 offen offset:1024 lds\n"
"v_mfma_f32_16x16x32_bf16 v[124:127], a[200:203], a[236:239], v[124:127]\n"
"v_mfma_f32_16x16x32_bf16 v[148:151], a[204:207], a[236:239], v[148:151]\n"
"ds_read_b128 a[236:239], v17 offset:49408\n"
"v_mfma_f32_16x16x32_bf16 v[80:83], a[192:195], a[240:243], v[80:83]\n"
"v_mfma_f32_16x16x32_bf16 v[104:107], a[196:199], a[240:243], v[104:107]\n"
"v_mfma_f32_16x16x32_bf16 v[128:131], a[200:203], a[240:243], v[128:131]\n"
"v_mfma_f32_16x16x32_bf16 v[152:155], a[204:207], a[240:243], v[152:155]\n"
"ds_read_b128 a[240:243], v21 offset:256\n"
"v_mfma_f32_16x16x32_bf16 v[84:87], a[192:195], a[244:247], v[84:87]\n"
"v_mfma_f32_16x16x32_bf16 v[108:111], a[196:199], a[244:247], v[108:111]\n"
"v_mfma_f32_16x16x32_bf16 v[132:135], a[200:203], a[244:247], v[132:135]\n"
"v_mfma_f32_16x16x32_bf16 v[156:159], a[204:207], a[244:247], v[156:159]\n"
"ds_read_b128 a[244:247], v21 offset:16640\n"
"s_waitcnt lgkmcnt(3)\n"
"v_mfma_f32_16x16x32_bf16 v[64:67], a[208:211], a[224:227], v[64:67]\n"
"v_mfma_f32_16x16x32_bf16 v[88:91], a[212:215], a[224:227], v[88:91]\n"
"s_waitcnt vmcnt(8)\n"
"v_mfma_f32_16x16x32_bf16 v[112:115], a[216:219], a[224:227], v[112:115]\n"
"ds_read_b64_tr_b16 a[192:193], v10 offset:0\n"
"ds_read_b64_tr_b16 a[194:195], v10 offset:256\n"
"v_mfma_f32_16x16x32_bf16 v[136:139], a[220:223], a[224:227], v[136:139]\n"
"ds_read_b128 a[224:227], v14 offset:512\n"
"v_mfma_f32_16x16x32_bf16 v[68:71], a[208:211], a[228:231], v[68:71]\n"
"ds_read_b64_tr_b16 a[196:197], v11 offset:0\n"
"ds_read_b64_tr_b16 a[198:199], v11 offset:256\n"
"v_mfma_f32_16x16x32_bf16 v[92:95], a[212:215], a[228:231], v[92:95]\n"
"v_mfma_f32_16x16x32_bf16 v[116:119], a[216:219], a[228:231], v[116:119]\n"
"ds_read_b64_tr_b16 a[200:201], v10 offset:2048\n"
"ds_read_b64_tr_b16 a[202:203], v10 offset:2304\n"
"v_mfma_f32_16x16x32_bf16 v[140:143], a[220:223], a[228:231], v[140:143]\n"
"ds_read_b128 a[228:231], v14 offset:16896\n"
"v_mfma_f32_16x16x32_bf16 v[72:75], a[208:211], a[232:235], v[72:75]\n"
"ds_read_b64_tr_b16 a[204:205], v11 offset:2048\n"
"ds_read_b64_tr_b16 a[206:207], v11 offset:2304\n"
"v_mfma_f32_16x16x32_bf16 v[96:99], a[212:215], a[232:235], v[96:99]\n"
"v_mfma_f32_16x16x32_bf16 v[120:123], a[216:219], a[232:235], v[120:123]\n"
"s_add_u32 s50, s62, 1261568\n"
"s_add_u32 m0, s49, 12288\n"
"v_mfma_f32_16x16x32_bf16 v[144:147], a[220:223], a[232:235], v[144:147]\n"
"buffer_load_dwordx4 v22, s[24:27], s50 offen lds\n"
"buffer_load_dwordx4 v23, s[24:27], s50 offen offset:1024 lds\n"
"ds_read_b128 a[232:235], v14 offset:33280\n"
"s_waitcnt lgkmcnt(11)\n"
"v_mfma_f32_16x16x32_bf16 v[76:79], a[208:211], a[236:239], v[76:79]\n"
"s_add_u32 m0, s49, 14336\n"
"s_nop 0\n"
"buffer_load_dwordx4 v22, s[84:87], s50 offen lds\n"
"v_mfma_f32_16x16x32_bf16 v[100:103], a[212:215], a[236:239], v[100:103]\n"
"buffer_load_dwordx4 v23, s[84:87], s50 offen offset:1024 lds\n"
"v_mfma_f32_16x16x32_bf16 v[124:127], a[216:219], a[236:239], v[124:127]\n"
"v_mfma_f32_16x16x32_bf16 v[148:151], a[220:223], a[236:239], v[148:151]\n"
"ds_read_b128 a[236:239], v14 offset:49664\n"
"v_mfma_f32_16x16x32_bf16 v[80:83], a[208:211], a[240:243], v[80:83]\n"
"v_mfma_f32_16x16x32_bf16 v[104:107], a[212:215], a[240:243], v[104:107]\n"
"v_mfma_f32_16x16x32_bf16 v[128:131], a[216:219], a[240:243], v[128:131]\n"
"v_mfma_f32_16x16x32_bf16 v[152:155], a[220:223], a[240:243], v[152:155]\n"
"ds_read_b128 a[240:243], v18 offset:512\n"
"v_mfma_f32_16x16x32_bf16 v[84:87], a[208:211], a[244:247], v[84:87]\n"
"v_mfma_f32_16x16x32_bf16 v[108:111], a[212:215], a[244:247], v[108:111]\n"
"v_mfma_f32_16x16x32_bf16 v[132:135], a[216:219], a[244:247], v[132:135]\n"
"v_mfma_f32_16x16x32_bf16 v[156:159], a[220:223], a[244:247], v[156:159]\n"
"ds_read_b128 a[244:247], v18 offset:16896\n"
"s_waitcnt lgkmcnt(3)\n"
"v_mfma_f32_16x16x32_bf16 v[64:67], a[192:195], a[224:227], v[64:67]\n"
"v_mfma_f32_16x16x32_bf16 v[88:91], a[196:199], a[224:227], v[88:91]\n"
"s_waitcnt vmcnt(8)\n"
"v_mfma_f32_16x16x32_bf16 v[112:115], a[200:203], a[224:227], v[112:115]\n"
"ds_read_b64_tr_b16 a[208:209], v10 offset:4096\n"
"ds_read_b64_tr_b16 a[210:211], v10 offset:4352\n"
"v_mfma_f32_16x16x32_bf16 v[136:139], a[204:207], a[224:227], v[136:139]\n"
"ds_read_b128 a[224:227], v15 offset:512\n"
"v_mfma_f32_16x16x32_bf16 v[68:71], a[192:195], a[228:231], v[68:71]\n"
"ds_read_b64_tr_b16 a[212:213], v11 offset:4096\n"
"ds_read_b64_tr_b16 a[214:215], v11 offset:4352\n"
"v_mfma_f32_16x16x32_bf16 v[92:95], a[196:199], a[228:231], v[92:95]\n"
"v_mfma_f32_16x16x32_bf16 v[116:119], a[200:203], a[228:231], v[116:119]\n"
"ds_read_b64_tr_b16 a[216:217], v10 offset:6144\n"
"ds_read_b64_tr_b16 a[218:219], v10 offset:6400\n"
"v_mfma_f32_16x16x32_bf16 v[140:143], a[204:207], a[228:231], v[140:143]\n"
"ds_read_b128 a[228:231], v15 offset:16896\n"
"v_mfma_f32_16x16x32_bf16 v[72:75], a[192:195], a[232:235], v[72:75]\n"
"ds_read_b64_tr_b16 a[220:221], v11 offset:6144\n"
"ds_read_b64_tr_b16 a[222:223], v11 offset:6400\n"
"v_mfma_f32_16x16x32_bf16 v[96:99], a[196:199], a[232:235], v[96:99]\n"
"v_mfma_f32_16x16x32_bf16 v[120:123], a[200:203], a[232:235], v[120:123]\n"
"s_add_u32 s50, s62, 1376256\n"
"s_add_u32 m0, s49, 0\n"
"v_mfma_f32_16x16x32_bf16 v[144:147], a[204:207], a[232:235], v[144:147]\n"
"buffer_load_dwordx4 v22, s[24:27], s50 offen lds\n"
"buffer_load_dwordx4 v23, s[24:27], s50 offen offset:1024 lds\n"
"ds_read_b128 a[232:235], v15 offset:33280\n"
"s_waitcnt lgkmcnt(11)\n"
"v_mfma_f32_16x16x32_bf16 v[76:79], a[192:195], a[236:239], v[76:79]\n"
"s_add_u32 m0, s49, 2048\n"
"s_nop 0\n"
"buffer_load_dwordx4 v22, s[84:87], s50 offen lds\n"
"v_mfma_f32_16x16x32_bf16 v[100:103], a[196:199], a[236:239], v[100:103]\n"
"buffer_load_dwordx4 v23, s[84:87], s50 offen offset:1024 lds\n"
"v_mfma_f32_16x16x32_bf16 v[124:127], a[200:203], a[236:239], v[124:127]\n"
"v_mfma_f32_16x16x32_bf16 v[148:151], a[204:207], a[236:239], v[148:151]\n"
"ds_read_b128 a[236:239], v15 offset:49664\n"
"v_mfma_f32_16x16x32_bf16 v[80:83], a[192:195], a[240:243], v[80:83]\n"
"v_mfma_f32_16x16x32_bf16 v[104:107], a[196:199], a[240:243], v[104:107]\n"
"v_mfma_f32_16x16x32_bf16 v[128:131], a[200:203], a[240:243], v[128:131]\n"
"v_mfma_f32_16x16x32_bf16 v[152:155], a[204:207], a[240:243], v[152:155]\n"
"ds_read_b128 a[240:243], v19 offset:512\n"
"v_mfma_f32_16x16x32_bf16 v[84:87], a[192:195], a[244:247], v[84:87]\n"
"v_mfma_f32_16x16x32_bf16 v[108:111], a[196:199], a[244:247], v[108:111]\n"
"v_mfma_f32_16x16x32_bf16 v[132:135], a[200:203], a[244:247], v[132:135]\n"
"v_mfma_f32_16x16x32_bf16 v[156:159], a[204:207], a[244:247], v[156:159]\n"
"ds_read_b128 a[244:247], v19 offset:16896\n"
"s_waitcnt lgkmcnt(3)\n"
"v_mfma_f32_16x16x32_bf16 v[64:67], a[208:211], a[224:227], v[64:67]\n"
"v_mfma_f32_16x16x32_bf16 v[88:91], a[212:215], a[224:227], v[88:91]\n"
"s_waitcnt vmcnt(8)\n"
"v_mfma_f32_16x16x32_bf16 v[112:115], a[216:219], a[224:227], v[112:115]\n"
"ds_read_b64_tr_b16 a[192:193], v10 offset:8192\n"
"ds_read_b64_tr_b16 a[194:195], v10 offset:8448\n"
"v_mfma_f32_16x16x32_bf16 v[136:139], a[220:223], a[224:227], v[136:139]\n"
"ds_read_b128 a[224:227], v16 offset:512\n"
"v_mfma_f32_16x16x32_bf16 v[68:71], a[208:211], a[228:231], v[68:71]\n"
"ds_read_b64_tr_b16 a[196:197], v11 offset:8192\n"
"ds_read_b64_tr_b16 a[198:199], v11 offset:8448\n"
"v_mfma_f32_16x16x32_bf16 v[92:95], a[212:215], a[228:231], v[92:95]\n"
"v_mfma_f32_16x16x32_bf16 v[116:119], a[216:219], a[228:231], v[116:119]\n"
"ds_read_b64_tr_b16 a[200:201], v10 offset:10240\n"
"ds_read_b64_tr_b16 a[202:203], v10 offset:10496\n"
"v_mfma_f32_16x16x32_bf16 v[140:143], a[220:223], a[228:231], v[140:143]\n"
"ds_read_b128 a[228:231], v16 offset:16896\n"
"v_mfma_f32_16x16x32_bf16 v[72:75], a[208:211], a[232:235], v[72:75]\n"
"ds_read_b64_tr_b16 a[204:205], v11 offset:10240\n"
"ds_read_b64_tr_b16 a[206:207], v11 offset:10496\n"
"v_mfma_f32_16x16x32_bf16 v[96:99], a[212:215], a[232:235], v[96:99]\n"
"v_mfma_f32_16x16x32_bf16 v[120:123], a[216:219], a[232:235], v[120:123]\n"
"s_add_u32 s50, s62, 1490944\n"
"s_add_u32 m0, s49, 4096\n"
"v_mfma_f32_16x16x32_bf16 v[144:147], a[220:223], a[232:235], v[144:147]\n"
"buffer_load_dwordx4 v22, s[24:27], s50 offen lds\n"
"buffer_load_dwordx4 v23, s[24:27], s50 offen offset:1024 lds\n"
"ds_read_b128 a[232:235], v16 offset:33280\n"
"s_waitcnt lgkmcnt(11)\n"
"v_mfma_f32_16x16x32_bf16 v[76:79], a[208:211], a[236:239], v[76:79]\n"
"s_add_u32 m0, s49, 6144\n"
"s_nop 0\n"
"buffer_load_dwordx4 v22, s[84:87], s50 offen lds\n"
"v_mfma_f32_16x16x32_bf16 v[100:103], a[212:215], a[236:239], v[100:103]\n"
"buffer_load_dwordx4 v23, s[84:87], s50 offen offset:1024 lds\n"
"v_mfma_f32_16x16x32_bf16 v[124:127], a[216:219], a[236:239], v[124:127]\n"
"v_mfma_f32_16x16x32_bf16 v[148:151], a[220:223], a[236:239], v[148:151]\n"
"ds_read_b128 a[236:239], v16 offset:49664\n"
"v_mfma_f32_16x16x32_bf16 v[80:83], a[208:211], a[240:243], v[80:83]\n"
"v_mfma_f32_16x16x32_bf16 v[104:107], a[212:215], a[240:243], v[104:107]\n"
"v_mfma_f32_16x16x32_bf16 v[128:131], a[216:219], a[240:243], v[128:131]\n"
"v_mfma_f32_16x16x32_bf16 v[152:155], a[220:223], a[240:243], v[152:155]\n"
"ds_read_b128 a[240:243], v20 offset:512\n"
"v_mfma_f32_16x16x32_bf16 v[84:87], a[208:211], a[244:247], v[84:87]\n"
"v_mfma_f32_16x16x32_bf16 v[108:111], a[212:215], a[244:247], v[108:111]\n"
"v_mfma_f32_16x16x32_bf16 v[132:135], a[216:219], a[244:247], v[132:135]\n"
"v_mfma_f32_16x16x32_bf16 v[156:159], a[220:223], a[244:247], v[156:159]\n"
"ds_read_b128 a[244:247], v20 offset:16896\n"
"s_waitcnt lgkmcnt(3)\n"
"v_mfma_f32_16x16x32_bf16 v[64:67], a[192:195], a[224:227], v[64:67]\n"
"v_mfma_f32_16x16x32_bf16 v[88:91], a[196:199], a[224:227], v[88:91]\n"
"s_waitcnt vmcnt(8)\n"
"v_mfma_f32_16x16x32_bf16 v[112:115], a[200:203], a[224:227], v[112:115]\n"
"ds_read_b64_tr_b16 a[208:209], v10 offset:12288\n"
"ds_read_b64_tr_b16 a[210:211], v10 offset:12544\n"
"v_mfma_f32_16x16x32_bf16 v[136:139], a[204:207], a[224:227], v[136:139]\n"
"ds_read_b128 a[224:227], v17 offset:512\n"
"v_mfma_f32_16x16x32_bf16 v[68:71], a[192:195], a[228:231], v[68:71]\n"
"ds_read_b64_tr_b16 a[212:213], v11 offset:12288\n"
"ds_read_b64_tr_b16 a[214:215], v11 offset:12544\n"
"v_mfma_f32_16x16x32_bf16 v[92:95], a[196:199], a[228:231], v[92:95]\n"
"v_mfma_f32_16x16x32_bf16 v[116:119], a[200:203], a[228:231], v[116:119]\n"
"ds_read_b64_tr_b16 a[216:217], v10 offset:14336\n"
"ds_read_b64_tr_b16 a[218:219], v10 offset:14592\n"
"v_mfma_f32_16x16x32_bf16 v[140:143], a[204:207], a[228:231], v[140:143]\n"
"ds_read_b128 a[228:231], v17 offset:16896\n"
"v_mfma_f32_16x16x32_bf16 v[72:75], a[192:195], a[232:235], v[72:75]\n"
"ds_read_b64_tr_b16 a[220:221], v11 offset:14336\n"
"ds_read_b64_tr_b16 a[222:223], v11 offset:14592\n"
"v_mfma_f32_16x16x32_bf16 v[96:99], a[196:199], a[232:235], v[96:99]\n"
"v_mfma_f32_16x16x32_bf16 v[120:123], a[200:203], a[232:235], v[120:123]\n"
"s_add_u32 s50, s62, 1605632\n"
"s_add_u32 m0, s49, 8192\n"
"v_mfma_f32_16x16x32_bf16 v[144:147], a[204:207], a[232:235], v[144:147]\n"
"buffer_load_dwordx4 v22, s[24:27], s50 offen lds\n"
"buffer_load_dwordx4 v23, s[24:27], s50 offen offset:1024 lds\n"
"ds_read_b128 a[232:235], v17 offset:33280\n"
"s_waitcnt lgkmcnt(11)\n"
"v_mfma_f32_16x16x32_bf16 v[76:79], a[192:195], a[236:239], v[76:79]\n"
"s_add_u32 m0, s49, 10240\n"
"s_nop 0\n"
"buffer_load_dwordx4 v22, s[84:87], s50 offen lds\n"
"v_mfma_f32_16x16x32_bf16 v[100:103], a[196:199], a[236:239], v[100:103]\n"
"buffer_load_dwordx4 v23, s[84:87], s50 offen offset:1024 lds\n"
"v_mfma_f32_16x16x32_bf16 v[124:127], a[200:203], a[236:239], v[124:127]\n"
"v_mfma_f32_16x16x32_bf16 v[148:151], a[204:207], a[236:239], v[148:151]\n"
"ds_read_b128 a[236:239], v17 offset:49664\n"
"v_mfma_f32_16x16x32_bf16 v[80:83], a[192:195], a[240:243], v[80:83]\n"
"v_mfma_f32_16x16x32_bf16 v[104:107], a[196:199], a[240:243], v[104:107]\n"
"v_mfma_f32_16x16x32_bf16 v[128:131], a[200:203], a[240:243], v[128:131]\n"
"v_mfma_f32_16x16x32_bf16 v[152:155], a[204:207], a[240:243], v[152:155]\n"
"ds_read_b128 a[240:243], v21 offset:512\n"
"v_mfma_f32_16x16x32_bf16 v[84:87], a[192:195], a[244:247], v[84:87]\n"
"v_mfma_f32_16x16x32_bf16 v[108:111], a[196:199], a[244:247], v[108:111]\n"
"v_mfma_f32_16x16x32_bf16 v[132:135], a[200:203], a[244:247], v[132:135]\n"
"v_mfma_f32_16x16x32_bf16 v[156:159], a[204:207], a[244:247], v[156:159]\n"
"ds_read_b128 a[244:247], v21 offset:16896\n"
"s_waitcnt lgkmcnt(3)\n"
"v_mfma_f32_16x16x32_bf16 v[64:67], a[208:211], a[224:227], v[64:67]\n"
"v_mfma_f32_16x16x32_bf16 v[88:91], a[212:215], a[224:227], v[88:91]\n"
"s_waitcnt vmcnt(8)\n"
"v_mfma_f32_16x16x32_bf16 v[112:115], a[216:219], a[224:227], v[112:115]\n"
"ds_read_b64_tr_b16 a[192:193], v10 offset:0\n"
"ds_read_b64_tr_b16 a[194:195], v10 offset:256\n"
"v_mfma_f32_16x16x32_bf16 v[136:139], a[220:223], a[224:227], v[136:139]\n"
"ds_read_b128 a[224:227], v14 offset:768\n"
"v_mfma_f32_16x16x32_bf16 v[68:71], a[208:211], a[228:231], v[68:71]\n"
"ds_read_b64_tr_b16 a[196:197], v11 offset:0\n"
"ds_read_b64_tr_b16 a[198:199], v11 offset:256\n"
"v_mfma_f32_16x16x32_bf16 v[92:95], a[212:215], a[228:231], v[92:95]\n"
"v_mfma_f32_16x16x32_bf16 v[116:119], a[216:219], a[228:231], v[116:119]\n"
"ds_read_b64_tr_b16 a[200:201], v10 offset:2048\n"
"ds_read_b64_tr_b16 a[202:203], v10 offset:2304\n"
"v_mfma_f32_16x16x32_bf16 v[140:143], a[220:223], a[228:231], v[140:143]\n"
"ds_read_b128 a[228:231], v14 offset:17152\n"
"v_mfma_f32_16x16x32_bf16 v[72:75], a[208:211], a[232:235], v[72:75]\n"
"ds_read_b64_tr_b16 a[204:205], v11 offset:2048\n"
"ds_read_b64_tr_b16 a[206:207], v11 offset:2304\n"
"v_mfma_f32_16x16x32_bf16 v[96:99], a[212:215], a[232:235], v[96:99]\n"
"v_mfma_f32_16x16x32_bf16 v[120:123], a[216:219], a[232:235], v[120:123]\n"
"s_add_u32 s50, s62, 1720320\n"
"s_add_u32 m0, s49, 12288\n"
"v_mfma_f32_16x16x32_bf16 v[144:147], a[220:223], a[232:235], v[144:147]\n"
"buffer_load_dwordx4 v22, s[24:27], s50 offen lds\n"
"buffer_load_dwordx4 v23, s[24:27], s50 offen offset:1024 lds\n"
"ds_read_b128 a[232:235], v14 offset:33536\n"
"s_waitcnt lgkmcnt(11)\n"
"v_mfma_f32_16x16x32_bf16 v[76:79], a[208:211], a[236:239], v[76:79]\n"
"s_add_u32 m0, s49, 14336\n"
"s_nop 0\n"
"buffer_load_dwordx4 v22, s[84:87], s50 offen lds\n"
"v_mfma_f32_16x16x32_bf16 v[100:103], a[212:215], a[236:239], v[100:103]\n"
"buffer_load_dwordx4 v23, s[84:87], s50 offen offset:1024 lds\n"
"v_mfma_f32_16x16x32_bf16 v[124:127], a[216:219], a[236:239], v[124:127]\n"
"v_mfma_f32_16x16x32_bf16 v[148:151], a[220:223], a[236:239], v[148:151]\n"
"ds_read_b128 a[236:239], v14 offset:49920\n"
"v_mfma_f32_16x16x32_bf16 v[80:83], a[208:211], a[240:243], v[80:83]\n"
"v_mfma_f32_16x16x32_bf16 v[104:107], a[212:215], a[240:243], v[104:107]\n"
"v_mfma_f32_16x16x32_bf16 v[128:131], a[216:219], a[240:243], v[128:131]\n"
"v_mfma_f32_16x16x32_bf16 v[152:155], a[220:223], a[240:243], v[152:155]\n"
"ds_read_b128 a[240:243], v18 offset:768\n"
"v_mfma_f32_16x16x32_bf16 v[84:87], a[208:211], a[244:247], v[84:87]\n"
"v_mfma_f32_16x16x32_bf16 v[108:111], a[212:215], a[244:247], v[108:111]\n"
"v_mfma_f32_16x16x32_bf16 v[132:135], a[216:219], a[244:247], v[132:135]\n"
"v_mfma_f32_16x16x32_bf16 v[156:159], a[220:223], a[244:247], v[156:159]\n"
"ds_read_b128 a[244:247], v18 offset:17152\n"
"s_waitcnt lgkmcnt(3)\n"
"v_mfma_f32_16x16x32_bf16 v[64:67], a[192:195], a[224:227], v[64:67]\n"
"v_mfma_f32_16x16x32_bf16 v[88:91], a[196:199], a[224:227], v[88:91]\n"
"s_waitcnt vmcnt(8)\n"
"v_mfma_f32_16x16x32_bf16 v[112:115], a[200:203], a[224:227], v[112:115]\n"
"ds_read_b64_tr_b16 a[208:209], v10 offset:4096\n"
"ds_read_b64_tr_b16 a[210:211], v10 offset:4352\n"
"v_mfma_f32_16x16x32_bf16 v[136:139], a[204:207], a[224:227], v[136:139]\n"
"ds_read_b128 a[224:227], v15 offset:768\n"
"v_mfma_f32_16x16x32_bf16 v[68:71], a[192:195], a[228:231], v[68:71]\n"
"ds_read_b64_tr_b16 a[212:213], v11 offset:4096\n"
"ds_read_b64_tr_b16 a[214:215], v11 offset:4352\n"
"v_mfma_f32_16x16x32_bf16 v[92:95], a[196:199], a[228:231], v[92:95]\n"
"v_mfma_f32_16x16x32_bf16 v[116:119], a[200:203], a[228:231], v[116:119]\n"
"ds_read_b64_tr_b16 a[216:217], v10 offset:6144\n"
"ds_read_b64_tr_b16 a[218:219], v10 offset:6400\n"
"v_mfma_f32_16x16x32_bf16 v[140:143], a[204:207], a[228:231], v[140:143]\n"
"ds_read_b128 a[228:231], v15 offset:17152\n"
"v_mfma_f32_16x16x32_bf16 v[72:75], a[192:195], a[232:235], v[72:75]\n"
"ds_read_b64_tr_b16 a[220:221], v11 offset:6144\n"
"ds_read_b64_tr_b16 a[222:223], v11 offset:6400\n"
"v_mfma_f32_16x16x32_bf16 v[96:99], a[196:199], a[232:235], v[96:99]\n"
"v_mfma_f32_16x16x32_bf16 v[120:123], a[200:203], a[232:235], v[120:123]\n"
"s_add_u32 s50, s62, 256\n"
"s_add_u32 m0, s49, 0\n"
"v_mfma_f32_16x16x32_bf16 v[144:147], a[204:207], a[232:235], v[144:147]\n"
"buffer_load_dwordx4 v22, s[24:27], s50 offen lds\n"
"buffer_load_dwordx4 v23, s[24:27], s50 offen offset:1024 lds\n"
"ds_read_b128 a[232:235], v15 offset:33536\n"
"s_waitcnt lgkmcnt(11)\n"
"v_mfma_f32_16x16x32_bf16 v[76:79], a[192:195], a[236:239], v[76:79]\n"
"s_add_u32 m0, s49, 2048\n"
"s_nop 0\n"
"buffer_load_dwordx4 v22, s[84:87], s50 offen lds\n"
"v_mfma_f32_16x16x32_bf16 v[100:103], a[196:199], a[236:239], v[100:103]\n"
"buffer_load_dwordx4 v23, s[84:87], s50 offen offset:1024 lds\n"
"v_mfma_f32_16x16x32_bf16 v[124:127], a[200:203], a[236:239], v[124:127]\n"
"v_mfma_f32_16x16x32_bf16 v[148:151], a[204:207], a[236:239], v[148:151]\n"
"ds_read_b128 a[236:239], v15 offset:49920\n"
"v_mfma_f32_16x16x32_bf16 v[80:83], a[192:195], a[240:243], v[80:83]\n"
"v_mfma_f32_16x16x32_bf16 v[104:107], a[196:199], a[240:243], v[104:107]\n"
"v_mfma_f32_16x16x32_bf16 v[128:131], a[200:203], a[240:243], v[128:131]\n"
"v_mfma_f32_16x16x32_bf16 v[152:155], a[204:207], a[240:243], v[152:155]\n"
"ds_read_b128 a[240:243], v19 offset:768\n"
"v_mfma_f32_16x16x32_bf16 v[84:87], a[192:195], a[244:247], v[84:87]\n"
"v_mfma_f32_16x16x32_bf16 v[108:111], a[196:199], a[244:247], v[108:111]\n"
"v_mfma_f32_16x16x32_bf16 v[132:135], a[200:203], a[244:247], v[132:135]\n"
"v_mfma_f32_16x16x32_bf16 v[156:159], a[204:207], a[244:247], v[156:159]\n"
"ds_read_b128 a[244:247], v19 offset:17152\n"
"s_waitcnt lgkmcnt(3)\n"
"v_mfma_f32_16x16x32_bf16 v[64:67], a[208:211], a[224:227], v[64:67]\n"
"v_mfma_f32_16x16x32_bf16 v[88:91], a[212:215], a[224:227], v[88:91]\n"
"s_waitcnt vmcnt(8)\n"
"v_mfma_f32_16x16x32_bf16 v[112:115], a[216:219], a[224:227], v[112:115]\n"
"ds_read_b64_tr_b16 a[192:193], v10 offset:8192\n"
"ds_read_b64_tr_b16 a[194:195], v10 offset:8448\n"
"v_mfma_f32_16x16x32_bf16 v[136:139], a[220:223], a[224:227], v[136:139]\n"
"ds_read_b128 a[224:227], v16 offset:768\n"
"v_mfma_f32_16x16x32_bf16 v[68:71], a[208:211], a[228:231], v[68:71]\n"
"ds_read_b64_tr_b16 a[196:197], v11 offset:8192\n"
"ds_read_b64_tr_b16 a[198:199], v11 offset:8448\n"
"v_mfma_f32_16x16x32_bf16 v[92:95], a[212:215], a[228:231], v[92:95]\n"
"v_mfma_f32_16x16x32_bf16 v[116:119], a[216:219], a[228:231], v[116:119]\n"
"ds_read_b64_tr_b16 a[200:201], v10 offset:10240\n"
"ds_read_b64_tr_b16 a[202:203], v10 offset:10496\n"
"v_mfma_f32_16x16x32_bf16 v[140:143], a[220:223], a[228:231], v[140:143]\n"
"ds_read_b128 a[228:231], v16 offset:17152\n"
"v_mfma_f32_16x16x32_bf16 v[72:75], a[208:211], a[232:235], v[72:75]\n"
"ds_read_b64_tr_b16 a[204:205], v11 offset:10240\n"
"ds_read_b64_tr_b16 a[206:207], v11 offset:10496\n"
"v_mfma_f32_16x16x32_bf16 v[96:99], a[212:215], a[232:235], v[96:99]\n"
"v_mfma_f32_16x16x32_bf16 v[120:123], a[216:219], a[232:235], v[120:123]\n"
"s_add_u32 s50, s62, 114944\n"
"s_add_u32 m0, s49, 4096\n"
"v_mfma_f32_16x16x32_bf16 v[144:147], a[220:223], a[232:235], v[144:147]\n"
"buffer_load_dwordx4 v22, s[24:27], s50 offen lds\n"
"buffer_load_dwordx4 v23, s[24:27], s50 offen offset:1024 lds\n"
"ds_read_b128 a[232:235], v16 offset:33536\n"
"s_waitcnt lgkmcnt(11)\n"
"v_mfma_f32_16x16x32_bf16 v[76:79], a[208:211], a[236:239], v[76:79]\n"
"s_add_u32 m0, s49, 6144\n"
"s_nop 0\n"
"buffer_load_dwordx4 v22, s[84:87], s50 offen lds\n"
"v_mfma_f32_16x16x32_bf16 v[100:103], a[212:215], a[236:239], v[100:103]\n"
"buffer_load_dwordx4 v23, s[84:87], s50 offen offset:1024 lds\n"
"v_mfma_f32_16x16x32_bf16 v[124:127], a[216:219], a[236:239], v[124:127]\n"
"v_mfma_f32_16x16x32_bf16 v[148:151], a[220:223], a[236:239], v[148:151]\n"
"ds_read_b128 a[236:239], v16 offset:49920\n"
"v_mfma_f32_16x16x32_bf16 v[80:83], a[208:211], a[240:243], v[80:83]\n"
"v_mfma_f32_16x16x32_bf16 v[104:107], a[212:215], a[240:243], v[104:107]\n"
"v_mfma_f32_16x16x32_bf16 v[128:131], a[216:219], a[240:243], v[128:131]\n"
"v_mfma_f32_16x16x32_bf16 v[152:155], a[220:223], a[240:243], v[152:155]\n"
"ds_read_b128 a[240:243], v20 offset:768\n"
"v_mfma_f32_16x16x32_bf16 v[84:87], a[208:211], a[244:247], v[84:87]\n"
"v_mfma_f32_16x16x32_bf16 v[108:111], a[212:215], a[244:247], v[108:111]\n"
"v_mfma_f32_16x16x32_bf16 v[132:135], a[216:219], a[244:247], v[132:135]\n"
"v_mfma_f32_16x16x32_bf16 v[156:159], a[220:223], a[244:247], v[156:159]\n"
"ds_read_b128 a[244:247], v20 offset:17152\n"
"s_waitcnt lgkmcnt(3)\n"
"v_mfma_f32_16x16x32_bf16 v[64:67], a[192:195], a[224:227], v[64:67]\n"
"v_mfma_f32_16x16x32_bf16 v[88:91], a[196:199], a[224:227], v[88:91]\n"
"s_waitcnt vmcnt(8)\n"
"v_mfma_f32_16x16x32_bf16 v[112:115], a[200:203], a[224:227], v[112:115]\n"
"ds_read_b64_tr_b16 a[208:209], v10 offset:12288\n"
"ds_read_b64_tr_b16 a[210:211], v10 offset:12544\n"
"v_mfma_f32_16x16x32_bf16 v[136:139], a[204:207], a[224:227], v[136:139]\n"
"ds_read_b128 a[224:227], v17 offset:768\n"
"v_mfma_f32_16x16x32_bf16 v[68:71], a[192:195], a[228:231], v[68:71]\n"
"ds_read_b64_tr_b16 a[212:213], v11 offset:12288\n"
"ds_read_b64_tr_b16 a[214:215], v11 offset:12544\n"
"v_mfma_f32_16x16x32_bf16 v[92:95], a[196:199], a[228:231], v[92:95]\n"
"v_mfma_f32_16x16x32_bf16 v[116:119], a[200:203], a[228:231], v[116:119]\n"
"ds_read_b64_tr_b16 a[216:217], v10 offset:14336\n"
"ds_read_b64_tr_b16 a[218:219], v10 offset:14592\n"
"v_mfma_f32_16x16x32_bf16 v[140:143], a[204:207], a[228:231], v[140:143]\n"
"ds_read_b128 a[228:231], v17 offset:17152\n"
"v_mfma_f32_16x16x32_bf16 v[72:75], a[192:195], a[232:235], v[72:75]\n"
"ds_read_b64_tr_b16 a[220:221], v11 offset:14336\n"
"ds_read_b64_tr_b16 a[222:223], v11 offset:14592\n"
"v_mfma_f32_16x16x32_bf16 v[96:99], a[196:199], a[232:235], v[96:99]\n"
"v_mfma_f32_16x16x32_bf16 v[120:123], a[200:203], a[232:235], v[120:123]\n"
"s_add_u32 s50, s62, 229632\n"
"s_add_u32 m0, s49, 8192\n"
"v_mfma_f32_16x16x32_bf16 v[144:147], a[204:207], a[232:235], v[144:147]\n"
"buffer_load_dwordx4 v22, s[24:27], s50 offen lds\n"
"buffer_load_dwordx4 v23, s[24:27], s50 offen offset:1024 lds\n"
"ds_read_b128 a[232:235], v17 offset:33536\n"
"s_waitcnt lgkmcnt(11)\n"
"v_mfma_f32_16x16x32_bf16 v[76:79], a[192:195], a[236:239], v[76:79]\n"
"s_add_u32 m0, s49, 10240\n"
"s_nop 0\n"
"buffer_load_dwordx4 v22, s[84:87], s50 offen lds\n"
"v_mfma_f32_16x16x32_bf16 v[100:103], a[196:199], a[236:239], v[100:103]\n"
"buffer_load_dwordx4 v23, s[84:87], s50 offen offset:1024 lds\n"
"v_mfma_f32_16x16x32_bf16 v[124:127], a[200:203], a[236:239], v[124:127]\n"
"v_mfma_f32_16x16x32_bf16 v[148:151], a[204:207], a[236:239], v[148:151]\n"
"ds_read_b128 a[236:239], v17 offset:49920\n"
"v_mfma_f32_16x16x32_bf16 v[80:83], a[192:195], a[240:243], v[80:83]\n"
"v_mfma_f32_16x16x32_bf16 v[104:107], a[196:199], a[240:243], v[104:107]\n"
"v_mfma_f32_16x16x32_bf16 v[128:131], a[200:203], a[240:243], v[128:131]\n"
"v_mfma_f32_16x16x32_bf16 v[152:155], a[204:207], a[240:243], v[152:155]\n"
"ds_read_b128 a[240:243], v21 offset:768\n"
"v_mfma_f32_16x16x32_bf16 v[84:87], a[192:195], a[244:247], v[84:87]\n"
"v_mfma_f32_16x16x32_bf16 v[108:111], a[196:199], a[244:247], v[108:111]\n"
"v_mfma_f32_16x16x32_bf16 v[132:135], a[200:203], a[244:247], v[132:135]\n"
"v_mfma_f32_16x16x32_bf16 v[156:159], a[204:207], a[244:247], v[156:159]\n"
"ds_read_b128 a[244:247], v21 offset:17152\n"
"s_waitcnt lgkmcnt(3)\n"
"v_mfma_f32_16x16x32_bf16 v[64:67], a[208:211], a[224:227], v[64:67]\n"
"v_mfma_f32_16x16x32_bf16 v[88:91], a[212:215], a[224:227], v[88:91]\n"
"s_waitcnt vmcnt(8)\n"
"v_mfma_f32_16x16x32_bf16 v[112:115], a[216:219], a[224:227], v[112:115]\n"
"ds_read_b64_tr_b16 a[192:193], v10 offset:0\n"
"ds_read_b64_tr_b16 a[194:195], v10 offset:256\n"
"v_mfma_f32_16x16x32_bf16 v[136:139], a[220:223], a[224:227], v[136:139]\n"
"ds_read_b128 a[224:227], v14 offset:0\n"
"v_mfma_f32_16x16x32_bf16 v[68:71], a[208:211], a[228:231], v[68:71]\n"
"ds_read_b64_tr_b16 a[196:197], v11 offset:0\n"
"ds_read_b64_tr_b16 a[198:199], v11 offset:256\n"
"v_mfma_f32_16x16x32_bf16 v[92:95], a[212:215], a[228:231], v[92:95]\n"
"v_mfma_f32_16x16x32_bf16 v[116:119], a[216:219], a[228:231], v[116:119]\n"
"ds_read_b64_tr_b16 a[200:201], v10 offset:2048\n"
"ds_read_b64_tr_b16 a[202:203], v10 offset:2304\n"
"v_mfma_f32_16x16x32_bf16 v[140:143], a[220:223], a[228:231], v[140:143]\n"
"ds_read_b128 a[228:231], v14 offset:16384\n"
"v_mfma_f32_16x16x32_bf16 v[72:75], a[208:211], a[232:235], v[72:75]\n"
"ds_read_b64_tr_b16 a[204:205], v11 offset:2048\n"
"ds_read_b64_tr_b16 a[206:207], v11 offset:2304\n"
"v_mfma_f32_16x16x32_bf16 v[96:99], a[212:215], a[232:235], v[96:99]\n"
"v_mfma_f32_16x16x32_bf16 v[120:123], a[216:219], a[232:235], v[120:123]\n"
"s_add_u32 s50, s62, 344320\n"
"s_add_u32 m0, s49, 12288\n"
"v_mfma_f32_16x16x32_bf16 v[144:147], a[220:223], a[232:235], v[144:147]\n"
"buffer_load_dwordx4 v22, s[24:27], s50 offen lds\n"
"buffer_load_dwordx4 v23, s[24:27], s50 offen offset:1024 lds\n"
"ds_read_b128 a[232:235], v14 offset:32768\n"
"s_waitcnt lgkmcnt(11)\n"
"v_mfma_f32_16x16x32_bf16 v[76:79], a[208:211], a[236:239], v[76:79]\n"
"s_add_u32 m0, s49, 14336\n"
"s_nop 0\n"
"buffer_load_dwordx4 v22, s[84:87], s50 offen lds\n"
"v_mfma_f32_16x16x32_bf16 v[100:103], a[212:215], a[236:239], v[100:103]\n"
"buffer_load_dwordx4 v23, s[84:87], s50 offen offset:1024 lds\n"
"v_mfma_f32_16x16x32_bf16 v[124:127], a[216:219], a[236:239], v[124:127]\n"
"v_mfma_f32_16x16x32_bf16 v[148:151], a[220:223], a[236:239], v[148:151]\n"
"ds_read_b128 a[236:239], v14 offset:49152\n"
"v_mfma_f32_16x16x32_bf16 v[80:83], a[208:211], a[240:243], v[80:83]\n"
"v_mfma_f32_16x16x32_bf16 v[104:107], a[212:215], a[240:243], v[104:107]\n"
"v_mfma_f32_16x16x32_bf16 v[128:131], a[216:219], a[240:243], v[128:131]\n"
"v_mfma_f32_16x16x32_bf16 v[152:155], a[220:223], a[240:243], v[152:155]\n"
"ds_read_b128 a[240:243], v18 offset:0\n"
"v_mfma_f32_16x16x32_bf16 v[84:87], a[208:211], a[244:247], v[84:87]\n"
"v_mfma_f32_16x16x32_bf16 v[108:111], a[212:215], a[244:247], v[108:111]\n"
"v_mfma_f32_16x16x32_bf16 v[132:135], a[216:219], a[244:247], v[132:135]\n"
"v_mfma_f32_16x16x32_bf16 v[156:159], a[220:223], a[244:247], v[156:159]\n"
"ds_read_b128 a[244:247], v18 offset:16384\n"
"s_add_u32 s62, s62, 256\n"
"; ---- end of section: p0\n"
"s_mov_b32 s47, 0\n"
"s_waitcnt lgkmcnt(0)\n"
"LFL_%=:\n"
"s_nop 7\n"
"s_nop 7\n"
"s_add_u32 s53, s47, s43\n"
"s_cmp_eq_u32 s53, 3\n"
"s_cselect_b32 s63, 1, 0\n"
"v_mfma_f32_16x16x32_bf16 v[160:163], a[192:195], a[224:227], 0\n"
"v_accvgpr_mov_b32 a0, a48\n"
"v_accvgpr_mov_b32 a1, a49\n"
"v_mfma_f32_16x16x32_bf16 v[184:187], a[196:199], a[224:227], 0\n"
"v_accvgpr_mov_b32 a2, a50\n"
"s_waitcnt vmcnt(8)\n"
"v_mfma_f32_16x16x32_bf16 v[208:211], a[200:203], a[224:227], 0\n"
"ds_read_b64_tr_b16 a[208:209], v10 offset:4096\n"
"ds_read_b64_tr_b16 a[210:211], v10 offset:4352\n"
"v_mfma_f32_16x16x32_bf16 v[232:235], a[204:207], a[224:227], 0\n"
"ds_read_b128 a[224:227], v15 offset:0\n"
"v_accvgpr_mov_b32 a3, a51\n"
"v_accvgpr_mov_b32 a4, a52\n"
"v_mfma_f32_16x16x32_bf16 v[164:167], a[192:195], a[228:231], 0\n"
"ds_read_b64_tr_b16 a[212:213], v11 offset:4096\n"
"ds_read_b64_tr_b16 a[214:215], v11 offset:4352\n"
"v_mfma_f32_16x16x32_bf16 v[188:191], a[196:199], a[228:231], 0\n"
"v_accvgpr_mov_b32 a5, a53\n"
"v_accvgpr_mov_b32 a6, a54\n"
"v_mfma_f32_16x16x32_bf16 v[212:215], a[200:203], a[228:231], 0\n"
"ds_read_b64_tr_b16 a[216:217], v10 offset:6144\n"
"ds_read_b64_tr_b16 a[218:219], v10 offset:6400\n"
"v_mfma_f32_16x16x32_bf16 v[236:239], a[204:207], a[228:231], 0\n"
"ds_read_b128 a[228:231], v15 offset:16384\n"
"v_accvgpr_mov_b32 a7, a55\n"
"v_mfma_f32_16x16x32_bf16 v[168:171], a[192:195], a[232:235], 0\n"
"ds_read_b64_tr_b16 a[220:221], v11 offset:6144\n"
"ds_read_b64_tr_b16 a[222:223], v11 offset:6400\n"
"v_mfma_f32_16x16x32_bf16 v[192:195], a[196:199], a[232:235], 0\n"
"v_accvgpr_mov_b32 a8, a56\n"
"v_accvgpr_mov_b32 a9, a57\n"
"v_mfma_f32_16x16x32_bf16 v[216:219], a[200:203], a[232:235], 0\n"
"s_add_u32 s50, s62, 458752\n"
"s_add_u32 m0, s49, 0\n"
"v_mfma_f32_16x16x32_bf16 v[240:243], a[204:207], a[232:235], 0\n"
"buffer_load_dwordx4 v22, s[24:27], s50 offen lds\n"
"buffer_load_dwordx4 v23, s[24:27], s50 offen offset:1024 lds\n"
"ds_read_b128 a[232:235], v15 offset:32768\n"
"v_mfma_f32_16x16x32_bf16 v[172:175], a[192:195], a[236:239], 0\n"
"s_add_u32 m0, s49, 2048\n"
"s_nop 0\n"
"buffer_load_dwordx4 v22, s[84:87], s50 offen lds\n"
"v_accvgpr_mov_b32 a10, a58\n"
"v_mfma_f32_16x16x32_bf16 v[196:199], a[196:199], a[236:239], 0\n"
"buffer_load_dwordx4 v23, s[84:87], s50 offen offset:1024 lds\n"
"v_accvgpr_mov_b32 a11, a59\n"
"v_mfma_f32_16x16x32_bf16 v[220:223], a[200:203], a[236:239], 0\n"
"v_accvgpr_mov_b32 a12, a60\n"
"v_accvgpr_mov_b32 a13, a61\n"
"v_mfma_f32_16x16x32_bf16 v[244:247], a[204:207], a[236:239], 0\n"
"ds_read_b128 a[236:239], v15 offset:49152\n"
"v_accvgpr_mov_b32 a14, a62\n"
"v_mfma_f32_16x16x32_bf16 v[176:179], a[192:195], a[240:243], 0\n"
"v_accvgpr_mov_b32 a15, a63\n"
"v_accvgpr_mov_b32 a16, a64\n"
"v_mfma_f32_16x16x32_bf16 v[200:203], a[196:199], a[240:243], 0\n"
"v_accvgpr_mov_b32 a17, a65\n"
"v_accvgpr_mov_b32 a18, a66\n"
"v_mfma_f32_16x16x32_bf16 v[224:227], a[200:203], a[240:243], 0\n"
"v_accvgpr_mov_b32 a19, a67\n"
"v_accvgpr_mov_b32 a20, a68\n"
"v_mfma_f32_16x16x32_bf16 v[248:251], a[204:207], a[240:243], 0\n"
"ds_read_b128 a[240:243], v19 offset:0\n"
"v_accvgpr_mov_b32 a21, a69\n"
"v_mfma_f32_16x16x32_bf16 v[180:183], a[192:195], a[244:247], 0\n"
"v_accvgpr_mov_b32 a22, a70\n"
"v_accvgpr_mov_b32 a23, a71\n"
"v_accvgpr_mov_b32 a24, a72\n"
"v_mfma_f32_16x16x32_bf16 v[204:207], a[196:199], a[244:247], 0\n"
"v_accvgpr_mov_b32 a25, a73\n"
"v_accvgpr_mov_b32 a26, a74\n"
"v_mfma_f32_16x16x32_bf16 v[228:231], a[200:203], a[244:247], 0\n"
"v_accvgpr_mov_b32 a27, a75\n"
"v_accvgpr_mov_b32 a28, a76\n"
"v_mfma_f32_16x16x32_bf16 v[252:255], a[204:207], a[244:247], 0\n"
"ds_read_b128 a[244:247], v19 offset:16384\n"
"v_accvgpr_mov_b32 a29, a77\n"
"s_waitcnt lgkmcnt(3)\n"
"v_mfma_f32_16x16x32_bf16 v[160:163], a[208:211], a[224:227], v[160:163]\n"
"v_accvgpr_mov_b32 a30, a78\n"
"v_accvgpr_mov_b32 a31, a79\n"
"v_mfma_f32_16x16x32_bf16 v[184:187], a[212:215], a[224:227], v[184:187]\n"
"v_accvgpr_mov_b32 a32, a80\n"
"s_waitcnt vmcnt(8)\n"
"v_mfma_f32_16x16x32_bf16 v[208:211], a[216:219], a[224:227], v[208:211]\n"
"ds_read_b64_tr_b16 a[192:193], v10 offset:8192\n"
"ds_read_b64_tr_b16 a[194:195], v10 offset:8448\n"
"v_mfma_f32_16x16x32_bf16 v[232:235], a[220:223], a[224:227], v[232:235]\n"
"ds_read_b128 a[224:227], v16 offset:0\n"
"v_accvgpr_mov_b32 a33, a81\n"
"v_accvgpr_mov_b32 a34, a82\n"
"v_mfma_f32_16x16x32_bf16 v[164:167], a[208:211], a[228:231], v[164:167]\n"
"ds_read_b64_tr_b16 a[196:197], v11 offset:8192\n"
"ds_read_b64_tr_b16 a[198:199], v11 offset:8448\n"
"v_mfma_f32_16x16x32_bf16 v[188:191], a[212:215], a[228:231], v[188:191]\n"
"v_accvgpr_mov_b32 a35, a83\n"
"v_accvgpr_mov_b32 a36, a84\n"
"v_mfma_f32_16x16x32_bf16 v[212:215], a[216:219], a[228:231], v[212:215]\n"
"ds_read_b64_tr_b16 a[200:201], v10 offset:10240\n"
"ds_read_b64_tr_b16 a[202:203], v10 offset:10496\n"
"v_mfma_f32_16x16x32_bf16 v[236:239], a[220:223], a[228:231], v[236:239]\n"
"ds_read_b128 a[228:231], v16 offset:16384\n"
"v_accvgpr_mov_b32 a37, a85\n"
"v_mfma_f32_16x16x32_bf16 v[168:171], a[208:211], a[232:235], v[168:171]\n"
"ds_read_b64_tr_b16 a[204:205], v11 offset:10240\n"
"ds_read_b64_tr_b16 a[206:207], v11 offset:10496\n"
"v_mfma_f32_16x16x32_bf16 v[192:195], a[212:215], a[232:235], v[192:195]\n"
"v_accvgpr_mov_b32 a38, a86\n"
"v_accvgpr_mov_b32 a39, a87\n"
"v_mfma_f32_16x16x32_bf16 v[216:219], a[216:219], a[232:235], v[216:219]\n"
"s_add_u32 s50, s62, 573440\n"
"s_add_u32 m0, s49, 4096\n"
"v_mfma_f32_16x16x32_bf16 v[240:243], a[220:223], a[232:235], v[240:243]\n"
"buffer_load_dwordx4 v22, s[24:27], s50 offen lds\n"
"buffer_load_dwordx4 v23, s[24:27], s50 offen offset:1024 lds\n"
"ds_read_b128 a[232:235], v16 offset:32768\n"
"s_waitcnt lgkmcnt(11)\n"
"v_mfma_f32_16x16x32_bf16 v[172:175], a[208:211], a[236:239], v[172:175]\n"
"s_add_u32 m0, s49, 6144\n"
"s_nop 0\n"
"buffer_load_dwordx4 v22, s[84:87], s50 offen lds\n"
"v_accvgpr_mov_b32 a40, a88\n"
"v_mfma_f32_16x16x32_bf16 v[196:199], a[212:215], a[236:239], v[196:199]\n"
"buffer_load_dwordx4 v23, s[84:87], s50 offen offset:1024 lds\n"
"v_accvgpr_mov_b32 a41, a89\n"
"v_mfma_f32_16x16x32_bf16 v[220:223], a[216:219], a[236:239], v[220:223]\n"
"v_accvgpr_mov_b32 a42, a90\n"
"v_accvgpr_mov_b32 a43, a91\n"
"v_mfma_f32_16x16x32_bf16 v[244:247], a[220:223], a[236:239], v[244:247]\n"
"ds_read_b128 a[236:239], v16 offset:49152\n"
"v_accvgpr_mov_b32 a44, a92\n"
"v_mfma_f32_16x16x32_bf16 v[176:179], a[208:211], a[240:243], v[176:179]\n"
"v_accvgpr_mov_b32 a45, a93\n"
"v_accvgpr_mov_b32 a46, a94\n"
"v_mfma_f32_16x16x32_bf16 v[200:203], a[212:215], a[240:243], v[200:203]\n"
"v_accvgpr_mov_b32 a47, a95\n"
"v_mul_f32 v2, v64, v64\n"
"v_mfma_f32_16x16x32_bf16 v[224:227], a[216:219], a[240:243], v[224:227]\n"
"v_mul_f32 v3, v65, v65\n"
"v_mul_f32 v4, v66, v66\n"
"v_mfma_f32_16x16x32_bf16 v[248:251], a[220:223], a[240:243], v[248:251]\n"
"ds_read_b128 a[240:243], v20 offset:0\n"
"v_mul_f32 v5, v67, v67\n"
"v_mfma_f32_16x16x32_bf16 v[180:183], a[208:211], a[244:247], v[180:183]\n"
"v_fma_f32 v2, v2, s61, v39\n"
"v_fma_f32 v3, v3, s61, v39\n"
"v_fma_f32 v4, v4, s61, v39\n"
"v_mfma_f32_16x16x32_bf16 v[204:207], a[212:215], a[244:247], v[204:207]\n"
"v_fma_f32 v5, v5, s61, v39\n"
"v_mul_f32 v2, v64, v2\n"
"v_mfma_f32_16x16x32_bf16 v[228:231], a[216:219], a[244:247], v[228:231]\n"
"v_mul_f32 v3, v65, v3\n"
"v_mul_f32 v4, v66, v4\n"
"v_mfma_f32_16x16x32_bf16 v[252:255], a[220:223], a[244:247], v[252:255]\n"
"ds_read_b128 a[244:247], v20 offset:16384\n"
"v_mul_f32 v5, v67, v5\n"
"s_waitcnt lgkmcnt(3)\n"
"v_mfma_f32_16x16x32_bf16 v[160:163], a[192:195], a[224:227], v[160:163]\n"
"v_exp_f32 v2, v2\n"
"v_mfma_f32_16x16x32_bf16 v[184:187], a[196:199], a[224:227], v[184:187]\n"
"v_exp_f32 v3, v3\n"
"s_waitcnt vmcnt(8)\n"
"v_mfma_f32_16x16x32_bf16 v[208:211], a[200:203], a[224:227], v[208:211]\n"
"ds_read_b64_tr_b16 a[208:209], v10 offset:12288\n"
"ds_read_b64_tr_b16 a[210:211], v10 offset:12544\n"
"v_mfma_f32_16x16x32_bf16 v[232:235], a[204:207], a[224:227], v[232:235]\n"
"ds_read_b128 a[224:227], v17 offset:0\n"
"v_exp_f32 v4, v4\n"
"v_mfma_f32_16x16x32_bf16 v[164:167], a[192:195], a[228:231], v[164:167]\n"
"ds_read_b64_tr_b16 a[212:213], v11 offset:12288\n"
"ds_read_b64_tr_b16 a[214:215], v11 offset:12544\n"
"v_mfma_f32_16x16x32_bf16 v[188:191], a[196:199], a[228:231], v[188:191]\n"
"v_exp_f32 v5, v5\n"
"v_mul_f32 v112, v64, v112\n"
"v_mfma_f32_16x16x32_bf16 v[212:215], a[200:203], a[228:231], v[212:215]\n"
"ds_read_b64_tr_b16 a[216:217], v10 offset:14336\n"
"ds_read_b64_tr_b16 a[218:219], v10 offset:14592\n"
"v_mfma_f32_16x16x32_bf16 v[236:239], a[204:207], a[228:231], v[236:239]\n"
"ds_read_b128 a[228:231], v17 offset:16384\n"
"v_mul_f32 v113, v65, v113\n"
"v_mul_f32 v114, v66, v114\n"
"v_mfma_f32_16x16x32_bf16 v[168:171], a[192:195], a[232:235], v[168:171]\n"
"ds_read_b64_tr_b16 a[220:221], v11 offset:14336\n"
"ds_read_b64_tr_b16 a[222:223], v11 offset:14592\n"
"v_mfma_f32_16x16x32_bf16 v[192:195], a[196:199], a[232:235], v[192:195]\n"
"v_mul_f32 v115, v67, v115\n"
"v_add_f32 v2, 1.0, v2\n"
"v_add_f32 v3, 1.0, v3\n"
"v_mfma_f32_16x16x32_bf16 v[216:219], a[200:203], a[232:235], v[216:219]\n"
"s_add_u32 s50, s62, 688128\n"
"s_add_u32 m0, s49, 8192\n"
"v_mfma_f32_16x16x32_bf16 v[240:243], a[204:207], a[232:235], v[240:243]\n"
"buffer_load_dwordx4 v22, s[24:27], s50 offen lds\n"
"buffer_load_dwordx4 v23, s[24:27], s50 offen offset:1024 lds\n"
"ds_read_b128 a[232:235], v17 offset:32768\n"
"s_waitcnt lgkmcnt(11)\n"
"v_mfma_f32_16x16x32_bf16 v[172:175], a[192:195], a[236:239], v[172:175]\n"
"s_add_u32 m0, s49, 10240\n"
"s_nop 0\n"
"buffer_load_dwordx4 v22, s[84:87], s50 offen lds\n"
"v_add_f32 v4, 1.0, v4\n"
"v_mfma_f32_16x16x32_bf16 v[196:199], a[196:199], a[236:239], v[196:199]\n"
"buffer_load_dwordx4 v23, s[84:87], s50 offen offset:1024 lds\n"
"v_add_f32 v5, 1.0, v5\n"
"v_mfma_f32_16x16x32_bf16 v[220:223], a[200:203], a[236:239], v[220:223]\n"
"v_rcp_f32 v2, v2\n"
"v_mfma_f32_16x16x32_bf16 v[244:247], a[204:207], a[236:239], v[244:247]\n"
"ds_read_b128 a[236:239], v17 offset:49152\n"
"v_rcp_f32 v3, v3\n"
"v_mfma_f32_16x16x32_bf16 v[176:179], a[192:195], a[240:243], v[176:179]\n"
"v_rcp_f32 v4, v4\n"
"v_mfma_f32_16x16x32_bf16 v[200:203], a[196:199], a[240:243], v[200:203]\n"
"v_rcp_f32 v5, v5\n"
"v_mul_f32 v112, v112, v2\n"
"v_mfma_f32_16x16x32_bf16 v[224:227], a[200:203], a[240:243], v[224:227]\n"
"v_mul_f32 v113, v113, v3\n"
"v_mul_f32 v114, v114, v4\n"
"v_mul_f32 v115, v115, v5\n"
"v_mfma_f32_16x16x32_bf16 v[248:251], a[204:207], a[240:243], v[248:251]\n"
"ds_read_b128 a[240:243], v21 offset:0\n"
"v_cvt_pk_bf16_f32 v64, v112, v113\n"
"v_mfma_f32_16x16x32_bf16 v[180:183], a[192:195], a[244:247], v[180:183]\n"
"v_cvt_pk_bf16_f32 v65, v114, v115\n"
"v_accvgpr_write_b32 a48, v64\n"
"v_accvgpr_write_b32 a49, v65\n"
"v_mfma_f32_16x16x32_bf16 v[204:207], a[196:199], a[244:247], v[204:207]\n"
"v_mul_f32 v2, v88, v88\n"
"v_mul_f32 v3, v89, v89\n"
"v_mfma_f32_16x16x32_bf16 v[228:231], a[200:203], a[244:247], v[228:231]\n"
"v_mul_f32 v4, v90, v90\n"
"v_mul_f32 v5, v91, v91\n"
"v_fma_f32 v2, v2, s61, v39\n"
"v_mfma_f32_16x16x32_bf16 v[252:255], a[204:207], a[244:247], v[252:255]\n"
"ds_read_b128 a[244:247], v21 offset:16384\n"
"v_fma_f32 v3, v3, s61, v39\n"
"s_waitcnt lgkmcnt(3)\n"
"v_mfma_f32_16x16x32_bf16 v[160:163], a[208:211], a[224:227], v[160:163]\n"
"v_fma_f32 v4, v4, s61, v39\n"
"v_fma_f32 v5, v5, s61, v39\n"
"v_mfma_f32_16x16x32_bf16 v[184:187], a[212:215], a[224:227], v[184:187]\n"
"v_mul_f32 v2, v88, v2\n"
"v_mul_f32 v3, v89, v3\n"
"s_waitcnt vmcnt(8)\n"
"v_mfma_f32_16x16x32_bf16 v[208:211], a[216:219], a[224:227], v[208:211]\n"
"ds_read_b64_tr_b16 a[192:193], v10 offset:0\n"
"ds_read_b64_tr_b16 a[194:195], v10 offset:256\n"
"v_mfma_f32_16x16x32_bf16 v[232:235], a[220:223], a[224:227], v[232:235]\n"
"ds_read_b128 a[224:227], v14 offset:256\n"
"v_mul_f32 v4, v90, v4\n"
"v_mul_f32 v5, v91, v5\n"
"v_mfma_f32_16x16x32_bf16 v[164:167], a[208:211], a[228:231], v[164:167]\n"
"ds_read_b64_tr_b16 a[196:197], v11 offset:0\n"
"ds_read_b64_tr_b16 a[198:199], v11 offset:256\n"
"v_mfma_f32_16x16x32_bf16 v[188:191], a[212:215], a[228:231], v[188:191]\n"
"v_exp_f32 v2, v2\n"
"v_mfma_f32_16x16x32_bf16 v[212:215], a[216:219], a[228:231], v[212:215]\n"
"ds_read_b64_tr_b16 a[200:201], v10 offset:2048\n"
"ds_read_b64_tr_b16 a[202:203], v10 offset:2304\n"
"v_mfma_f32_16x16x32_bf16 v[236:239], a[220:223], a[228:231], v[236:239]\n"
"ds_read_b128 a[228:231], v14 offset:16640\n"
"v_exp_f32 v3, v3\n"
"v_mfma_f32_16x16x32_bf16 v[168:171], a[208:211], a[232:235], v[168:171]\n"
"ds_read_b64_tr_b16 a[204:205], v11 offset:2048\n"
"ds_read_b64_tr_b16 a[206:207], v11 offset:2304\n"
"v_mfma_f32_16x16x32_bf16 v[192:195], a[212:215], a[232:235], v[192:195]\n"
"v_exp_f32 v4, v4\n"
"v_exp_f32 v5, v5\n"
"v_mfma_f32_16x16x32_bf16 v[216:219], a[216:219], a[232:235], v[216:219]\n"
"s_add_u32 s50, s62, 802816\n"
"s_add_u32 m0, s49, 12288\n"
"v_mfma_f32_16x16x32_bf16 v[240:243], a[220:223], a[232:235], v[240:243]\n"
"buffer_load_dwordx4 v22, s[24:27], s50 offen lds\n"
"buffer_load_dwordx4 v23, s[24:27], s50 offen offset:1024 lds\n"
"ds_read_b128 a[232:235], v14 offset:33024\n"
"s_waitcnt lgkmcnt(11)\n"
"v_mfma_f32_16x16x32_bf16 v[172:175], a[208:211], a[236:239], v[172:175]\n"
"s_add_u32 m0, s49, 14336\n"
"s_nop 0\n"
"buffer_load_dwordx4 v22, s[84:87], s50 offen lds\n"
"v_mul_f32 v136, v88, v136\n"
"v_mfma_f32_16x16x32_bf16 v[196:199], a[212:215], a[236:239], v[196:199]\n"
"buffer_load_dwordx4 v23, s[84:87], s50 offen offset:1024 lds\n"
"v_mul_f32 v137, v89, v137\n"
"v_mfma_f32_16x16x32_bf16 v[220:223], a[216:219], a[236:239], v[220:223]\n"
"v_mul_f32 v138, v90, v138\n"
"v_mul_f32 v139, v91, v139\n"
"v_add_f32 v2, 1.0, v2\n"
"v_mfma_f32_16x16x32_bf16 v[244:247], a[220:223], a[236:239], v[244:247]\n"
"ds_read_b128 a[236:239], v14 offset:49408\n"
"v_add_f32 v3, 1.0, v3\n"
"v_mfma_f32_16x16x32_bf16 v[176:179], a[208:211], a[240:243], v[176:179]\n"
"v_add_f32 v4, 1.0, v4\n"
"v_add_f32 v5, 1.0, v5\n"
"v_mfma_f32_16x16x32_bf16 v[200:203], a[212:215], a[240:243], v[200:203]\n"
"v_rcp_f32 v2, v2\n"
"v_mfma_f32_16x16x32_bf16 v[224:227], a[216:219], a[240:243], v[224:227]\n"
"v_rcp_f32 v3, v3\n"
"v_rcp_f32 v4, v4\n"
"v_mfma_f32_16x16x32_bf16 v[248:251], a[220:223], a[240:243], v[248:251]\n"
"ds_read_b128 a[240:243], v18 offset:256\n"
"v_mfma_f32_16x16x32_bf16 v[180:183], a[208:211], a[244:247], v[180:183]\n"
"v_rcp_f32 v5, v5\n"
"v_mul_f32 v136, v136, v2\n"
"v_mul_f32 v137, v137, v3\n"
"v_mfma_f32_16x16x32_bf16 v[204:207], a[212:215], a[244:247], v[204:207]\n"
"v_mul_f32 v138, v138, v4\n"
"v_mul_f32 v139, v139, v5\n"
"v_mfma_f32_16x16x32_bf16 v[228:231], a[216:219], a[244:247], v[228:231]\n"
"v_cvt_pk_bf16_f32 v88, v136, v137\n"
"v_cvt_pk_bf16_f32 v89, v138, v139\n"
"v_accvgpr_write_b32 a50, v88\n"
"v_mfma_f32_16x16x32_bf16 v[252:255], a[220:223], a[244:247], v[252:255]\n"
"ds_read_b128 a[244:247], v18 offset:16640\n"
"v_accvgpr_write_b32 a51, v89\n"
"s_waitcnt lgkmcnt(3)\n"
"v_mfma_f32_16x16x32_bf16 v[160:163], a[192:195], a[224:227], v[160:163]\n"
"v_mul_f32 v2, v68, v68\n"
"v_mul_f32 v3, v69, v69\n"
"v_mfma_f32_16x16x32_bf16 v[184:187], a[196:199], a[224:227], v[184:187]\n"
"v_mul_f32 v4, v70, v70\n"
"v_mul_f32 v5, v71, v71\n"
"s_waitcnt vmcnt(8)\n"
"v_mfma_f32_16x16x32_bf16 v[208:211], a[200:203], a[224:227], v[208:211]\n"
"ds_read_b64_tr_b16 a[208:209], v10 offset:4096\n"
"ds_read_b64_tr_b16 a[210:211], v10 offset:4352\n"
"v_mfma_f32_16x16x32_bf16 v[232:235], a[204:207], a[224:227], v[232:235]\n"
"ds_read_b128 a[224:227], v15 offset:256\n"
"v_fma_f32 v2, v2, s61, v39\n"
"v_fma_f32 v3, v3, s61, v39\n"
"v_mfma_f32_16x16x32_bf16 v[164:167], a[192:195], a[228:231], v[164:167]\n"
"ds_read_b64_tr_b16 a[212:213], v11 offset:4096\n"
"ds_read_b64_tr_b16 a[214:215], v11 offset:4352\n"
"v_mfma_f32_16x16x32_bf16 v[188:191], a[196:199], a[228:231], v[188:191]\n"
"v_fma_f32 v4, v4, s61, v39\n"
"v_fma_f32 v5, v5, s61, v39\n"
"v_mul_f32 v2, v68, v2\n"
"v_mfma_f32_16x16x32_bf16 v[212:215], a[200:203], a[228:231], v[212:215]\n"
"ds_read_b64_tr_b16 a[216:217], v10 offset:6144\n"
"ds_read_b64_tr_b16 a[218:219], v10 offset:6400\n"
"v_mfma_f32_16x16x32_bf16 v[236:239], a[204:207], a[228:231], v[236:239]\n"
"ds_read_b128 a[228:231], v15 offset:16640\n"
"v_mul_f32 v3, v69, v3\n"
"v_mul_f32 v4, v70, v4\n"
"v_mfma_f32_16x16x32_bf16 v[168:171], a[192:195], a[232:235], v[168:171]\n"
"ds_read_b64_tr_b16 a[220:221], v11 offset:6144\n"
"ds_read_b64_tr_b16 a[222:223], v11 offset:6400\n"
"v_mfma_f32_16x16x32_bf16 v[192:195], a[196:199], a[232:235], v[192:195]\n"
"v_mul_f32 v5, v71, v5\n"
"v_exp_f32 v2, v2\n"
"v_mfma_f32_16x16x32_bf16 v[216:219], a[200:203], a[232:235], v[216:219]\n"
"s_add_u32 s50, s62, 917504\n"
"s_add_u32 m0, s49, 0\n"
"v_mfma_f32_16x16x32_bf16 v[240:243], a[204:207], a[232:235], v[240:243]\n"
"buffer_load_dwordx4 v22, s[24:27], s50 offen lds\n"
"buffer_load_dwordx4 v23, s[24:27], s50 offen offset:1024 lds\n"
"ds_read_b128 a[232:235], v15 offset:33024\n"
"s_waitcnt lgkmcnt(11)\n"
"v_mfma_f32_16x16x32_bf16 v[172:175], a[192:195], a[236:239], v[172:175]\n"
"s_add_u32 m0, s49, 2048\n"
"s_nop 0\n"
"buffer_load_dwordx4 v22, s[84:87], s50 offen lds\n"
"v_mfma_f32_16x16x32_bf16 v[196:199], a[196:199], a[236:239], v[196:199]\n"
"buffer_load_dwordx4 v23, s[84:87], s50 offen offset:1024 lds\n"
"v_exp_f32 v3, v3\n"
"v_mfma_f32_16x16x32_bf16 v[220:223], a[200:203], a[236:239], v[220:223]\n"
"v_exp_f32 v4, v4\n"
"v_mfma_f32_16x16x32_bf16 v[244:247], a[204:207], a[236:239], v[244:247]\n"
"ds_read_b128 a[236:239], v15 offset:49408\n"
"v_exp_f32 v5, v5\n"
"v_mfma_f32_16x16x32_bf16 v[176:179], a[192:195], a[240:243], v[176:179]\n"
"v_mul_f32 v116, v68, v116\n"
"v_mul_f32 v117, v69, v117\n"
"v_mul_f32 v118, v70, v118\n"
"v_mfma_f32_16x16x32_bf16 v[200:203], a[196:199], a[240:243], v[200:203]\n"
"v_mul_f32 v119, v71, v119\n"
"v_add_f32 v2, 1.0, v2\n"
"v_mfma_f32_16x16x32_bf16 v[224:227], a[200:203], a[240:243], v[224:227]\n"
"v_add_f32 v3, 1.0, v3\n"
"v_add_f32 v4, 1.0, v4\n"
"v_add_f32 v5, 1.0, v5\n"
"v_mfma_f32_16x16x32_bf16 v[248:251], a[204:207], a[240:243], v[248:251]\n"
"ds_read_b128 a[240:243], v19 offset:256\n"
"v_mfma_f32_16x16x32_bf16 v[180:183], a[192:195], a[244:247], v[180:183]\n"
"v_rcp_f32 v2, v2\n"
"v_rcp_f32 v3, v3\n"
"v_mfma_f32_16x16x32_bf16 v[204:207], a[196:199], a[244:247], v[204:207]\n"
"v_rcp_f32 v4, v4\n"
"v_mfma_f32_16x16x32_bf16 v[228:231], a[200:203], a[244:247], v[228:231]\n"
"v_rcp_f32 v5, v5\n"
"v_mul_f32 v116, v116, v2\n"
"v_mfma_f32_16x16x32_bf16 v[252:255], a[204:207], a[244:247], v[252:255]\n"
"ds_read_b128 a[244:247], v19 offset:16640\n"
"v_mul_f32 v117, v117, v3\n"
"s_waitcnt lgkmcnt(3)\n"
"v_mfma_f32_16x16x32_bf16 v[160:163], a[208:211], a[224:227], v[160:163]\n"
"v_mul_f32 v118, v118, v4\n"
"v_mul_f32 v119, v119, v5\n"
"v_mfma_f32_16x16x32_bf16 v[184:187], a[212:215], a[224:227], v[184:187]\n"
"v_cvt_pk_bf16_f32 v68, v116, v117\n"
"v_cvt_pk_bf16_f32 v69, v118, v119\n"
"s_waitcnt vmcnt(8)\n"
"v_mfma_f32_16x16x32_bf16 v[208:211], a[216:219], a[224:227], v[208:211]\n"
"ds_read_b64_tr_b16 a[192:193], v10 offset:8192\n"
"ds_read_b64_tr_b16 a[194:195], v10 offset:8448\n"
"v_mfma_f32_16x16x32_bf16 v[232:235], a[220:223], a[224:227], v[232:235]\n"
"ds_read_b128 a[224:227], v16 offset:256\n"
"v_accvgpr_write_b32 a52, v68\n"
"v_mfma_f32_16x16x32_bf16 v[164:167], a[208:211], a[228:231], v[164:167]\n"
"ds_read_b64_tr_b16 a[196:197], v11 offset:8192\n"
"ds_read_b64_tr_b16 a[198:199], v11 offset:8448\n"
"v_accvgpr_write_b32 a53, v69\n"
"v_mfma_f32_16x16x32_bf16 v[188:191], a[212:215], a[228:231], v[188:191]\n"
"v_mul_f32 v2, v92, v92\n"
"v_mul_f32 v3, v93, v93\n"
"v_mfma_f32_16x16x32_bf16 v[212:215], a[216:219], a[228:231], v[212:215]\n"
"ds_read_b64_tr_b16 a[200:201], v10 offset:10240\n"
"ds_read_b64_tr_b16 a[202:203], v10 offset:10496\n"
"v_mfma_f32_16x16x32_bf16 v[236:239], a[220:223], a[228:231], v[236:239]\n"
"ds_read_b128 a[228:231], v16 offset:16640\n"
"v_mul_f32 v4, v94, v94\n"
"v_mfma_f32_16x16x32_bf16 v[168:171], a[208:211], a[232:235], v[168:171]\n"
"ds_read_b64_tr_b16 a[204:205], v11 offset:10240\n"
"ds_read_b64_tr_b16 a[206:207], v11 offset:10496\n"
"v_mul_f32 v5, v95, v95\n"
"v_mfma_f32_16x16x32_bf16 v[192:195], a[212:215], a[232:235], v[192:195]\n"
"v_fma_f32 v2, v2, s61, v39\n"
"v_fma_f32 v3, v3, s61, v39\n"
"v_mfma_f32_16x16x32_bf16 v[216:219], a[216:219], a[232:235], v[216:219]\n"
"s_add_u32 s50, s62, 1032192\n"
"s_add_u32 m0, s49, 4096\n"
"v_mfma_f32_16x16x32_bf16 v[240:243], a[220:223], a[232:235], v[240:243]\n"
"buffer_load_dwordx4 v22, s[24:27], s50 offen lds\n"
"buffer_load_dwordx4 v23, s[24:27], s50 offen offset:1024 lds\n"
"ds_read_b128 a[232:235], v16 offset:33024\n"
"s_waitcnt lgkmcnt(11)\n"
"v_mfma_f32_16x16x32_bf16 v[172:175], a[208:211], a[236:239], v[172:175]\n"
"s_add_u32 m0, s49, 6144\n"
"s_nop 0\n"
"buffer_load_dwordx4 v22, s[84:87], s50 offen lds\n"
"v_fma_f32 v4, v4, s61, v39\n"
"v_mfma_f32_16x16x32_bf16 v[196:199], a[212:215], a[236:239], v[196:199]\n"
"buffer_load_dwordx4 v23, s[84:87], s50 offen offset:1024 lds\n"
"v_fma_f32 v5, v5, s61, v39\n"
"v_mfma_f32_16x16x32_bf16 v[220:223], a[216:219], a[236:239], v[220:223]\n"
"v_mul_f32 v2, v92, v2\n"
"v_mul_f32 v3, v93, v3\n"
"v_mfma_f32_16x16x32_bf16 v[244:247], a[220:223], a[236:239], v[244:247]\n"
"ds_read_b128 a[236:239], v16 offset:49408\n"
"v_mul_f32 v4, v94, v4\n"
"v_mul_f32 v5, v95, v5\n"
"v_mfma_f32_16x16x32_bf16 v[176:179], a[208:211], a[240:243], v[176:179]\n"
"v_exp_f32 v2, v2\n"
"v_mfma_f32_16x16x32_bf16 v[200:203], a[212:215], a[240:243], v[200:203]\n"
"v_exp_f32 v3, v3\n"
"v_mfma_f32_16x16x32_bf16 v[224:227], a[216:219], a[240:243], v[224:227]\n"
"v_exp_f32 v4, v4\n"
"v_mfma_f32_16x16x32_bf16 v[248:251], a[220:223], a[240:243], v[248:251]\n"
"ds_read_b128 a[240:243], v20 offset:256\n"
"v_exp_f32 v5, v5\n"
"v_mfma_f32_16x16x32_bf16 v[180:183], a[208:211], a[244:247], v[180:183]\n"
"v_mul_f32 v140, v92, v140\n"
"v_mul_f32 v141, v93, v141\n"
"v_mfma_f32_16x16x32_bf16 v[204:207], a[212:215], a[244:247], v[204:207]\n"
"v_mul_f32 v142, v94, v142\n"
"v_mul_f32 v143, v95, v143\n"
"v_mfma_f32_16x16x32_bf16 v[228:231], a[216:219], a[244:247], v[228:231]\n"
"v_add_f32 v2, 1.0, v2\n"
"v_add_f32 v3, 1.0, v3\n"
"v_add_f32 v4, 1.0, v4\n"
"v_mfma_f32_16x16x32_bf16 v[252:255], a[220:223], a[244:247], v[252:255]\n"
"ds_read_b128 a[244:247], v20 offset:16640\n"
"v_add_f32 v5, 1.0, v5\n"
"s_waitcnt lgkmcnt(3)\n"
"v_mfma_f32_16x16x32_bf16 v[160:163], a[192:195], a[224:227], v[160:163]\n"
"v_rcp_f32 v2, v2\n"
"v_mfma_f32_16x16x32_bf16 v[184:187], a[196:199], a[224:227], v[184:187]\n"
"v_rcp_f32 v3, v3\n"
"s_waitcnt vmcnt(8)\n"
"v_mfma_f32_16x16x32_bf16 v[208:211], a[200:203], a[224:227], v[208:211]\n"
"ds_read_b64_tr_b16 a[208:209], v10 offset:12288\n"
"ds_read_b64_tr_b16 a[210:211], v10 offset:12544\n"
"v_mfma_f32_16x16x32_bf16 v[232:235], a[204:207], a[224:227], v[232:235]\n"
"ds_read_b128 a[224:227], v17 offset:256\n"
"v_rcp_f32 v4, v4\n"
"v_mfma_f32_16x16x32_bf16 v[164:167], a[192:195], a[228:231], v[164:167]\n"
"ds_read_b64_tr_b16 a[212:213], v11 offset:12288\n"
"ds_read_b64_tr_b16 a[214:215], v11 offset:12544\n"
"v_mfma_f32_16x16x32_bf16 v[188:191], a[196:199], a[228:231], v[188:191]\n"
"v_rcp_f32 v5, v5\n"
"v_mul_f32 v140, v140, v2\n"
"v_mfma_f32_16x16x32_bf16 v[212:215], a[200:203], a[228:231], v[212:215]\n"
"ds_read_b64_tr_b16 a[216:217], v10 offset:14336\n"
"ds_read_b64_tr_b16 a[218:219], v10 offset:14592\n"
"v_mfma_f32_16x16x32_bf16 v[236:239], a[204:207], a[228:231], v[236:239]\n"
"ds_read_b128 a[228:231], v17 offset:16640\n"
"v_mul_f32 v141, v141, v3\n"
"v_mul_f32 v142, v142, v4\n"
"v_mfma_f32_16x16x32_bf16 v[168:171], a[192:195], a[232:235], v[168:171]\n"
"ds_read_b64_tr_b16 a[220:221], v11 offset:14336\n"
"ds_read_b64_tr_b16 a[222:223], v11 offset:14592\n"
"v_mfma_f32_16x16x32_bf16 v[192:195], a[196:199], a[232:235], v[192:195]\n"
"v_mul_f32 v143, v143, v5\n"
"v_cvt_pk_bf16_f32 v92, v140, v141\n"
"v_cvt_pk_bf16_f32 v93, v142, v143\n"
"v_mfma_f32_16x16x32_bf16 v[216:219], a[200:203], a[232:235], v[216:219]\n"
"s_add_u32 s50, s62, 1146880\n"
"s_add_u32 m0, s49, 8192\n"
"v_mfma_f32_16x16x32_bf16 v[240:243], a[204:207], a[232:235], v[240:243]\n"
"buffer_load_dwordx4 v22, s[24:27], s50 offen lds\n"
"buffer_load_dwordx4 v23, s[24:27], s50 offen offset:1024 lds\n"
"ds_read_b128 a[232:235], v17 offset:33024\n"
"s_waitcnt lgkmcnt(11)\n"
"v_mfma_f32_16x16x32_bf16 v[172:175], a[192:195], a[236:239], v[172:175]\n"
"s_add_u32 m0, s49, 10240\n"
"s_nop 0\n"
"buffer_load_dwordx4 v22, s[84:87], s50 offen lds\n"
"v_accvgpr_write_b32 a54, v92\n"
"v_mfma_f32_16x16x32_bf16 v[196:199], a[196:199], a[236:239], v[196:199]\n"
"buffer_load_dwordx4 v23, s[84:87], s50 offen offset:1024 lds\n"
"v_accvgpr_write_b32 a55, v93\n"
"v_mfma_f32_16x16x32_bf16 v[220:223], a[200:203], a[236:239], v[220:223]\n"
"v_mul_f32 v2, v72, v72\n"
"v_mul_f32 v3, v73, v73\n"
"v_mul_f32 v4, v74, v74\n"
"v_mfma_f32_16x16x32_bf16 v[244:247], a[204:207], a[236:239], v[244:247]\n"
"ds_read_b128 a[236:239], v17 offset:49408\n"
"v_mul_f32 v5, v75, v75\n"
"v_mfma_f32_16x16x32_bf16 v[176:179], a[192:195], a[240:243], v[176:179]\n"
"v_fma_f32 v2, v2, s61, v39\n"
"v_fma_f32 v3, v3, s61, v39\n"
"v_fma_f32 v4, v4, s61, v39\n"
"v_mfma_f32_16x16x32_bf16 v[200:203], a[196:199], a[240:243], v[200:203]\n"
"v_fma_f32 v5, v5, s61, v39\n"
"v_mul_f32 v2, v72, v2\n"
"v_mfma_f32_16x16x32_bf16 v[224:227], a[200:203], a[240:243], v[224:227]\n"
"v_mul_f32 v3, v73, v3\n"
"v_mul_f32 v4, v74, v4\n"
"v_mul_f32 v5, v75, v5\n"
"v_mfma_f32_16x16x32_bf16 v[248:251], a[204:207], a[240:243], v[248:251]\n"
"ds_read_b128 a[240:243], v21 offset:256\n"
"v_mfma_f32_16x16x32_bf16 v[180:183], a[192:195], a[244:247], v[180:183]\n"
"v_exp_f32 v2, v2\n"
"v_exp_f32 v3, v3\n"
"v_mfma_f32_16x16x32_bf16 v[204:207], a[196:199], a[244:247], v[204:207]\n"
"v_exp_f32 v4, v4\n"
"v_mfma_f32_16x16x32_bf16 v[228:231], a[200:203], a[244:247], v[228:231]\n"
"v_exp_f32 v5, v5\n"
"v_mul_f32 v120, v72, v120\n"
"v_mfma_f32_16x16x32_bf16 v[252:255], a[204:207], a[244:247], v[252:255]\n"
"ds_read_b128 a[244:247], v21 offset:16640\n"
"v_mul_f32 v121, v73, v121\n"
"s_waitcnt lgkmcnt(3)\n"
"v_mfma_f32_16x16x32_bf16 v[160:163], a[208:211], a[224:227], v[160:163]\n"
"v_mul_f32 v122, v74, v122\n"
"v_mul_f32 v123, v75, v123\n"
"v_mfma_f32_16x16x32_bf16 v[184:187], a[212:215], a[224:227], v[184:187]\n"
"v_add_f32 v2, 1.0, v2\n"
"v_add_f32 v3, 1.0, v3\n"
"s_waitcnt vmcnt(8)\n"
"v_mfma_f32_16x16x32_bf16 v[208:211], a[216:219], a[224:227], v[208:211]\n"
"ds_read_b64_tr_b16 a[192:193], v10 offset:0\n"
"ds_read_b64_tr_b16 a[194:195], v10 offset:256\n"
"v_mfma_f32_16x16x32_bf16 v[232:235], a[220:223], a[224:227], v[232:235]\n"
"ds_read_b128 a[224:227], v14 offset:512\n"
"v_add_f32 v4, 1.0, v4\n"
"v_mfma_f32_16x16x32_bf16 v[164:167], a[208:211], a[228:231], v[164:167]\n"
"ds_read_b64_tr_b16 a[196:197], v11 offset:0\n"
"ds_read_b64_tr_b16 a[198:199], v11 offset:256\n"
"v_add_f32 v5, 1.0, v5\n"
"v_mfma_f32_16x16x32_bf16 v[188:191], a[212:215], a[228:231], v[188:191]\n"
"v_rcp_f32 v2, v2\n"
"v_mfma_f32_16x16x32_bf16 v[212:215], a[216:219], a[228:231], v[212:215]\n"
"ds_read_b64_tr_b16 a[200:201], v10 offset:2048\n"
"ds_read_b64_tr_b16 a[202:203], v10 offset:2304\n"
"v_mfma_f32_16x16x32_bf16 v[236:239], a[220:223], a[228:231], v[236:239]\n"
"ds_read_b128 a[228:231], v14 offset:16896\n"
"v_mfma_f32_16x16x32_bf16 v[168:171], a[208:211], a[232:235], v[168:171]\n"
"ds_read_b64_tr_b16 a[204:205], v11 offset:2048\n"
"ds_read_b64_tr_b16 a[206:207], v11 offset:2304\n"
"v_rcp_f32 v3, v3\n"
"v_mfma_f32_16x16x32_bf16 v[192:195], a[212:215], a[232:235], v[192:195]\n"
"v_rcp_f32 v4, v4\n"
"v_mfma_f32_16x16x32_bf16 v[216:219], a[216:219], a[232:235], v[216:219]\n"
"s_add_u32 s50, s62, 1261568\n"
"s_add_u32 m0, s49, 12288\n"
"v_mfma_f32_16x16x32_bf16 v[240:243], a[220:223], a[232:235], v[240:243]\n"
"buffer_load_dwordx4 v22, s[24:27], s50 offen lds\n"
"buffer_load_dwordx4 v23, s[24:27], s50 offen offset:1024 lds\n"
"ds_read_b128 a[232:235], v14 offset:33280\n"
"s_waitcnt lgkmcnt(11)\n"
"v_mfma_f32_16x16x32_bf16 v[172:175], a[208:211], a[236:239], v[172:175]\n"
"s_add_u32 m0, s49, 14336\n"
"s_nop 0\n"
"buffer_load_dwordx4 v22, s[84:87], s50 offen lds\n"
"v_mfma_f32_16x16x32_bf16 v[196:199], a[212:215], a[236:239], v[196:199]\n"
"buffer_load_dwordx4 v23, s[84:87], s50 offen offset:1024 lds\n"
"v_rcp_f32 v5, v5\n"
"v_mfma_f32_16x16x32_bf16 v[220:223], a[216:219], a[236:239], v[220:223]\n"
"v_mul_f32 v120, v120, v2\n"
"v_mul_f32 v121, v121, v3\n"
"v_mfma_f32_16x16x32_bf16 v[244:247], a[220:223], a[236:239], v[244:247]\n"
"ds_read_b128 a[236:239], v14 offset:49664\n"
"v_mul_f32 v122, v122, v4\n"
"v_mul_f32 v123, v123, v5\n"
"v_mfma_f32_16x16x32_bf16 v[176:179], a[208:211], a[240:243], v[176:179]\n"
"v_cvt_pk_bf16_f32 v72, v120, v121\n"
"v_cvt_pk_bf16_f32 v73, v122, v123\n"
"v_mfma_f32_16x16x32_bf16 v[200:203], a[212:215], a[240:243], v[200:203]\n"
"v_accvgpr_write_b32 a56, v72\n"
"v_accvgpr_write_b32 a57, v73\n"
"v_mfma_f32_16x16x32_bf16 v[224:227], a[216:219], a[240:243], v[224:227]\n"
"v_mul_f32 v2, v96, v96\n"
"v_mul_f32 v3, v97, v97\n"
"v_mfma_f32_16x16x32_bf16 v[248:251], a[220:223], a[240:243], v[248:251]\n"
"ds_read_b128 a[240:243], v18 offset:512\n"
"v_mul_f32 v4, v98, v98\n"
"v_mul_f32 v5, v99, v99\n"
"v_mfma_f32_16x16x32_bf16 v[180:183], a[208:211], a[244:247], v[180:183]\n"
"v_fma_f32 v2, v2, s61, v39\n"
"v_fma_f32 v3, v3, s61, v39\n"
"v_mfma_f32_16x16x32_bf16 v[204:207], a[212:215], a[244:247], v[204:207]\n"
"v_fma_f32 v4, v4, s61, v39\n"
"v_fma_f32 v5, v5, s61, v39\n"
"v_mfma_f32_16x16x32_bf16 v[228:231], a[216:219], a[244:247], v[228:231]\n"
"v_mul_f32 v2, v96, v2\n"
"v_mul_f32 v3, v97, v3\n"
"v_mul_f32 v4, v98, v4\n"
"v_mfma_f32_16x16x32_bf16 v[252:255], a[220:223], a[244:247], v[252:255]\n"
"ds_read_b128 a[244:247], v18 offset:16896\n"
"v_mul_f32 v5, v99, v5\n"
"s_waitcnt lgkmcnt(3)\n"
"v_mfma_f32_16x16x32_bf16 v[160:163], a[192:195], a[224:227], v[160:163]\n"
"v_exp_f32 v2, v2\n"
"v_mfma_f32_16x16x32_bf16 v[184:187], a[196:199], a[224:227], v[184:187]\n"
"v_exp_f32 v3, v3\n"
"s_waitcnt vmcnt(8)\n"
"v_mfma_f32_16x16x32_bf16 v[208:211], a[200:203], a[224:227], v[208:211]\n"
"ds_read_b64_tr_b16 a[208:209], v10 offset:4096\n"
"ds_read_b64_tr_b16 a[210:211], v10 offset:4352\n"
"v_mfma_f32_16x16x32_bf16 v[232:235], a[204:207], a[224:227], v[232:235]\n"
"ds_read_b128 a[224:227], v15 offset:512\n"
"v_exp_f32 v4, v4\n"
"v_mfma_f32_16x16x32_bf16 v[164:167], a[192:195], a[228:231], v[164:167]\n"
"ds_read_b64_tr_b16 a[212:213], v11 offset:4096\n"
"ds_read_b64_tr_b16 a[214:215], v11 offset:4352\n"
"v_mfma_f32_16x16x32_bf16 v[188:191], a[196:199], a[228:231], v[188:191]\n"
"v_exp_f32 v5, v5\n"
"v_mul_f32 v144, v96, v144\n"
"v_mfma_f32_16x16x32_bf16 v[212:215], a[200:203], a[228:231], v[212:215]\n"
"ds_read_b64_tr_b16 a[216:217], v10 offset:6144\n"
"ds_read_b64_tr_b16 a[218:219], v10 offset:6400\n"
"v_mfma_f32_16x16x32_bf16 v[236:239], a[204:207], a[228:231], v[236:239]\n"
"ds_read_b128 a[228:231], v15 offset:16896\n"
"v_mul_f32 v145, v97, v145\n"
"v_mul_f32 v146, v98, v146\n"
"v_mfma_f32_16x16x32_bf16 v[168:171], a[192:195], a[232:235], v[168:171]\n"
"ds_read_b64_tr_b16 a[220:221], v11 offset:6144\n"
"ds_read_b64_tr_b16 a[222:223], v11 offset:6400\n"
"v_mfma_f32_16x16x32_bf16 v[192:195], a[196:199], a[232:235], v[192:195]\n"
"v_mul_f32 v147, v99, v147\n"
"v_add_f32 v2, 1.0, v2\n"
"v_add_f32 v3, 1.0, v3\n"
"v_mfma_f32_16x16x32_bf16 v[216:219], a[200:203], a[232:235], v[216:219]\n"
"s_add_u32 s50, s62, 1376256\n"
"s_add_u32 m0, s49, 0\n"
"v_mfma_f32_16x16x32_bf16 v[240:243], a[204:207], a[232:235], v[240:243]\n"
"buffer_load_dwordx4 v22, s[24:27], s50 offen lds\n"
"buffer_load_dwordx4 v23, s[24:27], s50 offen offset:1024 lds\n"
"ds_read_b128 a[232:235], v15 offset:33280\n"
"s_waitcnt lgkmcnt(11)\n"
"v_mfma_f32_16x16x32_bf16 v[172:175], a[192:195], a[236:239], v[172:175]\n"
"s_add_u32 m0, s49, 2048\n"
"s_nop 0\n"
"buffer_load_dwordx4 v22, s[84:87], s50 offen lds\n"
"v_add_f32 v4, 1.0, v4\n"
"v_mfma_f32_16x16x32_bf16 v[196:199], a[196:199], a[236:239], v[196:199]\n"
"buffer_load_dwordx4 v23, s[84:87], s50 offen offset:1024 lds\n"
"v_add_f32 v5, 1.0, v5\n"
"v_mfma_f32_16x16x32_bf16 v[220:223], a[200:203], a[236:239], v[220:223]\n"
"v_rcp_f32 v2, v2\n"
"v_mfma_f32_16x16x32_bf16 v[244:247], a[204:207], a[236:239], v[244:247]\n"
"ds_read_b128 a[236:239], v15 offset:49664\n"
"v_rcp_f32 v3, v3\n"
"v_mfma_f32_16x16x32_bf16 v[176:179], a[192:195], a[240:243], v[176:179]\n"
"v_rcp_f32 v4, v4\n"
"v_mfma_f32_16x16x32_bf16 v[200:203], a[196:199], a[240:243], v[200:203]\n"
"v_rcp_f32 v5, v5\n"
"v_mul_f32 v144, v144, v2\n"
"v_mfma_f32_16x16x32_bf16 v[224:227], a[200:203], a[240:243], v[224:227]\n"
"v_mul_f32 v145, v145, v3\n"
"v_mul_f32 v146, v146, v4\n"
"v_mul_f32 v147, v147, v5\n"
"v_mfma_f32_16x16x32_bf16 v[248:251], a[204:207], a[240:243], v[248:251]\n"
"ds_read_b128 a[240:243], v19 offset:512\n"
"v_cvt_pk_bf16_f32 v96, v144, v145\n"
"v_mfma_f32_16x16x32_bf16 v[180:183], a[192:195], a[244:247], v[180:183]\n"
"v_cvt_pk_bf16_f32 v97, v146, v147\n"
"v_accvgpr_write_b32 a58, v96\n"
"v_accvgpr_write_b32 a59, v97\n"
"v_mfma_f32_16x16x32_bf16 v[204:207], a[196:199], a[244:247], v[204:207]\n"
"v_mul_f32 v2, v76, v76\n"
"v_mul_f32 v3, v77, v77\n"
"v_mfma_f32_16x16x32_bf16 v[228:231], a[200:203], a[244:247], v[228:231]\n"
"v_mul_f32 v4, v78, v78\n"
"v_mul_f32 v5, v79, v79\n"
"v_fma_f32 v2, v2, s61, v39\n"
"v_mfma_f32_16x16x32_bf16 v[252:255], a[204:207], a[244:247], v[252:255]\n"
"ds_read_b128 a[244:247], v19 offset:16896\n"
"v_fma_f32 v3, v3, s61, v39\n"
"s_waitcnt lgkmcnt(3)\n"
"v_mfma_f32_16x16x32_bf16 v[160:163], a[208:211], a[224:227], v[160:163]\n"
"v_fma_f32 v4, v4, s61, v39\n"
"v_fma_f32 v5, v5, s61, v39\n"
"v_mfma_f32_16x16x32_bf16 v[184:187], a[212:215], a[224:227], v[184:187]\n"
"v_mul_f32 v2, v76, v2\n"
"v_mul_f32 v3, v77, v3\n"
"s_waitcnt vmcnt(8)\n"
"v_mfma_f32_16x16x32_bf16 v[208:211], a[216:219], a[224:227], v[208:211]\n"
"ds_read_b64_tr_b16 a[192:193], v10 offset:8192\n"
"ds_read_b64_tr_b16 a[194:195], v10 offset:8448\n"
"v_mfma_f32_16x16x32_bf16 v[232:235], a[220:223], a[224:227], v[232:235]\n"
"ds_read_b128 a[224:227], v16 offset:512\n"
"v_mul_f32 v4, v78, v4\n"
"v_mul_f32 v5, v79, v5\n"
"v_mfma_f32_16x16x32_bf16 v[164:167], a[208:211], a[228:231], v[164:167]\n"
"ds_read_b64_tr_b16 a[196:197], v11 offset:8192\n"
"ds_read_b64_tr_b16 a[198:199], v11 offset:8448\n"
"v_mfma_f32_16x16x32_bf16 v[188:191], a[212:215], a[228:231], v[188:191]\n"
"v_exp_f32 v2, v2\n"
"v_mfma_f32_16x16x32_bf16 v[212:215], a[216:219], a[228:231], v[212:215]\n"
"ds_read_b64_tr_b16 a[200:201], v10 offset:10240\n"
"ds_read_b64_tr_b16 a[202:203], v10 offset:10496\n"
"v_mfma_f32_16x16x32_bf16 v[236:239], a[220:223], a[228:231], v[236:239]\n"
"ds_read_b128 a[228:231], v16 offset:16896\n"
"v_exp_f32 v3, v3\n"
"v_mfma_f32_16x16x32_bf16 v[168:171], a[208:211], a[232:235], v[168:171]\n"
"ds_read_b64_tr_b16 a[204:205], v11 offset:10240\n"
"ds_read_b64_tr_b16 a[206:207], v11 offset:10496\n"
"v_mfma_f32_16x16x32_bf16 v[192:195], a[212:215], a[232:235], v[192:195]\n"
"v_exp_f32 v4, v4\n"
"v_exp_f32 v5, v5\n"
"v_mfma_f32_16x16x32_bf16 v[216:219], a[216:219], a[232:235], v[216:219]\n"
"s_add_u32 s50, s62, 1490944\n"
"s_add_u32 m0, s49, 4096\n"
"v_mfma_f32_16x16x32_bf16 v[240:243], a[220:223], a[232:235], v[240:243]\n"
"buffer_load_dwordx4 v22, s[24:27], s50 offen lds\n"
"buffer_load_dwordx4 v23, s[24:27], s50 offen offset:1024 lds\n"
"ds_read_b128 a[232:235], v16 offset:33280\n"
"s_waitcnt lgkmcnt(11)\n"
"v_mfma_f32_16x16x32_bf16 v[172:175], a[208:211], a[236:239], v[172:175]\n"
"s_add_u32 m0, s49, 6144\n"
"s_nop 0\n"
"buffer_load_dwordx4 v22, s[84:87], s50 offen lds\n"
"v_mul_f32 v124, v76, v124\n"
"v_mfma_f32_16x16x32_bf16 v[196:199], a[212:215], a[236:239], v[196:199]\n"
"buffer_load_dwordx4 v23, s[84:87], s50 offen offset:1024 lds\n"
"v_mul_f32 v125, v77, v125\n"
"v_mfma_f32_16x16x32_bf16 v[220:223], a[216:219], a[236:239], v[220:223]\n"
"v_mul_f32 v126, v78, v126\n"
"v_mul_f32 v127, v79, v127\n"
"v_add_f32 v2, 1.0, v2\n"
"v_mfma_f32_16x16x32_bf16 v[244:247], a[220:223], a[236:239], v[244:247]\n"
"ds_read_b128 a[236:239], v16 offset:49664\n"
"v_add_f32 v3, 1.0, v3\n"
"v_mfma_f32_16x16x32_bf16 v[176:179], a[208:211], a[240:243], v[176:179]\n"
"v_add_f32 v4, 1.0, v4\n"
"v_add_f32 v5, 1.0, v5\n"
"v_mfma_f32_16x16x32_bf16 v[200:203], a[212:215], a[240:243], v[200:203]\n"
"v_rcp_f32 v2, v2\n"
"v_mfma_f32_16x16x32_bf16 v[224:227], a[216:219], a[240:243], v[224:227]\n"
"v_rcp_f32 v3, v3\n"
"v_rcp_f32 v4, v4\n"
"v_mfma_f32_16x16x32_bf16 v[248:251], a[220:223], a[240:243], v[248:251]\n"
"ds_read_b128 a[240:243], v20 offset:512\n"
"v_mfma_f32_16x16x32_bf16 v[180:183], a[208:211], a[244:247], v[180:183]\n"
"v_rcp_f32 v5, v5\n"
"v_mul_f32 v124, v124, v2\n"
"v_mul_f32 v125, v125, v3\n"
"v_mfma_f32_16x16x32_bf16 v[204:207], a[212:215], a[244:247], v[204:207]\n"
"v_mul_f32 v126, v126, v4\n"
"v_mul_f32 v127, v127, v5\n"
"v_mfma_f32_16x16x32_bf16 v[228:231], a[216:219], a[244:247], v[228:231]\n"
"v_cvt_pk_bf16_f32 v76, v124, v125\n"
"v_cvt_pk_bf16_f32 v77, v126, v127\n"
"v_accvgpr_write_b32 a60, v76\n"
"v_mfma_f32_16x16x32_bf16 v[252:255], a[220:223], a[244:247], v[252:255]\n"
"ds_read_b128 a[244:247], v20 offset:16896\n"
"v_accvgpr_write_b32 a61, v77\n"
"s_waitcnt lgkmcnt(3)\n"
"v_mfma_f32_16x16x32_bf16 v[160:163], a[192:195], a[224:227], v[160:163]\n"
"v_mul_f32 v2, v100, v100\n"
"v_mul_f32 v3, v101, v101\n"
"v_mfma_f32_16x16x32_bf16 v[184:187], a[196:199], a[224:227], v[184:187]\n"
"v_mul_f32 v4, v102, v102\n"
"v_mul_f32 v5, v103, v103\n"
"s_waitcnt vmcnt(8)\n"
"v_mfma_f32_16x16x32_bf16 v[208:211], a[200:203], a[224:227], v[208:211]\n"
"ds_read_b64_tr_b16 a[208:209], v10 offset:12288\n"
"ds_read_b64_tr_b16 a[210:211], v10 offset:12544\n"
"v_mfma_f32_16x16x32_bf16 v[232:235], a[204:207], a[224:227], v[232:235]\n"
"ds_read_b128 a[224:227], v17 offset:512\n"
"v_fma_f32 v2, v2, s61, v39\n"
"v_fma_f32 v3, v3, s61, v39\n"
"v_mfma_f32_16x16x32_bf16 v[164:167], a[192:195], a[228:231], v[164:167]\n"
"ds_read_b64_tr_b16 a[212:213], v11 offset:12288\n"
"ds_read_b64_tr_b16 a[214:215], v11 offset:12544\n"
"v_mfma_f32_16x16x32_bf16 v[188:191], a[196:199], a[228:231], v[188:191]\n"
"v_fma_f32 v4, v4, s61, v39\n"
"v_fma_f32 v5, v5, s61, v39\n"
"v_mul_f32 v2, v100, v2\n"
"v_mfma_f32_16x16x32_bf16 v[212:215], a[200:203], a[228:231], v[212:215]\n"
"ds_read_b64_tr_b16 a[216:217], v10 offset:14336\n"
"ds_read_b64_tr_b16 a[218:219], v10 offset:14592\n"
"v_mfma_f32_16x16x32_bf16 v[236:239], a[204:207], a[228:231], v[236:239]\n"
"ds_read_b128 a[228:231], v17 offset:16896\n"
"v_mul_f32 v3, v101, v3\n"
"v_mul_f32 v4, v102, v4\n"
"v_mfma_f32_16x16x32_bf16 v[168:171], a[192:195], a[232:235], v[168:171]\n"
"ds_read_b64_tr_b16 a[220:221], v11 offset:14336\n"
"ds_read_b64_tr_b16 a[222:223], v11 offset:14592\n"
"v_mfma_f32_16x16x32_bf16 v[192:195], a[196:199], a[232:235], v[192:195]\n"
"v_mul_f32 v5, v103, v5\n"
"v_exp_f32 v2, v2\n"
"v_mfma_f32_16x16x32_bf16 v[216:219], a[200:203], a[232:235], v[216:219]\n"
"s_add_u32 s50, s62, 1605632\n"
"s_add_u32 m0, s49, 8192\n"
"v_mfma_f32_16x16x32_bf16 v[240:243], a[204:207], a[232:235], v[240:243]\n"
"buffer_load_dwordx4 v22, s[24:27], s50 offen lds\n"
"buffer_load_dwordx4 v23, s[24:27], s50 offen offset:1024 lds\n"
"ds_read_b128 a[232:235], v17 offset:33280\n"
"s_waitcnt lgkmcnt(11)\n"
"v_mfma_f32_16x16x32_bf16 v[172:175], a[192:195], a[236:239], v[172:175]\n"
"s_add_u32 m0, s49, 10240\n"
"s_nop 0\n"
"buffer_load_dwordx4 v22, s[84:87], s50 offen lds\n"
"v_mfma_f32_16x16x32_bf16 v[196:199], a[196:199], a[236:239], v[196:199]\n"
"buffer_load_dwordx4 v23, s[84:87], s50 offen offset:1024 lds\n"
"v_exp_f32 v3, v3\n"
"v_mfma_f32_16x16x32_bf16 v[220:223], a[200:203], a[236:239], v[220:223]\n"
"v_exp_f32 v4, v4\n"
"v_mfma_f32_16x16x32_bf16 v[244:247], a[204:207], a[236:239], v[244:247]\n"
"ds_read_b128 a[236:239], v17 offset:49664\n"
"v_exp_f32 v5, v5\n"
"v_mfma_f32_16x16x32_bf16 v[176:179], a[192:195], a[240:243], v[176:179]\n"
"v_mul_f32 v148, v100, v148\n"
"v_mul_f32 v149, v101, v149\n"
"v_mul_f32 v150, v102, v150\n"
"v_mfma_f32_16x16x32_bf16 v[200:203], a[196:199], a[240:243], v[200:203]\n"
"v_mul_f32 v151, v103, v151\n"
"v_add_f32 v2, 1.0, v2\n"
"v_mfma_f32_16x16x32_bf16 v[224:227], a[200:203], a[240:243], v[224:227]\n"
"v_add_f32 v3, 1.0, v3\n"
"v_add_f32 v4, 1.0, v4\n"
"v_add_f32 v5, 1.0, v5\n"
"v_mfma_f32_16x16x32_bf16 v[248:251], a[204:207], a[240:243], v[248:251]\n"
"ds_read_b128 a[240:243], v21 offset:512\n"
"v_mfma_f32_16x16x32_bf16 v[180:183], a[192:195], a[244:247], v[180:183]\n"
"v_rcp_f32 v2, v2\n"
"v_rcp_f32 v3, v3\n"
"v_mfma_f32_16x16x32_bf16 v[204:207], a[196:199], a[244:247], v[204:207]\n"
"v_rcp_f32 v4, v4\n"
"v_mfma_f32_16x16x32_bf16 v[228:231], a[200:203], a[244:247], v[228:231]\n"
"v_rcp_f32 v5, v5\n"
"v_mul_f32 v148, v148, v2\n"
"v_mfma_f32_16x16x32_bf16 v[252:255], a[204:207], a[244:247], v[252:255]\n"
"ds_read_b128 a[244:247], v21 offset:16896\n"
"v_mul_f32 v149, v149, v3\n"
"s_waitcnt lgkmcnt(3)\n"
"v_mfma_f32_16x16x32_bf16 v[160:163], a[208:211], a[224:227], v[160:163]\n"
"v_mul_f32 v150, v150, v4\n"
"v_mul_f32 v151, v151, v5\n"
"v_mfma_f32_16x16x32_bf16 v[184:187], a[212:215], a[224:227], v[184:187]\n"
"v_cvt_pk_bf16_f32 v100, v148, v149\n"
"v_cvt_pk_bf16_f32 v101, v150, v151\n"
"s_waitcnt vmcnt(8)\n"
"v_mfma_f32_16x16x32_bf16 v[208:211], a[216:219], a[224:227], v[208:211]\n"
"ds_read_b64_tr_b16 a[192:193], v10 offset:0\n"
"ds_read_b64_tr_b16 a[194:195], v10 offset:256\n"
"v_mfma_f32_16x16x32_bf16 v[232:235], a[220:223], a[224:227], v[232:235]\n"
"ds_read_b128 a[224:227], v14 offset:768\n"
"v_accvgpr_write_b32 a62, v100\n"
"v_mfma_f32_16x16x32_bf16 v[164:167], a[208:211], a[228:231], v[164:167]\n"
"ds_read_b64_tr_b16 a[196:197], v11 offset:0\n"
"ds_read_b64_tr_b16 a[198:199], v11 offset:256\n"
"v_accvgpr_write_b32 a63, v101\n"
"v_mfma_f32_16x16x32_bf16 v[188:191], a[212:215], a[228:231], v[188:191]\n"
"v_mul_f32 v2, v80, v80\n"
"v_mul_f32 v3, v81, v81\n"
"v_mfma_f32_16x16x32_bf16 v[212:215], a[216:219], a[228:231], v[212:215]\n"
"ds_read_b64_tr_b16 a[200:201], v10 offset:2048\n"
"ds_read_b64_tr_b16 a[202:203], v10 offset:2304\n"
"v_mfma_f32_16x16x32_bf16 v[236:239], a[220:223], a[228:231], v[236:239]\n"
"ds_read_b128 a[228:231], v14 offset:17152\n"
"v_mul_f32 v4, v82, v82\n"
"v_mfma_f32_16x16x32_bf16 v[168:171], a[208:211], a[232:235], v[168:171]\n"
"ds_read_b64_tr_b16 a[204:205], v11 offset:2048\n"
"ds_read_b64_tr_b16 a[206:207], v11 offset:2304\n"
"v_mul_f32 v5, v83, v83\n"
"v_mfma_f32_16x16x32_bf16 v[192:195], a[212:215], a[232:235], v[192:195]\n"
"v_fma_f32 v2, v2, s61, v39\n"
"v_fma_f32 v3, v3, s61, v39\n"
"v_mfma_f32_16x16x32_bf16 v[216:219], a[216:219], a[232:235], v[216:219]\n"
"s_add_u32 s50, s62, 1720320\n"
"s_add_u32 m0, s49, 12288\n"
"v_mfma_f32_16x16x32_bf16 v[240:243], a[220:223], a[232:235], v[240:243]\n"
"buffer_load_dwordx4 v22, s[24:27], s50 offen lds\n"
"buffer_load_dwordx4 v23, s[24:27], s50 offen offset:1024 lds\n"
"ds_read_b128 a[232:235], v14 offset:33536\n"
"s_waitcnt lgkmcnt(11)\n"
"v_mfma_f32_16x16x32_bf16 v[172:175], a[208:211], a[236:239], v[172:175]\n"
"s_add_u32 m0, s49, 14336\n"
"s_nop 0\n"
"buffer_load_dwordx4 v22, s[84:87], s50 offen lds\n"
"v_fma_f32 v4, v4, s61, v39\n"
"v_mfma_f32_16x16x32_bf16 v[196:199], a[212:215], a[236:239], v[196:199]\n"
"buffer_load_dwordx4 v23, s[84:87], s50 offen offset:1024 lds\n"
"v_fma_f32 v5, v5, s61, v39\n"
"v_mfma_f32_16x16x32_bf16 v[220:223], a[216:219], a[236:239], v[220:223]\n"
"v_mul_f32 v2, v80, v2\n"
"v_mul_f32 v3, v81, v3\n"
"v_mfma_f32_16x16x32_bf16 v[244:247], a[220:223], a[236:239], v[244:247]\n"
"ds_read_b128 a[236:239], v14 offset:49920\n"
"v_mul_f32 v4, v82, v4\n"
"v_mul_f32 v5, v83, v5\n"
"v_mfma_f32_16x16x32_bf16 v[176:179], a[208:211], a[240:243], v[176:179]\n"
"v_exp_f32 v2, v2\n"
"v_mfma_f32_16x16x32_bf16 v[200:203], a[212:215], a[240:243], v[200:203]\n"
"v_exp_f32 v3, v3\n"
"v_mfma_f32_16x16x32_bf16 v[224:227], a[216:219], a[240:243], v[224:227]\n"
"v_exp_f32 v4, v4\n"
"v_mfma_f32_16x16x32_bf16 v[248:251], a[220:223], a[240:243], v[248:251]\n"
"ds_read_b128 a[240:243], v18 offset:768\n"
"v_exp_f32 v5, v5\n"
"v_mfma_f32_16x16x32_bf16 v[180:183], a[208:211], a[244:247], v[180:183]\n"
"v_mul_f32 v128, v80, v128\n"
"v_mul_f32 v129, v81, v129\n"
"v_mfma_f32_16x16x32_bf16 v[204:207], a[212:215], a[244:247], v[204:207]\n"
"v_mul_f32 v130, v82, v130\n"
"v_mul_f32 v131, v83, v131\n"
"v_mfma_f32_16x16x32_bf16 v[228:231], a[216:219], a[244:247], v[228:231]\n"
"v_add_f32 v2, 1.0, v2\n"
"v_add_f32 v3, 1.0, v3\n"
"v_add_f32 v4, 1.0, v4\n"
"v_mfma_f32_16x16x32_bf16 v[252:255], a[220:223], a[244:247], v[252:255]\n"
"ds_read_b128 a[244:247], v18 offset:17152\n"
"v_add_f32 v5, 1.0, v5\n"
"s_waitcnt lgkmcnt(3)\n"
"v_mfma_f32_16x16x32_bf16 v[160:163], a[192:195], a[224:227], v[160:163]\n"
"v_rcp_f32 v2, v2\n"
"v_mfma_f32_16x16x32_bf16 v[184:187], a[196:199], a[224:227], v[184:187]\n"
"v_rcp_f32 v3, v3\n"
"s_waitcnt vmcnt(8)\n"
"v_mfma_f32_16x16x32_bf16 v[208:211], a[200:203], a[224:227], v[208:211]\n"
"ds_read_b64_tr_b16 a[208:209], v10 offset:4096\n"
"ds_read_b64_tr_b16 a[210:211], v10 offset:4352\n"
"v_mfma_f32_16x16x32_bf16 v[232:235], a[204:207], a[224:227], v[232:235]\n"
"ds_read_b128 a[224:227], v15 offset:768\n"
"v_mfma_f32_16x16x32_bf16 v[164:167], a[192:195], a[228:231], v[164:167]\n"
"ds_read_b64_tr_b16 a[212:213], v11 offset:4096\n"
"ds_read_b64_tr_b16 a[214:215], v11 offset:4352\n"
"v_rcp_f32 v4, v4\n"
"v_mfma_f32_16x16x32_bf16 v[188:191], a[196:199], a[228:231], v[188:191]\n"
"v_rcp_f32 v5, v5\n"
"v_mfma_f32_16x16x32_bf16 v[212:215], a[200:203], a[228:231], v[212:215]\n"
"ds_read_b64_tr_b16 a[216:217], v10 offset:6144\n"
"ds_read_b64_tr_b16 a[218:219], v10 offset:6400\n"
"v_mfma_f32_16x16x32_bf16 v[236:239], a[204:207], a[228:231], v[236:239]\n"
"ds_read_b128 a[228:231], v15 offset:17152\n"
"v_mul_f32 v128, v128, v2\n"
"v_mfma_f32_16x16x32_bf16 v[168:171], a[192:195], a[232:235], v[168:171]\n"
"ds_read_b64_tr_b16 a[220:221], v11 offset:6144\n"
"ds_read_b64_tr_b16 a[222:223], v11 offset:6400\n"
"v_mul_f32 v129, v129, v3\n"
"v_mfma_f32_16x16x32_bf16 v[192:195], a[196:199], a[232:235], v[192:195]\n"
"v_mul_f32 v130, v130, v4\n"
"v_mul_f32 v131, v131, v5\n"
"v_mfma_f32_16x16x32_bf16 v[216:219], a[200:203], a[232:235], v[216:219]\n"
"s_cmp_eq_u32 s63, 1\n"
"s_cbranch_scc1 LNBa1_%=\n"
"s_add_u32 s50, s62, 256\n"
"s_add_u32 m0, s49, 0\n"
"s_nop 0\n"
"buffer_load_dwordx4 v22, s[24:27], s50 offen lds\n"
"buffer_load_dwordx4 v23, s[24:27], s50 offen offset:1024 lds\n"
"s_add_u32 m0, s49, 2048\n"
"s_nop 0\n"
"buffer_load_dwordx4 v22, s[84:87], s50 offen lds\n"
"buffer_load_dwordx4 v23, s[84:87], s50 offen offset:1024 lds\n"
"s_branch LNBb1_%=\n"
"LNBa1_%=:\n"
"s_add_u32 s50, s46, 0\n"
"s_add_u32 m0, s49, 0\n"
"s_nop 0\n"
"buffer_load_dwordx4 v30, s[28:31], s50 offen offset:0 lds\n"
"buffer_load_dwordx4 v31, s[28:31], s50 offen offset:1024 lds\n"
"buffer_load_dwordx4 v32, s[28:31], s50 offen offset:2048 lds\n"
"buffer_load_dwordx4 v33, s[28:31], s50 offen offset:3072 lds\n"
"LNBb1_%=:\n"
"v_mfma_f32_16x16x32_bf16 v[240:243], a[204:207], a[232:235], v[240:243]\n"
"ds_read_b128 a[232:235], v15 offset:33536\n"
"v_cvt_pk_bf16_f32 v80, v128, v129\n"
"s_waitcnt lgkmcnt(11)\n"
"v_mfma_f32_16x16x32_bf16 v[172:175], a[192:195], a[236:239], v[172:175]\n"
"v_cvt_pk_bf16_f32 v81, v130, v131\n"
"v_accvgpr_write_b32 a64, v80\n"
"v_accvgpr_write_b32 a65, v81\n"
"v_mfma_f32_16x16x32_bf16 v[196:199], a[196:199], a[236:239], v[196:199]\n"
"v_mul_f32 v2, v104, v104\n"
"v_mul_f32 v3, v105, v105\n"
"v_mfma_f32_16x16x32_bf16 v[220:223], a[200:203], a[236:239], v[220:223]\n"
"v_mul_f32 v4, v106, v106\n"
"v_mul_f32 v5, v107, v107\n"
"v_mfma_f32_16x16x32_bf16 v[244:247], a[204:207], a[236:239], v[244:247]\n"
"ds_read_b128 a[236:239], v15 offset:49920\n"
"v_fma_f32 v2, v2, s61, v39\n"
"v_fma_f32 v3, v3, s61, v39\n"
"v_mfma_f32_16x16x32_bf16 v[176:179], a[192:195], a[240:243], v[176:179]\n"
"v_fma_f32 v4, v4, s61, v39\n"
"v_fma_f32 v5, v5, s61, v39\n"
"v_mfma_f32_16x16x32_bf16 v[200:203], a[196:199], a[240:243], v[200:203]\n"
"v_mul_f32 v2, v104, v2\n"
"v_mul_f32 v3, v105, v3\n"
"v_mfma_f32_16x16x32_bf16 v[224:227], a[200:203], a[240:243], v[224:227]\n"
"v_mul_f32 v4, v106, v4\n"
"v_mul_f32 v5, v107, v5\n"
"v_mfma_f32_16x16x32_bf16 v[248:251], a[204:207], a[240:243], v[248:251]\n"
"ds_read_b128 a[240:243], v19 offset:768\n"
"v_exp_f32 v2, v2\n"
"v_mfma_f32_16x16x32_bf16 v[180:183], a[192:195], a[244:247], v[180:183]\n"
"v_exp_f32 v3, v3\n"
"v_mfma_f32_16x16x32_bf16 v[204:207], a[196:199], a[244:247], v[204:207]\n"
"v_exp_f32 v4, v4\n"
"v_mfma_f32_16x16x32_bf16 v[228:231], a[200:203], a[244:247], v[228:231]\n"
"v_exp_f32 v5, v5\n"
"v_mul_f32 v152, v104, v152\n"
"v_mfma_f32_16x16x32_bf16 v[252:255], a[204:207], a[244:247], v[252:255]\n"
"ds_read_b128 a[244:247], v19 offset:17152\n"
"v_mul_f32 v153, v105, v153\n"
"s_waitcnt lgkmcnt(3)\n"
"v_mfma_f32_16x16x32_bf16 v[160:163], a[208:211], a[224:227], v[160:163]\n"
"v_mul_f32 v154, v106, v154\n"
"v_mul_f32 v155, v107, v155\n"
"v_mfma_f32_16x16x32_bf16 v[184:187], a[212:215], a[224:227], v[184:187]\n"
"v_add_f32 v2, 1.0, v2\n"
"s_waitcnt vmcnt(8)\n"
"v_mfma_f32_16x16x32_bf16 v[208:211], a[216:219], a[224:227], v[208:211]\n"
"ds_read_b64_tr_b16 a[192:193], v10 offset:8192\n"
"ds_read_b64_tr_b16 a[194:195], v10 offset:8448\n"
"v_mfma_f32_16x16x32_bf16 v[232:235], a[220:223], a[224:227], v[232:235]\n"
"ds_read_b128 a[224:227], v16 offset:768\n"
"v_add_f32 v3, 1.0, v3\n"
"v_add_f32 v4, 1.0, v4\n"
"v_mfma_f32_16x16x32_bf16 v[164:167], a[208:211], a[228:231], v[164:167]\n"
"ds_read_b64_tr_b16 a[196:197], v11 offset:8192\n"
"ds_read_b64_tr_b16 a[198:199], v11 offset:8448\n"
"v_mfma_f32_16x16x32_bf16 v[188:191], a[212:215], a[228:231], v[188:191]\n"
"v_add_f32 v5, 1.0, v5\n"
"v_mfma_f32_16x16x32_bf16 v[212:215], a[216:219], a[228:231], v[212:215]\n"
"ds_read_b64_tr_b16 a[200:201], v10 offset:10240\n"
"ds_read_b64_tr_b16 a[202:203], v10 offset:10496\n"
"v_mfma_f32_16x16x32_bf16 v[236:239], a[220:223], a[228:231], v[236:239]\n"
"ds_read_b128 a[228:231], v16 offset:17152\n"
"v_rcp_f32 v2, v2\n"
"v_mfma_f32_16x16x32_bf16 v[168:171], a[208:211], a[232:235], v[168:171]\n"
"ds_read_b64_tr_b16 a[204:205], v11 offset:10240\n"
"ds_read_b64_tr_b16 a[206:207], v11 offset:10496\n"
"v_mfma_f32_16x16x32_bf16 v[192:195], a[212:215], a[232:235], v[192:195]\n"
"v_rcp_f32 v3, v3\n"
"v_mfma_f32_16x16x32_bf16 v[216:219], a[216:219], a[232:235], v[216:219]\n"
"s_cmp_eq_u32 s63, 1\n"
"s_cbranch_scc1 LNBa2_%=\n"
"s_add_u32 s50, s62, 114944\n"
"s_add_u32 m0, s49, 4096\n"
"s_nop 0\n"
"buffer_load_dwordx4 v22, s[24:27], s50 offen lds\n"
"buffer_load_dwordx4 v23, s[24:27], s50 offen offset:1024 lds\n"
"s_add_u32 m0, s49, 6144\n"
"s_nop 0\n"
"buffer_load_dwordx4 v22, s[84:87], s50 offen lds\n"
"buffer_load_dwordx4 v23, s[84:87], s50 offen offset:1024 lds\n"
"s_branch LNBb2_%=\n"
"LNBa2_%=:\n"
"s_add_u32 s50, s46, 32768\n"
"s_add_u32 m0, s49, 4096\n"
"s_nop 0\n"
"buffer_load_dwordx4 v30, s[28:31], s50 offen offset:0 lds\n"
"buffer_load_dwordx4 v31, s[28:31], s50 offen offset:1024 lds\n"
"buffer_load_dwordx4 v32, s[28:31], s50 offen offset:2048 lds\n"
"buffer_load_dwordx4 v33, s[28:31], s50 offen offset:3072 lds\n"
"LNBb2_%=:\n"
"v_mfma_f32_16x16x32_bf16 v[240:243], a[220:223], a[232:235], v[240:243]\n"
"ds_read_b128 a[232:235], v16 offset:33536\n"
"s_waitcnt lgkmcnt(11)\n"
"v_mfma_f32_16x16x32_bf16 v[172:175], a[208:211], a[236:239], v[172:175]\n"
"v_rcp_f32 v4, v4\n"
"v_rcp_f32 v5, v5\n"
"v_mfma_f32_16x16x32_bf16 v[196:199], a[212:215], a[236:239], v[196:199]\n"
"v_mul_f32 v152, v152, v2\n"
"v_mul_f32 v153, v153, v3\n"
"v_mfma_f32_16x16x32_bf16 v[220:223], a[216:219], a[236:239], v[220:223]\n"
"v_mul_f32 v154, v154, v4\n"
"v_mul_f32 v155, v155, v5\n"
"v_mfma_f32_16x16x32_bf16 v[244:247], a[220:223], a[236:239], v[244:247]\n"
"ds_read_b128 a[236:239], v16 offset:49920\n"
"v_cvt_pk_bf16_f32 v104, v152, v153\n"
"v_mfma_f32_16x16x32_bf16 v[176:179], a[208:211], a[240:243], v[176:179]\n"
"v_cvt_pk_bf16_f32 v105, v154, v155\n"
"v_accvgpr_write_b32 a66, v104\n"
"v_mfma_f32_16x16x32_bf16 v[200:203], a[212:215], a[240:243], v[200:203]\n"
"v_accvgpr_write_b32 a67, v105\n"
"v_mul_f32 v2, v84, v84\n"
"v_mfma_f32_16x16x32_bf16 v[224:227], a[216:219], a[240:243], v[224:227]\n"
"v_mul_f32 v3, v85, v85\n"
"v_mul_f32 v4, v86, v86\n"
"v_mfma_f32_16x16x32_bf16 v[248:251], a[220:223], a[240:243], v[248:251]\n"
"ds_read_b128 a[240:243], v20 offset:768\n"
"v_mul_f32 v5, v87, v87\n"
"v_mfma_f32_16x16x32_bf16 v[180:183], a[208:211], a[244:247], v[180:183]\n"
"v_fma_f32 v2, v2, s61, v39\n"
"v_fma_f32 v3, v3, s61, v39\n"
"v_fma_f32 v4, v4, s61, v39\n"
"v_mfma_f32_16x16x32_bf16 v[204:207], a[212:215], a[244:247], v[204:207]\n"
"v_fma_f32 v5, v5, s61, v39\n"
"v_mul_f32 v2, v84, v2\n"
"v_mfma_f32_16x16x32_bf16 v[228:231], a[216:219], a[244:247], v[228:231]\n"
"v_mul_f32 v3, v85, v3\n"
"v_mul_f32 v4, v86, v4\n"
"v_mfma_f32_16x16x32_bf16 v[252:255], a[220:223], a[244:247], v[252:255]\n"
"ds_read_b128 a[244:247], v20 offset:17152\n"
"v_mul_f32 v5, v87, v5\n"
"s_waitcnt lgkmcnt(3)\n"
"v_mfma_f32_16x16x32_bf16 v[160:163], a[192:195], a[224:227], v[160:163]\n"
"v_exp_f32 v2, v2\n"
"v_mfma_f32_16x16x32_bf16 v[184:187], a[196:199], a[224:227], v[184:187]\n"
"v_exp_f32 v3, v3\n"
"s_waitcnt vmcnt(8)\n"
"v_mfma_f32_16x16x32_bf16 v[208:211], a[200:203], a[224:227], v[208:211]\n"
"ds_read_b64_tr_b16 a[208:209], v10 offset:12288\n"
"ds_read_b64_tr_b16 a[210:211], v10 offset:12544\n"
"v_mfma_f32_16x16x32_bf16 v[232:235], a[204:207], a[224:227], v[232:235]\n"
"ds_read_b128 a[224:227], v17 offset:768\n"
"v_mfma_f32_16x16x32_bf16 v[164:167], a[192:195], a[228:231], v[164:167]\n"
"ds_read_b64_tr_b16 a[212:213], v11 offset:12288\n"
"ds_read_b64_tr_b16 a[214:215], v11 offset:12544\n"
"v_exp_f32 v4, v4\n"
"v_mfma_f32_16x16x32_bf16 v[188:191], a[196:199], a[228:231], v[188:191]\n"
"v_exp_f32 v5, v5\n"
"v_mfma_f32_16x16x32_bf16 v[212:215], a[200:203], a[228:231], v[212:215]\n"
"ds_read_b64_tr_b16 a[216:217], v10 offset:14336\n"
"ds_read_b64_tr_b16 a[218:219], v10 offset:14592\n"
"v_mfma_f32_16x16x32_bf16 v[236:239], a[204:207], a[228:231], v[236:239]\n"
"ds_read_b128 a[228:231], v17 offset:17152\n"
"v_mul_f32 v132, v84, v132\n"
"v_mfma_f32_16x16x32_bf16 v[168:171], a[192:195], a[232:235], v[168:171]\n"
"ds_read_b64_tr_b16 a[220:221], v11 offset:14336\n"
"ds_read_b64_tr_b16 a[222:223], v11 offset:14592\n"
"v_mul_f32 v133, v85, v133\n"
"v_mfma_f32_16x16x32_bf16 v[192:195], a[196:199], a[232:235], v[192:195]\n"
"v_mul_f32 v134, v86, v134\n"
"v_mul_f32 v135, v87, v135\n"
"v_mfma_f32_16x16x32_bf16 v[216:219], a[200:203], a[232:235], v[216:219]\n"
"s_cmp_eq_u32 s63, 1\n"
"s_cbranch_scc1 LNBa3_%=\n"
"s_add_u32 s50, s62, 229632\n"
"s_add_u32 m0, s49, 8192\n"
"s_nop 0\n"
"buffer_load_dwordx4 v22, s[24:27], s50 offen lds\n"
"buffer_load_dwordx4 v23, s[24:27], s50 offen offset:1024 lds\n"
"s_add_u32 m0, s49, 10240\n"
"s_nop 0\n"
"buffer_load_dwordx4 v22, s[84:87], s50 offen lds\n"
"buffer_load_dwordx4 v23, s[84:87], s50 offen offset:1024 lds\n"
"s_branch LNBb3_%=\n"
"LNBa3_%=:\n"
"s_add_u32 s50, s46, 65536\n"
"s_add_u32 m0, s49, 8192\n"
"s_nop 0\n"
"buffer_load_dwordx4 v30, s[28:31], s50 offen offset:0 lds\n"
"buffer_load_dwordx4 v31, s[28:31], s50 offen offset:1024 lds\n"
"buffer_load_dwordx4 v32, s[28:31], s50 offen offset:2048 lds\n"
"buffer_load_dwordx4 v33, s[28:31], s50 offen offset:3072 lds\n"
"LNBb3_%=:\n"
"v_mfma_f32_16x16x32_bf16 v[240:243], a[204:207], a[232:235], v[240:243]\n"
"ds_read_b128 a[232:235], v17 offset:33536\n"
"v_add_f32 v2, 1.0, v2\n"
"s_waitcnt lgkmcnt(11)\n"
"v_mfma_f32_16x16x32_bf16 v[172:175], a[192:195], a[236:239], v[172:175]\n"
"v_add_f32 v3, 1.0, v3\n"
"v_add_f32 v4, 1.0, v4\n"
"v_add_f32 v5, 1.0, v5\n"
"v_mfma_f32_16x16x32_bf16 v[196:199], a[196:199], a[236:239], v[196:199]\n"
"v_rcp_f32 v2, v2\n"
"v_mfma_f32_16x16x32_bf16 v[220:223], a[200:203], a[236:239], v[220:223]\n"
"v_rcp_f32 v3, v3\n"
"v_mfma_f32_16x16x32_bf16 v[244:247], a[204:207], a[236:239], v[244:247]\n"
"ds_read_b128 a[236:239], v17 offset:49920\n"
"v_rcp_f32 v4, v4\n"
"v_mfma_f32_16x16x32_bf16 v[176:179], a[192:195], a[240:243], v[176:179]\n"
"v_rcp_f32 v5, v5\n"
"v_mfma_f32_16x16x32_bf16 v[200:203], a[196:199], a[240:243], v[200:203]\n"
"v_mul_f32 v132, v132, v2\n"
"v_mul_f32 v133, v133, v3\n"
"v_mfma_f32_16x16x32_bf16 v[224:227], a[200:203], a[240:243], v[224:227]\n"
"v_mul_f32 v134, v134, v4\n"
"v_mul_f32 v135, v135, v5\n"
"v_mfma_f32_16x16x32_bf16 v[248:251], a[204:207], a[240:243], v[248:251]\n"
"ds_read_b128 a[240:243], v21 offset:768\n"
"v_cvt_pk_bf16_f32 v84, v132, v133\n"
"v_cvt_pk_bf16_f32 v85, v134, v135\n"
"v_mfma_f32_16x16x32_bf16 v[180:183], a[192:195], a[244:247], v[180:183]\n"
"v_accvgpr_write_b32 a68, v84\n"
"v_accvgpr_write_b32 a69, v85\n"
"v_mfma_f32_16x16x32_bf16 v[204:207], a[196:199], a[244:247], v[204:207]\n"
"v_mul_f32 v2, v108, v108\n"
"v_mul_f32 v3, v109, v109\n"
"v_mfma_f32_16x16x32_bf16 v[228:231], a[200:203], a[244:247], v[228:231]\n"
"v_mul_f32 v4, v110, v110\n"
"v_mul_f32 v5, v111, v111\n"
"v_fma_f32 v2, v2, s61, v39\n"
"v_mfma_f32_16x16x32_bf16 v[252:255], a[204:207], a[244:247], v[252:255]\n"
"ds_read_b128 a[244:247], v21 offset:17152\n"
"v_fma_f32 v3, v3, s61, v39\n"
"s_waitcnt lgkmcnt(3)\n"
"v_mfma_f32_16x16x32_bf16 v[160:163], a[208:211], a[224:227], v[160:163]\n"
"v_fma_f32 v4, v4, s61, v39\n"
"v_fma_f32 v5, v5, s61, v39\n"
"v_mfma_f32_16x16x32_bf16 v[184:187], a[212:215], a[224:227], v[184:187]\n"
"v_mul_f32 v2, v108, v2\n"
"v_mul_f32 v3, v109, v3\n"
"s_waitcnt vmcnt(8)\n"
"v_mfma_f32_16x16x32_bf16 v[208:211], a[216:219], a[224:227], v[208:211]\n"
"ds_read_b64_tr_b16 a[192:193], v10 offset:0\n"
"ds_read_b64_tr_b16 a[194:195], v10 offset:256\n"
"v_mfma_f32_16x16x32_bf16 v[232:235], a[220:223], a[224:227], v[232:235]\n"
"ds_read_b128 a[224:227], v14 offset:0\n"
"v_mul_f32 v4, v110, v4\n"
"v_mfma_f32_16x16x32_bf16 v[164:167], a[208:211], a[228:231], v[164:167]\n"
"ds_read_b64_tr_b16 a[196:197], v11 offset:0\n"
"ds_read_b64_tr_b16 a[198:199], v11 offset:256\n"
"v_mul_f32 v5, v111, v5\n"
"v_mfma_f32_16x16x32_bf16 v[188:191], a[212:215], a[228:231], v[188:191]\n"
"v_exp_f32 v2, v2\n"
"v_mfma_f32_16x16x32_bf16 v[212:215], a[216:219], a[228:231], v[212:215]\n"
"ds_read_b64_tr_b16 a[200:201], v10 offset:2048\n"
"ds_read_b64_tr_b16 a[202:203], v10 offset:2304\n"
"v_mfma_f32_16x16x32_bf16 v[236:239], a[220:223], a[228:231], v[236:239]\n"
"ds_read_b128 a[228:231], v14 offset:16384\n"
"v_mfma_f32_16x16x32_bf16 v[168:171], a[208:211], a[232:235], v[168:171]\n"
"ds_read_b64_tr_b16 a[204:205], v11 offset:2048\n"
"ds_read_b64_tr_b16 a[206:207], v11 offset:2304\n"
"v_exp_f32 v3, v3\n"
"v_mfma_f32_16x16x32_bf16 v[192:195], a[212:215], a[232:235], v[192:195]\n"
"v_exp_f32 v4, v4\n"
"v_mfma_f32_16x16x32_bf16 v[216:219], a[216:219], a[232:235], v[216:219]\n"
"s_cmp_eq_u32 s63, 1\n"
"s_cbranch_scc1 LNBa4_%=\n"
"s_add_u32 s50, s62, 344320\n"
"s_add_u32 m0, s49, 12288\n"
"s_nop 0\n"
"buffer_load_dwordx4 v22, s[24:27], s50 offen lds\n"
"buffer_load_dwordx4 v23, s[24:27], s50 offen offset:1024 lds\n"
"s_add_u32 m0, s49, 14336\n"
"s_nop 0\n"
"buffer_load_dwordx4 v22, s[84:87], s50 offen lds\n"
"buffer_load_dwordx4 v23, s[84:87], s50 offen offset:1024 lds\n"
"s_branch LNBb4_%=\n"
"LNBa4_%=:\n"
"s_add_u32 s50, s46, 98304\n"
"s_add_u32 m0, s49, 12288\n"
"s_nop 0\n"
"buffer_load_dwordx4 v30, s[28:31], s50 offen offset:0 lds\n"
"buffer_load_dwordx4 v31, s[28:31], s50 offen offset:1024 lds\n"
"buffer_load_dwordx4 v32, s[28:31], s50 offen offset:2048 lds\n"
"buffer_load_dwordx4 v33, s[28:31], s50 offen offset:3072 lds\n"
"LNBb4_%=:\n"
"v_mfma_f32_16x16x32_bf16 v[240:243], a[220:223], a[232:235], v[240:243]\n"
"ds_read_b128 a[232:235], v14 offset:32768\n"
"s_waitcnt lgkmcnt(11)\n"
"v_mfma_f32_16x16x32_bf16 v[172:175], a[208:211], a[236:239], v[172:175]\n"
"v_exp_f32 v5, v5\n"
"v_mul_f32 v156, v108, v156\n"
"v_mul_f32 v157, v109, v157\n"
"v_mfma_f32_16x16x32_bf16 v[196:199], a[212:215], a[236:239], v[196:199]\n"
"v_mul_f32 v158, v110, v158\n"
"v_mul_f32 v159, v111, v159\n"
"v_mfma_f32_16x16x32_bf16 v[220:223], a[216:219], a[236:239], v[220:223]\n"
"v_add_f32 v2, 1.0, v2\n"
"v_add_f32 v3, 1.0, v3\n"
"v_mfma_f32_16x16x32_bf16 v[244:247], a[220:223], a[236:239], v[244:247]\n"
"ds_read_b128 a[236:239], v14 offset:49152\n"
"v_add_f32 v4, 1.0, v4\n"
"v_add_f32 v5, 1.0, v5\n"
"v_mfma_f32_16x16x32_bf16 v[176:179], a[208:211], a[240:243], v[176:179]\n"
"v_rcp_f32 v2, v2\n"
"v_mfma_f32_16x16x32_bf16 v[200:203], a[212:215], a[240:243], v[200:203]\n"
"v_rcp_f32 v3, v3\n"
"v_mfma_f32_16x16x32_bf16 v[224:227], a[216:219], a[240:243], v[224:227]\n"
"v_rcp_f32 v4, v4\n"
"v_mfma_f32_16x16x32_bf16 v[248:251], a[220:223], a[240:243], v[248:251]\n"
"ds_read_b128 a[240:243], v18 offset:0\n"
"v_rcp_f32 v5, v5\n"
"v_mfma_f32_16x16x32_bf16 v[180:183], a[208:211], a[244:247], v[180:183]\n"
"v_mul_f32 v156, v156, v2\n"
"v_mul_f32 v157, v157, v3\n"
"v_mfma_f32_16x16x32_bf16 v[204:207], a[212:215], a[244:247], v[204:207]\n"
"v_mul_f32 v158, v158, v4\n"
"v_mul_f32 v159, v159, v5\n"
"v_mfma_f32_16x16x32_bf16 v[228:231], a[216:219], a[244:247], v[228:231]\n"
"v_cvt_pk_bf16_f32 v108, v156, v157\n"
"v_cvt_pk_bf16_f32 v109, v158, v159\n"
"v_accvgpr_write_b32 a70, v108\n"
"v_mfma_f32_16x16x32_bf16 v[252:255], a[220:223], a[244:247], v[252:255]\n"
"ds_read_b128 a[244:247], v18 offset:16384\n"
"v_accvgpr_write_b32 a71, v109\n"
"s_add_u32 s62, s62, 256\n"
"s_add_u32 s53, s47, s43\n"
"s_cmp_eq_u32 s53, 3\n"
"s_waitcnt lgkmcnt(0)\n"
"s_cbranch_scc1 LF4_%=\n"
"s_cmp_eq_u32 s47, 1\n"
"s_cselect_b32 s63, 1, 0\n"
"v_mfma_f32_16x16x32_bf16 v[64:67], a[192:195], a[224:227], 0\n"
"v_mul_f32 v2, v160, v160\n"
"v_mul_f32 v3, v161, v161\n"
"v_mfma_f32_16x16x32_bf16 v[88:91], a[196:199], a[224:227], 0\n"
"v_mul_f32 v4, v162, v162\n"
"v_mul_f32 v5, v163, v163\n"
"s_waitcnt vmcnt(8)\n"
"v_mfma_f32_16x16x32_bf16 v[112:115], a[200:203], a[224:227], 0\n"
"ds_read_b64_tr_b16 a[208:209], v10 offset:4096\n"
"ds_read_b64_tr_b16 a[210:211], v10 offset:4352\n"
"v_mfma_f32_16x16x32_bf16 v[136:139], a[204:207], a[224:227], 0\n"
"ds_read_b128 a[224:227], v15 offset:0\n"
"v_fma_f32 v2, v2, s61, v39\n"
"v_mfma_f32_16x16x32_bf16 v[68:71], a[192:195], a[228:231], 0\n"
"ds_read_b64_tr_b16 a[212:213], v11 offset:4096\n"
"ds_read_b64_tr_b16 a[214:215], v11 offset:4352\n"
"v_fma_f32 v3, v3, s61, v39\n"
"v_mfma_f32_16x16x32_bf16 v[92:95], a[196:199], a[228:231], 0\n"
"v_fma_f32 v4, v4, s61, v39\n"
"v_fma_f32 v5, v5, s61, v39\n"
"v_mfma_f32_16x16x32_bf16 v[116:119], a[200:203], a[228:231], 0\n"
"ds_read_b64_tr_b16 a[216:217], v10 offset:6144\n"
"ds_read_b64_tr_b16 a[218:219], v10 offset:6400\n"
"v_mfma_f32_16x16x32_bf16 v[140:143], a[204:207], a[228:231], 0\n"
"ds_read_b128 a[228:231], v15 offset:16384\n"
"v_mul_f32 v2, v160, v2\n"
"v_mfma_f32_16x16x32_bf16 v[72:75], a[192:195], a[232:235], 0\n"
"ds_read_b64_tr_b16 a[220:221], v11 offset:6144\n"
"ds_read_b64_tr_b16 a[222:223], v11 offset:6400\n"
"v_mul_f32 v3, v161, v3\n"
"v_mfma_f32_16x16x32_bf16 v[96:99], a[196:199], a[232:235], 0\n"
"v_mul_f32 v4, v162, v4\n"
"v_mul_f32 v5, v163, v5\n"
"v_mfma_f32_16x16x32_bf16 v[120:123], a[200:203], a[232:235], 0\n"
"s_add_u32 s50, s62, 458752\n"
"s_add_u32 m0, s49, 0\n"
"v_mfma_f32_16x16x32_bf16 v[144:147], a[204:207], a[232:235], 0\n"
"buffer_load_dwordx4 v22, s[24:27], s50 offen lds\n"
"buffer_load_dwordx4 v23, s[24:27], s50 offen offset:1024 lds\n"
"ds_read_b128 a[232:235], v15 offset:32768\n"
"v_mfma_f32_16x16x32_bf16 v[76:79], a[192:195], a[236:239], 0\n"
"s_add_u32 m0, s49, 2048\n"
"s_nop 0\n"
"buffer_load_dwordx4 v22, s[84:87], s50 offen lds\n"
"v_mfma_f32_16x16x32_bf16 v[100:103], a[196:199], a[236:239], 0\n"
"buffer_load_dwordx4 v23, s[84:87], s50 offen offset:1024 lds\n"
"v_exp_f32 v2, v2\n"
"v_mfma_f32_16x16x32_bf16 v[124:127], a[200:203], a[236:239], 0\n"
"v_exp_f32 v3, v3\n"
"v_mfma_f32_16x16x32_bf16 v[148:151], a[204:207], a[236:239], 0\n"
"ds_read_b128 a[236:239], v15 offset:49152\n"
"v_exp_f32 v4, v4\n"
"v_mfma_f32_16x16x32_bf16 v[80:83], a[192:195], a[240:243], 0\n"
"v_exp_f32 v5, v5\n"
"v_mfma_f32_16x16x32_bf16 v[104:107], a[196:199], a[240:243], 0\n"
"v_mul_f32 v208, v160, v208\n"
"v_mul_f32 v209, v161, v209\n"
"v_mfma_f32_16x16x32_bf16 v[128:131], a[200:203], a[240:243], 0\n"
"v_mul_f32 v210, v162, v210\n"
"v_mul_f32 v211, v163, v211\n"
"v_mfma_f32_16x16x32_bf16 v[152:155], a[204:207], a[240:243], 0\n"
"ds_read_b128 a[240:243], v19 offset:0\n"
"v_add_f32 v2, 1.0, v2\n"
"v_add_f32 v3, 1.0, v3\n"
"v_mfma_f32_16x16x32_bf16 v[84:87], a[192:195], a[244:247], 0\n"
"v_add_f32 v4, 1.0, v4\n"
"v_add_f32 v5, 1.0, v5\n"
"v_mfma_f32_16x16x32_bf16 v[108:111], a[196:199], a[244:247], 0\n"
"v_rcp_f32 v2, v2\n"
"v_mfma_f32_16x16x32_bf16 v[132:135], a[200:203], a[244:247], 0\n"
"v_rcp_f32 v3, v3\n"
"v_mfma_f32_16x16x32_bf16 v[156:159], a[204:207], a[244:247], 0\n"
"ds_read_b128 a[244:247], v19 offset:16384\n"
"v_rcp_f32 v4, v4\n"
"s_waitcnt lgkmcnt(3)\n"
"v_mfma_f32_16x16x32_bf16 v[64:67], a[208:211], a[224:227], v[64:67]\n"
"v_rcp_f32 v5, v5\n"
"v_mfma_f32_16x16x32_bf16 v[88:91], a[212:215], a[224:227], v[88:91]\n"
"v_mul_f32 v208, v208, v2\n"
"s_waitcnt vmcnt(8)\n"
"v_mfma_f32_16x16x32_bf16 v[112:115], a[216:219], a[224:227], v[112:115]\n"
"ds_read_b64_tr_b16 a[192:193], v10 offset:8192\n"
"ds_read_b64_tr_b16 a[194:195], v10 offset:8448\n"
"v_mul_f32 v209, v209, v3\n"
"v_mfma_f32_16x16x32_bf16 v[136:139], a[220:223], a[224:227], v[136:139]\n"
"ds_read_b128 a[224:227], v16 offset:0\n"
"v_mul_f32 v210, v210, v4\n"
"v_mfma_f32_16x16x32_bf16 v[68:71], a[208:211], a[228:231], v[68:71]\n"
"ds_read_b64_tr_b16 a[196:197], v11 offset:8192\n"
"ds_read_b64_tr_b16 a[198:199], v11 offset:8448\n"
"v_mfma_f32_16x16x32_bf16 v[92:95], a[212:215], a[228:231], v[92:95]\n"
"v_mul_f32 v211, v211, v5\n"
"v_cvt_pk_bf16_f32 v160, v208, v209\n"
"v_mfma_f32_16x16x32_bf16 v[116:119], a[216:219], a[228:231], v[116:119]\n"
"ds_read_b64_tr_b16 a[200:201], v10 offset:10240\n"
"ds_read_b64_tr_b16 a[202:203], v10 offset:10496\n"
"v_cvt_pk_bf16_f32 v161, v210, v211\n"
"v_mfma_f32_16x16x32_bf16 v[140:143], a[220:223], a[228:231], v[140:143]\n"
"ds_read_b128 a[228:231], v16 offset:16384\n"
"v_accvgpr_write_b32 a72, v160\n"
"v_mfma_f32_16x16x32_bf16 v[72:75], a[208:211], a[232:235], v[72:75]\n"
"ds_read_b64_tr_b16 a[204:205], v11 offset:10240\n"
"ds_read_b64_tr_b16 a[206:207], v11 offset:10496\n"
"v_mfma_f32_16x16x32_bf16 v[96:99], a[212:215], a[232:235], v[96:99]\n"
"v_accvgpr_write_b32 a73, v161\n"
"v_mul_f32 v2, v184, v184\n"
"v_mfma_f32_16x16x32_bf16 v[120:123], a[216:219], a[232:235], v[120:123]\n"
"s_add_u32 s50, s62, 573440\n"
"s_add_u32 m0, s49, 4096\n"
"v_mfma_f32_16x16x32_bf16 v[144:147], a[220:223], a[232:235], v[144:147]\n"
"buffer_load_dwordx4 v22, s[24:27], s50 offen lds\n"
"buffer_load_dwordx4 v23, s[24:27], s50 offen offset:1024 lds\n"
"ds_read_b128 a[232:235], v16 offset:32768\n"
"s_waitcnt lgkmcnt(11)\n"
"v_mfma_f32_16x16x32_bf16 v[76:79], a[208:211], a[236:239], v[76:79]\n"
"s_add_u32 m0, s49, 6144\n"
"s_nop 0\n"
"buffer_load_dwordx4 v22, s[84:87], s50 offen lds\n"
"v_mul_f32 v3, v185, v185\n"
"v_mfma_f32_16x16x32_bf16 v[100:103], a[212:215], a[236:239], v[100:103]\n"
"buffer_load_dwordx4 v23, s[84:87], s50 offen offset:1024 lds\n"
"v_mul_f32 v4, v186, v186\n"
"v_mfma_f32_16x16x32_bf16 v[124:127], a[216:219], a[236:239], v[124:127]\n"
"v_mul_f32 v5, v187, v187\n"
"v_fma_f32 v2, v2, s61, v39\n"
"v_mfma_f32_16x16x32_bf16 v[148:151], a[220:223], a[236:239], v[148:151]\n"
"ds_read_b128 a[236:239], v16 offset:49152\n"
"v_fma_f32 v3, v3, s61, v39\n"
"v_mfma_f32_16x16x32_bf16 v[80:83], a[208:211], a[240:243], v[80:83]\n"
"v_fma_f32 v4, v4, s61, v39\n"
"v_fma_f32 v5, v5, s61, v39\n"
"v_mfma_f32_16x16x32_bf16 v[104:107], a[212:215], a[240:243], v[104:107]\n"
"v_mul_f32 v2, v184, v2\n"
"v_mul_f32 v3, v185, v3\n"
"v_mul_f32 v4, v186, v4\n"
"v_mfma_f32_16x16x32_bf16 v[128:131], a[216:219], a[240:243], v[128:131]\n"
"v_mul_f32 v5, v187, v5\n"
"v_mfma_f32_16x16x32_bf16 v[152:155], a[220:223], a[240:243], v[152:155]\n"
"ds_read_b128 a[240:243], v20 offset:0\n"
"v_exp_f32 v2, v2\n"
"v_mfma_f32_16x16x32_bf16 v[84:87], a[208:211], a[244:247], v[84:87]\n"
"v_exp_f32 v3, v3\n"
"v_mfma_f32_16x16x32_bf16 v[108:111], a[212:215], a[244:247], v[108:111]\n"
"v_exp_f32 v4, v4\n"
"v_mfma_f32_16x16x32_bf16 v[132:135], a[216:219], a[244:247], v[132:135]\n"
"v_exp_f32 v5, v5\n"
"v_mul_f32 v232, v184, v232\n"
"v_mfma_f32_16x16x32_bf16 v[156:159], a[220:223], a[244:247], v[156:159]\n"
"ds_read_b128 a[244:247], v20 offset:16384\n"
"v_mul_f32 v233, v185, v233\n"
"s_waitcnt lgkmcnt(3)\n"
"v_mfma_f32_16x16x32_bf16 v[64:67], a[192:195], a[224:227], v[64:67]\n"
"v_mul_f32 v234, v186, v234\n"
"v_mul_f32 v235, v187, v235\n"
"v_mfma_f32_16x16x32_bf16 v[88:91], a[196:199], a[224:227], v[88:91]\n"
"v_add_f32 v2, 1.0, v2\n"
"s_waitcnt vmcnt(8)\n"
"v_mfma_f32_16x16x32_bf16 v[112:115], a[200:203], a[224:227], v[112:115]\n"
"ds_read_b64_tr_b16 a[208:209], v10 offset:12288\n"
"ds_read_b64_tr_b16 a[210:211], v10 offset:12544\n"
"v_add_f32 v3, 1.0, v3\n"
"v_mfma_f32_16x16x32_bf16 v[136:139], a[204:207], a[224:227], v[136:139]\n"
"ds_read_b128 a[224:227], v17 offset:0\n"
"v_add_f32 v4, 1.0, v4\n"
"v_mfma_f32_16x16x32_bf16 v[68:71], a[192:195], a[228:231], v[68:71]\n"
"ds_read_b64_tr_b16 a[212:213], v11 offset:12288\n"
"ds_read_b64_tr_b16 a[214:215], v11 offset:12544\n"
"v_mfma_f32_16x16x32_bf16 v[92:95], a[196:199], a[228:231], v[92:95]\n"
"v_add_f32 v5, 1.0, v5\n"
"v_mfma_f32_16x16x32_bf16 v[116:119], a[200:203], a[228:231], v[116:119]\n"
"ds_read_b64_tr_b16 a[216:217], v10 offset:14336\n"
"ds_read_b64_tr_b16 a[218:219], v10 offset:14592\n"
"v_mfma_f32_16x16x32_bf16 v[140:143], a[204:207], a[228:231], v[140:143]\n"
"ds_read_b128 a[228:231], v17 offset:16384\n"
"v_rcp_f32 v2, v2\n"
"v_mfma_f32_16x16x32_bf16 v[72:75], a[192:195], a[232:235], v[72:75]\n"
"ds_read_b64_tr_b16 a[220:221], v11 offset:14336\n"
"ds_read_b64_tr_b16 a[222:223], v11 offset:14592\n"
"v_mfma_f32_16x16x32_bf16 v[96:99], a[196:199], a[232:235], v[96:99]\n"
"v_rcp_f32 v3, v3\n"
"v_mfma_f32_16x16x32_bf16 v[120:123], a[200:203], a[232:235], v[120:123]\n"
"s_add_u32 s50, s62, 688128\n"
"s_add_u32 m0, s49, 8192\n"
"v_mfma_f32_16x16x32_bf16 v[144:147], a[204:207], a[232:235], v[144:147]\n"
"buffer_load_dwordx4 v22, s[24:27], s50 offen lds\n"
"buffer_load_dwordx4 v23, s[24:27], s50 offen offset:1024 lds\n"
"ds_read_b128 a[232:235], v17 offset:32768\n"
"s_waitcnt lgkmcnt(11)\n"
"v_mfma_f32_16x16x32_bf16 v[76:79], a[192:195], a[236:239], v[76:79]\n"
"s_add_u32 m0, s49, 10240\n"
"s_nop 0\n"
"buffer_load_dwordx4 v22, s[84:87], s50 offen lds\n"
"v_mfma_f32_16x16x32_bf16 v[100:103], a[196:199], a[236:239], v[100:103]\n"
"buffer_load_dwordx4 v23, s[84:87], s50 offen offset:1024 lds\n"
"v_rcp_f32 v4, v4\n"
"v_mfma_f32_16x16x32_bf16 v[124:127], a[200:203], a[236:239], v[124:127]\n"
"v_rcp_f32 v5, v5\n"
"v_mfma_f32_16x16x32_bf16 v[148:151], a[204:207], a[236:239], v[148:151]\n"
"ds_read_b128 a[236:239], v17 offset:49152\n"
"v_mul_f32 v232, v232, v2\n"
"v_mul_f32 v233, v233, v3\n"
"v_mfma_f32_16x16x32_bf16 v[80:83], a[192:195], a[240:243], v[80:83]\n"
"v_mul_f32 v234, v234, v4\n"
"v_mul_f32 v235, v235, v5\n"
"v_mfma_f32_16x16x32_bf16 v[104:107], a[196:199], a[240:243], v[104:107]\n"
"v_cvt_pk_bf16_f32 v184, v232, v233\n"
"v_cvt_pk_bf16_f32 v185, v234, v235\n"
"v_mfma_f32_16x16x32_bf16 v[128:131], a[200:203], a[240:243], v[128:131]\n"
"v_accvgpr_write_b32 a74, v184\n"
"v_accvgpr_write_b32 a75, v185\n"
"v_mfma_f32_16x16x32_bf16 v[152:155], a[204:207], a[240:243], v[152:155]\n"
"ds_read_b128 a[240:243], v21 offset:0\n"
"v_mul_f32 v2, v164, v164\n"
"v_mfma_f32_16x16x32_bf16 v[84:87], a[192:195], a[244:247], v[84:87]\n"
"v_mul_f32 v3, v165, v165\n"
"v_mul_f32 v4, v166, v166\n"
"v_mfma_f32_16x16x32_bf16 v[108:111], a[196:199], a[244:247], v[108:111]\n"
"v_mul_f32 v5, v167, v167\n"
"v_fma_f32 v2, v2, s61, v39\n"
"v_fma_f32 v3, v3, s61, v39\n"
"v_mfma_f32_16x16x32_bf16 v[132:135], a[200:203], a[244:247], v[132:135]\n"
"v_fma_f32 v4, v4, s61, v39\n"
"v_fma_f32 v5, v5, s61, v39\n"
"v_mfma_f32_16x16x32_bf16 v[156:159], a[204:207], a[244:247], v[156:159]\n"
"ds_read_b128 a[244:247], v21 offset:16384\n"
"v_mul_f32 v2, v164, v2\n"
"s_waitcnt lgkmcnt(3)\n"
"v_mfma_f32_16x16x32_bf16 v[64:67], a[208:211], a[224:227], v[64:67]\n"
"v_mul_f32 v3, v165, v3\n"
"v_mul_f32 v4, v166, v4\n"
"v_mfma_f32_16x16x32_bf16 v[88:91], a[212:215], a[224:227], v[88:91]\n"
"v_mul_f32 v5, v167, v5\n"
"s_waitcnt vmcnt(8)\n"
"v_mfma_f32_16x16x32_bf16 v[112:115], a[216:219], a[224:227], v[112:115]\n"
"ds_read_b64_tr_b16 a[192:193], v10 offset:0\n"
"ds_read_b64_tr_b16 a[194:195], v10 offset:256\n"
"v_mfma_f32_16x16x32_bf16 v[136:139], a[220:223], a[224:227], v[136:139]\n"
"ds_read_b128 a[224:227], v14 offset:256\n"
"v_exp_f32 v2, v2\n"
"v_mfma_f32_16x16x32_bf16 v[68:71], a[208:211], a[228:231], v[68:71]\n"
"ds_read_b64_tr_b16 a[196:197], v11 offset:0\n"
"ds_read_b64_tr_b16 a[198:199], v11 offset:256\n"
"v_mfma_f32_16x16x32_bf16 v[92:95], a[212:215], a[228:231], v[92:95]\n"
"v_exp_f32 v3, v3\n"
"v_mfma_f32_16x16x32_bf16 v[116:119], a[216:219], a[228:231], v[116:119]\n"
"ds_read_b64_tr_b16 a[200:201], v10 offset:2048\n"
"ds_read_b64_tr_b16 a[202:203], v10 offset:2304\n"
"v_mfma_f32_16x16x32_bf16 v[140:143], a[220:223], a[228:231], v[140:143]\n"
"ds_read_b128 a[228:231], v14 offset:16640\n"
"v_exp_f32 v4, v4\n"
"v_mfma_f32_16x16x32_bf16 v[72:75], a[208:211], a[232:235], v[72:75]\n"
"ds_read_b64_tr_b16 a[204:205], v11 offset:2048\n"
"ds_read_b64_tr_b16 a[206:207], v11 offset:2304\n"
"v_mfma_f32_16x16x32_bf16 v[96:99], a[212:215], a[232:235], v[96:99]\n"
"v_exp_f32 v5, v5\n"
"v_mul_f32 v212, v164, v212\n"
"v_mfma_f32_16x16x32_bf16 v[120:123], a[216:219], a[232:235], v[120:123]\n"
"s_add_u32 s50, s62, 802816\n"
"s_add_u32 m0, s49, 12288\n"
"v_mul_f32 v213, v165, v213\n"
"v_mfma_f32_16x16x32_bf16 v[144:147], a[220:223], a[232:235], v[144:147]\n"
"buffer_load_dwordx4 v22, s[24:27], s50 offen lds\n"
"buffer_load_dwordx4 v23, s[24:27], s50 offen offset:1024 lds\n"
"ds_read_b128 a[232:235], v14 offset:33024\n"
"s_waitcnt lgkmcnt(11)\n"
"v_mfma_f32_16x16x32_bf16 v[76:79], a[208:211], a[236:239], v[76:79]\n"
"s_add_u32 m0, s49, 14336\n"
"s_nop 0\n"
"buffer_load_dwordx4 v22, s[84:87], s50 offen lds\n"
"v_mfma_f32_16x16x32_bf16 v[100:103], a[212:215], a[236:239], v[100:103]\n"
"buffer_load_dwordx4 v23, s[84:87], s50 offen offset:1024 lds\n"
"v_mul_f32 v214, v166, v214\n"
"v_mul_f32 v215, v167, v215\n"
"v_mfma_f32_16x16x32_bf16 v[124:127], a[216:219], a[236:239], v[124:127]\n"
"v_add_f32 v2, 1.0, v2\n"
"v_add_f32 v3, 1.0, v3\n"
"v_mfma_f32_16x16x32_bf16 v[148:151], a[220:223], a[236:239], v[148:151]\n"
"ds_read_b128 a[236:239], v14 offset:49408\n"
"v_add_f32 v4, 1.0, v4\n"
"v_mfma_f32_16x16x32_bf16 v[80:83], a[208:211], a[240:243], v[80:83]\n"
"v_add_f32 v5, 1.0, v5\n"
"v_rcp_f32 v2, v2\n"
"v_mfma_f32_16x16x32_bf16 v[104:107], a[212:215], a[240:243], v[104:107]\n"
"v_rcp_f32 v3, v3\n"
"v_mfma_f32_16x16x32_bf16 v[128:131], a[216:219], a[240:243], v[128:131]\n"
"v_rcp_f32 v4, v4\n"
"v_mfma_f32_16x16x32_bf16 v[152:155], a[220:223], a[240:243], v[152:155]\n"
"ds_read_b128 a[240:243], v18 offset:256\n"
"v_rcp_f32 v5, v5\n"
"v_mfma_f32_16x16x32_bf16 v[84:87], a[208:211], a[244:247], v[84:87]\n"
"v_mul_f32 v212, v212, v2\n"
"v_mul_f32 v213, v213, v3\n"
"v_mfma_f32_16x16x32_bf16 v[108:111], a[212:215], a[244:247], v[108:111]\n"
"v_mul_f32 v214, v214, v4\n"
"v_mul_f32 v215, v215, v5\n"
"v_mfma_f32_16x16x32_bf16 v[132:135], a[216:219], a[244:247], v[132:135]\n"
"v_cvt_pk_bf16_f32 v164, v212, v213\n"
"v_cvt_pk_bf16_f32 v165, v214, v215\n"
"v_accvgpr_write_b32 a76, v164\n"
"v_mfma_f32_16x16x32_bf16 v[156:159], a[220:223], a[244:247], v[156:159]\n"
"ds_read_b128 a[244:247], v18 offset:16640\n"
"v_accvgpr_write_b32 a77, v165\n"
"s_waitcnt lgkmcnt(3)\n"
"v_mfma_f32_16x16x32_bf16 v[64:67], a[192:195], a[224:227], v[64:67]\n"
"v_mul_f32 v2, v188, v188\n"
"v_mul_f32 v3, v189, v189\n"
"v_mfma_f32_16x16x32_bf16 v[88:91], a[196:199], a[224:227], v[88:91]\n"
"v_mul_f32 v4, v190, v190\n"
"v_mul_f32 v5, v191, v191\n"
"s_waitcnt vmcnt(8)\n"
"v_mfma_f32_16x16x32_bf16 v[112:115], a[200:203], a[224:227], v[112:115]\n"
"ds_read_b64_tr_b16 a[208:209], v10 offset:4096\n"
"ds_read_b64_tr_b16 a[210:211], v10 offset:4352\n"
"v_mfma_f32_16x16x32_bf16 v[136:139], a[204:207], a[224:227], v[136:139]\n"
"ds_read_b128 a[224:227], v15 offset:256\n"
"v_fma_f32 v2, v2, s61, v39\n"
"v_mfma_f32_16x16x32_bf16 v[68:71], a[192:195], a[228:231], v[68:71]\n"
"ds_read_b64_tr_b16 a[212:213], v11 offset:4096\n"
"ds_read_b64_tr_b16 a[214:215], v11 offset:4352\n"
"v_fma_f32 v3, v3, s61, v39\n"
"v_mfma_f32_16x16x32_bf16 v[92:95], a[196:199], a[228:231], v[92:95]\n"
"v_fma_f32 v4, v4, s61, v39\n"
"v_fma_f32 v5, v5, s61, v39\n"
"v_mfma_f32_16x16x32_bf16 v[116:119], a[200:203], a[228:231], v[116:119]\n"
"ds_read_b64_tr_b16 a[216:217], v10 offset:6144\n"
"ds_read_b64_tr_b16 a[218:219], v10 offset:6400\n"
"v_mfma_f32_16x16x32_bf16 v[140:143], a[204:207], a[228:231], v[140:143]\n"
"ds_read_b128 a[228:231], v15 offset:16640\n"
"v_mul_f32 v2, v188, v2\n"
"v_mfma_f32_16x16x32_bf16 v[72:75], a[192:195], a[232:235], v[72:75]\n"
"ds_read_b64_tr_b16 a[220:221], v11 offset:6144\n"
"ds_read_b64_tr_b16 a[222:223], v11 offset:6400\n"
"v_mul_f32 v3, v189, v3\n"
"v_mfma_f32_16x16x32_bf16 v[96:99], a[196:199], a[232:235], v[96:99]\n"
"v_mul_f32 v4, v190, v4\n"
"v_mul_f32 v5, v191, v5\n"
"v_mfma_f32_16x16x32_bf16 v[120:123], a[200:203], a[232:235], v[120:123]\n"
"s_add_u32 s50, s62, 917504\n"
"s_add_u32 m0, s49, 0\n"
"v_mfma_f32_16x16x32_bf16 v[144:147], a[204:207], a[232:235], v[144:147]\n"
"buffer_load_dwordx4 v22, s[24:27], s50 offen lds\n"
"buffer_load_dwordx4 v23, s[24:27], s50 offen offset:1024 lds\n"
"ds_read_b128 a[232:235], v15 offset:33024\n"
"s_waitcnt lgkmcnt(11)\n"
"v_mfma_f32_16x16x32_bf16 v[76:79], a[192:195], a[236:239], v[76:79]\n"
"s_add_u32 m0, s49, 2048\n"
"s_nop 0\n"
"buffer_load_dwordx4 v22, s[84:87], s50 offen lds\n"
"v_mfma_f32_16x16x32_bf16 v[100:103], a[196:199], a[236:239], v[100:103]\n"
"buffer_load_dwordx4 v23, s[84:87], s50 offen offset:1024 lds\n"
"v_exp_f32 v2, v2\n"
"v_mfma_f32_16x16x32_bf16 v[124:127], a[200:203], a[236:239], v[124:127]\n"
"v_exp_f32 v3, v3\n"
"v_mfma_f32_16x16x32_bf16 v[148:151], a[204:207], a[236:239], v[148:151]\n"
"ds_read_b128 a[236:239], v15 offset:49408\n"
"v_exp_f32 v4, v4\n"
"v_mfma_f32_16x16x32_bf16 v[80:83], a[192:195], a[240:243], v[80:83]\n"
"v_exp_f32 v5, v5\n"
"v_mfma_f32_16x16x32_bf16 v[104:107], a[196:199], a[240:243], v[104:107]\n"
"v_mul_f32 v236, v188, v236\n"
"v_mul_f32 v237, v189, v237\n"
"v_mfma_f32_16x16x32_bf16 v[128:131], a[200:203], a[240:243], v[128:131]\n"
"v_mul_f32 v238, v190, v238\n"
"v_mul_f32 v239, v191, v239\n"
"v_mfma_f32_16x16x32_bf16 v[152:155], a[204:207], a[240:243], v[152:155]\n"
"ds_read_b128 a[240:243], v19 offset:256\n"
"v_add_f32 v2, 1.0, v2\n"
"v_add_f32 v3, 1.0, v3\n"
"v_mfma_f32_16x16x32_bf16 v[84:87], a[192:195], a[244:247], v[84:87]\n"
"v_add_f32 v4, 1.0, v4\n"
"v_add_f32 v5, 1.0, v5\n"
"v_mfma_f32_16x16x32_bf16 v[108:111], a[196:199], a[244:247], v[108:111]\n"
"v_rcp_f32 v2, v2\n"
"v_mfma_f32_16x16x32_bf16 v[132:135], a[200:203], a[244:247], v[132:135]\n"
"v_rcp_f32 v3, v3\n"
"v_mfma_f32_16x16x32_bf16 v[156:159], a[204:207], a[244:247], v[156:159]\n"
"ds_read_b128 a[244:247], v19 offset:16640\n"
"v_rcp_f32 v4, v4\n"
"s_waitcnt lgkmcnt(3)\n"
"v_mfma_f32_16x16x32_bf16 v[64:67], a[208:211], a[224:227], v[64:67]\n"
"v_rcp_f32 v5, v5\n"
"v_mfma_f32_16x16x32_bf16 v[88:91], a[212:215], a[224:227], v[88:91]\n"
"v_mul_f32 v236, v236, v2\n"
"s_waitcnt vmcnt(8)\n"
"v_mfma_f32_16x16x32_bf16 v[112:115], a[216:219], a[224:227], v[112:115]\n"
"ds_read_b64_tr_b16 a[192:193], v10 offset:8192\n"
"ds_read_b64_tr_b16 a[194:195], v10 offset:8448\n"
"v_mul_f32 v237, v237, v3\n"
"v_mfma_f32_16x16x32_bf16 v[136:139], a[220:223], a[224:227], v[136:139]\n"
"ds_read_b128 a[224:227], v16 offset:256\n"
"v_mul_f32 v238, v238, v4\n"
"v_mfma_f32_16x16x32_bf16 v[68:71], a[208:211], a[228:231], v[68:71]\n"
"ds_read_b64_tr_b16 a[196:197], v11 offset:8192\n"
"ds_read_b64_tr_b16 a[198:199], v11 offset:8448\n"
"v_mfma_f32_16x16x32_bf16 v[92:95], a[212:215], a[228:231], v[92:95]\n"
"v_mul_f32 v239, v239, v5\n"
"v_cvt_pk_bf16_f32 v188, v236, v237\n"
"v_mfma_f32_16x16x32_bf16 v[116:119], a[216:219], a[228:231], v[116:119]\n"
"ds_read_b64_tr_b16 a[200:201], v10 offset:10240\n"
"ds_read_b64_tr_b16 a[202:203], v10 offset:10496\n"
"v_cvt_pk_bf16_f32 v189, v238, v239\n"
"v_mfma_f32_16x16x32_bf16 v[140:143], a[220:223], a[228:231], v[140:143]\n"
"ds_read_b128 a[228:231], v16 offset:16640\n"
"v_accvgpr_write_b32 a78, v188\n"
"v_mfma_f32_16x16x32_bf16 v[72:75], a[208:211], a[232:235], v[72:75]\n"
"ds_read_b64_tr_b16 a[204:205], v11 offset:10240\n"
"ds_read_b64_tr_b16 a[206:207], v11 offset:10496\n"
"v_mfma_f32_16x16x32_bf16 v[96:99], a[212:215], a[232:235], v[96:99]\n"
"v_accvgpr_write_b32 a79, v189\n"
"v_mul_f32 v2, v168, v168\n"
"v_mfma_f32_16x16x32_bf16 v[120:123], a[216:219], a[232:235], v[120:123]\n"
"s_add_u32 s50, s62, 1032192\n"
"s_add_u32 m0, s49, 4096\n"
"v_mfma_f32_16x16x32_bf16 v[144:147], a[220:223], a[232:235], v[144:147]\n"
"buffer_load_dwordx4 v22, s[24:27], s50 offen lds\n"
"buffer_load_dwordx4 v23, s[24:27], s50 offen offset:1024 lds\n"
"ds_read_b128 a[232:235], v16 offset:33024\n"
"s_waitcnt lgkmcnt(11)\n"
"v_mfma_f32_16x16x32_bf16 v[76:79], a[208:211], a[236:239], v[76:79]\n"
"s_add_u32 m0, s49, 6144\n"
"s_nop 0\n"
"buffer_load_dwordx4 v22, s[84:87], s50 offen lds\n"
"v_mul_f32 v3, v169, v169\n"
"v_mfma_f32_16x16x32_bf16 v[100:103], a[212:215], a[236:239], v[100:103]\n"
"buffer_load_dwordx4 v23, s[84:87], s50 offen offset:1024 lds\n"
"v_mul_f32 v4, v170, v170\n"
"v_mfma_f32_16x16x32_bf16 v[124:127], a[216:219], a[236:239], v[124:127]\n"
"v_mul_f32 v5, v171, v171\n"
"v_fma_f32 v2, v2, s61, v39\n"
"v_mfma_f32_16x16x32_bf16 v[148:151], a[220:223], a[236:239], v[148:151]\n"
"ds_read_b128 a[236:239], v16 offset:49408\n"
"v_fma_f32 v3, v3, s61, v39\n"
"v_mfma_f32_16x16x32_bf16 v[80:83], a[208:211], a[240:243], v[80:83]\n"
"v_fma_f32 v4, v4, s61, v39\n"
"v_fma_f32 v5, v5, s61, v39\n"
"v_mfma_f32_16x16x32_bf16 v[104:107], a[212:215], a[240:243], v[104:107]\n"
"v_mul_f32 v2, v168, v2\n"
"v_mul_f32 v3, v169, v3\n"
"v_mul_f32 v4, v170, v4\n"
"v_mfma_f32_16x16x32_bf16 v[128:131], a[216:219], a[240:243], v[128:131]\n"
"v_mul_f32 v5, v171, v5\n"
"v_mfma_f32_16x16x32_bf16 v[152:155], a[220:223], a[240:243], v[152:155]\n"
"ds_read_b128 a[240:243], v20 offset:256\n"
"v_exp_f32 v2, v2\n"
"v_mfma_f32_16x16x32_bf16 v[84:87], a[208:211], a[244:247], v[84:87]\n"
"v_exp_f32 v3, v3\n"
"v_mfma_f32_16x16x32_bf16 v[108:111], a[212:215], a[244:247], v[108:111]\n"
"v_exp_f32 v4, v4\n"
"v_mfma_f32_16x16x32_bf16 v[132:135], a[216:219], a[244:247], v[132:135]\n"
"v_exp_f32 v5, v5\n"
"v_mul_f32 v216, v168, v216\n"
"v_mfma_f32_16x16x32_bf16 v[156:159], a[220:223], a[244:247], v[156:159]\n"
"ds_read_b128 a[244:247], v20 offset:16640\n"
"v_mul_f32 v217, v169, v217\n"
"s_waitcnt lgkmcnt(3)\n"
"v_mfma_f32_16x16x32_bf16 v[64:67], a[192:195], a[224:227], v[64:67]\n"
"v_mul_f32 v218, v170, v218\n"
"v_mul_f32 v219, v171, v219\n"
"v_mfma_f32_16x16x32_bf16 v[88:91], a[196:199], a[224:227], v[88:91]\n"
"v_add_f32 v2, 1.0, v2\n"
"s_waitcnt vmcnt(8)\n"
"v_mfma_f32_16x16x32_bf16 v[112:115], a[200:203], a[224:227], v[112:115]\n"
"ds_read_b64_tr_b16 a[208:209], v10 offset:12288\n"
"ds_read_b64_tr_b16 a[210:211], v10 offset:12544\n"
"v_add_f32 v3, 1.0, v3\n"
"v_mfma_f32_16x16x32_bf16 v[136:139], a[204:207], a[224:227], v[136:139]\n"
"ds_read_b128 a[224:227], v17 offset:256\n"
"v_add_f32 v4, 1.0, v4\n"
"v_mfma_f32_16x16x32_bf16 v[68:71], a[192:195], a[228:231], v[68:71]\n"
"ds_read_b64_tr_b16 a[212:213], v11 offset:12288\n"
"ds_read_b64_tr_b16 a[214:215], v11 offset:12544\n"
"v_mfma_f32_16x16x32_bf16 v[92:95], a[196:199], a[228:231], v[92:95]\n"
"v_add_f32 v5, 1.0, v5\n"
"v_mfma_f32_16x16x32_bf16 v[116:119], a[200:203], a[228:231], v[116:119]\n"
"ds_read_b64_tr_b16 a[216:217], v10 offset:14336\n"
"ds_read_b64_tr_b16 a[218:219], v10 offset:14592\n"
"v_mfma_f32_16x16x32_bf16 v[140:143], a[204:207], a[228:231], v[140:143]\n"
"ds_read_b128 a[228:231], v17 offset:16640\n"
"v_rcp_f32 v2, v2\n"
"v_mfma_f32_16x16x32_bf16 v[72:75], a[192:195], a[232:235], v[72:75]\n"
"ds_read_b64_tr_b16 a[220:221], v11 offset:14336\n"
"ds_read_b64_tr_b16 a[222:223], v11 offset:14592\n"
"v_mfma_f32_16x16x32_bf16 v[96:99], a[196:199], a[232:235], v[96:99]\n"
"v_rcp_f32 v3, v3\n"
"v_mfma_f32_16x16x32_bf16 v[120:123], a[200:203], a[232:235], v[120:123]\n"
"s_add_u32 s50, s62, 1146880\n"
"s_add_u32 m0, s49, 8192\n"
"v_mfma_f32_16x16x32_bf16 v[144:147], a[204:207], a[232:235], v[144:147]\n"
"buffer_load_dwordx4 v22, s[24:27], s50 offen lds\n"
"buffer_load_dwordx4 v23, s[24:27], s50 offen offset:1024 lds\n"
"ds_read_b128 a[232:235], v17 offset:33024\n"
"s_waitcnt lgkmcnt(11)\n"
"v_mfma_f32_16x16x32_bf16 v[76:79], a[192:195], a[236:239], v[76:79]\n"
"s_add_u32 m0, s49, 10240\n"
"s_nop 0\n"
"buffer_load_dwordx4 v22, s[84:87], s50 offen lds\n"
"v_mfma_f32_16x16x32_bf16 v[100:103], a[196:199], a[236:239], v[100:103]\n"
"buffer_load_dwordx4 v23, s[84:87], s50 offen offset:1024 lds\n"
"v_rcp_f32 v4, v4\n"
"v_mfma_f32_16x16x32_bf16 v[124:127], a[200:203], a[236:239], v[124:127]\n"
"v_rcp_f32 v5, v5\n"
"v_mfma_f32_16x16x32_bf16 v[148:151], a[204:207], a[236:239], v[148:151]\n"
"ds_read_b128 a[236:239], v17 offset:49408\n"
"v_mul_f32 v216, v216, v2\n"
"v_mul_f32 v217, v217, v3\n"
"v_mfma_f32_16x16x32_bf16 v[80:83], a[192:195], a[240:243], v[80:83]\n"
"v_mul_f32 v218, v218, v4\n"
"v_mul_f32 v219, v219, v5\n"
"v_mfma_f32_16x16x32_bf16 v[104:107], a[196:199], a[240:243], v[104:107]\n"
"v_cvt_pk_bf16_f32 v168, v216, v217\n"
"v_cvt_pk_bf16_f32 v169, v218, v219\n"
"v_mfma_f32_16x16x32_bf16 v[128:131], a[200:203], a[240:243], v[128:131]\n"
"v_accvgpr_write_b32 a80, v168\n"
"v_accvgpr_write_b32 a81, v169\n"
"v_mfma_f32_16x16x32_bf16 v[152:155], a[204:207], a[240:243], v[152:155]\n"
"ds_read_b128 a[240:243], v21 offset:256\n"
"v_mul_f32 v2, v192, v192\n"
"v_mfma_f32_16x16x32_bf16 v[84:87], a[192:195], a[244:247], v[84:87]\n"
"v_mul_f32 v3, v193, v193\n"
"v_mul_f32 v4, v194, v194\n"
"v_mfma_f32_16x16x32_bf16 v[108:111], a[196:199], a[244:247], v[108:111]\n"
"v_mul_f32 v5, v195, v195\n"
"v_fma_f32 v2, v2, s61, v39\n"
"v_fma_f32 v3, v3, s61, v39\n"
"v_mfma_f32_16x16x32_bf16 v[132:135], a[200:203], a[244:247], v[132:135]\n"
"v_fma_f32 v4, v4, s61, v39\n"
"v_fma_f32 v5, v5, s61, v39\n"
"v_mfma_f32_16x16x32_bf16 v[156:159], a[204:207], a[244:247], v[156:159]\n"
"ds_read_b128 a[244:247], v21 offset:16640\n"
"v_mul_f32 v2, v192, v2\n"
"s_waitcnt lgkmcnt(3)\n"
"v_mfma_f32_16x16x32_bf16 v[64:67], a[208:211], a[224:227], v[64:67]\n"
"v_mul_f32 v3, v193, v3\n"
"v_mul_f32 v4, v194, v4\n"
"v_mfma_f32_16x16x32_bf16 v[88:91], a[212:215], a[224:227], v[88:91]\n"
"v_mul_f32 v5, v195, v5\n"
"s_waitcnt vmcnt(8)\n"
"v_mfma_f32_16x16x32_bf16 v[112:115], a[216:219], a[224:227], v[112:115]\n"
"ds_read_b64_tr_b16 a[192:193], v10 offset:0\n"
"ds_read_b64_tr_b16 a[194:195], v10 offset:256\n"
"v_mfma_f32_16x16x32_bf16 v[136:139], a[220:223], a[224:227], v[136:139]\n"
"ds_read_b128 a[224:227], v14 offset:512\n"
"v_exp_f32 v2, v2\n"
"v_mfma_f32_16x16x32_bf16 v[68:71], a[208:211], a[228:231], v[68:71]\n"
"ds_read_b64_tr_b16 a[196:197], v11 offset:0\n"
"ds_read_b64_tr_b16 a[198:199], v11 offset:256\n"
"v_mfma_f32_16x16x32_bf16 v[92:95], a[212:215], a[228:231], v[92:95]\n"
"v_exp_f32 v3, v3\n"
"v_mfma_f32_16x16x32_bf16 v[116:119], a[216:219], a[228:231], v[116:119]\n"
"ds_read_b64_tr_b16 a[200:201], v10 offset:2048\n"
"ds_read_b64_tr_b16 a[202:203], v10 offset:2304\n"
"v_mfma_f32_16x16x32_bf16 v[140:143], a[220:223], a[228:231], v[140:143]\n"
"ds_read_b128 a[228:231], v14 offset:16896\n"
"v_exp_f32 v4, v4\n"
"v_mfma_f32_16x16x32_bf16 v[72:75], a[208:211], a[232:235], v[72:75]\n"
"ds_read_b64_tr_b16 a[204:205], v11 offset:2048\n"
"ds_read_b64_tr_b16 a[206:207], v11 offset:2304\n"
"v_mfma_f32_16x16x32_bf16 v[96:99], a[212:215], a[232:235], v[96:99]\n"
"v_exp_f32 v5, v5\n"
"v_mul_f32 v240, v192, v240\n"
"v_mfma_f32_16x16x32_bf16 v[120:123], a[216:219], a[232:235], v[120:123]\n"
"s_add_u32 s50, s62, 1261568\n"
"s_add_u32 m0, s49, 12288\n"
"v_mul_f32 v241, v193, v241\n"
"v_mfma_f32_16x16x32_bf16 v[144:147], a[220:223], a[232:235], v[144:147]\n"
"buffer_load_dwordx4 v22, s[24:27], s50 offen lds\n"
"buffer_load_dwordx4 v23, s[24:27], s50 offen offset:1024 lds\n"
"ds_read_b128 a[232:235], v14 offset:33280\n"
"s_waitcnt lgkmcnt(11)\n"
"v_mfma_f32_16x16x32_bf16 v[76:79], a[208:211], a[236:239], v[76:79]\n"
"s_add_u32 m0, s49, 14336\n"
"s_nop 0\n"
"buffer_load_dwordx4 v22, s[84:87], s50 offen lds\n"
"v_mfma_f32_16x16x32_bf16 v[100:103], a[212:215], a[236:239], v[100:103]\n"
"buffer_load_dwordx4 v23, s[84:87], s50 offen offset:1024 lds\n"
"v_mul_f32 v242, v194, v242\n"
"v_mul_f32 v243, v195, v243\n"
"v_mfma_f32_16x16x32_bf16 v[124:127], a[216:219], a[236:239], v[124:127]\n"
"v_add_f32 v2, 1.0, v2\n"
"v_add_f32 v3, 1.0, v3\n"
"v_mfma_f32_16x16x32_bf16 v[148:151], a[220:223], a[236:239], v[148:151]\n"
"ds_read_b128 a[236:239], v14 offset:49664\n"
"v_add_f32 v4, 1.0, v4\n"
"v_mfma_f32_16x16x32_bf16 v[80:83], a[208:211], a[240:243], v[80:83]\n"
"v_add_f32 v5, 1.0, v5\n"
"v_rcp_f32 v2, v2\n"
"v_mfma_f32_16x16x32_bf16 v[104:107], a[212:215], a[240:243], v[104:107]\n"
"v_rcp_f32 v3, v3\n"
"v_mfma_f32_16x16x32_bf16 v[128:131], a[216:219], a[240:243], v[128:131]\n"
"v_rcp_f32 v4, v4\n"
"v_mfma_f32_16x16x32_bf16 v[152:155], a[220:223], a[240:243], v[152:155]\n"
"ds_read_b128 a[240:243], v18 offset:512\n"
"v_rcp_f32 v5, v5\n"
"v_mfma_f32_16x16x32_bf16 v[84:87], a[208:211], a[244:247], v[84:87]\n"
"v_mul_f32 v240, v240, v2\n"
"v_mul_f32 v241, v241, v3\n"
"v_mfma_f32_16x16x32_bf16 v[108:111], a[212:215], a[244:247], v[108:111]\n"
"v_mul_f32 v242, v242, v4\n"
"v_mul_f32 v243, v243, v5\n"
"v_mfma_f32_16x16x32_bf16 v[132:135], a[216:219], a[244:247], v[132:135]\n"
"v_cvt_pk_bf16_f32 v192, v240, v241\n"
"v_cvt_pk_bf16_f32 v193, v242, v243\n"
"v_accvgpr_write_b32 a82, v192\n"
"v_mfma_f32_16x16x32_bf16 v[156:159], a[220:223], a[244:247], v[156:159]\n"
"ds_read_b128 a[244:247], v18 offset:16896\n"
"v_accvgpr_write_b32 a83, v193\n"
"s_waitcnt lgkmcnt(3)\n"
"v_mfma_f32_16x16x32_bf16 v[64:67], a[192:195], a[224:227], v[64:67]\n"
"v_mul_f32 v2, v172, v172\n"
"v_mul_f32 v3, v173, v173\n"
"v_mfma_f32_16x16x32_bf16 v[88:91], a[196:199], a[224:227], v[88:91]\n"
"v_mul_f32 v4, v174, v174\n"
"v_mul_f32 v5, v175, v175\n"
"s_waitcnt vmcnt(8)\n"
"v_mfma_f32_16x16x32_bf16 v[112:115], a[200:203], a[224:227], v[112:115]\n"
"ds_read_b64_tr_b16 a[208:209], v10 offset:4096\n"
"ds_read_b64_tr_b16 a[210:211], v10 offset:4352\n"
"v_mfma_f32_16x16x32_bf16 v[136:139], a[204:207], a[224:227], v[136:139]\n"
"ds_read_b128 a[224:227], v15 offset:512\n"
"v_fma_f32 v2, v2, s61, v39\n"
"v_mfma_f32_16x16x32_bf16 v[68:71], a[192:195], a[228:231], v[68:71]\n"
"ds_read_b64_tr_b16 a[212:213], v11 offset:4096\n"
"ds_read_b64_tr_b16 a[214:215], v11 offset:4352\n"
"v_fma_f32 v3, v3, s61, v39\n"
"v_mfma_f32_16x16x32_bf16 v[92:95], a[196:199], a[228:231], v[92:95]\n"
"v_fma_f32 v4, v4, s61, v39\n"
"v_fma_f32 v5, v5, s61, v39\n"
"v_mfma_f32_16x16x32_bf16 v[116:119], a[200:203], a[228:231], v[116:119]\n"
"ds_read_b64_tr_b16 a[216:217], v10 offset:6144\n"
"ds_read_b64_tr_b16 a[218:219], v10 offset:6400\n"
"v_mfma_f32_16x16x32_bf16 v[140:143], a[204:207], a[228:231], v[140:143]\n"
"ds_read_b128 a[228:231], v15 offset:16896\n"
"v_mul_f32 v2, v172, v2\n"
"v_mfma_f32_16x16x32_bf16 v[72:75], a[192:195], a[232:235], v[72:75]\n"
"ds_read_b64_tr_b16 a[220:221], v11 offset:6144\n"
"ds_read_b64_tr_b16 a[222:223], v11 offset:6400\n"
"v_mul_f32 v3, v173, v3\n"
"v_mfma_f32_16x16x32_bf16 v[96:99], a[196:199], a[232:235], v[96:99]\n"
"v_mul_f32 v4, v174, v4\n"
"v_mul_f32 v5, v175, v5\n"
"v_mfma_f32_16x16x32_bf16 v[120:123], a[200:203], a[232:235], v[120:123]\n"
"s_add_u32 s50, s62, 1376256\n"
"s_add_u32 m0, s49, 0\n"
"v_mfma_f32_16x16x32_bf16 v[144:147], a[204:207], a[232:235], v[144:147]\n"
"buffer_load_dwordx4 v22, s[24:27], s50 offen lds\n"
"buffer_load_dwordx4 v23, s[24:27], s50 offen offset:1024 lds\n"
"ds_read_b128 a[232:235], v15 offset:33280\n"
"s_waitcnt lgkmcnt(11)\n"
"v_mfma_f32_16x16x32_bf16 v[76:79], a[192:195], a[236:239], v[76:79]\n"
"s_add_u32 m0, s49, 2048\n"
"s_nop 0\n"
"buffer_load_dwordx4 v22, s[84:87], s50 offen lds\n"
"v_mfma_f32_16x16x32_bf16 v[100:103], a[196:199], a[236:239], v[100:103]\n"
"buffer_load_dwordx4 v23, s[84:87], s50 offen offset:1024 lds\n"
"v_exp_f32 v2, v2\n"
"v_mfma_f32_16x16x32_bf16 v[124:127], a[200:203], a[236:239], v[124:127]\n"
"v_exp_f32 v3, v3\n"
"v_mfma_f32_16x16x32_bf16 v[148:151], a[204:207], a[236:239], v[148:151]\n"
"ds_read_b128 a[236:239], v15 offset:49664\n"
"v_exp_f32 v4, v4\n"
"v_mfma_f32_16x16x32_bf16 v[80:83], a[192:195], a[240:243], v[80:83]\n"
"v_exp_f32 v5, v5\n"
"v_mfma_f32_16x16x32_bf16 v[104:107], a[196:199], a[240:243], v[104:107]\n"
"v_mul_f32 v220, v172, v220\n"
"v_mul_f32 v221, v173, v221\n"
"v_mfma_f32_16x16x32_bf16 v[128:131], a[200:203], a[240:243], v[128:131]\n"
"v_mul_f32 v222, v174, v222\n"
"v_mul_f32 v223, v175, v223\n"
"v_mfma_f32_16x16x32_bf16 v[152:155], a[204:207], a[240:243], v[152:155]\n"
"ds_read_b128 a[240:243], v19 offset:512\n"
"v_add_f32 v2, 1.0, v2\n"
"v_add_f32 v3, 1.0, v3\n"
"v_mfma_f32_16x16x32_bf16 v[84:87], a[192:195], a[244:247], v[84:87]\n"
"v_add_f32 v4, 1.0, v4\n"
"v_add_f32 v5, 1.0, v5\n"
"v_mfma_f32_16x16x32_bf16 v[108:111], a[196:199], a[244:247], v[108:111]\n"
"v_rcp_f32 v2, v2\n"
"v_mfma_f32_16x16x32_bf16 v[132:135], a[200:203], a[244:247], v[132:135]\n"
"v_rcp_f32 v3, v3\n"
"v_mfma_f32_16x16x32_bf16 v[156:159], a[204:207], a[244:247], v[156:159]\n"
"ds_read_b128 a[244:247], v19 offset:16896\n"
"v_rcp_f32 v4, v4\n"
"s_waitcnt lgkmcnt(3)\n"
"v_mfma_f32_16x16x32_bf16 v[64:67], a[208:211], a[224:227], v[64:67]\n"
"v_rcp_f32 v5, v5\n"
"v_mfma_f32_16x16x32_bf16 v[88:91], a[212:215], a[224:227], v[88:91]\n"
"v_mul_f32 v220, v220, v2\n"
"s_waitcnt vmcnt(8)\n"
"v_mfma_f32_16x16x32_bf16 v[112:115], a[216:219], a[224:227], v[112:115]\n"
"ds_read_b64_tr_b16 a[192:193], v10 offset:8192\n"
"ds_read_b64_tr_b16 a[194:195], v10 offset:8448\n"
"v_mul_f32 v221, v221, v3\n"
"v_mfma_f32_16x16x32_bf16 v[136:139], a[220:223], a[224:227], v[136:139]\n"
"ds_read_b128 a[224:227], v16 offset:512\n"
"v_mul_f32 v222, v222, v4\n"
"v_mfma_f32_16x16x32_bf16 v[68:71], a[208:211], a[228:231], v[68:71]\n"
"ds_read_b64_tr_b16 a[196:197], v11 offset:8192\n"
"ds_read_b64_tr_b16 a[198:199], v11 offset:8448\n"
"v_mfma_f32_16x16x32_bf16 v[92:95], a[212:215], a[228:231], v[92:95]\n"
"v_mul_f32 v223, v223, v5\n"
"v_cvt_pk_bf16_f32 v172, v220, v221\n"
"v_mfma_f32_16x16x32_bf16 v[116:119], a[216:219], a[228:231], v[116:119]\n"
"ds_read_b64_tr_b16 a[200:201], v10 offset:10240\n"
"ds_read_b64_tr_b16 a[202:203], v10 offset:10496\n"
"v_cvt_pk_bf16_f32 v173, v222, v223\n"
"v_mfma_f32_16x16x32_bf16 v[140:143], a[220:223], a[228:231], v[140:143]\n"
"ds_read_b128 a[228:231], v16 offset:16896\n"
"v_accvgpr_write_b32 a84, v172\n"
"v_mfma_f32_16x16x32_bf16 v[72:75], a[208:211], a[232:235], v[72:75]\n"
"ds_read_b64_tr_b16 a[204:205], v11 offset:10240\n"
"ds_read_b64_tr_b16 a[206:207], v11 offset:10496\n"
"v_mfma_f32_16x16x32_bf16 v[96:99], a[212:215], a[232:235], v[96:99]\n"
"v_accvgpr_write_b32 a85, v173\n"
"v_mul_f32 v2, v196, v196\n"
"v_mfma_f32_16x16x32_bf16 v[120:123], a[216:219], a[232:235], v[120:123]\n"
"s_add_u32 s50, s62, 1490944\n"
"s_add_u32 m0, s49, 4096\n"
"v_mfma_f32_16x16x32_bf16 v[144:147], a[220:223], a[232:235], v[144:147]\n"
"buffer_load_dwordx4 v22, s[24:27], s50 offen lds\n"
"buffer_load_dwordx4 v23, s[24:27], s50 offen offset:1024 lds\n"
"ds_read_b128 a[232:235], v16 offset:33280\n"
"s_waitcnt lgkmcnt(11)\n"
"v_mfma_f32_16x16x32_bf16 v[76:79], a[208:211], a[236:239], v[76:79]\n"
"s_add_u32 m0, s49, 6144\n"
"s_nop 0\n"
"buffer_load_dwordx4 v22, s[84:87], s50 offen lds\n"
"v_mul_f32 v3, v197, v197\n"
"v_mfma_f32_16x16x32_bf16 v[100:103], a[212:215], a[236:239], v[100:103]\n"
"buffer_load_dwordx4 v23, s[84:87], s50 offen offset:1024 lds\n"
"v_mul_f32 v4, v198, v198\n"
"v_mfma_f32_16x16x32_bf16 v[124:127], a[216:219], a[236:239], v[124:127]\n"
"v_mul_f32 v5, v199, v199\n"
"v_fma_f32 v2, v2, s61, v39\n"
"v_mfma_f32_16x16x32_bf16 v[148:151], a[220:223], a[236:239], v[148:151]\n"
"ds_read_b128 a[236:239], v16 offset:49664\n"
"v_fma_f32 v3, v3, s61, v39\n"
"v_mfma_f32_16x16x32_bf16 v[80:83], a[208:211], a[240:243], v[80:83]\n"
"v_fma_f32 v4, v4, s61, v39\n"
"v_fma_f32 v5, v5, s61, v39\n"
"v_mfma_f32_16x16x32_bf16 v[104:107], a[212:215], a[240:243], v[104:107]\n"
"v_mul_f32 v2, v196, v2\n"
"v_mul_f32 v3, v197, v3\n"
"v_mul_f32 v4, v198, v4\n"
"v_mfma_f32_16x16x32_bf16 v[128:131], a[216:219], a[240:243], v[128:131]\n"
"v_mul_f32 v5, v199, v5\n"
"v_mfma_f32_16x16x32_bf16 v[152:155], a[220:223], a[240:243], v[152:155]\n"
"ds_read_b128 a[240:243], v20 offset:512\n"
"v_exp_f32 v2, v2\n"
"v_mfma_f32_16x16x32_bf16 v[84:87], a[208:211], a[244:247], v[84:87]\n"
"v_exp_f32 v3, v3\n"
"v_mfma_f32_16x16x32_bf16 v[108:111], a[212:215], a[244:247], v[108:111]\n"
"v_exp_f32 v4, v4\n"
"v_mfma_f32_16x16x32_bf16 v[132:135], a[216:219], a[244:247], v[132:135]\n"
"v_exp_f32 v5, v5\n"
"v_mul_f32 v244, v196, v244\n"
"v_mfma_f32_16x16x32_bf16 v[156:159], a[220:223], a[244:247], v[156:159]\n"
"ds_read_b128 a[244:247], v20 offset:16896\n"
"v_mul_f32 v245, v197, v245\n"
"s_waitcnt lgkmcnt(3)\n"
"v_mfma_f32_16x16x32_bf16 v[64:67], a[192:195], a[224:227], v[64:67]\n"
"v_mul_f32 v246, v198, v246\n"
"v_mul_f32 v247, v199, v247\n"
"v_mfma_f32_16x16x32_bf16 v[88:91], a[196:199], a[224:227], v[88:91]\n"
"v_add_f32 v2, 1.0, v2\n"
"s_waitcnt vmcnt(8)\n"
"v_mfma_f32_16x16x32_bf16 v[112:115], a[200:203], a[224:227], v[112:115]\n"
"ds_read_b64_tr_b16 a[208:209], v10 offset:12288\n"
"ds_read_b64_tr_b16 a[210:211], v10 offset:12544\n"
"v_add_f32 v3, 1.0, v3\n"
"v_mfma_f32_16x16x32_bf16 v[136:139], a[204:207], a[224:227], v[136:139]\n"
"ds_read_b128 a[224:227], v17 offset:512\n"
"v_add_f32 v4, 1.0, v4\n"
"v_mfma_f32_16x16x32_bf16 v[68:71], a[192:195], a[228:231], v[68:71]\n"
"ds_read_b64_tr_b16 a[212:213], v11 offset:12288\n"
"ds_read_b64_tr_b16 a[214:215], v11 offset:12544\n"
"v_mfma_f32_16x16x32_bf16 v[92:95], a[196:199], a[228:231], v[92:95]\n"
"v_add_f32 v5, 1.0, v5\n"
"v_mfma_f32_16x16x32_bf16 v[116:119], a[200:203], a[228:231], v[116:119]\n"
"ds_read_b64_tr_b16 a[216:217], v10 offset:14336\n"
"ds_read_b64_tr_b16 a[218:219], v10 offset:14592\n"
"v_mfma_f32_16x16x32_bf16 v[140:143], a[204:207], a[228:231], v[140:143]\n"
"ds_read_b128 a[228:231], v17 offset:16896\n"
"v_rcp_f32 v2, v2\n"
"v_mfma_f32_16x16x32_bf16 v[72:75], a[192:195], a[232:235], v[72:75]\n"
"ds_read_b64_tr_b16 a[220:221], v11 offset:14336\n"
"ds_read_b64_tr_b16 a[222:223], v11 offset:14592\n"
"v_mfma_f32_16x16x32_bf16 v[96:99], a[196:199], a[232:235], v[96:99]\n"
"v_rcp_f32 v3, v3\n"
"v_mfma_f32_16x16x32_bf16 v[120:123], a[200:203], a[232:235], v[120:123]\n"
"s_add_u32 s50, s62, 1605632\n"
"s_add_u32 m0, s49, 8192\n"
"v_mfma_f32_16x16x32_bf16 v[144:147], a[204:207], a[232:235], v[144:147]\n"
"buffer_load_dwordx4 v22, s[24:27], s50 offen lds\n"
"buffer_load_dwordx4 v23, s[24:27], s50 offen offset:1024 lds\n"
"ds_read_b128 a[232:235], v17 offset:33280\n"
"s_waitcnt lgkmcnt(11)\n"
"v_mfma_f32_16x16x32_bf16 v[76:79], a[192:195], a[236:239], v[76:79]\n"
"s_add_u32 m0, s49, 10240\n"
"s_nop 0\n"
"buffer_load_dwordx4 v22, s[84:87], s50 offen lds\n"
"v_mfma_f32_16x16x32_bf16 v[100:103], a[196:199], a[236:239], v[100:103]\n"
"buffer_load_dwordx4 v23, s[84:87], s50 offen offset:1024 lds\n"
"v_rcp_f32 v4, v4\n"
"v_mfma_f32_16x16x32_bf16 v[124:127], a[200:203], a[236:239], v[124:127]\n"
"v_rcp_f32 v5, v5\n"
"v_mfma_f32_16x16x32_bf16 v[148:151], a[204:207], a[236:239], v[148:151]\n"
"ds_read_b128 a[236:239], v17 offset:49664\n"
"v_mul_f32 v244, v244, v2\n"
"v_mul_f32 v245, v245, v3\n"
"v_mfma_f32_16x16x32_bf16 v[80:83], a[192:195], a[240:243], v[80:83]\n"
"v_mul_f32 v246, v246, v4\n"
"v_mul_f32 v247, v247, v5\n"
"v_mfma_f32_16x16x32_bf16 v[104:107], a[196:199], a[240:243], v[104:107]\n"
"v_cvt_pk_bf16_f32 v196, v244, v245\n"
"v_cvt_pk_bf16_f32 v197, v246, v247\n"
"v_mfma_f32_16x16x32_bf16 v[128:131], a[200:203], a[240:243], v[128:131]\n"
"v_accvgpr_write_b32 a86, v196\n"
"v_accvgpr_write_b32 a87, v197\n"
"v_mfma_f32_16x16x32_bf16 v[152:155], a[204:207], a[240:243], v[152:155]\n"
"ds_read_b128 a[240:243], v21 offset:512\n"
"v_mul_f32 v2, v176, v176\n"
"v_mfma_f32_16x16x32_bf16 v[84:87], a[192:195], a[244:247], v[84:87]\n"
"v_mul_f32 v3, v177, v177\n"
"v_mul_f32 v4, v178, v178\n"
"v_mfma_f32_16x16x32_bf16 v[108:111], a[196:199], a[244:247], v[108:111]\n"
"v_mul_f32 v5, v179, v179\n"
"v_fma_f32 v2, v2, s61, v39\n"
"v_fma_f32 v3, v3, s61, v39\n"
"v_mfma_f32_16x16x32_bf16 v[132:135], a[200:203], a[244:247], v[132:135]\n"
"v_fma_f32 v4, v4, s61, v39\n"
"v_fma_f32 v5, v5, s61, v39\n"
"v_mfma_f32_16x16x32_bf16 v[156:159], a[204:207], a[244:247], v[156:159]\n"
"ds_read_b128 a[244:247], v21 offset:16896\n"
"v_mul_f32 v2, v176, v2\n"
"s_waitcnt lgkmcnt(3)\n"
"v_mfma_f32_16x16x32_bf16 v[64:67], a[208:211], a[224:227], v[64:67]\n"
"v_mul_f32 v3, v177, v3\n"
"v_mul_f32 v4, v178, v4\n"
"v_mfma_f32_16x16x32_bf16 v[88:91], a[212:215], a[224:227], v[88:91]\n"
"v_mul_f32 v5, v179, v5\n"
"s_waitcnt vmcnt(8)\n"
"v_mfma_f32_16x16x32_bf16 v[112:115], a[216:219], a[224:227], v[112:115]\n"
"ds_read_b64_tr_b16 a[192:193], v10 offset:0\n"
"ds_read_b64_tr_b16 a[194:195], v10 offset:256\n"
"v_mfma_f32_16x16x32_bf16 v[136:139], a[220:223], a[224:227], v[136:139]\n"
"ds_read_b128 a[224:227], v14 offset:768\n"
"v_exp_f32 v2, v2\n"
"v_mfma_f32_16x16x32_bf16 v[68:71], a[208:211], a[228:231], v[68:71]\n"
"ds_read_b64_tr_b16 a[196:197], v11 offset:0\n"
"ds_read_b64_tr_b16 a[198:199], v11 offset:256\n"
"v_mfma_f32_16x16x32_bf16 v[92:95], a[212:215], a[228:231], v[92:95]\n"
"v_exp_f32 v3, v3\n"
"v_mfma_f32_16x16x32_bf16 v[116:119], a[216:219], a[228:231], v[116:119]\n"
"ds_read_b64_tr_b16 a[200:201], v10 offset:2048\n"
"ds_read_b64_tr_b16 a[202:203], v10 offset:2304\n"
"v_mfma_f32_16x16x32_bf16 v[140:143], a[220:223], a[228:231], v[140:143]\n"
"ds_read_b128 a[228:231], v14 offset:17152\n"
"v_exp_f32 v4, v4\n"
"v_mfma_f32_16x16x32_bf16 v[72:75], a[208:211], a[232:235], v[72:75]\n"
"ds_read_b64_tr_b16 a[204:205], v11 offset:2048\n"
"ds_read_b64_tr_b16 a[206:207], v11 offset:2304\n"
"v_mfma_f32_16x16x32_bf16 v[96:99], a[212:215], a[232:235], v[96:99]\n"
"v_exp_f32 v5, v5\n"
"v_mul_f32 v224, v176, v224\n"
"v_mfma_f32_16x16x32_bf16 v[120:123], a[216:219], a[232:235], v[120:123]\n"
"s_add_u32 s50, s62, 1720320\n"
"s_add_u32 m0, s49, 12288\n"
"v_mul_f32 v225, v177, v225\n"
"v_mfma_f32_16x16x32_bf16 v[144:147], a[220:223], a[232:235], v[144:147]\n"
"buffer_load_dwordx4 v22, s[24:27], s50 offen lds\n"
"buffer_load_dwordx4 v23, s[24:27], s50 offen offset:1024 lds\n"
"ds_read_b128 a[232:235], v14 offset:33536\n"
"s_waitcnt lgkmcnt(11)\n"
"v_mfma_f32_16x16x32_bf16 v[76:79], a[208:211], a[236:239], v[76:79]\n"
"s_add_u32 m0, s49, 14336\n"
"s_nop 0\n"
"buffer_load_dwordx4 v22, s[84:87], s50 offen lds\n"
"v_mfma_f32_16x16x32_bf16 v[100:103], a[212:215], a[236:239], v[100:103]\n"
"buffer_load_dwordx4 v23, s[84:87], s50 offen offset:1024 lds\n"
"v_mul_f32 v226, v178, v226\n"
"v_mul_f32 v227, v179, v227\n"
"v_mfma_f32_16x16x32_bf16 v[124:127], a[216:219], a[236:239], v[124:127]\n"
"v_add_f32 v2, 1.0, v2\n"
"v_add_f32 v3, 1.0, v3\n"
"v_mfma_f32_16x16x32_bf16 v[148:151], a[220:223], a[236:239], v[148:151]\n"
"ds_read_b128 a[236:239], v14 offset:49920\n"
"v_add_f32 v4, 1.0, v4\n"
"v_mfma_f32_16x16x32_bf16 v[80:83], a[208:211], a[240:243], v[80:83]\n"
"v_add_f32 v5, 1.0, v5\n"
"v_rcp_f32 v2, v2\n"
"v_mfma_f32_16x16x32_bf16 v[104:107], a[212:215], a[240:243], v[104:107]\n"
"v_rcp_f32 v3, v3\n"
"v_mfma_f32_16x16x32_bf16 v[128:131], a[216:219], a[240:243], v[128:131]\n"
"v_rcp_f32 v4, v4\n"
"v_mfma_f32_16x16x32_bf16 v[152:155], a[220:223], a[240:243], v[152:155]\n"
"ds_read_b128 a[240:243], v18 offset:768\n"
"v_rcp_f32 v5, v5\n"
"v_mfma_f32_16x16x32_bf16 v[84:87], a[208:211], a[244:247], v[84:87]\n"
"v_mul_f32 v224, v224, v2\n"
"v_mul_f32 v225, v225, v3\n"
"v_mfma_f32_16x16x32_bf16 v[108:111], a[212:215], a[244:247], v[108:111]\n"
"v_mul_f32 v226, v226, v4\n"
"v_mul_f32 v227, v227, v5\n"
"v_mfma_f32_16x16x32_bf16 v[132:135], a[216:219], a[244:247], v[132:135]\n"
"v_cvt_pk_bf16_f32 v176, v224, v225\n"
"v_cvt_pk_bf16_f32 v177, v226, v227\n"
"v_accvgpr_write_b32 a88, v176\n"
"v_mfma_f32_16x16x32_bf16 v[156:159], a[220:223], a[244:247], v[156:159]\n"
"ds_read_b128 a[244:247], v18 offset:17152\n"
"v_accvgpr_write_b32 a89, v177\n"
"s_waitcnt lgkmcnt(3)\n"
"v_mfma_f32_16x16x32_bf16 v[64:67], a[192:195], a[224:227], v[64:67]\n"
"v_mul_f32 v2, v200, v200\n"
"v_mul_f32 v3, v201, v201\n"
"v_mfma_f32_16x16x32_bf16 v[88:91], a[196:199], a[224:227], v[88:91]\n"
"v_mul_f32 v4, v202, v202\n"
"s_waitcnt vmcnt(8)\n"
"v_mfma_f32_16x16x32_bf16 v[112:115], a[200:203], a[224:227], v[112:115]\n"
"ds_read_b64_tr_b16 a[208:209], v10 offset:4096\n"
"ds_read_b64_tr_b16 a[210:211], v10 offset:4352\n"
"v_mfma_f32_16x16x32_bf16 v[136:139], a[204:207], a[224:227], v[136:139]\n"
"ds_read_b128 a[224:227], v15 offset:768\n"
"v_mul_f32 v5, v203, v203\n"
"v_fma_f32 v2, v2, s61, v39\n"
"v_mfma_f32_16x16x32_bf16 v[68:71], a[192:195], a[228:231], v[68:71]\n"
"ds_read_b64_tr_b16 a[212:213], v11 offset:4096\n"
"ds_read_b64_tr_b16 a[214:215], v11 offset:4352\n"
"v_mfma_f32_16x16x32_bf16 v[92:95], a[196:199], a[228:231], v[92:95]\n"
"v_fma_f32 v3, v3, s61, v39\n"
"v_fma_f32 v4, v4, s61, v39\n"
"v_mfma_f32_16x16x32_bf16 v[116:119], a[200:203], a[228:231], v[116:119]\n"
"ds_read_b64_tr_b16 a[216:217], v10 offset:6144\n"
"ds_read_b64_tr_b16 a[218:219], v10 offset:6400\n"
"v_mfma_f32_16x16x32_bf16 v[140:143], a[204:207], a[228:231], v[140:143]\n"
"ds_read_b128 a[228:231], v15 offset:17152\n"
"v_fma_f32 v5, v5, s61, v39\n"
"v_mfma_f32_16x16x32_bf16 v[72:75], a[192:195], a[232:235], v[72:75]\n"
"ds_read_b64_tr_b16 a[220:221], v11 offset:6144\n"
"ds_read_b64_tr_b16 a[222:223], v11 offset:6400\n"
"v_mfma_f32_16x16x32_bf16 v[96:99], a[196:199], a[232:235], v[96:99]\n"
"v_mul_f32 v2, v200, v2\n"
"v_mul_f32 v3, v201, v3\n"
"v_mfma_f32_16x16x32_bf16 v[120:123], a[200:203], a[232:235], v[120:123]\n"
"s_cmp_eq_u32 s63, 1\n"
"s_cbranch_scc1 LNBa5_%=\n"
"s_add_u32 s50, s62, 256\n"
"s_add_u32 m0, s49, 0\n"
"s_nop 0\n"
"buffer_load_dwordx4 v22, s[24:27], s50 offen lds\n"
"buffer_load_dwordx4 v23, s[24:27], s50 offen offset:1024 lds\n"
"s_add_u32 m0, s49, 2048\n"
"s_nop 0\n"
"buffer_load_dwordx4 v22, s[84:87], s50 offen lds\n"
"buffer_load_dwordx4 v23, s[84:87], s50 offen offset:1024 lds\n"
"s_branch LNBb5_%=\n"
"LNBa5_%=:\n"
"s_add_u32 s50, s46, 0\n"
"s_add_u32 m0, s49, 0\n"
"s_nop 0\n"
"buffer_load_dwordx4 v30, s[28:31], s50 offen offset:0 lds\n"
"buffer_load_dwordx4 v31, s[28:31], s50 offen offset:1024 lds\n"
"buffer_load_dwordx4 v32, s[28:31], s50 offen offset:2048 lds\n"
"buffer_load_dwordx4 v33, s[28:31], s50 offen offset:3072 lds\n"
"LNBb5_%=:\n"
"v_mfma_f32_16x16x32_bf16 v[144:147], a[204:207], a[232:235], v[144:147]\n"
"ds_read_b128 a[232:235], v15 offset:33536\n"
"v_mul_f32 v4, v202, v4\n"
"s_waitcnt lgkmcnt(11)\n"
"v_mfma_f32_16x16x32_bf16 v[76:79], a[192:195], a[236:239], v[76:79]\n"
"v_mul_f32 v5, v203, v5\n"
"v_exp_f32 v2, v2\n"
"v_mfma_f32_16x16x32_bf16 v[100:103], a[196:199], a[236:239], v[100:103]\n"
"v_exp_f32 v3, v3\n"
"v_mfma_f32_16x16x32_bf16 v[124:127], a[200:203], a[236:239], v[124:127]\n"
"v_exp_f32 v4, v4\n"
"v_mfma_f32_16x16x32_bf16 v[148:151], a[204:207], a[236:239], v[148:151]\n"
"ds_read_b128 a[236:239], v15 offset:49920\n"
"v_mfma_f32_16x16x32_bf16 v[80:83], a[192:195], a[240:243], v[80:83]\n"
"v_exp_f32 v5, v5\n"
"v_mul_f32 v248, v200, v248\n"
"v_mfma_f32_16x16x32_bf16 v[104:107], a[196:199], a[240:243], v[104:107]\n"
"v_mul_f32 v249, v201, v249\n"
"v_mul_f32 v250, v202, v250\n"
"v_mfma_f32_16x16x32_bf16 v[128:131], a[200:203], a[240:243], v[128:131]\n"
"v_mul_f32 v251, v203, v251\n"
"v_add_f32 v2, 1.0, v2\n"
"v_mfma_f32_16x16x32_bf16 v[152:155], a[204:207], a[240:243], v[152:155]\n"
"ds_read_b128 a[240:243], v19 offset:768\n"
"v_add_f32 v3, 1.0, v3\n"
"v_mfma_f32_16x16x32_bf16 v[84:87], a[192:195], a[244:247], v[84:87]\n"
"v_add_f32 v4, 1.0, v4\n"
"v_add_f32 v5, 1.0, v5\n"
"v_mfma_f32_16x16x32_bf16 v[108:111], a[196:199], a[244:247], v[108:111]\n"
"v_rcp_f32 v2, v2\n"
"v_mfma_f32_16x16x32_bf16 v[132:135], a[200:203], a[244:247], v[132:135]\n"
"v_rcp_f32 v3, v3\n"
"v_mfma_f32_16x16x32_bf16 v[156:159], a[204:207], a[244:247], v[156:159]\n"
"ds_read_b128 a[244:247], v19 offset:17152\n"
"v_rcp_f32 v4, v4\n"
"s_waitcnt lgkmcnt(3)\n"
"v_mfma_f32_16x16x32_bf16 v[64:67], a[208:211], a[224:227], v[64:67]\n"
"v_rcp_f32 v5, v5\n"
"v_mfma_f32_16x16x32_bf16 v[88:91], a[212:215], a[224:227], v[88:91]\n"
"v_mul_f32 v248, v248, v2\n"
"s_waitcnt vmcnt(8)\n"
"v_mfma_f32_16x16x32_bf16 v[112:115], a[216:219], a[224:227], v[112:115]\n"
"ds_read_b64_tr_b16 a[192:193], v10 offset:8192\n"
"ds_read_b64_tr_b16 a[194:195], v10 offset:8448\n"
"v_mfma_f32_16x16x32_bf16 v[136:139], a[220:223], a[224:227], v[136:139]\n"
"ds_read_b128 a[224:227], v16 offset:768\n"
"v_mul_f32 v249, v249, v3\n"
"v_mfma_f32_16x16x32_bf16 v[68:71], a[208:211], a[228:231], v[68:71]\n"
"ds_read_b64_tr_b16 a[196:197], v11 offset:8192\n"
"ds_read_b64_tr_b16 a[198:199], v11 offset:8448\n"
"v_mfma_f32_16x16x32_bf16 v[92:95], a[212:215], a[228:231], v[92:95]\n"
"v_mul_f32 v250, v250, v4\n"
"v_mul_f32 v251, v251, v5\n"
"v_mfma_f32_16x16x32_bf16 v[116:119], a[216:219], a[228:231], v[116:119]\n"
"ds_read_b64_tr_b16 a[200:201], v10 offset:10240\n"
"ds_read_b64_tr_b16 a[202:203], v10 offset:10496\n"
"v_mfma_f32_16x16x32_bf16 v[140:143], a[220:223], a[228:231], v[140:143]\n"
"ds_read_b128 a[228:231], v16 offset:17152\n"
"v_cvt_pk_bf16_f32 v200, v248, v249\n"
"v_mfma_f32_16x16x32_bf16 v[72:75], a[208:211], a[232:235], v[72:75]\n"
"ds_read_b64_tr_b16 a[204:205], v11 offset:10240\n"
"ds_read_b64_tr_b16 a[206:207], v11 offset:10496\n"
"v_mfma_f32_16x16x32_bf16 v[96:99], a[212:215], a[232:235], v[96:99]\n"
"v_cvt_pk_bf16_f32 v201, v250, v251\n"
"v_accvgpr_write_b32 a90, v200\n"
"v_mfma_f32_16x16x32_bf16 v[120:123], a[216:219], a[232:235], v[120:123]\n"
"s_cmp_eq_u32 s63, 1\n"
"s_cbranch_scc1 LNBa6_%=\n"
"s_add_u32 s50, s62, 114944\n"
"s_add_u32 m0, s49, 4096\n"
"s_nop 0\n"
"buffer_load_dwordx4 v22, s[24:27], s50 offen lds\n"
"buffer_load_dwordx4 v23, s[24:27], s50 offen offset:1024 lds\n"
"s_add_u32 m0, s49, 6144\n"
"s_nop 0\n"
"buffer_load_dwordx4 v22, s[84:87], s50 offen lds\n"
"buffer_load_dwordx4 v23, s[84:87], s50 offen offset:1024 lds\n"
"s_branch LNBb6_%=\n"
"LNBa6_%=:\n"
"s_add_u32 s50, s46, 32768\n"
"s_add_u32 m0, s49, 4096\n"
"s_nop 0\n"
"buffer_load_dwordx4 v30, s[28:31], s50 offen offset:0 lds\n"
"buffer_load_dwordx4 v31, s[28:31], s50 offen offset:1024 lds\n"
"buffer_load_dwordx4 v32, s[28:31], s50 offen offset:2048 lds\n"
"buffer_load_dwordx4 v33, s[28:31], s50 offen offset:3072 lds\n"
"LNBb6_%=:\n"
"v_mfma_f32_16x16x32_bf16 v[144:147], a[220:223], a[232:235], v[144:147]\n"
"ds_read_b128 a[232:235], v16 offset:33536\n"
"v_accvgpr_write_b32 a91, v201\n"
"s_waitcnt lgkmcnt(11)\n"
"v_mfma_f32_16x16x32_bf16 v[76:79], a[208:211], a[236:239], v[76:79]\n"
"v_mul_f32 v2, v180, v180\n"
"v_mul_f32 v3, v181, v181\n"
"v_mul_f32 v4, v182, v182\n"
"v_mfma_f32_16x16x32_bf16 v[100:103], a[212:215], a[236:239], v[100:103]\n"
"v_mul_f32 v5, v183, v183\n"
"v_fma_f32 v2, v2, s61, v39\n"
"v_mfma_f32_16x16x32_bf16 v[124:127], a[216:219], a[236:239], v[124:127]\n"
"v_fma_f32 v3, v3, s61, v39\n"
"v_fma_f32 v4, v4, s61, v39\n"
"v_mfma_f32_16x16x32_bf16 v[148:151], a[220:223], a[236:239], v[148:151]\n"
"ds_read_b128 a[236:239], v16 offset:49920\n"
"v_fma_f32 v5, v5, s61, v39\n"
"v_mfma_f32_16x16x32_bf16 v[80:83], a[208:211], a[240:243], v[80:83]\n"
"v_mul_f32 v2, v180, v2\n"
"v_mul_f32 v3, v181, v3\n"
"v_mfma_f32_16x16x32_bf16 v[104:107], a[212:215], a[240:243], v[104:107]\n"
"v_mul_f32 v4, v182, v4\n"
"v_mul_f32 v5, v183, v5\n"
"v_mfma_f32_16x16x32_bf16 v[128:131], a[216:219], a[240:243], v[128:131]\n"
"v_exp_f32 v2, v2\n"
"v_mfma_f32_16x16x32_bf16 v[152:155], a[220:223], a[240:243], v[152:155]\n"
"ds_read_b128 a[240:243], v20 offset:768\n"
"v_mfma_f32_16x16x32_bf16 v[84:87], a[208:211], a[244:247], v[84:87]\n"
"v_exp_f32 v3, v3\n"
"v_mfma_f32_16x16x32_bf16 v[108:111], a[212:215], a[244:247], v[108:111]\n"
"v_exp_f32 v4, v4\n"
"v_mfma_f32_16x16x32_bf16 v[132:135], a[216:219], a[244:247], v[132:135]\n"
"v_exp_f32 v5, v5\n"
"v_mul_f32 v228, v180, v228\n"
"v_mfma_f32_16x16x32_bf16 v[156:159], a[220:223], a[244:247], v[156:159]\n"
"ds_read_b128 a[244:247], v20 offset:17152\n"
"v_mul_f32 v229, v181, v229\n"
"s_waitcnt lgkmcnt(3)\n"
"v_mfma_f32_16x16x32_bf16 v[64:67], a[192:195], a[224:227], v[64:67]\n"
"v_mul_f32 v230, v182, v230\n"
"v_mul_f32 v231, v183, v231\n"
"v_mfma_f32_16x16x32_bf16 v[88:91], a[196:199], a[224:227], v[88:91]\n"
"v_add_f32 v2, 1.0, v2\n"
"s_waitcnt vmcnt(8)\n"
"v_mfma_f32_16x16x32_bf16 v[112:115], a[200:203], a[224:227], v[112:115]\n"
"ds_read_b64_tr_b16 a[208:209], v10 offset:12288\n"
"ds_read_b64_tr_b16 a[210:211], v10 offset:12544\n"
"v_mfma_f32_16x16x32_bf16 v[136:139], a[204:207], a[224:227], v[136:139]\n"
"ds_read_b128 a[224:227], v17 offset:768\n"
"v_add_f32 v3, 1.0, v3\n"
"v_mfma_f32_16x16x32_bf16 v[68:71], a[192:195], a[228:231], v[68:71]\n"
"ds_read_b64_tr_b16 a[212:213], v11 offset:12288\n"
"ds_read_b64_tr_b16 a[214:215], v11 offset:12544\n"
"v_mfma_f32_16x16x32_bf16 v[92:95], a[196:199], a[228:231], v[92:95]\n"
"v_add_f32 v4, 1.0, v4\n"
"v_add_f32 v5, 1.0, v5\n"
"v_mfma_f32_16x16x32_bf16 v[116:119], a[200:203], a[228:231], v[116:119]\n"
"ds_read_b64_tr_b16 a[216:217], v10 offset:14336\n"
"ds_read_b64_tr_b16 a[218:219], v10 offset:14592\n"
"v_mfma_f32_16x16x32_bf16 v[140:143], a[204:207], a[228:231], v[140:143]\n"
"ds_read_b128 a[228:231], v17 offset:17152\n"
"v_mfma_f32_16x16x32_bf16 v[72:75], a[192:195], a[232:235], v[72:75]\n"
"ds_read_b64_tr_b16 a[220:221], v11 offset:14336\n"
"ds_read_b64_tr_b16 a[222:223], v11 offset:14592\n"
"v_mfma_f32_16x16x32_bf16 v[96:99], a[196:199], a[232:235], v[96:99]\n"
"v_rcp_f32 v2, v2\n"
"v_mfma_f32_16x16x32_bf16 v[120:123], a[200:203], a[232:235], v[120:123]\n"
"s_cmp_eq_u32 s63, 1\n"
"s_cbranch_scc1 LNBa7_%=\n"
"s_add_u32 s50, s62, 229632\n"
"s_add_u32 m0, s49, 8192\n"
"s_nop 0\n"
"buffer_load_dwordx4 v22, s[24:27], s50 offen lds\n"
"buffer_load_dwordx4 v23, s[24:27], s50 offen offset:1024 lds\n"
"s_add_u32 m0, s49, 10240\n"
"s_nop 0\n"
"buffer_load_dwordx4 v22, s[84:87], s50 offen lds\n"
"buffer_load_dwordx4 v23, s[84:87], s50 offen offset:1024 lds\n"
"s_branch LNBb7_%=\n"
"LNBa7_%=:\n"
"s_add_u32 s50, s46, 65536\n"
"s_add_u32 m0, s49, 8192\n"
"s_nop 0\n"
"buffer_load_dwordx4 v30, s[28:31], s50 offen offset:0 lds\n"
"buffer_load_dwordx4 v31, s[28:31], s50 offen offset:1024 lds\n"
"buffer_load_dwordx4 v32, s[28:31], s50 offen offset:2048 lds\n"
"buffer_load_dwordx4 v33, s[28:31], s50 offen offset:3072 lds\n"
"LNBb7_%=:\n"
"v_mfma_f32_16x16x32_bf16 v[144:147], a[204:207], a[232:235], v[144:147]\n"
"ds_read_b128 a[232:235], v17 offset:33536\n"
"v_rcp_f32 v3, v3\n"
"s_waitcnt lgkmcnt(11)\n"
"v_mfma_f32_16x16x32_bf16 v[76:79], a[192:195], a[236:239], v[76:79]\n"
"v_rcp_f32 v4, v4\n"
"v_mfma_f32_16x16x32_bf16 v[100:103], a[196:199], a[236:239], v[100:103]\n"
"v_rcp_f32 v5, v5\n"
"v_mfma_f32_16x16x32_bf16 v[124:127], a[200:203], a[236:239], v[124:127]\n"
"v_mul_f32 v228, v228, v2\n"
"v_mul_f32 v229, v229, v3\n"
"v_mfma_f32_16x16x32_bf16 v[148:151], a[204:207], a[236:239], v[148:151]\n"
"ds_read_b128 a[236:239], v17 offset:49920\n"
"v_mul_f32 v230, v230, v4\n"
"v_mfma_f32_16x16x32_bf16 v[80:83], a[192:195], a[240:243], v[80:83]\n"
"v_mul_f32 v231, v231, v5\n"
"v_cvt_pk_bf16_f32 v180, v228, v229\n"
"v_mfma_f32_16x16x32_bf16 v[104:107], a[196:199], a[240:243], v[104:107]\n"
"v_cvt_pk_bf16_f32 v181, v230, v231\n"
"v_accvgpr_write_b32 a92, v180\n"
"v_mfma_f32_16x16x32_bf16 v[128:131], a[200:203], a[240:243], v[128:131]\n"
"v_accvgpr_write_b32 a93, v181\n"
"v_mul_f32 v2, v204, v204\n"
"v_mfma_f32_16x16x32_bf16 v[152:155], a[204:207], a[240:243], v[152:155]\n"
"ds_read_b128 a[240:243], v21 offset:768\n"
"v_mul_f32 v3, v205, v205\n"
"v_mfma_f32_16x16x32_bf16 v[84:87], a[192:195], a[244:247], v[84:87]\n"
"v_mul_f32 v4, v206, v206\n"
"v_mul_f32 v5, v207, v207\n"
"v_mfma_f32_16x16x32_bf16 v[108:111], a[196:199], a[244:247], v[108:111]\n"
"v_fma_f32 v2, v2, s61, v39\n"
"v_fma_f32 v3, v3, s61, v39\n"
"v_mfma_f32_16x16x32_bf16 v[132:135], a[200:203], a[244:247], v[132:135]\n"
"v_fma_f32 v4, v4, s61, v39\n"
"v_fma_f32 v5, v5, s61, v39\n"
"v_mfma_f32_16x16x32_bf16 v[156:159], a[204:207], a[244:247], v[156:159]\n"
"ds_read_b128 a[244:247], v21 offset:17152\n"
"v_mul_f32 v2, v204, v2\n"
"s_waitcnt lgkmcnt(3)\n"
"v_mfma_f32_16x16x32_bf16 v[64:67], a[208:211], a[224:227], v[64:67]\n"
"v_mul_f32 v3, v205, v3\n"
"v_mul_f32 v4, v206, v4\n"
"v_mfma_f32_16x16x32_bf16 v[88:91], a[212:215], a[224:227], v[88:91]\n"
"v_mul_f32 v5, v207, v5\n"
"s_waitcnt vmcnt(8)\n"
"v_mfma_f32_16x16x32_bf16 v[112:115], a[216:219], a[224:227], v[112:115]\n"
"ds_read_b64_tr_b16 a[192:193], v10 offset:0\n"
"ds_read_b64_tr_b16 a[194:195], v10 offset:256\n"
"v_mfma_f32_16x16x32_bf16 v[136:139], a[220:223], a[224:227], v[136:139]\n"
"ds_read_b128 a[224:227], v14 offset:0\n"
"v_exp_f32 v2, v2\n"
"v_mfma_f32_16x16x32_bf16 v[68:71], a[208:211], a[228:231], v[68:71]\n"
"ds_read_b64_tr_b16 a[196:197], v11 offset:0\n"
"ds_read_b64_tr_b16 a[198:199], v11 offset:256\n"
"v_mfma_f32_16x16x32_bf16 v[92:95], a[212:215], a[228:231], v[92:95]\n"
"v_exp_f32 v3, v3\n"
"v_mfma_f32_16x16x32_bf16 v[116:119], a[216:219], a[228:231], v[116:119]\n"
"ds_read_b64_tr_b16 a[200:201], v10 offset:2048\n"
"ds_read_b64_tr_b16 a[202:203], v10 offset:2304\n"
"v_mfma_f32_16x16x32_bf16 v[140:143], a[220:223], a[228:231], v[140:143]\n"
"ds_read_b128 a[228:231], v14 offset:16384\n"
"v_mfma_f32_16x16x32_bf16 v[72:75], a[208:211], a[232:235], v[72:75]\n"
"ds_read_b64_tr_b16 a[204:205], v11 offset:2048\n"
"ds_read_b64_tr_b16 a[206:207], v11 offset:2304\n"
"v_exp_f32 v4, v4\n"
"v_mfma_f32_16x16x32_bf16 v[96:99], a[212:215], a[232:235], v[96:99]\n"
"v_exp_f32 v5, v5\n"
"v_mfma_f32_16x16x32_bf16 v[120:123], a[216:219], a[232:235], v[120:123]\n"
"s_cmp_eq_u32 s63, 1\n"
"s_cbranch_scc1 LNBa8_%=\n"
"s_add_u32 s50, s62, 344320\n"
"s_add_u32 m0, s49, 12288\n"
"s_nop 0\n"
"buffer_load_dwordx4 v22, s[24:27], s50 offen lds\n"
"buffer_load_dwordx4 v23, s[24:27], s50 offen offset:1024 lds\n"
"s_add_u32 m0, s49, 14336\n"
"s_nop 0\n"
"buffer_load_dwordx4 v22, s[84:87], s50 offen lds\n"
"buffer_load_dwordx4 v23, s[84:87], s50 offen offset:1024 lds\n"
"s_branch LNBb8_%=\n"
"LNBa8_%=:\n"
"s_add_u32 s50, s46, 98304\n"
"s_add_u32 m0, s49, 12288\n"
"s_nop 0\n"
"buffer_load_dwordx4 v30, s[28:31], s50 offen offset:0 lds\n"
"buffer_load_dwordx4 v31, s[28:31], s50 offen offset:1024 lds\n"
"buffer_load_dwordx4 v32, s[28:31], s50 offen offset:2048 lds\n"
"buffer_load_dwordx4 v33, s[28:31], s50 offen offset:3072 lds\n"
"LNBb8_%=:\n"
"v_mfma_f32_16x16x32_bf16 v[144:147], a[220:223], a[232:235], v[144:147]\n"
"ds_read_b128 a[232:235], v14 offset:32768\n"
"v_mul_f32 v252, v204, v252\n"
"s_waitcnt lgkmcnt(11)\n"
"v_mfma_f32_16x16x32_bf16 v[76:79], a[208:211], a[236:239], v[76:79]\n"
"v_mul_f32 v253, v205, v253\n"
"v_mul_f32 v254, v206, v254\n"
"v_mfma_f32_16x16x32_bf16 v[100:103], a[212:215], a[236:239], v[100:103]\n"
"v_mul_f32 v255, v207, v255\n"
"v_add_f32 v2, 1.0, v2\n"
"v_mfma_f32_16x16x32_bf16 v[124:127], a[216:219], a[236:239], v[124:127]\n"
"v_add_f32 v3, 1.0, v3\n"
"v_add_f32 v4, 1.0, v4\n"
"v_mfma_f32_16x16x32_bf16 v[148:151], a[220:223], a[236:239], v[148:151]\n"
"ds_read_b128 a[236:239], v14 offset:49152\n"
"v_add_f32 v5, 1.0, v5\n"
"v_mfma_f32_16x16x32_bf16 v[80:83], a[208:211], a[240:243], v[80:83]\n"
"v_rcp_f32 v2, v2\n"
"v_mfma_f32_16x16x32_bf16 v[104:107], a[212:215], a[240:243], v[104:107]\n"
"v_rcp_f32 v3, v3\n"
"v_mfma_f32_16x16x32_bf16 v[128:131], a[216:219], a[240:243], v[128:131]\n"
"v_rcp_f32 v4, v4\n"
"v_mfma_f32_16x16x32_bf16 v[152:155], a[220:223], a[240:243], v[152:155]\n"
"ds_read_b128 a[240:243], v18 offset:0\n"
"v_rcp_f32 v5, v5\n"
"v_mfma_f32_16x16x32_bf16 v[84:87], a[208:211], a[244:247], v[84:87]\n"
"v_mul_f32 v252, v252, v2\n"
"v_mul_f32 v253, v253, v3\n"
"v_mfma_f32_16x16x32_bf16 v[108:111], a[212:215], a[244:247], v[108:111]\n"
"v_mul_f32 v254, v254, v4\n"
"v_mul_f32 v255, v255, v5\n"
"v_cvt_pk_bf16_f32 v204, v252, v253\n"
"v_mfma_f32_16x16x32_bf16 v[132:135], a[216:219], a[244:247], v[132:135]\n"
"v_cvt_pk_bf16_f32 v205, v254, v255\n"
"v_accvgpr_write_b32 a94, v204\n"
"v_mfma_f32_16x16x32_bf16 v[156:159], a[220:223], a[244:247], v[156:159]\n"
"ds_read_b128 a[244:247], v18 offset:16384\n"
"v_accvgpr_write_b32 a95, v205\n"
"s_add_u32 s62, s62, 256\n"
"s_add_u32 s47, s47, 1\n"
"s_cmp_lt_u32 s47, 2\n"
"s_waitcnt lgkmcnt(0)\n"
"s_cbranch_scc1 LFL_%=\n"
"s_branch LF5_%=\n"
"LF4_%=:\n"
"s_nop 7\n"
"s_nop 7\n"
"s_nop 7\n"
"v_mul_f32 v2, v160, v160\n"
"v_mul_f32 v3, v161, v161\n"
"v_mul_f32 v4, v162, v162\n"
"v_mul_f32 v5, v163, v163\n"
"v_fma_f32 v2, v2, s61, v39\n"
"v_fma_f32 v3, v3, s61, v39\n"
"v_fma_f32 v4, v4, s61, v39\n"
"v_fma_f32 v5, v5, s61, v39\n"
"v_mul_f32 v2, v160, v2\n"
"v_mul_f32 v3, v161, v3\n"
"v_mul_f32 v4, v162, v4\n"
"v_mul_f32 v5, v163, v5\n"
"v_exp_f32 v2, v2\n"
"v_exp_f32 v3, v3\n"
"v_exp_f32 v4, v4\n"
"v_exp_f32 v5, v5\n"
"v_mul_f32 v208, v160, v208\n"
"v_mul_f32 v209, v161, v209\n"
"v_mul_f32 v210, v162, v210\n"
"v_mul_f32 v211, v163, v211\n"
"v_add_f32 v2, 1.0, v2\n"
"v_add_f32 v3, 1.0, v3\n"
"v_add_f32 v4, 1.0, v4\n"
"v_add_f32 v5, 1.0, v5\n"
"v_rcp_f32 v2, v2\n"
"v_rcp_f32 v3, v3\n"
"v_rcp_f32 v4, v4\n"
"v_rcp_f32 v5, v5\n"
"v_mul_f32 v208, v208, v2\n"
"v_mul_f32 v209, v209, v3\n"
"v_mul_f32 v210, v210, v4\n"
"v_mul_f32 v211, v211, v5\n"
"v_cvt_pk_bf16_f32 v160, v208, v209\n"
"v_cvt_pk_bf16_f32 v161, v210, v211\n"
"v_accvgpr_write_b32 a72, v160\n"
"v_accvgpr_write_b32 a73, v161\n"
"v_mul_f32 v2, v184, v184\n"
"v_mul_f32 v3, v185, v185\n"
"v_mul_f32 v4, v186, v186\n"
"v_mul_f32 v5, v187, v187\n"
"v_fma_f32 v2, v2, s61, v39\n"
"v_fma_f32 v3, v3, s61, v39\n"
"v_fma_f32 v4, v4, s61, v39\n"
"v_fma_f32 v5, v5, s61, v39\n"
"v_mul_f32 v2, v184, v2\n"
"v_mul_f32 v3, v185, v3\n"
"v_mul_f32 v4, v186, v4\n"
"v_mul_f32 v5, v187, v5\n"
"v_exp_f32 v2, v2\n"
"v_exp_f32 v3, v3\n"
"v_exp_f32 v4, v4\n"
"v_exp_f32 v5, v5\n"
"v_mul_f32 v232, v184, v232\n"
"v_mul_f32 v233, v185, v233\n"
"v_mul_f32 v234, v186, v234\n"
"v_mul_f32 v235, v187, v235\n"
"v_add_f32 v2, 1.0, v2\n"
"v_add_f32 v3, 1.0, v3\n"
"v_add_f32 v4, 1.0, v4\n"
"v_add_f32 v5, 1.0, v5\n"
"v_rcp_f32 v2, v2\n"
"v_rcp_f32 v3, v3\n"
"v_rcp_f32 v4, v4\n"
"v_rcp_f32 v5, v5\n"
"v_mul_f32 v232, v232, v2\n"
"v_mul_f32 v233, v233, v3\n"
"v_mul_f32 v234, v234, v4\n"
"v_mul_f32 v235, v235, v5\n"
"v_cvt_pk_bf16_f32 v184, v232, v233\n"
"v_cvt_pk_bf16_f32 v185, v234, v235\n"
"v_accvgpr_write_b32 a74, v184\n"
"v_accvgpr_write_b32 a75, v185\n"
"v_mul_f32 v2, v164, v164\n"
"v_mul_f32 v3, v165, v165\n"
"v_mul_f32 v4, v166, v166\n"
"v_mul_f32 v5, v167, v167\n"
"v_fma_f32 v2, v2, s61, v39\n"
"v_fma_f32 v3, v3, s61, v39\n"
"v_fma_f32 v4, v4, s61, v39\n"
"v_fma_f32 v5, v5, s61, v39\n"
"v_mul_f32 v2, v164, v2\n"
"v_mul_f32 v3, v165, v3\n"
"v_mul_f32 v4, v166, v4\n"
"v_mul_f32 v5, v167, v5\n"
"v_exp_f32 v2, v2\n"
"v_exp_f32 v3, v3\n"
"v_exp_f32 v4, v4\n"
"v_exp_f32 v5, v5\n"
"v_mul_f32 v212, v164, v212\n"
"v_mul_f32 v213, v165, v213\n"
"v_mul_f32 v214, v166, v214\n"
"v_mul_f32 v215, v167, v215\n"
"v_add_f32 v2, 1.0, v2\n"
"v_add_f32 v3, 1.0, v3\n"
"v_add_f32 v4, 1.0, v4\n"
"v_add_f32 v5, 1.0, v5\n"
"v_rcp_f32 v2, v2\n"
"v_rcp_f32 v3, v3\n"
"v_rcp_f32 v4, v4\n"
"v_rcp_f32 v5, v5\n"
"v_mul_f32 v212, v212, v2\n"
"v_mul_f32 v213, v213, v3\n"
"v_mul_f32 v214, v214, v4\n"
"v_mul_f32 v215, v215, v5\n"
"v_cvt_pk_bf16_f32 v164, v212, v213\n"
"v_cvt_pk_bf16_f32 v165, v214, v215\n"
"v_accvgpr_write_b32 a76, v164\n"
"v_accvgpr_write_b32 a77, v165\n"
"v_mul_f32 v2, v188, v188\n"
"v_mul_f32 v3, v189, v189\n"
"v_mul_f32 v4, v190, v190\n"
"v_mul_f32 v5, v191, v191\n"
"v_fma_f32 v2, v2, s61, v39\n"
"v_fma_f32 v3, v3, s61, v39\n"
"v_fma_f32 v4, v4, s61, v39\n"
"v_fma_f32 v5, v5, s61, v39\n"
"v_mul_f32 v2, v188, v2\n"
"v_mul_f32 v3, v189, v3\n"
"v_mul_f32 v4, v190, v4\n"
"v_mul_f32 v5, v191, v5\n"
"v_exp_f32 v2, v2\n"
"v_exp_f32 v3, v3\n"
"v_exp_f32 v4, v4\n"
"v_exp_f32 v5, v5\n"
"v_mul_f32 v236, v188, v236\n"
"v_mul_f32 v237, v189, v237\n"
"v_mul_f32 v238, v190, v238\n"
"v_mul_f32 v239, v191, v239\n"
"v_add_f32 v2, 1.0, v2\n"
"v_add_f32 v3, 1.0, v3\n"
"v_add_f32 v4, 1.0, v4\n"
"v_add_f32 v5, 1.0, v5\n"
"v_rcp_f32 v2, v2\n"
"v_rcp_f32 v3, v3\n"
"v_rcp_f32 v4, v4\n"
"v_rcp_f32 v5, v5\n"
"v_mul_f32 v236, v236, v2\n"
"v_mul_f32 v237, v237, v3\n"
"v_mul_f32 v238, v238, v4\n"
"v_mul_f32 v239, v239, v5\n"
"v_cvt_pk_bf16_f32 v188, v236, v237\n"
"v_cvt_pk_bf16_f32 v189, v238, v239\n"
"v_accvgpr_write_b32 a78, v188\n"
"v_accvgpr_write_b32 a79, v189\n"
"v_mul_f32 v2, v168, v168\n"
"v_mul_f32 v3, v169, v169\n"
"v_mul_f32 v4, v170, v170\n"
"v_mul_f32 v5, v171, v171\n"
"v_fma_f32 v2, v2, s61, v39\n"
"v_fma_f32 v3, v3, s61, v39\n"
"v_fma_f32 v4, v4, s61, v39\n"
"v_fma_f32 v5, v5, s61, v39\n"
"v_mul_f32 v2, v168, v2\n"
"v_mul_f32 v3, v169, v3\n"
"v_mul_f32 v4, v170, v4\n"
"v_mul_f32 v5, v171, v5\n"
"v_exp_f32 v2, v2\n"
"v_exp_f32 v3, v3\n"
"v_exp_f32 v4, v4\n"
"v_exp_f32 v5, v5\n"
"v_mul_f32 v216, v168, v216\n"
"v_mul_f32 v217, v169, v217\n"
"v_mul_f32 v218, v170, v218\n"
"v_mul_f32 v219, v171, v219\n"
"v_add_f32 v2, 1.0, v2\n"
"v_add_f32 v3, 1.0, v3\n"
"v_add_f32 v4, 1.0, v4\n"
"v_add_f32 v5, 1.0, v5\n"
"v_rcp_f32 v2, v2\n"
"v_rcp_f32 v3, v3\n"
"v_rcp_f32 v4, v4\n"
"v_rcp_f32 v5, v5\n"
"v_mul_f32 v216, v216, v2\n"
"v_mul_f32 v217, v217, v3\n"
"v_mul_f32 v218, v218, v4\n"
"v_mul_f32 v219, v219, v5\n"
"v_cvt_pk_bf16_f32 v168, v216, v217\n"
"v_cvt_pk_bf16_f32 v169, v218, v219\n"
"v_accvgpr_write_b32 a80, v168\n"
"v_accvgpr_write_b32 a81, v169\n"
"v_mul_f32 v2, v192, v192\n"
"v_mul_f32 v3, v193, v193\n"
"v_mul_f32 v4, v194, v194\n"
"v_mul_f32 v5, v195, v195\n"
"v_fma_f32 v2, v2, s61, v39\n"
"v_fma_f32 v3, v3, s61, v39\n"
"v_fma_f32 v4, v4, s61, v39\n"
"v_fma_f32 v5, v5, s61, v39\n"
"v_mul_f32 v2, v192, v2\n"
"v_mul_f32 v3, v193, v3\n"
"v_mul_f32 v4, v194, v4\n"
"v_mul_f32 v5, v195, v5\n"
"v_exp_f32 v2, v2\n"
"v_exp_f32 v3, v3\n"
"v_exp_f32 v4, v4\n"
"v_exp_f32 v5, v5\n"
"v_mul_f32 v240, v192, v240\n"
"v_mul_f32 v241, v193, v241\n"
"v_mul_f32 v242, v194, v242\n"
"v_mul_f32 v243, v195, v243\n"
"v_add_f32 v2, 1.0, v2\n"
"v_add_f32 v3, 1.0, v3\n"
"v_add_f32 v4, 1.0, v4\n"
"v_add_f32 v5, 1.0, v5\n"
"v_rcp_f32 v2, v2\n"
"v_rcp_f32 v3, v3\n"
"v_rcp_f32 v4, v4\n"
"v_rcp_f32 v5, v5\n"
"v_mul_f32 v240, v240, v2\n"
"v_mul_f32 v241, v241, v3\n"
"v_mul_f32 v242, v242, v4\n"
"v_mul_f32 v243, v243, v5\n"
"v_cvt_pk_bf16_f32 v192, v240, v241\n"
"v_cvt_pk_bf16_f32 v193, v242, v243\n"
"v_accvgpr_write_b32 a82, v192\n"
"v_accvgpr_write_b32 a83, v193\n"
"v_mul_f32 v2, v172, v172\n"
"v_mul_f32 v3, v173, v173\n"
"v_mul_f32 v4, v174, v174\n"
"v_mul_f32 v5, v175, v175\n"
"v_fma_f32 v2, v2, s61, v39\n"
"v_fma_f32 v3, v3, s61, v39\n"
"v_fma_f32 v4, v4, s61, v39\n"
"v_fma_f32 v5, v5, s61, v39\n"
"v_mul_f32 v2, v172, v2\n"
"v_mul_f32 v3, v173, v3\n"
"v_mul_f32 v4, v174, v4\n"
"v_mul_f32 v5, v175, v5\n"
"v_exp_f32 v2, v2\n"
"v_exp_f32 v3, v3\n"
"v_exp_f32 v4, v4\n"
"v_exp_f32 v5, v5\n"
"v_mul_f32 v220, v172, v220\n"
"v_mul_f32 v221, v173, v221\n"
"v_mul_f32 v222, v174, v222\n"
"v_mul_f32 v223, v175, v223\n"
"v_add_f32 v2, 1.0, v2\n"
"v_add_f32 v3, 1.0, v3\n"
"v_add_f32 v4, 1.0, v4\n"
"v_add_f32 v5, 1.0, v5\n"
"v_rcp_f32 v2, v2\n"
"v_rcp_f32 v3, v3\n"
"v_rcp_f32 v4, v4\n"
"v_rcp_f32 v5, v5\n"
"v_mul_f32 v220, v220, v2\n"
"v_mul_f32 v221, v221, v3\n"
"v_mul_f32 v222, v222, v4\n"
"v_mul_f32 v223, v223, v5\n"
"v_cvt_pk_bf16_f32 v172, v220, v221\n"
"v_cvt_pk_bf16_f32 v173, v222, v223\n"
"v_accvgpr_write_b32 a84, v172\n"
"v_accvgpr_write_b32 a85, v173\n"
"v_mul_f32 v2, v196, v196\n"
"v_mul_f32 v3, v197, v197\n"
"v_mul_f32 v4, v198, v198\n"
"v_mul_f32 v5, v199, v199\n"
"v_fma_f32 v2, v2, s61, v39\n"
"v_fma_f32 v3, v3, s61, v39\n"
"v_fma_f32 v4, v4, s61, v39\n"
"v_fma_f32 v5, v5, s61, v39\n"
"v_mul_f32 v2, v196, v2\n"
"v_mul_f32 v3, v197, v3\n"
"v_mul_f32 v4, v198, v4\n"
"v_mul_f32 v5, v199, v5\n"
"v_exp_f32 v2, v2\n"
"v_exp_f32 v3, v3\n"
"v_exp_f32 v4, v4\n"
"v_exp_f32 v5, v5\n"
"v_mul_f32 v244, v196, v244\n"
"v_mul_f32 v245, v197, v245\n"
"v_mul_f32 v246, v198, v246\n"
"v_mul_f32 v247, v199, v247\n"
"v_add_f32 v2, 1.0, v2\n"
"v_add_f32 v3, 1.0, v3\n"
"v_add_f32 v4, 1.0, v4\n"
"v_add_f32 v5, 1.0, v5\n"
"v_rcp_f32 v2, v2\n"
"v_rcp_f32 v3, v3\n"
"v_rcp_f32 v4, v4\n"
"v_rcp_f32 v5, v5\n"
"v_mul_f32 v244, v244, v2\n"
"v_mul_f32 v245, v245, v3\n"
"v_mul_f32 v246, v246, v4\n"
"v_mul_f32 v247, v247, v5\n"
"v_cvt_pk_bf16_f32 v196, v244, v245\n"
"v_cvt_pk_bf16_f32 v197, v246, v247\n"
"v_accvgpr_write_b32 a86, v196\n"
"v_accvgpr_write_b32 a87, v197\n"
"v_mul_f32 v2, v176, v176\n"
"v_mul_f32 v3, v177, v177\n"
"v_mul_f32 v4, v178, v178\n"
"v_mul_f32 v5, v179, v179\n"
"v_fma_f32 v2, v2, s61, v39\n"
"v_fma_f32 v3, v3, s61, v39\n"
"v_fma_f32 v4, v4, s61, v39\n"
"v_fma_f32 v5, v5, s61, v39\n"
"v_mul_f32 v2, v176, v2\n"
"v_mul_f32 v3, v177, v3\n"
"v_mul_f32 v4, v178, v4\n"
"v_mul_f32 v5, v179, v5\n"
"v_exp_f32 v2, v2\n"
"v_exp_f32 v3, v3\n"
"v_exp_f32 v4, v4\n"
"v_exp_f32 v5, v5\n"
"v_mul_f32 v224, v176, v224\n"
"v_mul_f32 v225, v177, v225\n"
"v_mul_f32 v226, v178, v226\n"
"v_mul_f32 v227, v179, v227\n"
"v_add_f32 v2, 1.0, v2\n"
"v_add_f32 v3, 1.0, v3\n"
"v_add_f32 v4, 1.0, v4\n"
"v_add_f32 v5, 1.0, v5\n"
"v_rcp_f32 v2, v2\n"
"v_rcp_f32 v3, v3\n"
"v_rcp_f32 v4, v4\n"
"v_rcp_f32 v5, v5\n"
"v_mul_f32 v224, v224, v2\n"
"v_mul_f32 v225, v225, v3\n"
"v_mul_f32 v226, v226, v4\n"
"v_mul_f32 v227, v227, v5\n"
"v_cvt_pk_bf16_f32 v176, v224, v225\n"
"v_cvt_pk_bf16_f32 v177, v226, v227\n"
"v_accvgpr_write_b32 a88, v176\n"
"v_accvgpr_write_b32 a89, v177\n"
"v_mul_f32 v2, v200, v200\n"
"v_mul_f32 v3, v201, v201\n"
"v_mul_f32 v4, v202, v202\n"
"v_mul_f32 v5, v203, v203\n"
"v_fma_f32 v2, v2, s61, v39\n"
"v_fma_f32 v3, v3, s61, v39\n"
"v_fma_f32 v4, v4, s61, v39\n"
"v_fma_f32 v5, v5, s61, v39\n"
"v_mul_f32 v2, v200, v2\n"
"v_mul_f32 v3, v201, v3\n"
"v_mul_f32 v4, v202, v4\n"
"v_mul_f32 v5, v203, v5\n"
"v_exp_f32 v2, v2\n"
"v_exp_f32 v3, v3\n"
"v_exp_f32 v4, v4\n"
"v_exp_f32 v5, v5\n"
"v_mul_f32 v248, v200, v248\n"
"v_mul_f32 v249, v201, v249\n"
"v_mul_f32 v250, v202, v250\n"
"v_mul_f32 v251, v203, v251\n"
"v_add_f32 v2, 1.0, v2\n"
"v_add_f32 v3, 1.0, v3\n"
"v_add_f32 v4, 1.0, v4\n"
"v_add_f32 v5, 1.0, v5\n"
"v_rcp_f32 v2, v2\n"
"v_rcp_f32 v3, v3\n"
"v_rcp_f32 v4, v4\n"
"v_rcp_f32 v5, v5\n"
"v_mul_f32 v248, v248, v2\n"
"v_mul_f32 v249, v249, v3\n"
"v_mul_f32 v250, v250, v4\n"
"v_mul_f32 v251, v251, v5\n"
"v_cvt_pk_bf16_f32 v200, v248, v249\n"
"v_cvt_pk_bf16_f32 v201, v250, v251\n"
"v_accvgpr_write_b32 a90, v200\n"
"v_accvgpr_write_b32 a91, v201\n"
"v_mul_f32 v2, v180, v180\n"
"v_mul_f32 v3, v181, v181\n"
"v_mul_f32 v4, v182, v182\n"
"v_mul_f32 v5, v183, v183\n"
"v_fma_f32 v2, v2, s61, v39\n"
"v_fma_f32 v3, v3, s61, v39\n"
"v_fma_f32 v4, v4, s61, v39\n"
"v_fma_f32 v5, v5, s61, v39\n"
"v_mul_f32 v2, v180, v2\n"
"v_mul_f32 v3, v181, v3\n"
"v_mul_f32 v4, v182, v4\n"
"v_mul_f32 v5, v183, v5\n"
"v_exp_f32 v2, v2\n"
"v_exp_f32 v3, v3\n"
"v_exp_f32 v4, v4\n"
"v_exp_f32 v5, v5\n"
"v_mul_f32 v228, v180, v228\n"
"v_mul_f32 v229, v181, v229\n"
"v_mul_f32 v230, v182, v230\n"
"v_mul_f32 v231, v183, v231\n"
"v_add_f32 v2, 1.0, v2\n"
"v_add_f32 v3, 1.0, v3\n"
"v_add_f32 v4, 1.0, v4\n"
"v_add_f32 v5, 1.0, v5\n"
"v_rcp_f32 v2, v2\n"
"v_rcp_f32 v3, v3\n"
"v_rcp_f32 v4, v4\n"
"v_rcp_f32 v5, v5\n"
"v_mul_f32 v228, v228, v2\n"
"v_mul_f32 v229, v229, v3\n"
"v_mul_f32 v230, v230, v4\n"
"v_mul_f32 v231, v231, v5\n"
"v_cvt_pk_bf16_f32 v180, v228, v229\n"
"v_cvt_pk_bf16_f32 v181, v230, v231\n"
"v_accvgpr_write_b32 a92, v180\n"
"v_accvgpr_write_b32 a93, v181\n"
"v_mul_f32 v2, v204, v204\n"
"v_mul_f32 v3, v205, v205\n"
"v_mul_f32 v4, v206, v206\n"
"v_mul_f32 v5, v207, v207\n"
"v_fma_f32 v2, v2, s61, v39\n"
"v_fma_f32 v3, v3, s61, v39\n"
"v_fma_f32 v4, v4, s61, v39\n"
"v_fma_f32 v5, v5, s61, v39\n"
"v_mul_f32 v2, v204, v2\n"
"v_mul_f32 v3, v205, v3\n"
"v_mul_f32 v4, v206, v4\n"
"v_mul_f32 v5, v207, v5\n"
"v_exp_f32 v2, v2\n"
"v_exp_f32 v3, v3\n"
"v_exp_f32 v4, v4\n"
"v_exp_f32 v5, v5\n"
"v_mul_f32 v252, v204, v252\n"
"v_mul_f32 v253, v205, v253\n"
"v_mul_f32 v254, v206, v254\n"
"v_mul_f32 v255, v207, v255\n"
"v_add_f32 v2, 1.0, v2\n"
"v_add_f32 v3, 1.0, v3\n"
"v_add_f32 v4, 1.0, v4\n"
"v_add_f32 v5, 1.0, v5\n"
"v_rcp_f32 v2, v2\n"
"v_rcp_f32 v3, v3\n"
"v_rcp_f32 v4, v4\n"
"v_rcp_f32 v5, v5\n"
"v_mul_f32 v252, v252, v2\n"
"v_mul_f32 v253, v253, v3\n"
"v_mul_f32 v254, v254, v4\n"
"v_mul_f32 v255, v255, v5\n"
"v_cvt_pk_bf16_f32 v204, v252, v253\n"
"v_cvt_pk_bf16_f32 v205, v254, v255\n"
"v_accvgpr_write_b32 a94, v204\n"
"v_accvgpr_write_b32 a95, v205\n"
"LF5_%=:\n"
"s_nop 7\n"
"s_nop 7\n"
"; ---- end of section: up\n"
"v_and_b32 v2, 15, v1\n"
"v_lshrrev_b32 v3, 4, v1\n"
"s_lshl_b32 s53, s42, 2\n"
"v_add_u32 v3, s53, v3\n"
"v_xor_b32 v3, v3, v2\n"
"v_lshlrev_b32 v3, 4, v3\n"
"v_lshl_add_u32 v8, v2, 10, v3\n"
"v_add_u32 v9, 0x10000, v8\n"
"v_bfe_u32 v2, v1, 2, 3\n"
"v_lshrrev_b32 v3, 5, v1\n"
"v_and_b32 v4, 3, v1\n"
"v_lshlrev_b32 v3, 10, v3\n"
"v_lshl_add_u32 v3, v2, 7, v3\n"
"v_lshl_add_u32 v3, v4, 3, v3\n"
"v_add_u32 v3, s49, v3\n"
"v_bfe_u32 v2, v2, 1, 2\n"
"v_xor_b32 v4, 0, v2\n"
"v_lshl_add_u32 v10, v4, 5, v3\n"
"v_xor_b32 v4, 1, v2\n"
"v_lshl_add_u32 v11, v4, 5, v3\n"
"v_xor_b32 v4, 2, v2\n"
"v_lshl_add_u32 v12, v4, 5, v3\n"
"v_xor_b32 v4, 3, v2\n"
"v_lshl_add_u32 v13, v4, 5, v3\n"
"s_waitcnt vmcnt(0)\n"
"s_barrier\n"
"ds_write_b128 v8, a[0:3] offset:0\n"
"ds_write_b128 v8, a[4:7] offset:16384\n"
"ds_write_b128 v8, a[8:11] offset:32768\n"
"ds_write_b128 v8, a[12:15] offset:49152\n"
"ds_write_b128 v9, a[16:19] offset:0\n"
"ds_write_b128 v9, a[20:23] offset:16384\n"
"ds_write_b128 v8, a[24:27] offset:256\n"
"ds_write_b128 v8, a[28:31] offset:16640\n"
"ds_write_b128 v8, a[32:35] offset:33024\n"
"ds_write_b128 v8, a[36:39] offset:49408\n"
"ds_write_b128 v9, a[40:43] offset:256\n"
"ds_write_b128 v9, a[44:47] offset:16640\n"
"ds_write_b128 v8, a[48:51] offset:512\n"
"ds_write_b128 v8, a[52:55] offset:16896\n"
"ds_write_b128 v8, a[56:59] offset:33280\n"
"ds_write_b128 v8, a[60:63] offset:49664\n"
"ds_write_b128 v9, a[64:67] offset:512\n"
"ds_write_b128 v9, a[68:71] offset:16896\n"
"ds_write_b128 v8, a[72:75] offset:768\n"
"ds_write_b128 v8, a[76:79] offset:17152\n"
"ds_write_b128 v8, a[80:83] offset:33536\n"
"ds_write_b128 v8, a[84:87] offset:49920\n"
"ds_write_b128 v9, a[88:91] offset:768\n"
"ds_write_b128 v9, a[92:95] offset:17152\n"
"s_waitcnt lgkmcnt(0)\n"
"s_barrier\n"
"ds_read_b64_tr_b16 a[192:193], v10 offset:0\n"
"ds_read_b64_tr_b16 a[194:195], v10 offset:2048\n"
"ds_read_b64_tr_b16 a[196:197], v11 offset:0\n"
"ds_read_b64_tr_b16 a[198:199], v11 offset:2048\n"
"ds_read_b64_tr_b16 a[200:201], v12 offset:0\n"
"ds_read_b64_tr_b16 a[202:203], v12 offset:2048\n"
"ds_read_b64_tr_b16 a[204:205], v13 offset:0\n"
"ds_read_b64_tr_b16 a[206:207], v13 offset:2048\n"
"ds_read_b128 a[224:227], v14 offset:0\n"
"ds_read_b128 a[228:231], v14 offset:16384\n"
"ds_read_b128 a[232:235], v14 offset:32768\n"
"ds_read_b128 a[236:239], v14 offset:49152\n"
"ds_read_b128 a[240:243], v18 offset:0\n"
"ds_read_b128 a[244:247], v18 offset:16384\n"
"s_mul_i32 s53, s43, 22\n"
"s_add_u32 s53, s53, s44\n"
"s_add_u32 s53, s53, 176\n"
"s_lshl_b32 s53, s53, 2\n"
"s_add_u32 s53, s53, s42\n"
"s_mul_i32 s53, s53, 24576\n"
"v_mov_b32 v24, s53\n"
"s_mov_b64 exec, 1\n"
"global_load_dword v24, v24, s[80:81]\n"
"s_mov_b64 exec, -1\n"
"; ---- end of section: hwr\n"
"s_waitcnt lgkmcnt(5)\n"
"s_nop 2\n"
"v_mfma_f32_16x16x32_bf16 a[0:3], a[192:195], a[224:227], 0\n"
"v_mul_f32 v2, v64, v64\n"
"v_mul_f32 v3, v65, v65\n"
"v_mfma_f32_16x16x32_bf16 a[4:7], a[196:199], a[224:227], 0\n"
"v_mul_f32 v4, v66, v66\n"
"v_mul_f32 v5, v67, v67\n"
"v_mfma_f32_16x16x32_bf16 a[48:51], a[200:203], a[224:227], 0\n"
"ds_read_b64_tr_b16 a[208:209], v10 offset:4096\n"
"ds_read_b64_tr_b16 a[210:211], v10 offset:6144\n"
"v_mfma_f32_16x16x32_bf16 a[52:55], a[204:207], a[224:227], 0\n"
"ds_read_b128 a[224:227], v15 offset:0\n"
"s_waitcnt lgkmcnt(7)\n"
"v_mfma_f32_16x16x32_bf16 a[8:11], a[192:195], a[228:231], 0\n"
"ds_read_b64_tr_b16 a[212:213], v11 offset:4096\n"
"ds_read_b64_tr_b16 a[214:215], v11 offset:6144\n"
"v_mfma_f32_16x16x32_bf16 a[12:15], a[196:199], a[228:231], 0\n"
"v_fma_f32 v2, v2, s61, v39\n"
"v_fma_f32 v3, v3, s61, v39\n"
"v_mfma_f32_16x16x32_bf16 a[56:59], a[200:203], a[228:231], 0\n"
"ds_read_b64_tr_b16 a[216:217], v12 offset:4096\n"
"ds_read_b64_tr_b16 a[218:219], v12 offset:6144\n"
"v_mfma_f32_16x16x32_bf16 a[60:63], a[204:207], a[228:231], 0\n"
"ds_read_b128 a[228:231], v15 offset:16384\n"
"v_fma_f32 v4, v4, s61, v39\n"
"s_waitcnt lgkmcnt(11)\n"
"v_mfma_f32_16x16x32_bf16 a[16:19], a[192:195], a[232:235], 0\n"
"ds_read_b64_tr_b16 a[220:221], v13 offset:4096\n"
"ds_read_b64_tr_b16 a[222:223], v13 offset:6144\n"
"v_mfma_f32_16x16x32_bf16 a[20:23], a[196:199], a[232:235], 0\n"
"v_fma_f32 v5, v5, s61, v39\n"
"v_mul_f32 v2, v64, v2\n"
"v_mfma_f32_16x16x32_bf16 a[64:67], a[200:203], a[232:235], 0\n"
"s_add_u32 s50, s46, 131072\n"
"s_add_u32 m0, s49, 0\n"
"v_mfma_f32_16x16x32_bf16 a[68:71], a[204:207], a[232:235], 0\n"
"ds_read_b128 a[232:235], v15 offset:32768\n"
"buffer_load_dwordx4 v30, s[28:31], s50 offen offset:0 lds\n"
"s_waitcnt lgkmcnt(11)\n"
"v_mfma_f32_16x16x32_bf16 a[24:27], a[192:195], a[236:239], 0\n"
"buffer_load_dwordx4 v31, s[28:31], s50 offen offset:1024 lds\n"
"buffer_load_dwordx4 v32, s[28:31], s50 offen offset:2048 lds\n"
"v_mfma_f32_16x16x32_bf16 a[28:31], a[196:199], a[236:239], 0\n"
"buffer_load_dwordx4 v33, s[28:31], s50 offen offset:3072 lds\n"
"v_mul_f32 v3, v65, v3\n"
"v_mfma_f32_16x16x32_bf16 a[72:75], a[200:203], a[236:239], 0\n"
"v_mul_f32 v4, v66, v4\n"
"v_mul_f32 v5, v67, v5\n"
"v_mfma_f32_16x16x32_bf16 a[76:79], a[204:207], a[236:239], 0\n"
"ds_read_b128 a[236:239], v15 offset:49152\n"
"v_mfma_f32_16x16x32_bf16 a[32:35], a[192:195], a[240:243], 0\n"
"v_exp_f32 v2, v2\n"
"v_mfma_f32_16x16x32_bf16 a[36:39], a[196:199], a[240:243], 0\n"
"v_exp_f32 v3, v3\n"
"v_mfma_f32_16x16x32_bf16 a[80:83], a[200:203], a[240:243], 0\n"
"v_exp_f32 v4, v4\n"
"v_mfma_f32_16x16x32_bf16 a[84:87], a[204:207], a[240:243], 0\n"
"ds_read_b128 a[240:243], v19 offset:0\n"
"v_mfma_f32_16x16x32_bf16 a[40:43], a[192:195], a[244:247], 0\n"
"v_exp_f32 v5, v5\n"
"v_mul_f32 v112, v64, v112\n"
"v_mfma_f32_16x16x32_bf16 a[44:47], a[196:199], a[244:247], 0\n"
"v_mul_f32 v113, v65, v113\n"
"v_mfma_f32_16x16x32_bf16 a[88:91], a[200:203], a[244:247], 0\n"
"v_mul_f32 v114, v66, v114\n"
"v_mul_f32 v115, v67, v115\n"
"v_mfma_f32_16x16x32_bf16 a[92:95], a[204:207], a[244:247], 0\n"
"ds_read_b128 a[244:247], v19 offset:16384\n"
"v_add_f32 v2, 1.0, v2\n"
"s_waitcnt lgkmcnt(3)\n"
"v_mfma_f32_16x16x32_bf16 a[0:3], a[208:211], a[224:227], a[0:3]\n"
"v_add_f32 v3, 1.0, v3\n"
"v_add_f32 v4, 1.0, v4\n"
"v_mfma_f32_16x16x32_bf16 a[4:7], a[212:215], a[224:227], a[4:7]\n"
"v_add_f32 v5, 1.0, v5\n"
"v_mfma_f32_16x16x32_bf16 a[48:51], a[216:219], a[224:227], a[48:51]\n"
"ds_read_b64_tr_b16 a[192:193], v10 offset:8192\n"
"ds_read_b64_tr_b16 a[194:195], v10 offset:10240\n"
"v_mfma_f32_16x16x32_bf16 a[52:55], a[220:223], a[224:227], a[52:55]\n"
"ds_read_b128 a[224:227], v16 offset:0\n"
"v_mfma_f32_16x16x32_bf16 a[8:11], a[208:211], a[228:231], a[8:11]\n"
"ds_read_b64_tr_b16 a[196:197], v11 offset:8192\n"
"ds_read_b64_tr_b16 a[198:199], v11 offset:10240\n"
"v_mfma_f32_16x16x32_bf16 a[12:15], a[212:215], a[228:231], a[12:15]\n"
"v_rcp_f32 v2, v2\n"
"v_mfma_f32_16x16x32_bf16 a[56:59], a[216:219], a[228:231], a[56:59]\n"
"ds_read_b64_tr_b16 a[200:201], v12 offset:8192\n"
"ds_read_b64_tr_b16 a[202:203], v12 offset:10240\n"
"v_mfma_f32_16x16x32_bf16 a[60:63], a[220:223], a[228:231], a[60:63]\n"
"ds_read_b128 a[228:231], v16 offset:16384\n"
"v_rcp_f32 v3, v3\n"
"v_mfma_f32_16x16x32_bf16 a[16:19], a[208:211], a[232:235], a[16:19]\n"
"ds_read_b64_tr_b16 a[204:205], v13 offset:8192\n"
"ds_read_b64_tr_b16 a[206:207], v13 offset:10240\n"
"v_mfma_f32_16x16x32_bf16 a[20:23], a[212:215], a[232:235], a[20:23]\n"
"v_rcp_f32 v4, v4\n"
"v_mfma_f32_16x16x32_bf16 a[64:67], a[216:219], a[232:235], a[64:67]\n"
"s_add_u32 s50, s46, 163840\n"
"s_add_u32 m0, s49, 4096\n"
"v_mfma_f32_16x16x32_bf16 a[68:71], a[220:223], a[232:235], a[68:71]\n"
"ds_read_b128 a[232:235], v16 offset:32768\n"
"buffer_load_dwordx4 v30, s[28:31], s50 offen offset:0 lds\n"
"s_waitcnt lgkmcnt(11)\n"
"v_mfma_f32_16x16x32_bf16 a[24:27], a[208:211], a[236:239], a[24:27]\n"
"buffer_load_dwordx4 v31, s[28:31], s50 offen offset:1024 lds\n"
"buffer_load_dwordx4 v32, s[28:31], s50 offen offset:2048 lds\n"
"v_mfma_f32_16x16x32_bf16 a[28:31], a[212:215], a[236:239], a[28:31]\n"
"buffer_load_dwordx4 v33, s[28:31], s50 offen offset:3072 lds\n"
"v_mfma_f32_16x16x32_bf16 a[72:75], a[216:219], a[236:239], a[72:75]\n"
"v_rcp_f32 v5, v5\n"
"v_mul_f32 v112, v112, v2\n"
"v_mfma_f32_16x16x32_bf16 a[76:79], a[220:223], a[236:239], a[76:79]\n"
"ds_read_b128 a[236:239], v16 offset:49152\n"
"v_mfma_f32_16x16x32_bf16 a[32:35], a[208:211], a[240:243], a[32:35]\n"
"v_mul_f32 v113, v113, v3\n"
"v_mul_f32 v114, v114, v4\n"
"v_mfma_f32_16x16x32_bf16 a[36:39], a[212:215], a[240:243], a[36:39]\n"
"v_mul_f32 v115, v115, v5\n"
"v_cvt_pk_bf16_f32 v40, v112, v113\n"
"v_mfma_f32_16x16x32_bf16 a[80:83], a[216:219], a[240:243], a[80:83]\n"
"v_cvt_pk_bf16_f32 v41, v114, v115\n"
"v_mul_f32 v2, v88, v88\n"
"v_mfma_f32_16x16x32_bf16 a[84:87], a[220:223], a[240:243], a[84:87]\n"
"ds_read_b128 a[240:243], v20 offset:0\n"
"v_mul_f32 v3, v89, v89\n"
"v_mfma_f32_16x16x32_bf16 a[40:43], a[208:211], a[244:247], a[40:43]\n"
"v_mul_f32 v4, v90, v90\n"
"v_mul_f32 v5, v91, v91\n"
"v_mfma_f32_16x16x32_bf16 a[44:47], a[212:215], a[244:247], a[44:47]\n"
"v_fma_f32 v2, v2, s61, v39\n"
"v_mfma_f32_16x16x32_bf16 a[88:91], a[216:219], a[244:247], a[88:91]\n"
"v_fma_f32 v3, v3, s61, v39\n"
"v_fma_f32 v4, v4, s61, v39\n"
"v_mfma_f32_16x16x32_bf16 a[92:95], a[220:223], a[244:247], a[92:95]\n"
"ds_read_b128 a[244:247], v20 offset:16384\n"
"v_fma_f32 v5, v5, s61, v39\n"
"s_waitcnt lgkmcnt(3)\n"
"v_mfma_f32_16x16x32_bf16 a[0:3], a[192:195], a[224:227], a[0:3]\n"
"v_mul_f32 v2, v88, v2\n"
"v_mul_f32 v3, v89, v3\n"
"v_mfma_f32_16x16x32_bf16 a[4:7], a[196:199], a[224:227], a[4:7]\n"
"v_mul_f32 v4, v90, v4\n"
"v_mul_f32 v5, v91, v5\n"
"v_mfma_f32_16x16x32_bf16 a[48:51], a[200:203], a[224:227], a[48:51]\n"
"ds_read_b64_tr_b16 a[208:209], v10 offset:12288\n"
"ds_read_b64_tr_b16 a[210:211], v10 offset:14336\n"
"v_mfma_f32_16x16x32_bf16 a[52:55], a[204:207], a[224:227], a[52:55]\n"
"ds_read_b128 a[224:227], v17 offset:0\n"
"v_mfma_f32_16x16x32_bf16 a[8:11], a[192:195], a[228:231], a[8:11]\n"
"ds_read_b64_tr_b16 a[212:213], v11 offset:12288\n"
"ds_read_b64_tr_b16 a[214:215], v11 offset:14336\n"
"v_mfma_f32_16x16x32_bf16 a[12:15], a[196:199], a[228:231], a[12:15]\n"
"v_exp_f32 v2, v2\n"
"v_mfma_f32_16x16x32_bf16 a[56:59], a[200:203], a[228:231], a[56:59]\n"
"ds_read_b64_tr_b16 a[216:217], v12 offset:12288\n"
"ds_read_b64_tr_b16 a[218:219], v12 offset:14336\n"
"v_mfma_f32_16x16x32_bf16 a[60:63], a[204:207], a[228:231], a[60:63]\n"
"ds_read_b128 a[228:231], v17 offset:16384\n"
"v_exp_f32 v3, v3\n"
"v_mfma_f32_16x16x32_bf16 a[16:19], a[192:195], a[232:235], a[16:19]\n"
"ds_read_b64_tr_b16 a[220:221], v13 offset:12288\n"
"ds_read_b64_tr_b16 a[222:223], v13 offset:14336\n"
"v_mfma_f32_16x16x32_bf16 a[20:23], a[196:199], a[232:235], a[20:23]\n"
"v_exp_f32 v4, v4\n"
"v_mfma_f32_16x16x32_bf16 a[64:67], a[200:203], a[232:235], a[64:67]\n"
"s_add_u32 s50, s46, 196608\n"
"s_add_u32 m0, s49, 8192\n"
"v_mfma_f32_16x16x32_bf16 a[68:71], a[204:207], a[232:235], a[68:71]\n"
"ds_read_b128 a[232:235], v17 offset:32768\n"
"buffer_load_dwordx4 v30, s[28:31], s50 offen offset:0 lds\n"
"s_waitcnt lgkmcnt(11)\n"
"v_mfma_f32_16x16x32_bf16 a[24:27], a[192:195], a[236:239], a[24:27]\n"
"buffer_load_dwordx4 v31, s[28:31], s50 offen offset:1024 lds\n"
"buffer_load_dwordx4 v32, s[28:31], s50 offen offset:2048 lds\n"
"v_mfma_f32_16x16x32_bf16 a[28:31], a[196:199], a[236:239], a[28:31]\n"
"buffer_load_dwordx4 v33, s[28:31], s50 offen offset:3072 lds\n"
"v_exp_f32 v5, v5\n"
"v_mfma_f32_16x16x32_bf16 a[72:75], a[200:203], a[236:239], a[72:75]\n"
"v_mul_f32 v136, v88, v136\n"
"v_mul_f32 v137, v89, v137\n"
"v_mfma_f32_16x16x32_bf16 a[76:79], a[204:207], a[236:239], a[76:79]\n"
"ds_read_b128 a[236:239], v17 offset:49152\n"
"v_mul_f32 v138, v90, v138\n"
"v_mfma_f32_16x16x32_bf16 a[32:35], a[192:195], a[240:243], a[32:35]\n"
"v_mul_f32 v139, v91, v139\n"
"v_add_f32 v2, 1.0, v2\n"
"v_mfma_f32_16x16x32_bf16 a[36:39], a[196:199], a[240:243], a[36:39]\n"
"v_add_f32 v3, 1.0, v3\n"
"v_add_f32 v4, 1.0, v4\n"
"v_mfma_f32_16x16x32_bf16 a[80:83], a[200:203], a[240:243], a[80:83]\n"
"v_add_f32 v5, 1.0, v5\n"
"v_mfma_f32_16x16x32_bf16 a[84:87], a[204:207], a[240:243], a[84:87]\n"
"ds_read_b128 a[240:243], v21 offset:0\n"
"v_rcp_f32 v2, v2\n"
"v_mfma_f32_16x16x32_bf16 a[40:43], a[192:195], a[244:247], a[40:43]\n"
"v_rcp_f32 v3, v3\n"
"v_mfma_f32_16x16x32_bf16 a[44:47], a[196:199], a[244:247], a[44:47]\n"
"v_rcp_f32 v4, v4\n"
"v_mfma_f32_16x16x32_bf16 a[88:91], a[200:203], a[244:247], a[88:91]\n"
"v_rcp_f32 v5, v5\n"
"v_mfma_f32_16x16x32_bf16 a[92:95], a[204:207], a[244:247], a[92:95]\n"
"ds_read_b128 a[244:247], v21 offset:16384\n"
"v_mul_f32 v136, v136, v2\n"
"s_waitcnt lgkmcnt(3)\n"
"v_mfma_f32_16x16x32_bf16 a[0:3], a[208:211], a[224:227], a[0:3]\n"
"v_mul_f32 v137, v137, v3\n"
"v_mul_f32 v138, v138, v4\n"
"v_mfma_f32_16x16x32_bf16 a[4:7], a[212:215], a[224:227], a[4:7]\n"
"v_mul_f32 v139, v139, v5\n"
"s_waitcnt vmcnt(8)\n"
"v_mfma_f32_16x16x32_bf16 a[48:51], a[216:219], a[224:227], a[48:51]\n"
"ds_read_b64_tr_b16 a[192:193], v10 offset:0\n"
"ds_read_b64_tr_b16 a[194:195], v10 offset:2048\n"
"v_mfma_f32_16x16x32_bf16 a[52:55], a[220:223], a[224:227], a[52:55]\n"
"ds_read_b128 a[224:227], v14 offset:256\n"
"v_cvt_pk_bf16_f32 v42, v136, v137\n"
"v_mfma_f32_16x16x32_bf16 a[8:11], a[208:211], a[228:231], a[8:11]\n"
"ds_read_b64_tr_b16 a[196:197], v11 offset:0\n"
"ds_read_b64_tr_b16 a[198:199], v11 offset:2048\n"
"v_mfma_f32_16x16x32_bf16 a[12:15], a[212:215], a[228:231], a[12:15]\n"
"v_cvt_pk_bf16_f32 v43, v138, v139\n"
"v_mul_f32 v2, v68, v68\n"
"v_mfma_f32_16x16x32_bf16 a[56:59], a[216:219], a[228:231], a[56:59]\n"
"ds_read_b64_tr_b16 a[200:201], v12 offset:0\n"
"ds_read_b64_tr_b16 a[202:203], v12 offset:2048\n"
"v_mfma_f32_16x16x32_bf16 a[60:63], a[220:223], a[228:231], a[60:63]\n"
"ds_read_b128 a[228:231], v14 offset:16640\n"
"v_mfma_f32_16x16x32_bf16 a[16:19], a[208:211], a[232:235], a[16:19]\n"
"ds_read_b64_tr_b16 a[204:205], v13 offset:0\n"
"ds_read_b64_tr_b16 a[206:207], v13 offset:2048\n"
"v_mfma_f32_16x16x32_bf16 a[20:23], a[212:215], a[232:235], a[20:23]\n"
"v_mul_f32 v3, v69, v69\n"
"v_mul_f32 v4, v70, v70\n"
"v_mfma_f32_16x16x32_bf16 a[64:67], a[216:219], a[232:235], a[64:67]\n"
"s_add_u32 s50, s46, 229376\n"
"s_add_u32 m0, s49, 12288\n"
"v_mfma_f32_16x16x32_bf16 a[68:71], a[220:223], a[232:235], a[68:71]\n"
"ds_read_b128 a[232:235], v14 offset:33024\n"
"buffer_load_dwordx4 v30, s[28:31], s50 offen offset:0 lds\n"
"s_waitcnt lgkmcnt(11)\n"
"v_mfma_f32_16x16x32_bf16 a[24:27], a[208:211], a[236:239], a[24:27]\n"
"buffer_load_dwordx4 v31, s[28:31], s50 offen offset:1024 lds\n"
"buffer_load_dwordx4 v32, s[28:31], s50 offen offset:2048 lds\n"
"v_mfma_f32_16x16x32_bf16 a[28:31], a[212:215], a[236:239], a[28:31]\n"
"buffer_load_dwordx4 v33, s[28:31], s50 offen offset:3072 lds\n"
"v_mul_f32 v5, v71, v71\n"
"v_mfma_f32_16x16x32_bf16 a[72:75], a[216:219], a[236:239], a[72:75]\n"
"v_fma_f32 v2, v2, s61, v39\n"
"v_fma_f32 v3, v3, s61, v39\n"
"v_mfma_f32_16x16x32_bf16 a[76:79], a[220:223], a[236:239], a[76:79]\n"
"ds_read_b128 a[236:239], v14 offset:49408\n"
"v_fma_f32 v4, v4, s61, v39\n"
"v_mfma_f32_16x16x32_bf16 a[32:35], a[208:211], a[240:243], a[32:35]\n"
"v_fma_f32 v5, v5, s61, v39\n"
"v_mul_f32 v2, v68, v2\n"
"v_mfma_f32_16x16x32_bf16 a[36:39], a[212:215], a[240:243], a[36:39]\n"
"v_mul_f32 v3, v69, v3\n"
"v_mul_f32 v4, v70, v4\n"
"v_mfma_f32_16x16x32_bf16 a[80:83], a[216:219], a[240:243], a[80:83]\n"
"v_mul_f32 v5, v71, v5\n"
"v_mfma_f32_16x16x32_bf16 a[84:87], a[220:223], a[240:243], a[84:87]\n"
"ds_read_b128 a[240:243], v18 offset:256\n"
"v_mfma_f32_16x16x32_bf16 a[40:43], a[208:211], a[244:247], a[40:43]\n"
"v_exp_f32 v2, v2\n"
"v_mfma_f32_16x16x32_bf16 a[44:47], a[212:215], a[244:247], a[44:47]\n"
"v_exp_f32 v3, v3\n"
"v_mfma_f32_16x16x32_bf16 a[88:91], a[216:219], a[244:247], a[88:91]\n"
"v_exp_f32 v4, v4\n"
"v_mfma_f32_16x16x32_bf16 a[92:95], a[220:223], a[244:247], a[92:95]\n"
"ds_read_b128 a[244:247], v18 offset:16640\n"
"v_exp_f32 v5, v5\n"
"s_waitcnt lgkmcnt(3)\n"
"v_mfma_f32_16x16x32_bf16 a[0:3], a[192:195], a[224:227], a[0:3]\n"
"v_mul_f32 v116, v68, v116\n"
"v_mul_f32 v117, v69, v117\n"
"v_mfma_f32_16x16x32_bf16 a[4:7], a[196:199], a[224:227], a[4:7]\n"
"v_mul_f32 v118, v70, v118\n"
"s_waitcnt vmcnt(8)\n"
"v_mfma_f32_16x16x32_bf16 a[48:51], a[200:203], a[224:227], a[48:51]\n"
"ds_read_b64_tr_b16 a[208:209], v10 offset:4096\n"
"ds_read_b64_tr_b16 a[210:211], v10 offset:6144\n"
"v_mfma_f32_16x16x32_bf16 a[52:55], a[204:207], a[224:227], a[52:55]\n"
"ds_read_b128 a[224:227], v15 offset:256\n"
"v_mul_f32 v119, v71, v119\n"
"v_mfma_f32_16x16x32_bf16 a[8:11], a[192:195], a[228:231], a[8:11]\n"
"ds_read_b64_tr_b16 a[212:213], v11 offset:4096\n"
"ds_read_b64_tr_b16 a[214:215], v11 offset:6144\n"
"v_mfma_f32_16x16x32_bf16 a[12:15], a[196:199], a[228:231], a[12:15]\n"
"v_add_f32 v2, 1.0, v2\n"
"v_add_f32 v3, 1.0, v3\n"
"v_mfma_f32_16x16x32_bf16 a[56:59], a[200:203], a[228:231], a[56:59]\n"
"ds_read_b64_tr_b16 a[216:217], v12 offset:4096\n"
"ds_read_b64_tr_b16 a[218:219], v12 offset:6144\n"
"v_mfma_f32_16x16x32_bf16 a[60:63], a[204:207], a[228:231], a[60:63]\n"
"ds_read_b128 a[228:231], v15 offset:16640\n"
"v_mfma_f32_16x16x32_bf16 a[16:19], a[192:195], a[232:235], a[16:19]\n"
"ds_read_b64_tr_b16 a[220:221], v13 offset:4096\n"
"ds_read_b64_tr_b16 a[222:223], v13 offset:6144\n"
"v_mfma_f32_16x16x32_bf16 a[20:23], a[196:199], a[232:235], a[20:23]\n"
"v_add_f32 v4, 1.0, v4\n"
"v_add_f32 v5, 1.0, v5\n"
"v_mfma_f32_16x16x32_bf16 a[64:67], a[200:203], a[232:235], a[64:67]\n"
"s_add_u32 s50, s46, 262144\n"
"s_add_u32 m0, s49, 0\n"
"v_mfma_f32_16x16x32_bf16 a[68:71], a[204:207], a[232:235], a[68:71]\n"
"ds_read_b128 a[232:235], v15 offset:33024\n"
"buffer_load_dwordx4 v30, s[28:31], s50 offen offset:0 lds\n"
"s_waitcnt lgkmcnt(11)\n"
"v_mfma_f32_16x16x32_bf16 a[24:27], a[192:195], a[236:239], a[24:27]\n"
"buffer_load_dwordx4 v31, s[28:31], s50 offen offset:1024 lds\n"
"buffer_load_dwordx4 v32, s[28:31], s50 offen offset:2048 lds\n"
"v_mfma_f32_16x16x32_bf16 a[28:31], a[196:199], a[236:239], a[28:31]\n"
"buffer_load_dwordx4 v33, s[28:31], s50 offen offset:3072 lds\n"
"v_mfma_f32_16x16x32_bf16 a[72:75], a[200:203], a[236:239], a[72:75]\n"
"v_rcp_f32 v2, v2\n"
"v_mfma_f32_16x16x32_bf16 a[76:79], a[204:207], a[236:239], a[76:79]\n"
"ds_read_b128 a[236:239], v15 offset:49408\n"
"v_rcp_f32 v3, v3\n"
"v_mfma_f32_16x16x32_bf16 a[32:35], a[192:195], a[240:243], a[32:35]\n"
"v_rcp_f32 v4, v4\n"
"v_mfma_f32_16x16x32_bf16 a[36:39], a[196:199], a[240:243], a[36:39]\n"
"v_rcp_f32 v5, v5\n"
"v_mfma_f32_16x16x32_bf16 a[80:83], a[200:203], a[240:243], a[80:83]\n"
"v_mul_f32 v116, v116, v2\n"
"v_mul_f32 v117, v117, v3\n"
"v_mfma_f32_16x16x32_bf16 a[84:87], a[204:207], a[240:243], a[84:87]\n"
"ds_read_b128 a[240:243], v19 offset:256\n"
"v_mfma_f32_16x16x32_bf16 a[40:43], a[192:195], a[244:247], a[40:43]\n"
"v_mul_f32 v118, v118, v4\n"
"v_mul_f32 v119, v119, v5\n"
"v_mfma_f32_16x16x32_bf16 a[44:47], a[196:199], a[244:247], a[44:47]\n"
"v_cvt_pk_bf16_f32 v44, v116, v117\n"
"v_cvt_pk_bf16_f32 v45, v118, v119\n"
"v_mfma_f32_16x16x32_bf16 a[88:91], a[200:203], a[244:247], a[88:91]\n"
"v_mul_f32 v2, v92, v92\n"
"v_mul_f32 v3, v93, v93\n"
"v_mfma_f32_16x16x32_bf16 a[92:95], a[204:207], a[244:247], a[92:95]\n"
"ds_read_b128 a[244:247], v19 offset:16640\n"
"v_mul_f32 v4, v94, v94\n"
"s_waitcnt lgkmcnt(3)\n"
"v_mfma_f32_16x16x32_bf16 a[0:3], a[208:211], a[224:227], a[0:3]\n"
"v_mul_f32 v5, v95, v95\n"
"v_fma_f32 v2, v2, s61, v39\n"
"v_mfma_f32_16x16x32_bf16 a[4:7], a[212:215], a[224:227], a[4:7]\n"
"v_fma_f32 v3, v3, s61, v39\n"
"s_waitcnt vmcnt(8)\n"
"v_mfma_f32_16x16x32_bf16 a[48:51], a[216:219], a[224:227], a[48:51]\n"
"ds_read_b64_tr_b16 a[192:193], v10 offset:8192\n"
"ds_read_b64_tr_b16 a[194:195], v10 offset:10240\n"
"v_mfma_f32_16x16x32_bf16 a[52:55], a[220:223], a[224:227], a[52:55]\n"
"ds_read_b128 a[224:227], v16 offset:256\n"
"v_fma_f32 v4, v4, s61, v39\n"
"v_mfma_f32_16x16x32_bf16 a[8:11], a[208:211], a[228:231], a[8:11]\n"
"ds_read_b64_tr_b16 a[196:197], v11 offset:8192\n"
"ds_read_b64_tr_b16 a[198:199], v11 offset:10240\n"
"v_mfma_f32_16x16x32_bf16 a[12:15], a[212:215], a[228:231], a[12:15]\n"
"v_fma_f32 v5, v5, s61, v39\n"
"v_mul_f32 v2, v92, v2\n"
"v_mfma_f32_16x16x32_bf16 a[56:59], a[216:219], a[228:231], a[56:59]\n"
"ds_read_b64_tr_b16 a[200:201], v12 offset:8192\n"
"ds_read_b64_tr_b16 a[202:203], v12 offset:10240\n"
"v_mfma_f32_16x16x32_bf16 a[60:63], a[220:223], a[228:231], a[60:63]\n"
"ds_read_b128 a[228:231], v16 offset:16640\n"
"v_mfma_f32_16x16x32_bf16 a[16:19], a[208:211], a[232:235], a[16:19]\n"
"ds_read_b64_tr_b16 a[204:205], v13 offset:8192\n"
"ds_read_b64_tr_b16 a[206:207], v13 offset:10240\n"
"v_mfma_f32_16x16x32_bf16 a[20:23], a[212:215], a[232:235], a[20:23]\n"
"v_mul_f32 v3, v93, v3\n"
"v_mul_f32 v4, v94, v4\n"
"v_mfma_f32_16x16x32_bf16 a[64:67], a[216:219], a[232:235], a[64:67]\n"
"s_add_u32 s50, s46, 294912\n"
"s_add_u32 m0, s49, 4096\n"
"v_mfma_f32_16x16x32_bf16 a[68:71], a[220:223], a[232:235], a[68:71]\n"
"ds_read_b128 a[232:235], v16 offset:33024\n"
"buffer_load_dwordx4 v30, s[28:31], s50 offen offset:0 lds\n"
"s_waitcnt lgkmcnt(11)\n"
"v_mfma_f32_16x16x32_bf16 a[24:27], a[208:211], a[236:239], a[24:27]\n"
"buffer_load_dwordx4 v31, s[28:31], s50 offen offset:1024 lds\n"
"buffer_load_dwordx4 v32, s[28:31], s50 offen offset:2048 lds\n"
"v_mfma_f32_16x16x32_bf16 a[28:31], a[212:215], a[236:239], a[28:31]\n"
"buffer_load_dwordx4 v33, s[28:31], s50 offen offset:3072 lds\n"
"v_mul_f32 v5, v95, v5\n"
"v_mfma_f32_16x16x32_bf16 a[72:75], a[216:219], a[236:239], a[72:75]\n"
"v_exp_f32 v2, v2\n"
"v_mfma_f32_16x16x32_bf16 a[76:79], a[220:223], a[236:239], a[76:79]\n"
"ds_read_b128 a[236:239], v16 offset:49408\n"
"v_mfma_f32_16x16x32_bf16 a[32:35], a[208:211], a[240:243], a[32:35]\n"
"v_exp_f32 v3, v3\n"
"v_mfma_f32_16x16x32_bf16 a[36:39], a[212:215], a[240:243], a[36:39]\n"
"v_exp_f32 v4, v4\n"
"v_mfma_f32_16x16x32_bf16 a[80:83], a[216:219], a[240:243], a[80:83]\n"
"v_exp_f32 v5, v5\n"
"v_mul_f32 v140, v92, v140\n"
"v_mfma_f32_16x16x32_bf16 a[84:87], a[220:223], a[240:243], a[84:87]\n"
"ds_read_b128 a[240:243], v20 offset:256\n"
"v_mfma_f32_16x16x32_bf16 a[40:43], a[208:211], a[244:247], a[40:43]\n"
"v_mul_f32 v141, v93, v141\n"
"v_mul_f32 v142, v94, v142\n"
"v_mfma_f32_16x16x32_bf16 a[44:47], a[212:215], a[244:247], a[44:47]\n"
"v_mul_f32 v143, v95, v143\n"
"v_add_f32 v2, 1.0, v2\n"
"v_mfma_f32_16x16x32_bf16 a[88:91], a[216:219], a[244:247], a[88:91]\n"
"v_add_f32 v3, 1.0, v3\n"
"v_add_f32 v4, 1.0, v4\n"
"v_mfma_f32_16x16x32_bf16 a[92:95], a[220:223], a[244:247], a[92:95]\n"
"ds_read_b128 a[244:247], v20 offset:16640\n"
"v_add_f32 v5, 1.0, v5\n"
"s_waitcnt lgkmcnt(3)\n"
"v_mfma_f32_16x16x32_bf16 a[0:3], a[192:195], a[224:227], a[0:3]\n"
"v_rcp_f32 v2, v2\n"
"v_mfma_f32_16x16x32_bf16 a[4:7], a[196:199], a[224:227], a[4:7]\n"
"s_waitcnt vmcnt(8)\n"
"v_mfma_f32_16x16x32_bf16 a[48:51], a[200:203], a[224:227], a[48:51]\n"
"ds_read_b64_tr_b16 a[208:209], v10 offset:12288\n"
"ds_read_b64_tr_b16 a[210:211], v10 offset:14336\n"
"v_mfma_f32_16x16x32_bf16 a[52:55], a[204:207], a[224:227], a[52:55]\n"
"ds_read_b128 a[224:227], v17 offset:256\n"
"v_rcp_f32 v3, v3\n"
"v_mfma_f32_16x16x32_bf16 a[8:11], a[192:195], a[228:231], a[8:11]\n"
"ds_read_b64_tr_b16 a[212:213], v11 offset:12288\n"
"ds_read_b64_tr_b16 a[214:215], v11 offset:14336\n"
"v_mfma_f32_16x16x32_bf16 a[12:15], a[196:199], a[228:231], a[12:15]\n"
"v_rcp_f32 v4, v4\n"
"v_mfma_f32_16x16x32_bf16 a[56:59], a[200:203], a[228:231], a[56:59]\n"
"ds_read_b64_tr_b16 a[216:217], v12 offset:12288\n"
"ds_read_b64_tr_b16 a[218:219], v12 offset:14336\n"
"v_mfma_f32_16x16x32_bf16 a[60:63], a[204:207], a[228:231], a[60:63]\n"
"ds_read_b128 a[228:231], v17 offset:16640\n"
"v_mfma_f32_16x16x32_bf16 a[16:19], a[192:195], a[232:235], a[16:19]\n"
"ds_read_b64_tr_b16 a[220:221], v13 offset:12288\n"
"ds_read_b64_tr_b16 a[222:223], v13 offset:14336\n"
"v_mfma_f32_16x16x32_bf16 a[20:23], a[196:199], a[232:235], a[20:23]\n"
"v_rcp_f32 v5, v5\n"
"v_mfma_f32_16x16x32_bf16 a[64:67], a[200:203], a[232:235], a[64:67]\n"
"s_add_u32 s50, s46, 327680\n"
"s_add_u32 m0, s49, 8192\n"
"v_mfma_f32_16x16x32_bf16 a[68:71], a[204:207], a[232:235], a[68:71]\n"
"ds_read_b128 a[232:235], v17 offset:33024\n"
"buffer_load_dwordx4 v30, s[28:31], s50 offen offset:0 lds\n"
"s_waitcnt lgkmcnt(11)\n"
"v_mfma_f32_16x16x32_bf16 a[24:27], a[192:195], a[236:239], a[24:27]\n"
"buffer_load_dwordx4 v31, s[28:31], s50 offen offset:1024 lds\n"
"buffer_load_dwordx4 v32, s[28:31], s50 offen offset:2048 lds\n"
"v_mfma_f32_16x16x32_bf16 a[28:31], a[196:199], a[236:239], a[28:31]\n"
"buffer_load_dwordx4 v33, s[28:31], s50 offen offset:3072 lds\n"
"v_mul_f32 v140, v140, v2\n"
"v_mfma_f32_16x16x32_bf16 a[72:75], a[200:203], a[236:239], a[72:75]\n"
"v_mul_f32 v141, v141, v3\n"
"v_mul_f32 v142, v142, v4\n"
"v_mfma_f32_16x16x32_bf16 a[76:79], a[204:207], a[236:239], a[76:79]\n"
"ds_read_b128 a[236:239], v17 offset:49408\n"
"v_mul_f32 v143, v143, v5\n"
"v_mfma_f32_16x16x32_bf16 a[32:35], a[192:195], a[240:243], a[32:35]\n"
"v_cvt_pk_bf16_f32 v46, v140, v141\n"
"v_cvt_pk_bf16_f32 v47, v142, v143\n"
"v_mfma_f32_16x16x32_bf16 a[36:39], a[196:199], a[240:243], a[36:39]\n"
"v_mul_f32 v2, v72, v72\n"
"v_mul_f32 v3, v73, v73\n"
"v_mfma_f32_16x16x32_bf16 a[80:83], a[200:203], a[240:243], a[80:83]\n"
"v_mul_f32 v4, v74, v74\n"
"v_mul_f32 v5, v75, v75\n"
"v_mfma_f32_16x16x32_bf16 a[84:87], a[204:207], a[240:243], a[84:87]\n"
"ds_read_b128 a[240:243], v21 offset:256\n"
"v_mfma_f32_16x16x32_bf16 a[40:43], a[192:195], a[244:247], a[40:43]\n"
"v_fma_f32 v2, v2, s61, v39\n"
"v_fma_f32 v3, v3, s61, v39\n"
"v_mfma_f32_16x16x32_bf16 a[44:47], a[196:199], a[244:247], a[44:47]\n"
"v_fma_f32 v4, v4, s61, v39\n"
"v_fma_f32 v5, v5, s61, v39\n"
"v_mfma_f32_16x16x32_bf16 a[88:91], a[200:203], a[244:247], a[88:91]\n"
"v_mul_f32 v2, v72, v2\n"
"v_mul_f32 v3, v73, v3\n"
"v_mfma_f32_16x16x32_bf16 a[92:95], a[204:207], a[244:247], a[92:95]\n"
"ds_read_b128 a[244:247], v21 offset:16640\n"
"v_mul_f32 v4, v74, v4\n"
"s_waitcnt lgkmcnt(3)\n"
"v_mfma_f32_16x16x32_bf16 a[0:3], a[208:211], a[224:227], a[0:3]\n"
"v_mul_f32 v5, v75, v5\n"
"v_mfma_f32_16x16x32_bf16 a[4:7], a[212:215], a[224:227], a[4:7]\n"
"v_exp_f32 v2, v2\n"
"s_waitcnt vmcnt(8)\n"
"v_mfma_f32_16x16x32_bf16 a[48:51], a[216:219], a[224:227], a[48:51]\n"
"ds_read_b64_tr_b16 a[192:193], v10 offset:0\n"
"ds_read_b64_tr_b16 a[194:195], v10 offset:2048\n"
"v_mfma_f32_16x16x32_bf16 a[52:55], a[220:223], a[224:227], a[52:55]\n"
"ds_read_b128 a[224:227], v14 offset:512\n"
"v_mfma_f32_16x16x32_bf16 a[8:11], a[208:211], a[228:231], a[8:11]\n"
"ds_read_b64_tr_b16 a[196:197], v11 offset:0\n"
"ds_read_b64_tr_b16 a[198:199], v11 offset:2048\n"
"v_mfma_f32_16x16x32_bf16 a[12:15], a[212:215], a[228:231], a[12:15]\n"
"v_exp_f32 v3, v3\n"
"v_exp_f32 v4, v4\n"
"v_mfma_f32_16x16x32_bf16 a[56:59], a[216:219], a[228:231], a[56:59]\n"
"ds_read_b64_tr_b16 a[200:201], v12 offset:0\n"
"ds_read_b64_tr_b16 a[202:203], v12 offset:2048\n"
"v_mfma_f32_16x16x32_bf16 a[60:63], a[220:223], a[228:231], a[60:63]\n"
"ds_read_b128 a[228:231], v14 offset:16896\n"
"v_mfma_f32_16x16x32_bf16 a[16:19], a[208:211], a[232:235], a[16:19]\n"
"ds_read_b64_tr_b16 a[204:205], v13 offset:0\n"
"ds_read_b64_tr_b16 a[206:207], v13 offset:2048\n"
"v_mfma_f32_16x16x32_bf16 a[20:23], a[212:215], a[232:235], a[20:23]\n"
"v_exp_f32 v5, v5\n"
"v_mul_f32 v120, v72, v120\n"
"v_mfma_f32_16x16x32_bf16 a[64:67], a[216:219], a[232:235], a[64:67]\n"
"s_add_u32 s50, s46, 360448\n"
"s_add_u32 m0, s49, 12288\n"
"v_mfma_f32_16x16x32_bf16 a[68:71], a[220:223], a[232:235], a[68:71]\n"
"ds_read_b128 a[232:235], v14 offset:33280\n"
"buffer_load_dwordx4 v30, s[28:31], s50 offen offset:0 lds\n"
"s_waitcnt lgkmcnt(11)\n"
"v_mfma_f32_16x16x32_bf16 a[24:27], a[208:211], a[236:239], a[24:27]\n"
"buffer_load_dwordx4 v31, s[28:31], s50 offen offset:1024 lds\n"
"buffer_load_dwordx4 v32, s[28:31], s50 offen offset:2048 lds\n"
"v_mfma_f32_16x16x32_bf16 a[28:31], a[212:215], a[236:239], a[28:31]\n"
"buffer_load_dwordx4 v33, s[28:31], s50 offen offset:3072 lds\n"
"v_mul_f32 v121, v73, v121\n"
"v_mfma_f32_16x16x32_bf16 a[72:75], a[216:219], a[236:239], a[72:75]\n"
"v_mul_f32 v122, v74, v122\n"
"v_mul_f32 v123, v75, v123\n"
"v_mfma_f32_16x16x32_bf16 a[76:79], a[220:223], a[236:239], a[76:79]\n"
"ds_read_b128 a[236:239], v14 offset:49664\n"
"v_add_f32 v2, 1.0, v2\n"
"v_mfma_f32_16x16x32_bf16 a[32:35], a[208:211], a[240:243], a[32:35]\n"
"v_add_f32 v3, 1.0, v3\n"
"v_add_f32 v4, 1.0, v4\n"
"v_mfma_f32_16x16x32_bf16 a[36:39], a[212:215], a[240:243], a[36:39]\n"
"v_add_f32 v5, 1.0, v5\n"
"v_rcp_f32 v2, v2\n"
"v_mfma_f32_16x16x32_bf16 a[80:83], a[216:219], a[240:243], a[80:83]\n"
"v_rcp_f32 v3, v3\n"
"v_mfma_f32_16x16x32_bf16 a[84:87], a[220:223], a[240:243], a[84:87]\n"
"ds_read_b128 a[240:243], v18 offset:512\n"
"v_mfma_f32_16x16x32_bf16 a[40:43], a[208:211], a[244:247], a[40:43]\n"
"v_rcp_f32 v4, v4\n"
"v_mfma_f32_16x16x32_bf16 a[44:47], a[212:215], a[244:247], a[44:47]\n"
"v_rcp_f32 v5, v5\n"
"v_mul_f32 v120, v120, v2\n"
"v_mfma_f32_16x16x32_bf16 a[88:91], a[216:219], a[244:247], a[88:91]\n"
"v_mul_f32 v121, v121, v3\n"
"v_mul_f32 v122, v122, v4\n"
"v_mfma_f32_16x16x32_bf16 a[92:95], a[220:223], a[244:247], a[92:95]\n"
"ds_read_b128 a[244:247], v18 offset:16896\n"
"v_mul_f32 v123, v123, v5\n"
"s_waitcnt lgkmcnt(3)\n"
"v_mfma_f32_16x16x32_bf16 a[0:3], a[192:195], a[224:227], a[0:3]\n"
"v_cvt_pk_bf16_f32 v48, v120, v121\n"
"v_cvt_pk_bf16_f32 v49, v122, v123\n"
"v_mfma_f32_16x16x32_bf16 a[4:7], a[196:199], a[224:227], a[4:7]\n"
"v_mul_f32 v2, v96, v96\n"
"s_waitcnt vmcnt(8)\n"
"v_mfma_f32_16x16x32_bf16 a[48:51], a[200:203], a[224:227], a[48:51]\n"
"ds_read_b64_tr_b16 a[208:209], v10 offset:4096\n"
"ds_read_b64_tr_b16 a[210:211], v10 offset:6144\n"
"v_mfma_f32_16x16x32_bf16 a[52:55], a[204:207], a[224:227], a[52:55]\n"
"ds_read_b128 a[224:227], v15 offset:512\n"
"v_mul_f32 v3, v97, v97\n"
"v_mfma_f32_16x16x32_bf16 a[8:11], a[192:195], a[228:231], a[8:11]\n"
"ds_read_b64_tr_b16 a[212:213], v11 offset:4096\n"
"ds_read_b64_tr_b16 a[214:215], v11 offset:6144\n"
"v_mfma_f32_16x16x32_bf16 a[12:15], a[196:199], a[228:231], a[12:15]\n"
"v_mul_f32 v4, v98, v98\n"
"v_mul_f32 v5, v99, v99\n"
"v_mfma_f32_16x16x32_bf16 a[56:59], a[200:203], a[228:231], a[56:59]\n"
"ds_read_b64_tr_b16 a[216:217], v12 offset:4096\n"
"ds_read_b64_tr_b16 a[218:219], v12 offset:6144\n"
"v_mfma_f32_16x16x32_bf16 a[60:63], a[204:207], a[228:231], a[60:63]\n"
"ds_read_b128 a[228:231], v15 offset:16896\n"
"v_mfma_f32_16x16x32_bf16 a[16:19], a[192:195], a[232:235], a[16:19]\n"
"ds_read_b64_tr_b16 a[220:221], v13 offset:4096\n"
"ds_read_b64_tr_b16 a[222:223], v13 offset:6144\n"
"v_mfma_f32_16x16x32_bf16 a[20:23], a[196:199], a[232:235], a[20:23]\n"
"v_fma_f32 v2, v2, s61, v39\n"
"v_fma_f32 v3, v3, s61, v39\n"
"v_mfma_f32_16x16x32_bf16 a[64:67], a[200:203], a[232:235], a[64:67]\n"
"s_add_u32 s50, s46, 393216\n"
"s_add_u32 m0, s49, 0\n"
"v_mfma_f32_16x16x32_bf16 a[68:71], a[204:207], a[232:235], a[68:71]\n"
"ds_read_b128 a[232:235], v15 offset:33280\n"
"buffer_load_dwordx4 v30, s[28:31], s50 offen offset:0 lds\n"
"s_waitcnt lgkmcnt(11)\n"
"v_mfma_f32_16x16x32_bf16 a[24:27], a[192:195], a[236:239], a[24:27]\n"
"buffer_load_dwordx4 v31, s[28:31], s50 offen offset:1024 lds\n"
"buffer_load_dwordx4 v32, s[28:31], s50 offen offset:2048 lds\n"
"v_mfma_f32_16x16x32_bf16 a[28:31], a[196:199], a[236:239], a[28:31]\n"
"buffer_load_dwordx4 v33, s[28:31], s50 offen offset:3072 lds\n"
"v_fma_f32 v4, v4, s61, v39\n"
"v_mfma_f32_16x16x32_bf16 a[72:75], a[200:203], a[236:239], a[72:75]\n"
"v_fma_f32 v5, v5, s61, v39\n"
"v_mul_f32 v2, v96, v2\n"
"v_mfma_f32_16x16x32_bf16 a[76:79], a[204:207], a[236:239], a[76:79]\n"
"ds_read_b128 a[236:239], v15 offset:49664\n"
"v_mul_f32 v3, v97, v3\n"
"v_mfma_f32_16x16x32_bf16 a[32:35], a[192:195], a[240:243], a[32:35]\n"
"v_mul_f32 v4, v98, v4\n"
"v_mul_f32 v5, v99, v5\n"
"v_mfma_f32_16x16x32_bf16 a[36:39], a[196:199], a[240:243], a[36:39]\n"
"v_exp_f32 v2, v2\n"
"v_mfma_f32_16x16x32_bf16 a[80:83], a[200:203], a[240:243], a[80:83]\n"
"v_exp_f32 v3, v3\n"
"v_mfma_f32_16x16x32_bf16 a[84:87], a[204:207], a[240:243], a[84:87]\n"
"ds_read_b128 a[240:243], v19 offset:512\n"
"v_mfma_f32_16x16x32_bf16 a[40:43], a[192:195], a[244:247], a[40:43]\n"
"v_exp_f32 v4, v4\n"
"v_mfma_f32_16x16x32_bf16 a[44:47], a[196:199], a[244:247], a[44:47]\n"
"v_exp_f32 v5, v5\n"
"v_mfma_f32_16x16x32_bf16 a[88:91], a[200:203], a[244:247], a[88:91]\n"
"v_mul_f32 v144, v96, v144\n"
"v_mul_f32 v145, v97, v145\n"
"v_mfma_f32_16x16x32_bf16 a[92:95], a[204:207], a[244:247], a[92:95]\n"
"ds_read_b128 a[244:247], v19 offset:16896\n"
"v_mul_f32 v146, v98, v146\n"
"s_waitcnt lgkmcnt(3)\n"
"v_mfma_f32_16x16x32_bf16 a[0:3], a[208:211], a[224:227], a[0:3]\n"
"v_mul_f32 v147, v99, v147\n"
"v_add_f32 v2, 1.0, v2\n"
"v_mfma_f32_16x16x32_bf16 a[4:7], a[212:215], a[224:227], a[4:7]\n"
"v_add_f32 v3, 1.0, v3\n"
"s_waitcnt vmcnt(8)\n"
"v_mfma_f32_16x16x32_bf16 a[48:51], a[216:219], a[224:227], a[48:51]\n"
"ds_read_b64_tr_b16 a[192:193], v10 offset:8192\n"
"ds_read_b64_tr_b16 a[194:195], v10 offset:10240\n"
"v_mfma_f32_16x16x32_bf16 a[52:55], a[220:223], a[224:227], a[52:55]\n"
"ds_read_b128 a[224:227], v16 offset:512\n"
"v_add_f32 v4, 1.0, v4\n"
"v_mfma_f32_16x16x32_bf16 a[8:11], a[208:211], a[228:231], a[8:11]\n"
"ds_read_b64_tr_b16 a[196:197], v11 offset:8192\n"
"ds_read_b64_tr_b16 a[198:199], v11 offset:10240\n"
"v_mfma_f32_16x16x32_bf16 a[12:15], a[212:215], a[228:231], a[12:15]\n"
"v_add_f32 v5, 1.0, v5\n"
"v_mfma_f32_16x16x32_bf16 a[56:59], a[216:219], a[228:231], a[56:59]\n"
"ds_read_b64_tr_b16 a[200:201], v12 offset:8192\n"
"ds_read_b64_tr_b16 a[202:203], v12 offset:10240\n"
"v_mfma_f32_16x16x32_bf16 a[60:63], a[220:223], a[228:231], a[60:63]\n"
"ds_read_b128 a[228:231], v16 offset:16896\n"
"v_mfma_f32_16x16x32_bf16 a[16:19], a[208:211], a[232:235], a[16:19]\n"
"ds_read_b64_tr_b16 a[204:205], v13 offset:8192\n"
"ds_read_b64_tr_b16 a[206:207], v13 offset:10240\n"
"v_mfma_f32_16x16x32_bf16 a[20:23], a[212:215], a[232:235], a[20:23]\n"
"v_rcp_f32 v2, v2\n"
"v_mfma_f32_16x16x32_bf16 a[64:67], a[216:219], a[232:235], a[64:67]\n"
"s_add_u32 s50, s46, 425984\n"
"s_add_u32 m0, s49, 4096\n"
"v_mfma_f32_16x16x32_bf16 a[68:71], a[220:223], a[232:235], a[68:71]\n"
"ds_read_b128 a[232:235], v16 offset:33280\n"
"buffer_load_dwordx4 v30, s[28:31], s50 offen offset:0 lds\n"
"s_waitcnt lgkmcnt(11)\n"
"v_mfma_f32_16x16x32_bf16 a[24:27], a[208:211], a[236:239], a[24:27]\n"
"buffer_load_dwordx4 v31, s[28:31], s50 offen offset:1024 lds\n"
"buffer_load_dwordx4 v32, s[28:31], s50 offen offset:2048 lds\n"
"v_mfma_f32_16x16x32_bf16 a[28:31], a[212:215], a[236:239], a[28:31]\n"
"buffer_load_dwordx4 v33, s[28:31], s50 offen offset:3072 lds\n"
"v_rcp_f32 v3, v3\n"
"v_mfma_f32_16x16x32_bf16 a[72:75], a[216:219], a[236:239], a[72:75]\n"
"v_rcp_f32 v4, v4\n"
"v_mfma_f32_16x16x32_bf16 a[76:79], a[220:223], a[236:239], a[76:79]\n"
"ds_read_b128 a[236:239], v16 offset:49664\n"
"v_mfma_f32_16x16x32_bf16 a[32:35], a[208:211], a[240:243], a[32:35]\n"
"v_rcp_f32 v5, v5\n"
"v_mul_f32 v144, v144, v2\n"
"v_mfma_f32_16x16x32_bf16 a[36:39], a[212:215], a[240:243], a[36:39]\n"
"v_mul_f32 v145, v145, v3\n"
"v_mul_f32 v146, v146, v4\n"
"v_mfma_f32_16x16x32_bf16 a[80:83], a[216:219], a[240:243], a[80:83]\n"
"v_mul_f32 v147, v147, v5\n"
"v_cvt_pk_bf16_f32 v50, v144, v145\n"
"v_mfma_f32_16x16x32_bf16 a[84:87], a[220:223], a[240:243], a[84:87]\n"
"ds_read_b128 a[240:243], v20 offset:512\n"
"v_mfma_f32_16x16x32_bf16 a[40:43], a[208:211], a[244:247], a[40:43]\n"
"v_cvt_pk_bf16_f32 v51, v146, v147\n"
"v_mul_f32 v2, v76, v76\n"
"v_mfma_f32_16x16x32_bf16 a[44:47], a[212:215], a[244:247], a[44:47]\n"
"v_mul_f32 v3, v77, v77\n"
"v_mul_f32 v4, v78, v78\n"
"v_mfma_f32_16x16x32_bf16 a[88:91], a[216:219], a[244:247], a[88:91]\n"
"v_mul_f32 v5, v79, v79\n"
"v_fma_f32 v2, v2, s61, v39\n"
"v_mfma_f32_16x16x32_bf16 a[92:95], a[220:223], a[244:247], a[92:95]\n"
"ds_read_b128 a[244:247], v20 offset:16896\n"
"v_fma_f32 v3, v3, s61, v39\n"
"s_waitcnt lgkmcnt(3)\n"
"v_mfma_f32_16x16x32_bf16 a[0:3], a[192:195], a[224:227], a[0:3]\n"
"v_fma_f32 v4, v4, s61, v39\n"
"v_fma_f32 v5, v5, s61, v39\n"
"v_mfma_f32_16x16x32_bf16 a[4:7], a[196:199], a[224:227], a[4:7]\n"
"v_mul_f32 v2, v76, v2\n"
"s_waitcnt vmcnt(8)\n"
"v_mfma_f32_16x16x32_bf16 a[48:51], a[200:203], a[224:227], a[48:51]\n"
"ds_read_b64_tr_b16 a[208:209], v10 offset:12288\n"
"ds_read_b64_tr_b16 a[210:211], v10 offset:14336\n"
"v_mfma_f32_16x16x32_bf16 a[52:55], a[204:207], a[224:227], a[52:55]\n"
"ds_read_b128 a[224:227], v17 offset:512\n"
"v_mul_f32 v3, v77, v3\n"
"v_mfma_f32_16x16x32_bf16 a[8:11], a[192:195], a[228:231], a[8:11]\n"
"ds_read_b64_tr_b16 a[212:213], v11 offset:12288\n"
"ds_read_b64_tr_b16 a[214:215], v11 offset:14336\n"
"v_mfma_f32_16x16x32_bf16 a[12:15], a[196:199], a[228:231], a[12:15]\n"
"v_mul_f32 v4, v78, v4\n"
"v_mul_f32 v5, v79, v5\n"
"v_mfma_f32_16x16x32_bf16 a[56:59], a[200:203], a[228:231], a[56:59]\n"
"ds_read_b64_tr_b16 a[216:217], v12 offset:12288\n"
"ds_read_b64_tr_b16 a[218:219], v12 offset:14336\n"
"v_mfma_f32_16x16x32_bf16 a[60:63], a[204:207], a[228:231], a[60:63]\n"
"ds_read_b128 a[228:231], v17 offset:16896\n"
"v_mfma_f32_16x16x32_bf16 a[16:19], a[192:195], a[232:235], a[16:19]\n"
"ds_read_b64_tr_b16 a[220:221], v13 offset:12288\n"
"ds_read_b64_tr_b16 a[222:223], v13 offset:14336\n"
"v_mfma_f32_16x16x32_bf16 a[20:23], a[196:199], a[232:235], a[20:23]\n"
"v_exp_f32 v2, v2\n"
"v_mfma_f32_16x16x32_bf16 a[64:67], a[200:203], a[232:235], a[64:67]\n"
"s_add_u32 s50, s46, 458752\n"
"s_add_u32 m0, s49, 8192\n"
"v_mfma_f32_16x16x32_bf16 a[68:71], a[204:207], a[232:235], a[68:71]\n"
"ds_read_b128 a[232:235], v17 offset:33280\n"
"buffer_load_dwordx4 v30, s[28:31], s50 offen offset:0 lds\n"
"v_exp_f32 v3, v3\n"
"s_waitcnt lgkmcnt(11)\n"
"v_mfma_f32_16x16x32_bf16 a[24:27], a[192:195], a[236:239], a[24:27]\n"
"buffer_load_dwordx4 v31, s[28:31], s50 offen offset:1024 lds\n"
"buffer_load_dwordx4 v32, s[28:31], s50 offen offset:2048 lds\n"
"v_mfma_f32_16x16x32_bf16 a[28:31], a[196:199], a[236:239], a[28:31]\n"
"buffer_load_dwordx4 v33, s[28:31], s50 offen offset:3072 lds\n"
"v_mfma_f32_16x16x32_bf16 a[72:75], a[200:203], a[236:239], a[72:75]\n"
"v_exp_f32 v4, v4\n"
"v_mfma_f32_16x16x32_bf16 a[76:79], a[204:207], a[236:239], a[76:79]\n"
"ds_read_b128 a[236:239], v17 offset:49664\n"
"v_exp_f32 v5, v5\n"
"v_mfma_f32_16x16x32_bf16 a[32:35], a[192:195], a[240:243], a[32:35]\n"
"v_mul_f32 v124, v76, v124\n"
"v_mul_f32 v125, v77, v125\n"
"v_mfma_f32_16x16x32_bf16 a[36:39], a[196:199], a[240:243], a[36:39]\n"
"v_mul_f32 v126, v78, v126\n"
"v_mul_f32 v127, v79, v127\n"
"v_mfma_f32_16x16x32_bf16 a[80:83], a[200:203], a[240:243], a[80:83]\n"
"v_add_f32 v2, 1.0, v2\n"
"v_add_f32 v3, 1.0, v3\n"
"v_mfma_f32_16x16x32_bf16 a[84:87], a[204:207], a[240:243], a[84:87]\n"
"ds_read_b128 a[240:243], v21 offset:512\n"
"v_add_f32 v4, 1.0, v4\n"
"v_mfma_f32_16x16x32_bf16 a[40:43], a[192:195], a[244:247], a[40:43]\n"
"v_add_f32 v5, 1.0, v5\n"
"v_mfma_f32_16x16x32_bf16 a[44:47], a[196:199], a[244:247], a[44:47]\n"
"v_rcp_f32 v2, v2\n"
"v_mfma_f32_16x16x32_bf16 a[88:91], a[200:203], a[244:247], a[88:91]\n"
"v_rcp_f32 v3, v3\n"
"v_mfma_f32_16x16x32_bf16 a[92:95], a[204:207], a[244:247], a[92:95]\n"
"ds_read_b128 a[244:247], v21 offset:16896\n"
"v_rcp_f32 v4, v4\n"
"s_waitcnt lgkmcnt(3)\n"
"v_mfma_f32_16x16x32_bf16 a[0:3], a[208:211], a[224:227], a[0:3]\n"
"v_rcp_f32 v5, v5\n"
"v_mfma_f32_16x16x32_bf16 a[4:7], a[212:215], a[224:227], a[4:7]\n"
"v_mul_f32 v124, v124, v2\n"
"s_waitcnt vmcnt(8)\n"
"v_mfma_f32_16x16x32_bf16 a[48:51], a[216:219], a[224:227], a[48:51]\n"
"ds_read_b64_tr_b16 a[192:193], v10 offset:0\n"
"ds_read_b64_tr_b16 a[194:195], v10 offset:2048\n"
"v_mfma_f32_16x16x32_bf16 a[52:55], a[220:223], a[224:227], a[52:55]\n"
"ds_read_b128 a[224:227], v14 offset:768\n"
"v_mfma_f32_16x16x32_bf16 a[8:11], a[208:211], a[228:231], a[8:11]\n"
"ds_read_b64_tr_b16 a[196:197], v11 offset:0\n"
"ds_read_b64_tr_b16 a[198:199], v11 offset:2048\n"
"v_mfma_f32_16x16x32_bf16 a[12:15], a[212:215], a[228:231], a[12:15]\n"
"v_mul_f32 v125, v125, v3\n"
"v_mul_f32 v126, v126, v4\n"
"v_mfma_f32_16x16x32_bf16 a[56:59], a[216:219], a[228:231], a[56:59]\n"
"ds_read_b64_tr_b16 a[200:201], v12 offset:0\n"
"ds_read_b64_tr_b16 a[202:203], v12 offset:2048\n"
"v_mfma_f32_16x16x32_bf16 a[60:63], a[220:223], a[228:231], a[60:63]\n"
"ds_read_b128 a[228:231], v14 offset:17152\n"
"v_mul_f32 v127, v127, v5\n"
"v_mfma_f32_16x16x32_bf16 a[16:19], a[208:211], a[232:235], a[16:19]\n"
"ds_read_b64_tr_b16 a[204:205], v13 offset:0\n"
"ds_read_b64_tr_b16 a[206:207], v13 offset:2048\n"
"v_mfma_f32_16x16x32_bf16 a[20:23], a[212:215], a[232:235], a[20:23]\n"
"v_cvt_pk_bf16_f32 v52, v124, v125\n"
"v_cvt_pk_bf16_f32 v53, v126, v127\n"
"v_mfma_f32_16x16x32_bf16 a[64:67], a[216:219], a[232:235], a[64:67]\n"
"s_add_u32 s50, s46, 491520\n"
"s_add_u32 m0, s49, 12288\n"
"v_mfma_f32_16x16x32_bf16 a[68:71], a[220:223], a[232:235], a[68:71]\n"
"ds_read_b128 a[232:235], v14 offset:33536\n"
"buffer_load_dwordx4 v30, s[28:31], s50 offen offset:0 lds\n"
"s_waitcnt lgkmcnt(11)\n"
"v_mfma_f32_16x16x32_bf16 a[24:27], a[208:211], a[236:239], a[24:27]\n"
"buffer_load_dwordx4 v31, s[28:31], s50 offen offset:1024 lds\n"
"buffer_load_dwordx4 v32, s[28:31], s50 offen offset:2048 lds\n"
"v_mfma_f32_16x16x32_bf16 a[28:31], a[212:215], a[236:239], a[28:31]\n"
"buffer_load_dwordx4 v33, s[28:31], s50 offen offset:3072 lds\n"
"v_mul_f32 v2, v100, v100\n"
"v_mfma_f32_16x16x32_bf16 a[72:75], a[216:219], a[236:239], a[72:75]\n"
"v_mul_f32 v3, v101, v101\n"
"v_mul_f32 v4, v102, v102\n"
"v_mfma_f32_16x16x32_bf16 a[76:79], a[220:223], a[236:239], a[76:79]\n"
"ds_read_b128 a[236:239], v14 offset:49920\n"
"v_mfma_f32_16x16x32_bf16 a[32:35], a[208:211], a[240:243], a[32:35]\n"
"v_mul_f32 v5, v103, v103\n"
"v_fma_f32 v2, v2, s61, v39\n"
"v_mfma_f32_16x16x32_bf16 a[36:39], a[212:215], a[240:243], a[36:39]\n"
"v_fma_f32 v3, v3, s61, v39\n"
"v_fma_f32 v4, v4, s61, v39\n"
"v_mfma_f32_16x16x32_bf16 a[80:83], a[216:219], a[240:243], a[80:83]\n"
"v_fma_f32 v5, v5, s61, v39\n"
"v_mul_f32 v2, v100, v2\n"
"v_mfma_f32_16x16x32_bf16 a[84:87], a[220:223], a[240:243], a[84:87]\n"
"ds_read_b128 a[240:243], v18 offset:768\n"
"v_mul_f32 v3, v101, v3\n"
"v_mfma_f32_16x16x32_bf16 a[40:43], a[208:211], a[244:247], a[40:43]\n"
"v_mul_f32 v4, v102, v4\n"
"v_mul_f32 v5, v103, v5\n"
"v_mfma_f32_16x16x32_bf16 a[44:47], a[212:215], a[244:247], a[44:47]\n"
"v_mfma_f32_16x16x32_bf16 a[88:91], a[216:219], a[244:247], a[88:91]\n"
"v_exp_f32 v2, v2\n"
"v_mfma_f32_16x16x32_bf16 a[92:95], a[220:223], a[244:247], a[92:95]\n"
"ds_read_b128 a[244:247], v18 offset:17152\n"
"v_exp_f32 v3, v3\n"
"s_waitcnt lgkmcnt(3)\n"
"v_mfma_f32_16x16x32_bf16 a[0:3], a[192:195], a[224:227], a[0:3]\n"
"v_exp_f32 v4, v4\n"
"v_mfma_f32_16x16x32_bf16 a[4:7], a[196:199], a[224:227], a[4:7]\n"
"s_waitcnt vmcnt(8)\n"
"v_mfma_f32_16x16x32_bf16 a[48:51], a[200:203], a[224:227], a[48:51]\n"
"ds_read_b64_tr_b16 a[208:209], v10 offset:4096\n"
"ds_read_b64_tr_b16 a[210:211], v10 offset:6144\n"
"v_mfma_f32_16x16x32_bf16 a[52:55], a[204:207], a[224:227], a[52:55]\n"
"ds_read_b128 a[224:227], v15 offset:768\n"
"v_exp_f32 v5, v5\n"
"v_mfma_f32_16x16x32_bf16 a[8:11], a[192:195], a[228:231], a[8:11]\n"
"ds_read_b64_tr_b16 a[212:213], v11 offset:4096\n"
"ds_read_b64_tr_b16 a[214:215], v11 offset:6144\n"
"v_mfma_f32_16x16x32_bf16 a[12:15], a[196:199], a[228:231], a[12:15]\n"
"v_mul_f32 v148, v100, v148\n"
"v_mul_f32 v149, v101, v149\n"
"v_mfma_f32_16x16x32_bf16 a[56:59], a[200:203], a[228:231], a[56:59]\n"
"ds_read_b64_tr_b16 a[216:217], v12 offset:4096\n"
"ds_read_b64_tr_b16 a[218:219], v12 offset:6144\n"
"v_mfma_f32_16x16x32_bf16 a[60:63], a[204:207], a[228:231], a[60:63]\n"
"ds_read_b128 a[228:231], v15 offset:17152\n"
"v_mul_f32 v150, v102, v150\n"
"v_mfma_f32_16x16x32_bf16 a[16:19], a[192:195], a[232:235], a[16:19]\n"
"ds_read_b64_tr_b16 a[220:221], v13 offset:4096\n"
"ds_read_b64_tr_b16 a[222:223], v13 offset:6144\n"
"v_mfma_f32_16x16x32_bf16 a[20:23], a[196:199], a[232:235], a[20:23]\n"
"v_mul_f32 v151, v103, v151\n"
"v_add_f32 v2, 1.0, v2\n"
"v_mfma_f32_16x16x32_bf16 a[64:67], a[200:203], a[232:235], a[64:67]\n"
"s_add_u32 s50, s46, 0\n"
"s_add_u32 m0, s49, 0\n"
"v_mfma_f32_16x16x32_bf16 a[68:71], a[204:207], a[232:235], a[68:71]\n"
"ds_read_b128 a[232:235], v15 offset:33536\n"
"buffer_load_dwordx4 v34, s[28:31], s50 offen offset:0 lds\n"
"s_waitcnt lgkmcnt(11)\n"
"v_mfma_f32_16x16x32_bf16 a[24:27], a[192:195], a[236:239], a[24:27]\n"
"buffer_load_dwordx4 v35, s[28:31], s50 offen offset:1024 lds\n"
"buffer_load_dwordx4 v36, s[28:31], s50 offen offset:2048 lds\n"
"v_mfma_f32_16x16x32_bf16 a[28:31], a[196:199], a[236:239], a[28:31]\n"
"buffer_load_dwordx4 v37, s[28:31], s50 offen offset:3072 lds\n"
"v_add_f32 v3, 1.0, v3\n"
"v_mfma_f32_16x16x32_bf16 a[72:75], a[200:203], a[236:239], a[72:75]\n"
"v_add_f32 v4, 1.0, v4\n"
"v_add_f32 v5, 1.0, v5\n"
"v_mfma_f32_16x16x32_bf16 a[76:79], a[204:207], a[236:239], a[76:79]\n"
"ds_read_b128 a[236:239], v15 offset:49920\n"
"v_mfma_f32_16x16x32_bf16 a[32:35], a[192:195], a[240:243], a[32:35]\n"
"v_rcp_f32 v2, v2\n"
"v_mfma_f32_16x16x32_bf16 a[36:39], a[196:199], a[240:243], a[36:39]\n"
"v_rcp_f32 v3, v3\n"
"v_mfma_f32_16x16x32_bf16 a[80:83], a[200:203], a[240:243], a[80:83]\n"
"v_rcp_f32 v4, v4\n"
"v_mfma_f32_16x16x32_bf16 a[84:87], a[204:207], a[240:243], a[84:87]\n"
"ds_read_b128 a[240:243], v19 offset:768\n"
"v_rcp_f32 v5, v5\n"
"v_mfma_f32_16x16x32_bf16 a[40:43], a[192:195], a[244:247], a[40:43]\n"
"v_mul_f32 v148, v148, v2\n"
"v_mul_f32 v149, v149, v3\n"
"v_mfma_f32_16x16x32_bf16 a[44:47], a[196:199], a[244:247], a[44:47]\n"
"v_mul_f32 v150, v150, v4\n"
"v_mul_f32 v151, v151, v5\n"
"v_mfma_f32_16x16x32_bf16 a[88:91], a[200:203], a[244:247], a[88:91]\n"
"v_cvt_pk_bf16_f32 v54, v148, v149\n"
"v_cvt_pk_bf16_f32 v55, v150, v151\n"
"v_mfma_f32_16x16x32_bf16 a[92:95], a[204:207], a[244:247], a[92:95]\n"
"ds_read_b128 a[244:247], v19 offset:17152\n"
"v_mul_f32 v2, v80, v80\n"
"s_waitcnt lgkmcnt(3)\n"
"v_mfma_f32_16x16x32_bf16 a[0:3], a[208:211], a[224:227], a[0:3]\n"
"v_mul_f32 v3, v81, v81\n"
"v_mul_f32 v4, v82, v82\n"
"v_mfma_f32_16x16x32_bf16 a[4:7], a[212:215], a[224:227], a[4:7]\n"
"v_mul_f32 v5, v83, v83\n"
"s_waitcnt vmcnt(8)\n"
"v_mfma_f32_16x16x32_bf16 a[48:51], a[216:219], a[224:227], a[48:51]\n"
"ds_read_b64_tr_b16 a[192:193], v10 offset:8192\n"
"ds_read_b64_tr_b16 a[194:195], v10 offset:10240\n"
"v_mfma_f32_16x16x32_bf16 a[52:55], a[220:223], a[224:227], a[52:55]\n"
"ds_read_b128 a[224:227], v16 offset:768\n"
"v_fma_f32 v2, v2, s61, v39\n"
"v_mfma_f32_16x16x32_bf16 a[8:11], a[208:211], a[228:231], a[8:11]\n"
"ds_read_b64_tr_b16 a[196:197], v11 offset:8192\n"
"ds_read_b64_tr_b16 a[198:199], v11 offset:10240\n"
"v_mfma_f32_16x16x32_bf16 a[12:15], a[212:215], a[228:231], a[12:15]\n"
"v_fma_f32 v3, v3, s61, v39\n"
"v_fma_f32 v4, v4, s61, v39\n"
"v_mfma_f32_16x16x32_bf16 a[56:59], a[216:219], a[228:231], a[56:59]\n"
"ds_read_b64_tr_b16 a[200:201], v12 offset:8192\n"
"ds_read_b64_tr_b16 a[202:203], v12 offset:10240\n"
"v_mfma_f32_16x16x32_bf16 a[60:63], a[220:223], a[228:231], a[60:63]\n"
"ds_read_b128 a[228:231], v16 offset:17152\n"
"v_mfma_f32_16x16x32_bf16 a[16:19], a[208:211], a[232:235], a[16:19]\n"
"ds_read_b64_tr_b16 a[204:205], v13 offset:8192\n"
"ds_read_b64_tr_b16 a[206:207], v13 offset:10240\n"
"v_mfma_f32_16x16x32_bf16 a[20:23], a[212:215], a[232:235], a[20:23]\n"
"v_fma_f32 v5, v5, s61, v39\n"
"v_mul_f32 v2, v80, v2\n"
"v_mfma_f32_16x16x32_bf16 a[64:67], a[216:219], a[232:235], a[64:67]\n"
"s_add_u32 s50, s46, 32768\n"
"s_add_u32 m0, s49, 4096\n"
"v_mfma_f32_16x16x32_bf16 a[68:71], a[220:223], a[232:235], a[68:71]\n"
"ds_read_b128 a[232:235], v16 offset:33536\n"
"buffer_load_dwordx4 v34, s[28:31], s50 offen offset:0 lds\n"
"s_waitcnt lgkmcnt(11)\n"
"v_mfma_f32_16x16x32_bf16 a[24:27], a[208:211], a[236:239], a[24:27]\n"
"buffer_load_dwordx4 v35, s[28:31], s50 offen offset:1024 lds\n"
"buffer_load_dwordx4 v36, s[28:31], s50 offen offset:2048 lds\n"
"v_mfma_f32_16x16x32_bf16 a[28:31], a[212:215], a[236:239], a[28:31]\n"
"buffer_load_dwordx4 v37, s[28:31], s50 offen offset:3072 lds\n"
"v_mul_f32 v3, v81, v3\n"
"v_mfma_f32_16x16x32_bf16 a[72:75], a[216:219], a[236:239], a[72:75]\n"
"v_mul_f32 v4, v82, v4\n"
"v_mul_f32 v5, v83, v5\n"
"v_mfma_f32_16x16x32_bf16 a[76:79], a[220:223], a[236:239], a[76:79]\n"
"ds_read_b128 a[236:239], v16 offset:49920\n"
"v_mfma_f32_16x16x32_bf16 a[32:35], a[208:211], a[240:243], a[32:35]\n"
"v_exp_f32 v2, v2\n"
"v_mfma_f32_16x16x32_bf16 a[36:39], a[212:215], a[240:243], a[36:39]\n"
"v_exp_f32 v3, v3\n"
"v_mfma_f32_16x16x32_bf16 a[80:83], a[216:219], a[240:243], a[80:83]\n"
"v_exp_f32 v4, v4\n"
"v_mfma_f32_16x16x32_bf16 a[84:87], a[220:223], a[240:243], a[84:87]\n"
"ds_read_b128 a[240:243], v20 offset:768\n"
"v_mfma_f32_16x16x32_bf16 a[40:43], a[208:211], a[244:247], a[40:43]\n"
"v_exp_f32 v5, v5\n"
"v_mul_f32 v128, v80, v128\n"
"v_mfma_f32_16x16x32_bf16 a[44:47], a[212:215], a[244:247], a[44:47]\n"
"v_mul_f32 v129, v81, v129\n"
"v_mul_f32 v130, v82, v130\n"
"v_mfma_f32_16x16x32_bf16 a[88:91], a[216:219], a[244:247], a[88:91]\n"
"v_mul_f32 v131, v83, v131\n"
"v_add_f32 v2, 1.0, v2\n"
"v_mfma_f32_16x16x32_bf16 a[92:95], a[220:223], a[244:247], a[92:95]\n"
"ds_read_b128 a[244:247], v20 offset:17152\n"
"v_add_f32 v3, 1.0, v3\n"
"s_waitcnt lgkmcnt(3)\n"
"v_mfma_f32_16x16x32_bf16 a[0:3], a[192:195], a[224:227], a[0:3]\n"
"v_add_f32 v4, 1.0, v4\n"
"v_add_f32 v5, 1.0, v5\n"
"v_mfma_f32_16x16x32_bf16 a[4:7], a[196:199], a[224:227], a[4:7]\n"
"s_waitcnt vmcnt(8)\n"
"v_mfma_f32_16x16x32_bf16 a[48:51], a[200:203], a[224:227], a[48:51]\n"
"ds_read_b64_tr_b16 a[208:209], v10 offset:12288\n"
"ds_read_b64_tr_b16 a[210:211], v10 offset:14336\n"
"v_mfma_f32_16x16x32_bf16 a[52:55], a[204:207], a[224:227], a[52:55]\n"
"ds_read_b128 a[224:227], v17 offset:768\n"
"v_rcp_f32 v2, v2\n"
"v_mfma_f32_16x16x32_bf16 a[8:11], a[192:195], a[228:231], a[8:11]\n"
"ds_read_b64_tr_b16 a[212:213], v11 offset:12288\n"
"ds_read_b64_tr_b16 a[214:215], v11 offset:14336\n"
"v_mfma_f32_16x16x32_bf16 a[12:15], a[196:199], a[228:231], a[12:15]\n"
"v_rcp_f32 v3, v3\n"
"v_mfma_f32_16x16x32_bf16 a[56:59], a[200:203], a[228:231], a[56:59]\n"
"ds_read_b64_tr_b16 a[216:217], v12 offset:12288\n"
"ds_read_b64_tr_b16 a[218:219], v12 offset:14336\n"
"v_mfma_f32_16x16x32_bf16 a[60:63], a[204:207], a[228:231], a[60:63]\n"
"ds_read_b128 a[228:231], v17 offset:17152\n"
"v_mfma_f32_16x16x32_bf16 a[16:19], a[192:195], a[232:235], a[16:19]\n"
"ds_read_b64_tr_b16 a[220:221], v13 offset:12288\n"
"ds_read_b64_tr_b16 a[222:223], v13 offset:14336\n"
"v_mfma_f32_16x16x32_bf16 a[20:23], a[196:199], a[232:235], a[20:23]\n"
"v_rcp_f32 v4, v4\n"
"v_mfma_f32_16x16x32_bf16 a[64:67], a[200:203], a[232:235], a[64:67]\n"
"s_add_u32 s50, s46, 65536\n"
"s_add_u32 m0, s49, 8192\n"
"v_mfma_f32_16x16x32_bf16 a[68:71], a[204:207], a[232:235], a[68:71]\n"
"ds_read_b128 a[232:235], v17 offset:33536\n"
"buffer_load_dwordx4 v34, s[28:31], s50 offen offset:0 lds\n"
"s_waitcnt lgkmcnt(11)\n"
"v_mfma_f32_16x16x32_bf16 a[24:27], a[192:195], a[236:239], a[24:27]\n"
"buffer_load_dwordx4 v35, s[28:31], s50 offen offset:1024 lds\n"
"buffer_load_dwordx4 v36, s[28:31], s50 offen offset:2048 lds\n"
"v_mfma_f32_16x16x32_bf16 a[28:31], a[196:199], a[236:239], a[28:31]\n"
"buffer_load_dwordx4 v37, s[28:31], s50 offen offset:3072 lds\n"
"v_mfma_f32_16x16x32_bf16 a[72:75], a[200:203], a[236:239], a[72:75]\n"
"v_rcp_f32 v5, v5\n"
"v_mul_f32 v128, v128, v2\n"
"v_mfma_f32_16x16x32_bf16 a[76:79], a[204:207], a[236:239], a[76:79]\n"
"ds_read_b128 a[236:239], v17 offset:49920\n"
"v_mul_f32 v129, v129, v3\n"
"v_mfma_f32_16x16x32_bf16 a[32:35], a[192:195], a[240:243], a[32:35]\n"
"v_mul_f32 v130, v130, v4\n"
"v_mul_f32 v131, v131, v5\n"
"v_mfma_f32_16x16x32_bf16 a[36:39], a[196:199], a[240:243], a[36:39]\n"
"v_cvt_pk_bf16_f32 v56, v128, v129\n"
"v_cvt_pk_bf16_f32 v57, v130, v131\n"
"v_mfma_f32_16x16x32_bf16 a[80:83], a[200:203], a[240:243], a[80:83]\n"
"v_mul_f32 v2, v104, v104\n"
"v_mul_f32 v3, v105, v105\n"
"v_mfma_f32_16x16x32_bf16 a[84:87], a[204:207], a[240:243], a[84:87]\n"
"ds_read_b128 a[240:243], v21 offset:768\n"
"v_mfma_f32_16x16x32_bf16 a[40:43], a[192:195], a[244:247], a[40:43]\n"
"v_mul_f32 v4, v106, v106\n"
"v_mul_f32 v5, v107, v107\n"
"v_mfma_f32_16x16x32_bf16 a[44:47], a[196:199], a[244:247], a[44:47]\n"
"v_fma_f32 v2, v2, s61, v39\n"
"v_fma_f32 v3, v3, s61, v39\n"
"v_mfma_f32_16x16x32_bf16 a[88:91], a[200:203], a[244:247], a[88:91]\n"
"v_fma_f32 v4, v4, s61, v39\n"
"v_fma_f32 v5, v5, s61, v39\n"
"v_mfma_f32_16x16x32_bf16 a[92:95], a[204:207], a[244:247], a[92:95]\n"
"ds_read_b128 a[244:247], v21 offset:17152\n"
"v_mul_f32 v2, v104, v2\n"
"s_waitcnt lgkmcnt(3)\n"
"v_mfma_f32_16x16x32_bf16 a[0:3], a[208:211], a[224:227], a[0:3]\n"
"v_mul_f32 v3, v105, v3\n"
"v_mul_f32 v4, v106, v4\n"
"v_mfma_f32_16x16x32_bf16 a[4:7], a[212:215], a[224:227], a[4:7]\n"
"v_mul_f32 v5, v107, v5\n"
"s_waitcnt vmcnt(8)\n"
"v_mfma_f32_16x16x32_bf16 a[48:51], a[216:219], a[224:227], a[48:51]\n"
"ds_read_b64_tr_b16 a[192:193], v10 offset:0\n"
"ds_read_b64_tr_b16 a[194:195], v10 offset:2048\n"
"v_mfma_f32_16x16x32_bf16 a[52:55], a[220:223], a[224:227], a[52:55]\n"
"ds_read_b128 a[224:227], v14 offset:0\n"
"v_mfma_f32_16x16x32_bf16 a[8:11], a[208:211], a[228:231], a[8:11]\n"
"ds_read_b64_tr_b16 a[196:197], v11 offset:0\n"
"ds_read_b64_tr_b16 a[198:199], v11 offset:2048\n"
"v_mfma_f32_16x16x32_bf16 a[12:15], a[212:215], a[228:231], a[12:15]\n"
"v_exp_f32 v2, v2\n"
"v_exp_f32 v3, v3\n"
"v_mfma_f32_16x16x32_bf16 a[56:59], a[216:219], a[228:231], a[56:59]\n"
"ds_read_b64_tr_b16 a[200:201], v12 offset:0\n"
"ds_read_b64_tr_b16 a[202:203], v12 offset:2048\n"
"v_mfma_f32_16x16x32_bf16 a[60:63], a[220:223], a[228:231], a[60:63]\n"
"ds_read_b128 a[228:231], v14 offset:16384\n"
"v_mfma_f32_16x16x32_bf16 a[16:19], a[208:211], a[232:235], a[16:19]\n"
"ds_read_b64_tr_b16 a[204:205], v13 offset:0\n"
"ds_read_b64_tr_b16 a[206:207], v13 offset:2048\n"
"v_mfma_f32_16x16x32_bf16 a[20:23], a[212:215], a[232:235], a[20:23]\n"
"v_exp_f32 v4, v4\n"
"v_mfma_f32_16x16x32_bf16 a[64:67], a[216:219], a[232:235], a[64:67]\n"
"s_add_u32 s50, s46, 98304\n"
"s_add_u32 m0, s49, 12288\n"
"v_mfma_f32_16x16x32_bf16 a[68:71], a[220:223], a[232:235], a[68:71]\n"
"ds_read_b128 a[232:235], v14 offset:32768\n"
"buffer_load_dwordx4 v34, s[28:31], s50 offen offset:0 lds\n"
"s_waitcnt lgkmcnt(11)\n"
"v_mfma_f32_16x16x32_bf16 a[24:27], a[208:211], a[236:239], a[24:27]\n"
"buffer_load_dwordx4 v35, s[28:31], s50 offen offset:1024 lds\n"
"buffer_load_dwordx4 v36, s[28:31], s50 offen offset:2048 lds\n"
"v_mfma_f32_16x16x32_bf16 a[28:31], a[212:215], a[236:239], a[28:31]\n"
"buffer_load_dwordx4 v37, s[28:31], s50 offen offset:3072 lds\n"
"v_exp_f32 v5, v5\n"
"v_mfma_f32_16x16x32_bf16 a[72:75], a[216:219], a[236:239], a[72:75]\n"
"v_mul_f32 v152, v104, v152\n"
"v_mul_f32 v153, v105, v153\n"
"v_mfma_f32_16x16x32_bf16 a[76:79], a[220:223], a[236:239], a[76:79]\n"
"ds_read_b128 a[236:239], v14 offset:49152\n"
"v_mul_f32 v154, v106, v154\n"
"v_mfma_f32_16x16x32_bf16 a[32:35], a[208:211], a[240:243], a[32:35]\n"
"v_mul_f32 v155, v107, v155\n"
"v_add_f32 v2, 1.0, v2\n"
"v_mfma_f32_16x16x32_bf16 a[36:39], a[212:215], a[240:243], a[36:39]\n"
"v_add_f32 v3, 1.0, v3\n"
"v_add_f32 v4, 1.0, v4\n"
"v_add_f32 v5, 1.0, v5\n"
"v_mfma_f32_16x16x32_bf16 a[80:83], a[216:219], a[240:243], a[80:83]\n"
"v_rcp_f32 v2, v2\n"
"v_mfma_f32_16x16x32_bf16 a[84:87], a[220:223], a[240:243], a[84:87]\n"
"ds_read_b128 a[240:243], v18 offset:0\n"
"v_mfma_f32_16x16x32_bf16 a[40:43], a[208:211], a[244:247], a[40:43]\n"
"v_rcp_f32 v3, v3\n"
"v_mfma_f32_16x16x32_bf16 a[44:47], a[212:215], a[244:247], a[44:47]\n"
"v_rcp_f32 v4, v4\n"
"v_mfma_f32_16x16x32_bf16 a[88:91], a[216:219], a[244:247], a[88:91]\n"
"v_rcp_f32 v5, v5\n"
"v_mul_f32 v152, v152, v2\n"
"v_mfma_f32_16x16x32_bf16 a[92:95], a[220:223], a[244:247], a[92:95]\n"
"ds_read_b128 a[244:247], v18 offset:16384\n"
"v_mul_f32 v153, v153, v3\n"
"s_waitcnt lgkmcnt(3)\n"
"v_mfma_f32_16x16x32_bf16 a[96:99], a[192:195], a[224:227], 0\n"
"v_mul_f32 v154, v154, v4\n"
"v_mul_f32 v155, v155, v5\n"
"v_mfma_f32_16x16x32_bf16 a[100:103], a[196:199], a[224:227], 0\n"
"v_cvt_pk_bf16_f32 v58, v152, v153\n"
"s_waitcnt vmcnt(8)\n"
"v_mfma_f32_16x16x32_bf16 a[144:147], a[200:203], a[224:227], 0\n"
"ds_read_b64_tr_b16 a[208:209], v10 offset:4096\n"
"ds_read_b64_tr_b16 a[210:211], v10 offset:6144\n"
"v_mfma_f32_16x16x32_bf16 a[148:151], a[204:207], a[224:227], 0\n"
"ds_read_b128 a[224:227], v15 offset:0\n"
"v_cvt_pk_bf16_f32 v59, v154, v155\n"
"v_mfma_f32_16x16x32_bf16 a[104:107], a[192:195], a[228:231], 0\n"
"ds_read_b64_tr_b16 a[212:213], v11 offset:4096\n"
"ds_read_b64_tr_b16 a[214:215], v11 offset:6144\n"
"v_mfma_f32_16x16x32_bf16 a[108:111], a[196:199], a[228:231], 0\n"
"v_mul_f32 v2, v84, v84\n"
"v_mul_f32 v3, v85, v85\n"
"v_mfma_f32_16x16x32_bf16 a[152:155], a[200:203], a[228:231], 0\n"
"ds_read_b64_tr_b16 a[216:217], v12 offset:4096\n"
"ds_read_b64_tr_b16 a[218:219], v12 offset:6144\n"
"v_mfma_f32_16x16x32_bf16 a[156:159], a[204:207], a[228:231], 0\n"
"ds_read_b128 a[228:231], v15 offset:16384\n"
"v_mfma_f32_16x16x32_bf16 a[112:115], a[192:195], a[232:235], 0\n"
"ds_read_b64_tr_b16 a[220:221], v13 offset:4096\n"
"ds_read_b64_tr_b16 a[222:223], v13 offset:6144\n"
"v_mfma_f32_16x16x32_bf16 a[116:119], a[196:199], a[232:235], 0\n"
"v_mul_f32 v4, v86, v86\n"
"v_mul_f32 v5, v87, v87\n"
"v_mfma_f32_16x16x32_bf16 a[160:163], a[200:203], a[232:235], 0\n"
"s_add_u32 s50, s46, 131072\n"
"s_add_u32 m0, s49, 0\n"
"v_mfma_f32_16x16x32_bf16 a[164:167], a[204:207], a[232:235], 0\n"
"ds_read_b128 a[232:235], v15 offset:32768\n"
"buffer_load_dwordx4 v34, s[28:31], s50 offen offset:0 lds\n"
"s_waitcnt lgkmcnt(11)\n"
"v_mfma_f32_16x16x32_bf16 a[120:123], a[192:195], a[236:239], 0\n"
"buffer_load_dwordx4 v35, s[28:31], s50 offen offset:1024 lds\n"
"buffer_load_dwordx4 v36, s[28:31], s50 offen offset:2048 lds\n"
"v_mfma_f32_16x16x32_bf16 a[124:127], a[196:199], a[236:239], 0\n"
"buffer_load_dwordx4 v37, s[28:31], s50 offen offset:3072 lds\n"
"v_fma_f32 v2, v2, s61, v39\n"
"v_mfma_f32_16x16x32_bf16 a[168:171], a[200:203], a[236:239], 0\n"
"v_fma_f32 v3, v3, s61, v39\n"
"v_fma_f32 v4, v4, s61, v39\n"
"v_mfma_f32_16x16x32_bf16 a[172:175], a[204:207], a[236:239], 0\n"
"ds_read_b128 a[236:239], v15 offset:49152\n"
"v_fma_f32 v5, v5, s61, v39\n"
"v_mfma_f32_16x16x32_bf16 a[128:131], a[192:195], a[240:243], 0\n"
"v_mul_f32 v2, v84, v2\n"
"v_mul_f32 v3, v85, v3\n"
"v_mfma_f32_16x16x32_bf16 a[132:135], a[196:199], a[240:243], 0\n"
"v_mul_f32 v4, v86, v4\n"
"v_mul_f32 v5, v87, v5\n"
"v_mfma_f32_16x16x32_bf16 a[176:179], a[200:203], a[240:243], 0\n"
"v_exp_f32 v2, v2\n"
"v_mfma_f32_16x16x32_bf16 a[180:183], a[204:207], a[240:243], 0\n"
"ds_read_b128 a[240:243], v19 offset:0\n"
"v_mfma_f32_16x16x32_bf16 a[136:139], a[192:195], a[244:247], 0\n"
"v_exp_f32 v3, v3\n"
"v_mfma_f32_16x16x32_bf16 a[140:143], a[196:199], a[244:247], 0\n"
"v_exp_f32 v4, v4\n"
"v_mfma_f32_16x16x32_bf16 a[184:187], a[200:203], a[244:247], 0\n"
"v_exp_f32 v5, v5\n"
"v_mfma_f32_16x16x32_bf16 a[188:191], a[204:207], a[244:247], 0\n"
"ds_read_b128 a[244:247], v19 offset:16384\n"
"v_mul_f32 v132, v84, v132\n"
"s_waitcnt lgkmcnt(3)\n"
"v_mfma_f32_16x16x32_bf16 a[96:99], a[208:211], a[224:227], a[96:99]\n"
"v_mul_f32 v133, v85, v133\n"
"v_mul_f32 v134, v86, v134\n"
"v_mfma_f32_16x16x32_bf16 a[100:103], a[212:215], a[224:227], a[100:103]\n"
"v_mul_f32 v135, v87, v135\n"
"s_waitcnt vmcnt(8)\n"
"v_mfma_f32_16x16x32_bf16 a[144:147], a[216:219], a[224:227], a[144:147]\n"
"ds_read_b64_tr_b16 a[192:193], v10 offset:8192\n"
"ds_read_b64_tr_b16 a[194:195], v10 offset:10240\n"
"v_mfma_f32_16x16x32_bf16 a[148:151], a[220:223], a[224:227], a[148:151]\n"
"ds_read_b128 a[224:227], v16 offset:0\n"
"v_add_f32 v2, 1.0, v2\n"
"v_mfma_f32_16x16x32_bf16 a[104:107], a[208:211], a[228:231], a[104:107]\n"
"ds_read_b64_tr_b16 a[196:197], v11 offset:8192\n"
"ds_read_b64_tr_b16 a[198:199], v11 offset:10240\n"
"v_mfma_f32_16x16x32_bf16 a[108:111], a[212:215], a[228:231], a[108:111]\n"
"v_add_f32 v3, 1.0, v3\n"
"v_add_f32 v4, 1.0, v4\n"
"v_mfma_f32_16x16x32_bf16 a[152:155], a[216:219], a[228:231], a[152:155]\n"
"ds_read_b64_tr_b16 a[200:201], v12 offset:8192\n"
"ds_read_b64_tr_b16 a[202:203], v12 offset:10240\n"
"v_mfma_f32_16x16x32_bf16 a[156:159], a[220:223], a[228:231], a[156:159]\n"
"ds_read_b128 a[228:231], v16 offset:16384\n"
"v_mfma_f32_16x16x32_bf16 a[112:115], a[208:211], a[232:235], a[112:115]\n"
"ds_read_b64_tr_b16 a[204:205], v13 offset:8192\n"
"ds_read_b64_tr_b16 a[206:207], v13 offset:10240\n"
"v_mfma_f32_16x16x32_bf16 a[116:119], a[212:215], a[232:235], a[116:119]\n"
"v_add_f32 v5, 1.0, v5\n"
"v_mfma_f32_16x16x32_bf16 a[160:163], a[216:219], a[232:235], a[160:163]\n"
"s_add_u32 s50, s46, 163840\n"
"s_add_u32 m0, s49, 4096\n"
"v_mfma_f32_16x16x32_bf16 a[164:167], a[220:223], a[232:235], a[164:167]\n"
"ds_read_b128 a[232:235], v16 offset:32768\n"
"buffer_load_dwordx4 v34, s[28:31], s50 offen offset:0 lds\n"
"s_waitcnt lgkmcnt(11)\n"
"v_mfma_f32_16x16x32_bf16 a[120:123], a[208:211], a[236:239], a[120:123]\n"
"buffer_load_dwordx4 v35, s[28:31], s50 offen offset:1024 lds\n"
"buffer_load_dwordx4 v36, s[28:31], s50 offen offset:2048 lds\n"
"v_mfma_f32_16x16x32_bf16 a[124:127], a[212:215], a[236:239], a[124:127]\n"
"buffer_load_dwordx4 v37, s[28:31], s50 offen offset:3072 lds\n"
"v_rcp_f32 v2, v2\n"
"v_mfma_f32_16x16x32_bf16 a[168:171], a[216:219], a[236:239], a[168:171]\n"
"v_rcp_f32 v3, v3\n"
"v_mfma_f32_16x16x32_bf16 a[172:175], a[220:223], a[236:239], a[172:175]\n"
"ds_read_b128 a[236:239], v16 offset:49152\n"
"v_mfma_f32_16x16x32_bf16 a[128:131], a[208:211], a[240:243], a[128:131]\n"
"v_rcp_f32 v4, v4\n"
"v_mfma_f32_16x16x32_bf16 a[132:135], a[212:215], a[240:243], a[132:135]\n"
"v_rcp_f32 v5, v5\n"
"v_mul_f32 v132, v132, v2\n"
"v_mfma_f32_16x16x32_bf16 a[176:179], a[216:219], a[240:243], a[176:179]\n"
"v_mul_f32 v133, v133, v3\n"
"v_mul_f32 v134, v134, v4\n"
"v_mfma_f32_16x16x32_bf16 a[180:183], a[220:223], a[240:243], a[180:183]\n"
"ds_read_b128 a[240:243], v20 offset:0\n"
"v_mfma_f32_16x16x32_bf16 a[136:139], a[208:211], a[244:247], a[136:139]\n"
"v_mul_f32 v135, v135, v5\n"
"v_cvt_pk_bf16_f32 v60, v132, v133\n"
"v_mfma_f32_16x16x32_bf16 a[140:143], a[212:215], a[244:247], a[140:143]\n"
"v_cvt_pk_bf16_f32 v61, v134, v135\n"
"v_mul_f32 v2, v108, v108\n"
"v_mfma_f32_16x16x32_bf16 a[184:187], a[216:219], a[244:247], a[184:187]\n"
"v_mul_f32 v3, v109, v109\n"
"v_mul_f32 v4, v110, v110\n"
"v_mfma_f32_16x16x32_bf16 a[188:191], a[220:223], a[244:247], a[188:191]\n"
"ds_read_b128 a[244:247], v20 offset:16384\n"
"v_mul_f32 v5, v111, v111\n"
"s_waitcnt lgkmcnt(3)\n"
"v_mfma_f32_16x16x32_bf16 a[96:99], a[192:195], a[224:227], a[96:99]\n"
"v_fma_f32 v2, v2, s61, v39\n"
"v_fma_f32 v3, v3, s61, v39\n"
"v_mfma_f32_16x16x32_bf16 a[100:103], a[196:199], a[224:227], a[100:103]\n"
"v_fma_f32 v4, v4, s61, v39\n"
"s_waitcnt vmcnt(8)\n"
"v_mfma_f32_16x16x32_bf16 a[144:147], a[200:203], a[224:227], a[144:147]\n"
"ds_read_b64_tr_b16 a[208:209], v10 offset:12288\n"
"ds_read_b64_tr_b16 a[210:211], v10 offset:14336\n"
"v_mfma_f32_16x16x32_bf16 a[148:151], a[204:207], a[224:227], a[148:151]\n"
"ds_read_b128 a[224:227], v17 offset:0\n"
"v_fma_f32 v5, v5, s61, v39\n"
"v_mfma_f32_16x16x32_bf16 a[104:107], a[192:195], a[228:231], a[104:107]\n"
"ds_read_b64_tr_b16 a[212:213], v11 offset:12288\n"
"ds_read_b64_tr_b16 a[214:215], v11 offset:14336\n"
"v_mfma_f32_16x16x32_bf16 a[108:111], a[196:199], a[228:231], a[108:111]\n"
"v_mul_f32 v2, v108, v2\n"
"v_mul_f32 v3, v109, v3\n"
"v_mfma_f32_16x16x32_bf16 a[152:155], a[200:203], a[228:231], a[152:155]\n"
"ds_read_b64_tr_b16 a[216:217], v12 offset:12288\n"
"ds_read_b64_tr_b16 a[218:219], v12 offset:14336\n"
"v_mfma_f32_16x16x32_bf16 a[156:159], a[204:207], a[228:231], a[156:159]\n"
"ds_read_b128 a[228:231], v17 offset:16384\n"
"v_mul_f32 v4, v110, v4\n"
"v_mfma_f32_16x16x32_bf16 a[112:115], a[192:195], a[232:235], a[112:115]\n"
"ds_read_b64_tr_b16 a[220:221], v13 offset:12288\n"
"ds_read_b64_tr_b16 a[222:223], v13 offset:14336\n"
"v_mfma_f32_16x16x32_bf16 a[116:119], a[196:199], a[232:235], a[116:119]\n"
"v_mul_f32 v5, v111, v5\n"
"v_mfma_f32_16x16x32_bf16 a[160:163], a[200:203], a[232:235], a[160:163]\n"
"s_add_u32 s50, s46, 196608\n"
"s_add_u32 m0, s49, 8192\n"
"v_mfma_f32_16x16x32_bf16 a[164:167], a[204:207], a[232:235], a[164:167]\n"
"ds_read_b128 a[232:235], v17 offset:32768\n"
"buffer_load_dwordx4 v34, s[28:31], s50 offen offset:0 lds\n"
"s_waitcnt lgkmcnt(11)\n"
"v_mfma_f32_16x16x32_bf16 a[120:123], a[192:195], a[236:239], a[120:123]\n"
"buffer_load_dwordx4 v35, s[28:31], s50 offen offset:1024 lds\n"
"buffer_load_dwordx4 v36, s[28:31], s50 offen offset:2048 lds\n"
"v_mfma_f32_16x16x32_bf16 a[124:127], a[196:199], a[236:239], a[124:127]\n"
"buffer_load_dwordx4 v37, s[28:31], s50 offen offset:3072 lds\n"
"v_exp_f32 v2, v2\n"
"v_mfma_f32_16x16x32_bf16 a[168:171], a[200:203], a[236:239], a[168:171]\n"
"v_exp_f32 v3, v3\n"
"v_mfma_f32_16x16x32_bf16 a[172:175], a[204:207], a[236:239], a[172:175]\n"
"ds_read_b128 a[236:239], v17 offset:49152\n"
"v_mfma_f32_16x16x32_bf16 a[128:131], a[192:195], a[240:243], a[128:131]\n"
"v_exp_f32 v4, v4\n"
"v_mfma_f32_16x16x32_bf16 a[132:135], a[196:199], a[240:243], a[132:135]\n"
"v_exp_f32 v5, v5\n"
"v_mfma_f32_16x16x32_bf16 a[176:179], a[200:203], a[240:243], a[176:179]\n"
"v_mul_f32 v156, v108, v156\n"
"v_mul_f32 v157, v109, v157\n"
"v_mfma_f32_16x16x32_bf16 a[180:183], a[204:207], a[240:243], a[180:183]\n"
"ds_read_b128 a[240:243], v21 offset:0\n"
"v_mul_f32 v158, v110, v158\n"
"v_mfma_f32_16x16x32_bf16 a[136:139], a[192:195], a[244:247], a[136:139]\n"
"v_mul_f32 v159, v111, v159\n"
"v_add_f32 v2, 1.0, v2\n"
"v_mfma_f32_16x16x32_bf16 a[140:143], a[196:199], a[244:247], a[140:143]\n"
"v_add_f32 v3, 1.0, v3\n"
"v_add_f32 v4, 1.0, v4\n"
"v_mfma_f32_16x16x32_bf16 a[184:187], a[200:203], a[244:247], a[184:187]\n"
"v_add_f32 v5, 1.0, v5\n"
"v_mfma_f32_16x16x32_bf16 a[188:191], a[204:207], a[244:247], a[188:191]\n"
"ds_read_b128 a[244:247], v21 offset:16384\n"
"v_rcp_f32 v2, v2\n"
"s_waitcnt lgkmcnt(3)\n"
"v_mfma_f32_16x16x32_bf16 a[96:99], a[208:211], a[224:227], a[96:99]\n"
"v_mfma_f32_16x16x32_bf16 a[100:103], a[212:215], a[224:227], a[100:103]\n"
"s_waitcnt vmcnt(8)\n"
"v_mfma_f32_16x16x32_bf16 a[144:147], a[216:219], a[224:227], a[144:147]\n"
"ds_read_b64_tr_b16 a[192:193], v10 offset:0\n"
"ds_read_b64_tr_b16 a[194:195], v10 offset:2048\n"
"v_mfma_f32_16x16x32_bf16 a[148:151], a[220:223], a[224:227], a[148:151]\n"
"ds_read_b128 a[224:227], v14 offset:256\n"
"v_mfma_f32_16x16x32_bf16 a[104:107], a[208:211], a[228:231], a[104:107]\n"
"ds_read_b64_tr_b16 a[196:197], v11 offset:0\n"
"ds_read_b64_tr_b16 a[198:199], v11 offset:2048\n"
"v_mfma_f32_16x16x32_bf16 a[108:111], a[212:215], a[228:231], a[108:111]\n"
"v_rcp_f32 v3, v3\n"
"v_mfma_f32_16x16x32_bf16 a[152:155], a[216:219], a[228:231], a[152:155]\n"
"ds_read_b64_tr_b16 a[200:201], v12 offset:0\n"
"ds_read_b64_tr_b16 a[202:203], v12 offset:2048\n"
"v_mfma_f32_16x16x32_bf16 a[156:159], a[220:223], a[228:231], a[156:159]\n"
"ds_read_b128 a[228:231], v14 offset:16640\n"
"v_mfma_f32_16x16x32_bf16 a[112:115], a[208:211], a[232:235], a[112:115]\n"
"ds_read_b64_tr_b16 a[204:205], v13 offset:0\n"
"ds_read_b64_tr_b16 a[206:207], v13 offset:2048\n"
"v_mfma_f32_16x16x32_bf16 a[116:119], a[212:215], a[232:235], a[116:119]\n"
"v_rcp_f32 v4, v4\n"
"v_mfma_f32_16x16x32_bf16 a[160:163], a[216:219], a[232:235], a[160:163]\n"
"s_add_u32 s50, s46, 229376\n"
"s_add_u32 m0, s49, 12288\n"
"v_mfma_f32_16x16x32_bf16 a[164:167], a[220:223], a[232:235], a[164:167]\n"
"ds_read_b128 a[232:235], v14 offset:33024\n"
"buffer_load_dwordx4 v34, s[28:31], s50 offen offset:0 lds\n"
"s_waitcnt lgkmcnt(11)\n"
"v_mfma_f32_16x16x32_bf16 a[120:123], a[208:211], a[236:239], a[120:123]\n"
"buffer_load_dwordx4 v35, s[28:31], s50 offen offset:1024 lds\n"
"buffer_load_dwordx4 v36, s[28:31], s50 offen offset:2048 lds\n"
"v_mfma_f32_16x16x32_bf16 a[124:127], a[212:215], a[236:239], a[124:127]\n"
"buffer_load_dwordx4 v37, s[28:31], s50 offen offset:3072 lds\n"
"v_mfma_f32_16x16x32_bf16 a[168:171], a[216:219], a[236:239], a[168:171]\n"
"v_mfma_f32_16x16x32_bf16 a[172:175], a[220:223], a[236:239], a[172:175]\n"
"ds_read_b128 a[236:239], v14 offset:49408\n"
"v_mfma_f32_16x16x32_bf16 a[128:131], a[208:211], a[240:243], a[128:131]\n"
"v_rcp_f32 v5, v5\n"
"v_mfma_f32_16x16x32_bf16 a[132:135], a[212:215], a[240:243], a[132:135]\n"
"v_mul_f32 v156, v156, v2\n"
"v_mfma_f32_16x16x32_bf16 a[176:179], a[216:219], a[240:243], a[176:179]\n"
"v_mul_f32 v157, v157, v3\n"
"v_mfma_f32_16x16x32_bf16 a[180:183], a[220:223], a[240:243], a[180:183]\n"
"ds_read_b128 a[240:243], v18 offset:256\n"
"v_mul_f32 v158, v158, v4\n"
"v_mfma_f32_16x16x32_bf16 a[136:139], a[208:211], a[244:247], a[136:139]\n"
"v_mul_f32 v159, v159, v5\n"
"v_mfma_f32_16x16x32_bf16 a[140:143], a[212:215], a[244:247], a[140:143]\n"
"v_cvt_pk_bf16_f32 v62, v156, v157\n"
"v_mfma_f32_16x16x32_bf16 a[184:187], a[216:219], a[244:247], a[184:187]\n"
"v_cvt_pk_bf16_f32 v63, v158, v159\n"
"v_mfma_f32_16x16x32_bf16 a[188:191], a[220:223], a[244:247], a[188:191]\n"
"ds_read_b128 a[244:247], v18 offset:16640\n"
"s_waitcnt lgkmcnt(3)\n"
"v_mfma_f32_16x16x32_bf16 a[96:99], a[192:195], a[224:227], a[96:99]\n"
"v_mfma_f32_16x16x32_bf16 a[100:103], a[196:199], a[224:227], a[100:103]\n"
"s_waitcnt vmcnt(8)\n"
"v_mfma_f32_16x16x32_bf16 a[144:147], a[200:203], a[224:227], a[144:147]\n"
"ds_read_b64_tr_b16 a[208:209], v10 offset:4096\n"
"ds_read_b64_tr_b16 a[210:211], v10 offset:6144\n"
"v_mfma_f32_16x16x32_bf16 a[148:151], a[204:207], a[224:227], a[148:151]\n"
"ds_read_b128 a[224:227], v15 offset:256\n"
"v_mfma_f32_16x16x32_bf16 a[104:107], a[192:195], a[228:231], a[104:107]\n"
"ds_read_b64_tr_b16 a[212:213], v11 offset:4096\n"
"ds_read_b64_tr_b16 a[214:215], v11 offset:6144\n"
"v_mfma_f32_16x16x32_bf16 a[108:111], a[196:199], a[228:231], a[108:111]\n"
"v_mfma_f32_16x16x32_bf16 a[152:155], a[200:203], a[228:231], a[152:155]\n"
"ds_read_b64_tr_b16 a[216:217], v12 offset:4096\n"
"ds_read_b64_tr_b16 a[218:219], v12 offset:6144\n"
"v_mfma_f32_16x16x32_bf16 a[156:159], a[204:207], a[228:231], a[156:159]\n"
"ds_read_b128 a[228:231], v15 offset:16640\n"
"v_mfma_f32_16x16x32_bf16 a[112:115], a[192:195], a[232:235], a[112:115]\n"
"ds_read_b64_tr_b16 a[220:221], v13 offset:4096\n"
"ds_read_b64_tr_b16 a[222:223], v13 offset:6144\n"
"v_mfma_f32_16x16x32_bf16 a[116:119], a[196:199], a[232:235], a[116:119]\n"
"v_mfma_f32_16x16x32_bf16 a[160:163], a[200:203], a[232:235], a[160:163]\n"
"s_add_u32 s50, s46, 262144\n"
"s_add_u32 m0, s49, 0\n"
"v_mfma_f32_16x16x32_bf16 a[164:167], a[204:207], a[232:235], a[164:167]\n"
"ds_read_b128 a[232:235], v15 offset:33024\n"
"buffer_load_dwordx4 v34, s[28:31], s50 offen offset:0 lds\n"
"s_waitcnt lgkmcnt(11)\n"
"v_mfma_f32_16x16x32_bf16 a[120:123], a[192:195], a[236:239], a[120:123]\n"
"buffer_load_dwordx4 v35, s[28:31], s50 offen offset:1024 lds\n"
"buffer_load_dwordx4 v36, s[28:31], s50 offen offset:2048 lds\n"
"v_mfma_f32_16x16x32_bf16 a[124:127], a[196:199], a[236:239], a[124:127]\n"
"buffer_load_dwordx4 v37, s[28:31], s50 offen offset:3072 lds\n"
"v_mfma_f32_16x16x32_bf16 a[168:171], a[200:203], a[236:239], a[168:171]\n"
"v_mfma_f32_16x16x32_bf16 a[172:175], a[204:207], a[236:239], a[172:175]\n"
"ds_read_b128 a[236:239], v15 offset:49408\n"
"v_mfma_f32_16x16x32_bf16 a[128:131], a[192:195], a[240:243], a[128:131]\n"
"v_mfma_f32_16x16x32_bf16 a[132:135], a[196:199], a[240:243], a[132:135]\n"
"v_mfma_f32_16x16x32_bf16 a[176:179], a[200:203], a[240:243], a[176:179]\n"
"v_mfma_f32_16x16x32_bf16 a[180:183], a[204:207], a[240:243], a[180:183]\n"
"ds_read_b128 a[240:243], v19 offset:256\n"
"v_mfma_f32_16x16x32_bf16 a[136:139], a[192:195], a[244:247], a[136:139]\n"
"v_mfma_f32_16x16x32_bf16 a[140:143], a[196:199], a[244:247], a[140:143]\n"
"v_mfma_f32_16x16x32_bf16 a[184:187], a[200:203], a[244:247], a[184:187]\n"
"v_mfma_f32_16x16x32_bf16 a[188:191], a[204:207], a[244:247], a[188:191]\n"
"ds_read_b128 a[244:247], v19 offset:16640\n"
"s_waitcnt lgkmcnt(3)\n"
"v_mfma_f32_16x16x32_bf16 a[96:99], a[208:211], a[224:227], a[96:99]\n"
"v_mfma_f32_16x16x32_bf16 a[100:103], a[212:215], a[224:227], a[100:103]\n"
"s_waitcnt vmcnt(8)\n"
"v_mfma_f32_16x16x32_bf16 a[144:147], a[216:219], a[224:227], a[144:147]\n"
"ds_read_b64_tr_b16 a[192:193], v10 offset:8192\n"
"ds_read_b64_tr_b16 a[194:195], v10 offset:10240\n"
"v_mfma_f32_16x16x32_bf16 a[148:151], a[220:223], a[224:227], a[148:151]\n"
"ds_read_b128 a[224:227], v16 offset:256\n"
"v_mfma_f32_16x16x32_bf16 a[104:107], a[208:211], a[228:231], a[104:107]\n"
"ds_read_b64_tr_b16 a[196:197], v11 offset:8192\n"
"ds_read_b64_tr_b16 a[198:199], v11 offset:10240\n"
"v_mfma_f32_16x16x32_bf16 a[108:111], a[212:215], a[228:231], a[108:111]\n"
"v_mfma_f32_16x16x32_bf16 a[152:155], a[216:219], a[228:231], a[152:155]\n"
"ds_read_b64_tr_b16 a[200:201], v12 offset:8192\n"
"ds_read_b64_tr_b16 a[202:203], v12 offset:10240\n"
"v_mfma_f32_16x16x32_bf16 a[156:159], a[220:223], a[228:231], a[156:159]\n"
"ds_read_b128 a[228:231], v16 offset:16640\n"
"v_mfma_f32_16x16x32_bf16 a[112:115], a[208:211], a[232:235], a[112:115]\n"
"ds_read_b64_tr_b16 a[204:205], v13 offset:8192\n"
"ds_read_b64_tr_b16 a[206:207], v13 offset:10240\n"
"v_mfma_f32_16x16x32_bf16 a[116:119], a[212:215], a[232:235], a[116:119]\n"
"v_mfma_f32_16x16x32_bf16 a[160:163], a[216:219], a[232:235], a[160:163]\n"
"s_add_u32 s50, s46, 294912\n"
"s_add_u32 m0, s49, 4096\n"
"v_mfma_f32_16x16x32_bf16 a[164:167], a[220:223], a[232:235], a[164:167]\n"
"ds_read_b128 a[232:235], v16 offset:33024\n"
"buffer_load_dwordx4 v34, s[28:31], s50 offen offset:0 lds\n"
"s_waitcnt lgkmcnt(11)\n"
"v_mfma_f32_16x16x32_bf16 a[120:123], a[208:211], a[236:239], a[120:123]\n"
"buffer_load_dwordx4 v35, s[28:31], s50 offen offset:1024 lds\n"
"buffer_load_dwordx4 v36, s[28:31], s50 offen offset:2048 lds\n"
"v_mfma_f32_16x16x32_bf16 a[124:127], a[212:215], a[236:239], a[124:127]\n"
"buffer_load_dwordx4 v37, s[28:31], s50 offen offset:3072 lds\n"
"v_mfma_f32_16x16x32_bf16 a[168:171], a[216:219], a[236:239], a[168:171]\n"
"v_mfma_f32_16x16x32_bf16 a[172:175], a[220:223], a[236:239], a[172:175]\n"
"ds_read_b128 a[236:239], v16 offset:49408\n"
"v_mfma_f32_16x16x32_bf16 a[128:131], a[208:211], a[240:243], a[128:131]\n"
"v_mfma_f32_16x16x32_bf16 a[132:135], a[212:215], a[240:243], a[132:135]\n"
"v_mfma_f32_16x16x32_bf16 a[176:179], a[216:219], a[240:243], a[176:179]\n"
"v_mfma_f32_16x16x32_bf16 a[180:183], a[220:223], a[240:243], a[180:183]\n"
"ds_read_b128 a[240:243], v20 offset:256\n"
"v_mfma_f32_16x16x32_bf16 a[136:139], a[208:211], a[244:247], a[136:139]\n"
"v_mfma_f32_16x16x32_bf16 a[140:143], a[212:215], a[244:247], a[140:143]\n"
"v_mfma_f32_16x16x32_bf16 a[184:187], a[216:219], a[244:247], a[184:187]\n"
"v_mfma_f32_16x16x32_bf16 a[188:191], a[220:223], a[244:247], a[188:191]\n"
"ds_read_b128 a[244:247], v20 offset:16640\n"
"s_waitcnt lgkmcnt(3)\n"
"v_mfma_f32_16x16x32_bf16 a[96:99], a[192:195], a[224:227], a[96:99]\n"
"v_mfma_f32_16x16x32_bf16 a[100:103], a[196:199], a[224:227], a[100:103]\n"
"s_waitcnt vmcnt(8)\n"
"v_mfma_f32_16x16x32_bf16 a[144:147], a[200:203], a[224:227], a[144:147]\n"
"ds_read_b64_tr_b16 a[208:209], v10 offset:12288\n"
"ds_read_b64_tr_b16 a[210:211], v10 offset:14336\n"
"v_mfma_f32_16x16x32_bf16 a[148:151], a[204:207], a[224:227], a[148:151]\n"
"ds_read_b128 a[224:227], v17 offset:256\n"
"v_mfma_f32_16x16x32_bf16 a[104:107], a[192:195], a[228:231], a[104:107]\n"
"ds_read_b64_tr_b16 a[212:213], v11 offset:12288\n"
"ds_read_b64_tr_b16 a[214:215], v11 offset:14336\n"
"v_mfma_f32_16x16x32_bf16 a[108:111], a[196:199], a[228:231], a[108:111]\n"
"v_mfma_f32_16x16x32_bf16 a[152:155], a[200:203], a[228:231], a[152:155]\n"
"ds_read_b64_tr_b16 a[216:217], v12 offset:12288\n"
"ds_read_b64_tr_b16 a[218:219], v12 offset:14336\n"
"v_mfma_f32_16x16x32_bf16 a[156:159], a[204:207], a[228:231], a[156:159]\n"
"ds_read_b128 a[228:231], v17 offset:16640\n"
"v_mfma_f32_16x16x32_bf16 a[112:115], a[192:195], a[232:235], a[112:115]\n"
"ds_read_b64_tr_b16 a[220:221], v13 offset:12288\n"
"ds_read_b64_tr_b16 a[222:223], v13 offset:14336\n"
"v_mfma_f32_16x16x32_bf16 a[116:119], a[196:199], a[232:235], a[116:119]\n"
"v_mfma_f32_16x16x32_bf16 a[160:163], a[200:203], a[232:235], a[160:163]\n"
"s_add_u32 s50, s46, 327680\n"
"s_add_u32 m0, s49, 8192\n"
"v_mfma_f32_16x16x32_bf16 a[164:167], a[204:207], a[232:235], a[164:167]\n"
"ds_read_b128 a[232:235], v17 offset:33024\n"
"buffer_load_dwordx4 v34, s[28:31], s50 offen offset:0 lds\n"
"s_waitcnt lgkmcnt(11)\n"
"v_mfma_f32_16x16x32_bf16 a[120:123], a[192:195], a[236:239], a[120:123]\n"
"buffer_load_dwordx4 v35, s[28:31], s50 offen offset:1024 lds\n"
"buffer_load_dwordx4 v36, s[28:31], s50 offen offset:2048 lds\n"
"v_mfma_f32_16x16x32_bf16 a[124:127], a[196:199], a[236:239], a[124:127]\n"
"buffer_load_dwordx4 v37, s[28:31], s50 offen offset:3072 lds\n"
"v_mfma_f32_16x16x32_bf16 a[168:171], a[200:203], a[236:239], a[168:171]\n"
"v_mfma_f32_16x16x32_bf16 a[172:175], a[204:207], a[236:239], a[172:175]\n"
"ds_read_b128 a[236:239], v17 offset:49408\n"
"v_mfma_f32_16x16x32_bf16 a[128:131], a[192:195], a[240:243], a[128:131]\n"
"v_mfma_f32_16x16x32_bf16 a[132:135], a[196:199], a[240:243], a[132:135]\n"
"v_mfma_f32_16x16x32_bf16 a[176:179], a[200:203], a[240:243], a[176:179]\n"
"v_mfma_f32_16x16x32_bf16 a[180:183], a[204:207], a[240:243], a[180:183]\n"
"ds_read_b128 a[240:243], v21 offset:256\n"
"v_mfma_f32_16x16x32_bf16 a[136:139], a[192:195], a[244:247], a[136:139]\n"
"v_mfma_f32_16x16x32_bf16 a[140:143], a[196:199], a[244:247], a[140:143]\n"
"v_mfma_f32_16x16x32_bf16 a[184:187], a[200:203], a[244:247], a[184:187]\n"
"v_mfma_f32_16x16x32_bf16 a[188:191], a[204:207], a[244:247], a[188:191]\n"
"ds_read_b128 a[244:247], v21 offset:16640\n"
"s_waitcnt lgkmcnt(3)\n"
"v_mfma_f32_16x16x32_bf16 a[96:99], a[208:211], a[224:227], a[96:99]\n"
"v_mfma_f32_16x16x32_bf16 a[100:103], a[212:215], a[224:227], a[100:103]\n"
"s_waitcnt vmcnt(8)\n"
"v_mfma_f32_16x16x32_bf16 a[144:147], a[216:219], a[224:227], a[144:147]\n"
"ds_read_b64_tr_b16 a[192:193], v10 offset:0\n"
"ds_read_b64_tr_b16 a[194:195], v10 offset:2048\n"
"v_mfma_f32_16x16x32_bf16 a[148:151], a[220:223], a[224:227], a[148:151]\n"
"ds_read_b128 a[224:227], v14 offset:512\n"
"v_mfma_f32_16x16x32_bf16 a[104:107], a[208:211], a[228:231], a[104:107]\n"
"ds_read_b64_tr_b16 a[196:197], v11 offset:0\n"
"ds_read_b64_tr_b16 a[198:199], v11 offset:2048\n"
"v_mfma_f32_16x16x32_bf16 a[108:111], a[212:215], a[228:231], a[108:111]\n"
"v_mfma_f32_16x16x32_bf16 a[152:155], a[216:219], a[228:231], a[152:155]\n"
"ds_read_b64_tr_b16 a[200:201], v12 offset:0\n"
"ds_read_b64_tr_b16 a[202:203], v12 offset:2048\n"
"v_mfma_f32_16x16x32_bf16 a[156:159], a[220:223], a[228:231], a[156:159]\n"
"ds_read_b128 a[228:231], v14 offset:16896\n"
"v_mfma_f32_16x16x32_bf16 a[112:115], a[208:211], a[232:235], a[112:115]\n"
"ds_read_b64_tr_b16 a[204:205], v13 offset:0\n"
"ds_read_b64_tr_b16 a[206:207], v13 offset:2048\n"
"v_mfma_f32_16x16x32_bf16 a[116:119], a[212:215], a[232:235], a[116:119]\n"
"v_mfma_f32_16x16x32_bf16 a[160:163], a[216:219], a[232:235], a[160:163]\n"
"s_add_u32 s50, s46, 360448\n"
"s_add_u32 m0, s49, 12288\n"
"v_mfma_f32_16x16x32_bf16 a[164:167], a[220:223], a[232:235], a[164:167]\n"
"ds_read_b128 a[232:235], v14 offset:33280\n"
"buffer_load_dwordx4 v34, s[28:31], s50 offen offset:0 lds\n"
"s_waitcnt lgkmcnt(11)\n"
"v_mfma_f32_16x16x32_bf16 a[120:123], a[208:211], a[236:239], a[120:123]\n"
"buffer_load_dwordx4 v35, s[28:31], s50 offen offset:1024 lds\n"
"buffer_load_dwordx4 v36, s[28:31], s50 offen offset:2048 lds\n"
"v_mfma_f32_16x16x32_bf16 a[124:127], a[212:215], a[236:239], a[124:127]\n"
"buffer_load_dwordx4 v37, s[28:31], s50 offen offset:3072 lds\n"
"v_mfma_f32_16x16x32_bf16 a[168:171], a[216:219], a[236:239], a[168:171]\n"
"v_mfma_f32_16x16x32_bf16 a[172:175], a[220:223], a[236:239], a[172:175]\n"
"ds_read_b128 a[236:239], v14 offset:49664\n"
"v_mfma_f32_16x16x32_bf16 a[128:131], a[208:211], a[240:243], a[128:131]\n"
"v_mfma_f32_16x16x32_bf16 a[132:135], a[212:215], a[240:243], a[132:135]\n"
"v_mfma_f32_16x16x32_bf16 a[176:179], a[216:219], a[240:243], a[176:179]\n"
"v_mfma_f32_16x16x32_bf16 a[180:183], a[220:223], a[240:243], a[180:183]\n"
"ds_read_b128 a[240:243], v18 offset:512\n"
"v_mfma_f32_16x16x32_bf16 a[136:139], a[208:211], a[244:247], a[136:139]\n"
"v_mfma_f32_16x16x32_bf16 a[140:143], a[212:215], a[244:247], a[140:143]\n"
"v_mfma_f32_16x16x32_bf16 a[184:187], a[216:219], a[244:247], a[184:187]\n"
"v_mfma_f32_16x16x32_bf16 a[188:191], a[220:223], a[244:247], a[188:191]\n"
"ds_read_b128 a[244:247], v18 offset:16896\n"
"s_waitcnt lgkmcnt(3)\n"
"v_mfma_f32_16x16x32_bf16 a[96:99], a[192:195], a[224:227], a[96:99]\n"
"v_mfma_f32_16x16x32_bf16 a[100:103], a[196:199], a[224:227], a[100:103]\n"
"s_waitcnt vmcnt(8)\n"
"v_mfma_f32_16x16x32_bf16 a[144:147], a[200:203], a[224:227], a[144:147]\n"
"ds_read_b64_tr_b16 a[208:209], v10 offset:4096\n"
"ds_read_b64_tr_b16 a[210:211], v10 offset:6144\n"
"v_mfma_f32_16x16x32_bf16 a[148:151], a[204:207], a[224:227], a[148:151]\n"
"ds_read_b128 a[224:227], v15 offset:512\n"
"v_mfma_f32_16x16x32_bf16 a[104:107], a[192:195], a[228:231], a[104:107]\n"
"ds_read_b64_tr_b16 a[212:213], v11 offset:4096\n"
"ds_read_b64_tr_b16 a[214:215], v11 offset:6144\n"
"v_mfma_f32_16x16x32_bf16 a[108:111], a[196:199], a[228:231], a[108:111]\n"
"v_mfma_f32_16x16x32_bf16 a[152:155], a[200:203], a[228:231], a[152:155]\n"
"ds_read_b64_tr_b16 a[216:217], v12 offset:4096\n"
"ds_read_b64_tr_b16 a[218:219], v12 offset:6144\n"
"v_mfma_f32_16x16x32_bf16 a[156:159], a[204:207], a[228:231], a[156:159]\n"
"ds_read_b128 a[228:231], v15 offset:16896\n"
"v_mfma_f32_16x16x32_bf16 a[112:115], a[192:195], a[232:235], a[112:115]\n"
"ds_read_b64_tr_b16 a[220:221], v13 offset:4096\n"
"ds_read_b64_tr_b16 a[222:223], v13 offset:6144\n"
"v_mfma_f32_16x16x32_bf16 a[116:119], a[196:199], a[232:235], a[116:119]\n"
"v_mfma_f32_16x16x32_bf16 a[160:163], a[200:203], a[232:235], a[160:163]\n"
"s_add_u32 s50, s46, 393216\n"
"s_add_u32 m0, s49, 0\n"
"v_mfma_f32_16x16x32_bf16 a[164:167], a[204:207], a[232:235], a[164:167]\n"
"ds_read_b128 a[232:235], v15 offset:33280\n"
"buffer_load_dwordx4 v34, s[28:31], s50 offen offset:0 lds\n"
"s_waitcnt lgkmcnt(11)\n"
"v_mfma_f32_16x16x32_bf16 a[120:123], a[192:195], a[236:239], a[120:123]\n"
"buffer_load_dwordx4 v35, s[28:31], s50 offen offset:1024 lds\n"
"buffer_load_dwordx4 v36, s[28:31], s50 offen offset:2048 lds\n"
"v_mfma_f32_16x16x32_bf16 a[124:127], a[196:199], a[236:239], a[124:127]\n"
"buffer_load_dwordx4 v37, s[28:31], s50 offen offset:3072 lds\n"
"v_mfma_f32_16x16x32_bf16 a[168:171], a[200:203], a[236:239], a[168:171]\n"
"v_mfma_f32_16x16x32_bf16 a[172:175], a[204:207], a[236:239], a[172:175]\n"
"ds_read_b128 a[236:239], v15 offset:49664\n"
"v_mfma_f32_16x16x32_bf16 a[128:131], a[192:195], a[240:243], a[128:131]\n"
"v_mfma_f32_16x16x32_bf16 a[132:135], a[196:199], a[240:243], a[132:135]\n"
"v_mfma_f32_16x16x32_bf16 a[176:179], a[200:203], a[240:243], a[176:179]\n"
"v_mfma_f32_16x16x32_bf16 a[180:183], a[204:207], a[240:243], a[180:183]\n"
"ds_read_b128 a[240:243], v19 offset:512\n"
"v_mfma_f32_16x16x32_bf16 a[136:139], a[192:195], a[244:247], a[136:139]\n"
"v_mfma_f32_16x16x32_bf16 a[140:143], a[196:199], a[244:247], a[140:143]\n"
"v_mfma_f32_16x16x32_bf16 a[184:187], a[200:203], a[244:247], a[184:187]\n"
"v_mfma_f32_16x16x32_bf16 a[188:191], a[204:207], a[244:247], a[188:191]\n"
"ds_read_b128 a[244:247], v19 offset:16896\n"
"s_waitcnt lgkmcnt(3)\n"
"v_mfma_f32_16x16x32_bf16 a[96:99], a[208:211], a[224:227], a[96:99]\n"
"v_mfma_f32_16x16x32_bf16 a[100:103], a[212:215], a[224:227], a[100:103]\n"
"s_waitcnt vmcnt(8)\n"
"v_mfma_f32_16x16x32_bf16 a[144:147], a[216:219], a[224:227], a[144:147]\n"
"ds_read_b64_tr_b16 a[192:193], v10 offset:8192\n"
"ds_read_b64_tr_b16 a[194:195], v10 offset:10240\n"
"v_mfma_f32_16x16x32_bf16 a[148:151], a[220:223], a[224:227], a[148:151]\n"
"ds_read_b128 a[224:227], v16 offset:512\n"
"v_mfma_f32_16x16x32_bf16 a[104:107], a[208:211], a[228:231], a[104:107]\n"
"ds_read_b64_tr_b16 a[196:197], v11 offset:8192\n"
"ds_read_b64_tr_b16 a[198:199], v11 offset:10240\n"
"v_mfma_f32_16x16x32_bf16 a[108:111], a[212:215], a[228:231], a[108:111]\n"
"v_mfma_f32_16x16x32_bf16 a[152:155], a[216:219], a[228:231], a[152:155]\n"
"ds_read_b64_tr_b16 a[200:201], v12 offset:8192\n"
"ds_read_b64_tr_b16 a[202:203], v12 offset:10240\n"
"v_mfma_f32_16x16x32_bf16 a[156:159], a[220:223], a[228:231], a[156:159]\n"
"ds_read_b128 a[228:231], v16 offset:16896\n"
"v_mfma_f32_16x16x32_bf16 a[112:115], a[208:211], a[232:235], a[112:115]\n"
"ds_read_b64_tr_b16 a[204:205], v13 offset:8192\n"
"ds_read_b64_tr_b16 a[206:207], v13 offset:10240\n"
"v_mfma_f32_16x16x32_bf16 a[116:119], a[212:215], a[232:235], a[116:119]\n"
"v_mfma_f32_16x16x32_bf16 a[160:163], a[216:219], a[232:235], a[160:163]\n"
"s_add_u32 s50, s46, 425984\n"
"s_add_u32 m0, s49, 4096\n"
"v_mfma_f32_16x16x32_bf16 a[164:167], a[220:223], a[232:235], a[164:167]\n"
"ds_read_b128 a[232:235], v16 offset:33280\n"
"buffer_load_dwordx4 v34, s[28:31], s50 offen offset:0 lds\n"
"s_waitcnt lgkmcnt(11)\n"
"v_mfma_f32_16x16x32_bf16 a[120:123], a[208:211], a[236:239], a[120:123]\n"
"buffer_load_dwordx4 v35, s[28:31], s50 offen offset:1024 lds\n"
"buffer_load_dwordx4 v36, s[28:31], s50 offen offset:2048 lds\n"
"v_mfma_f32_16x16x32_bf16 a[124:127], a[212:215], a[236:239], a[124:127]\n"
"buffer_load_dwordx4 v37, s[28:31], s50 offen offset:3072 lds\n"
"v_mfma_f32_16x16x32_bf16 a[168:171], a[216:219], a[236:239], a[168:171]\n"
"v_mfma_f32_16x16x32_bf16 a[172:175], a[220:223], a[236:239], a[172:175]\n"
"ds_read_b128 a[236:239], v16 offset:49664\n"
"v_mfma_f32_16x16x32_bf16 a[128:131], a[208:211], a[240:243], a[128:131]\n"
"v_mfma_f32_16x16x32_bf16 a[132:135], a[212:215], a[240:243], a[132:135]\n"
"v_mfma_f32_16x16x32_bf16 a[176:179], a[216:219], a[240:243], a[176:179]\n"
"v_mfma_f32_16x16x32_bf16 a[180:183], a[220:223], a[240:243], a[180:183]\n"
"ds_read_b128 a[240:243], v20 offset:512\n"
"v_mfma_f32_16x16x32_bf16 a[136:139], a[208:211], a[244:247], a[136:139]\n"
"v_mfma_f32_16x16x32_bf16 a[140:143], a[212:215], a[244:247], a[140:143]\n"
"v_mfma_f32_16x16x32_bf16 a[184:187], a[216:219], a[244:247], a[184:187]\n"
"v_mfma_f32_16x16x32_bf16 a[188:191], a[220:223], a[244:247], a[188:191]\n"
"ds_read_b128 a[244:247], v20 offset:16896\n"
"s_waitcnt lgkmcnt(3)\n"
"v_mfma_f32_16x16x32_bf16 a[96:99], a[192:195], a[224:227], a[96:99]\n"
"v_mfma_f32_16x16x32_bf16 a[100:103], a[196:199], a[224:227], a[100:103]\n"
"s_waitcnt vmcnt(8)\n"
"v_mfma_f32_16x16x32_bf16 a[144:147], a[200:203], a[224:227], a[144:147]\n"
"ds_read_b64_tr_b16 a[208:209], v10 offset:12288\n"
"ds_read_b64_tr_b16 a[210:211], v10 offset:14336\n"
"v_mfma_f32_16x16x32_bf16 a[148:151], a[204:207], a[224:227], a[148:151]\n"
"ds_read_b128 a[224:227], v17 offset:512\n"
"v_mfma_f32_16x16x32_bf16 a[104:107], a[192:195], a[228:231], a[104:107]\n"
"ds_read_b64_tr_b16 a[212:213], v11 offset:12288\n"
"ds_read_b64_tr_b16 a[214:215], v11 offset:14336\n"
"v_mfma_f32_16x16x32_bf16 a[108:111], a[196:199], a[228:231], a[108:111]\n"
"v_mfma_f32_16x16x32_bf16 a[152:155], a[200:203], a[228:231], a[152:155]\n"
"ds_read_b64_tr_b16 a[216:217], v12 offset:12288\n"
"ds_read_b64_tr_b16 a[218:219], v12 offset:14336\n"
"v_mfma_f32_16x16x32_bf16 a[156:159], a[204:207], a[228:231], a[156:159]\n"
"ds_read_b128 a[228:231], v17 offset:16896\n"
"v_mfma_f32_16x16x32_bf16 a[112:115], a[192:195], a[232:235], a[112:115]\n"
"ds_read_b64_tr_b16 a[220:221], v13 offset:12288\n"
"ds_read_b64_tr_b16 a[222:223], v13 offset:14336\n"
"v_mfma_f32_16x16x32_bf16 a[116:119], a[196:199], a[232:235], a[116:119]\n"
"v_mfma_f32_16x16x32_bf16 a[160:163], a[200:203], a[232:235], a[160:163]\n"
"s_add_u32 s50, s46, 458752\n"
"s_add_u32 m0, s49, 8192\n"
"v_mfma_f32_16x16x32_bf16 a[164:167], a[204:207], a[232:235], a[164:167]\n"
"ds_read_b128 a[232:235], v17 offset:33280\n"
"buffer_load_dwordx4 v34, s[28:31], s50 offen offset:0 lds\n"
"s_waitcnt lgkmcnt(11)\n"
"v_mfma_f32_16x16x32_bf16 a[120:123], a[192:195], a[236:239], a[120:123]\n"
"buffer_load_dwordx4 v35, s[28:31], s50 offen offset:1024 lds\n"
"buffer_load_dwordx4 v36, s[28:31], s50 offen offset:2048 lds\n"
"v_mfma_f32_16x16x32_bf16 a[124:127], a[196:199], a[236:239], a[124:127]\n"
"buffer_load_dwordx4 v37, s[28:31], s50 offen offset:3072 lds\n"
"v_mfma_f32_16x16x32_bf16 a[168:171], a[200:203], a[236:239], a[168:171]\n"
"v_mfma_f32_16x16x32_bf16 a[172:175], a[204:207], a[236:239], a[172:175]\n"
"ds_read_b128 a[236:239], v17 offset:49664\n"
"v_mfma_f32_16x16x32_bf16 a[128:131], a[192:195], a[240:243], a[128:131]\n"
"v_mfma_f32_16x16x32_bf16 a[132:135], a[196:199], a[240:243], a[132:135]\n"
"v_mfma_f32_16x16x32_bf16 a[176:179], a[200:203], a[240:243], a[176:179]\n"
"v_mfma_f32_16x16x32_bf16 a[180:183], a[204:207], a[240:243], a[180:183]\n"
"ds_read_b128 a[240:243], v21 offset:512\n"
"v_mfma_f32_16x16x32_bf16 a[136:139], a[192:195], a[244:247], a[136:139]\n"
"v_mfma_f32_16x16x32_bf16 a[140:143], a[196:199], a[244:247], a[140:143]\n"
"v_mfma_f32_16x16x32_bf16 a[184:187], a[200:203], a[244:247], a[184:187]\n"
"v_mfma_f32_16x16x32_bf16 a[188:191], a[204:207], a[244:247], a[188:191]\n"
"ds_read_b128 a[244:247], v21 offset:16896\n"
"s_waitcnt lgkmcnt(3)\n"
"v_mfma_f32_16x16x32_bf16 a[96:99], a[208:211], a[224:227], a[96:99]\n"
"v_mfma_f32_16x16x32_bf16 a[100:103], a[212:215], a[224:227], a[100:103]\n"
"s_waitcnt vmcnt(8)\n"
"v_mfma_f32_16x16x32_bf16 a[144:147], a[216:219], a[224:227], a[144:147]\n"
"ds_read_b64_tr_b16 a[192:193], v10 offset:0\n"
"ds_read_b64_tr_b16 a[194:195], v10 offset:2048\n"
"v_mfma_f32_16x16x32_bf16 a[148:151], a[220:223], a[224:227], a[148:151]\n"
"ds_read_b128 a[224:227], v14 offset:768\n"
"v_mfma_f32_16x16x32_bf16 a[104:107], a[208:211], a[228:231], a[104:107]\n"
"ds_read_b64_tr_b16 a[196:197], v11 offset:0\n"
"ds_read_b64_tr_b16 a[198:199], v11 offset:2048\n"
"v_mfma_f32_16x16x32_bf16 a[108:111], a[212:215], a[228:231], a[108:111]\n"
"v_mfma_f32_16x16x32_bf16 a[152:155], a[216:219], a[228:231], a[152:155]\n"
"ds_read_b64_tr_b16 a[200:201], v12 offset:0\n"
"ds_read_b64_tr_b16 a[202:203], v12 offset:2048\n"
"v_mfma_f32_16x16x32_bf16 a[156:159], a[220:223], a[228:231], a[156:159]\n"
"ds_read_b128 a[228:231], v14 offset:17152\n"
"v_mfma_f32_16x16x32_bf16 a[112:115], a[208:211], a[232:235], a[112:115]\n"
"ds_read_b64_tr_b16 a[204:205], v13 offset:0\n"
"ds_read_b64_tr_b16 a[206:207], v13 offset:2048\n"
"v_mfma_f32_16x16x32_bf16 a[116:119], a[212:215], a[232:235], a[116:119]\n"
"v_mfma_f32_16x16x32_bf16 a[160:163], a[216:219], a[232:235], a[160:163]\n"
"s_add_u32 s50, s46, 491520\n"
"s_add_u32 m0, s49, 12288\n"
"v_mfma_f32_16x16x32_bf16 a[164:167], a[220:223], a[232:235], a[164:167]\n"
"ds_read_b128 a[232:235], v14 offset:33536\n"
"buffer_load_dwordx4 v34, s[28:31], s50 offen offset:0 lds\n"
"s_waitcnt lgkmcnt(11)\n"
"v_mfma_f32_16x16x32_bf16 a[120:123], a[208:211], a[236:239], a[120:123]\n"
"buffer_load_dwordx4 v35, s[28:31], s50 offen offset:1024 lds\n"
"buffer_load_dwordx4 v36, s[28:31], s50 offen offset:2048 lds\n"
"v_mfma_f32_16x16x32_bf16 a[124:127], a[212:215], a[236:239], a[124:127]\n"
"buffer_load_dwordx4 v37, s[28:31], s50 offen offset:3072 lds\n"
"v_mfma_f32_16x16x32_bf16 a[168:171], a[216:219], a[236:239], a[168:171]\n"
"v_mfma_f32_16x16x32_bf16 a[172:175], a[220:223], a[236:239], a[172:175]\n"
"ds_read_b128 a[236:239], v14 offset:49920\n"
"v_mfma_f32_16x16x32_bf16 a[128:131], a[208:211], a[240:243], a[128:131]\n"
"v_mfma_f32_16x16x32_bf16 a[132:135], a[212:215], a[240:243], a[132:135]\n"
"v_mfma_f32_16x16x32_bf16 a[176:179], a[216:219], a[240:243], a[176:179]\n"
"v_mfma_f32_16x16x32_bf16 a[180:183], a[220:223], a[240:243], a[180:183]\n"
"ds_read_b128 a[240:243], v18 offset:768\n"
"v_mfma_f32_16x16x32_bf16 a[136:139], a[208:211], a[244:247], a[136:139]\n"
"v_mfma_f32_16x16x32_bf16 a[140:143], a[212:215], a[244:247], a[140:143]\n"
"v_mfma_f32_16x16x32_bf16 a[184:187], a[216:219], a[244:247], a[184:187]\n"
"v_mfma_f32_16x16x32_bf16 a[188:191], a[220:223], a[244:247], a[188:191]\n"
"ds_read_b128 a[244:247], v18 offset:17152\n"
"s_waitcnt lgkmcnt(3)\n"
"v_mfma_f32_16x16x32_bf16 a[96:99], a[192:195], a[224:227], a[96:99]\n"
"v_mfma_f32_16x16x32_bf16 a[100:103], a[196:199], a[224:227], a[100:103]\n"
"s_waitcnt vmcnt(8)\n"
"v_mfma_f32_16x16x32_bf16 a[144:147], a[200:203], a[224:227], a[144:147]\n"
"ds_read_b64_tr_b16 a[208:209], v10 offset:4096\n"
"ds_read_b64_tr_b16 a[210:211], v10 offset:6144\n"
"v_mfma_f32_16x16x32_bf16 a[148:151], a[204:207], a[224:227], a[148:151]\n"
"ds_read_b128 a[224:227], v15 offset:768\n"
"v_mfma_f32_16x16x32_bf16 a[104:107], a[192:195], a[228:231], a[104:107]\n"
"ds_read_b64_tr_b16 a[212:213], v11 offset:4096\n"
"ds_read_b64_tr_b16 a[214:215], v11 offset:6144\n"
"v_mfma_f32_16x16x32_bf16 a[108:111], a[196:199], a[228:231], a[108:111]\n"
"v_mfma_f32_16x16x32_bf16 a[152:155], a[200:203], a[228:231], a[152:155]\n"
"ds_read_b64_tr_b16 a[216:217], v12 offset:4096\n"
"ds_read_b64_tr_b16 a[218:219], v12 offset:6144\n"
"v_mfma_f32_16x16x32_bf16 a[156:159], a[204:207], a[228:231], a[156:159]\n"
"ds_read_b128 a[228:231], v15 offset:17152\n"
"v_mfma_f32_16x16x32_bf16 a[112:115], a[192:195], a[232:235], a[112:115]\n"
"ds_read_b64_tr_b16 a[220:221], v13 offset:4096\n"
"ds_read_b64_tr_b16 a[222:223], v13 offset:6144\n"
"v_mfma_f32_16x16x32_bf16 a[116:119], a[196:199], a[232:235], a[116:119]\n"
"v_mfma_f32_16x16x32_bf16 a[160:163], a[200:203], a[232:235], a[160:163]\n"
"s_add_u32 s50, s46, 524288\n"
"s_add_u32 m0, s49, 0\n"
"v_mfma_f32_16x16x32_bf16 a[164:167], a[204:207], a[232:235], a[164:167]\n"
"ds_read_b128 a[232:235], v15 offset:33536\n"
"buffer_load_dwordx4 v30, s[28:31], s50 offen offset:0 lds\n"
"s_waitcnt lgkmcnt(11)\n"
"v_mfma_f32_16x16x32_bf16 a[120:123], a[192:195], a[236:239], a[120:123]\n"
"buffer_load_dwordx4 v31, s[28:31], s50 offen offset:1024 lds\n"
"buffer_load_dwordx4 v32, s[28:31], s50 offen offset:2048 lds\n"
"v_mfma_f32_16x16x32_bf16 a[124:127], a[196:199], a[236:239], a[124:127]\n"
"buffer_load_dwordx4 v33, s[28:31], s50 offen offset:3072 lds\n"
"v_mfma_f32_16x16x32_bf16 a[168:171], a[200:203], a[236:239], a[168:171]\n"
"v_mfma_f32_16x16x32_bf16 a[172:175], a[204:207], a[236:239], a[172:175]\n"
"ds_read_b128 a[236:239], v15 offset:49920\n"
"v_mfma_f32_16x16x32_bf16 a[128:131], a[192:195], a[240:243], a[128:131]\n"
"v_mfma_f32_16x16x32_bf16 a[132:135], a[196:199], a[240:243], a[132:135]\n"
"v_mfma_f32_16x16x32_bf16 a[176:179], a[200:203], a[240:243], a[176:179]\n"
"v_mfma_f32_16x16x32_bf16 a[180:183], a[204:207], a[240:243], a[180:183]\n"
"ds_read_b128 a[240:243], v19 offset:768\n"
"v_mfma_f32_16x16x32_bf16 a[136:139], a[192:195], a[244:247], a[136:139]\n"
"v_mfma_f32_16x16x32_bf16 a[140:143], a[196:199], a[244:247], a[140:143]\n"
"v_mfma_f32_16x16x32_bf16 a[184:187], a[200:203], a[244:247], a[184:187]\n"
"v_mfma_f32_16x16x32_bf16 a[188:191], a[204:207], a[244:247], a[188:191]\n"
"ds_read_b128 a[244:247], v19 offset:17152\n"
"s_waitcnt lgkmcnt(3)\n"
"v_mfma_f32_16x16x32_bf16 a[96:99], a[208:211], a[224:227], a[96:99]\n"
"v_mfma_f32_16x16x32_bf16 a[100:103], a[212:215], a[224:227], a[100:103]\n"
"s_waitcnt vmcnt(8)\n"
"v_mfma_f32_16x16x32_bf16 a[144:147], a[216:219], a[224:227], a[144:147]\n"
"ds_read_b64_tr_b16 a[192:193], v10 offset:8192\n"
"ds_read_b64_tr_b16 a[194:195], v10 offset:10240\n"
"v_mfma_f32_16x16x32_bf16 a[148:151], a[220:223], a[224:227], a[148:151]\n"
"ds_read_b128 a[224:227], v16 offset:768\n"
"v_mfma_f32_16x16x32_bf16 a[104:107], a[208:211], a[228:231], a[104:107]\n"
"ds_read_b64_tr_b16 a[196:197], v11 offset:8192\n"
"ds_read_b64_tr_b16 a[198:199], v11 offset:10240\n"
"v_mfma_f32_16x16x32_bf16 a[108:111], a[212:215], a[228:231], a[108:111]\n"
"v_mfma_f32_16x16x32_bf16 a[152:155], a[216:219], a[228:231], a[152:155]\n"
"ds_read_b64_tr_b16 a[200:201], v12 offset:8192\n"
"ds_read_b64_tr_b16 a[202:203], v12 offset:10240\n"
"v_mfma_f32_16x16x32_bf16 a[156:159], a[220:223], a[228:231], a[156:159]\n"
"ds_read_b128 a[228:231], v16 offset:17152\n"
"v_mfma_f32_16x16x32_bf16 a[112:115], a[208:211], a[232:235], a[112:115]\n"
"ds_read_b64_tr_b16 a[204:205], v13 offset:8192\n"
"ds_read_b64_tr_b16 a[206:207], v13 offset:10240\n"
"v_mfma_f32_16x16x32_bf16 a[116:119], a[212:215], a[232:235], a[116:119]\n"
"v_mfma_f32_16x16x32_bf16 a[160:163], a[216:219], a[232:235], a[160:163]\n"
"s_add_u32 s50, s46, 557056\n"
"s_add_u32 m0, s49, 4096\n"
"v_mfma_f32_16x16x32_bf16 a[164:167], a[220:223], a[232:235], a[164:167]\n"
"ds_read_b128 a[232:235], v16 offset:33536\n"
"buffer_load_dwordx4 v30, s[28:31], s50 offen offset:0 lds\n"
"s_waitcnt lgkmcnt(11)\n"
"v_mfma_f32_16x16x32_bf16 a[120:123], a[208:211], a[236:239], a[120:123]\n"
"buffer_load_dwordx4 v31, s[28:31], s50 offen offset:1024 lds\n"
"buffer_load_dwordx4 v32, s[28:31], s50 offen offset:2048 lds\n"
"v_mfma_f32_16x16x32_bf16 a[124:127], a[212:215], a[236:239], a[124:127]\n"
"buffer_load_dwordx4 v33, s[28:31], s50 offen offset:3072 lds\n"
"v_mfma_f32_16x16x32_bf16 a[168:171], a[216:219], a[236:239], a[168:171]\n"
"v_mfma_f32_16x16x32_bf16 a[172:175], a[220:223], a[236:239], a[172:175]\n"
"ds_read_b128 a[236:239], v16 offset:49920\n"
"v_mfma_f32_16x16x32_bf16 a[128:131], a[208:211], a[240:243], a[128:131]\n"
"v_mfma_f32_16x16x32_bf16 a[132:135], a[212:215], a[240:243], a[132:135]\n"
"v_mfma_f32_16x16x32_bf16 a[176:179], a[216:219], a[240:243], a[176:179]\n"
"v_mfma_f32_16x16x32_bf16 a[180:183], a[220:223], a[240:243], a[180:183]\n"
"ds_read_b128 a[240:243], v20 offset:768\n"
"v_mfma_f32_16x16x32_bf16 a[136:139], a[208:211], a[244:247], a[136:139]\n"
"v_mfma_f32_16x16x32_bf16 a[140:143], a[212:215], a[244:247], a[140:143]\n"
"v_mfma_f32_16x16x32_bf16 a[184:187], a[216:219], a[244:247], a[184:187]\n"
"v_mfma_f32_16x16x32_bf16 a[188:191], a[220:223], a[244:247], a[188:191]\n"
"ds_read_b128 a[244:247], v20 offset:17152\n"
"s_waitcnt lgkmcnt(3)\n"
"v_mfma_f32_16x16x32_bf16 a[96:99], a[192:195], a[224:227], a[96:99]\n"
"v_mfma_f32_16x16x32_bf16 a[100:103], a[196:199], a[224:227], a[100:103]\n"
"s_waitcnt vmcnt(8)\n"
"v_mfma_f32_16x16x32_bf16 a[144:147], a[200:203], a[224:227], a[144:147]\n"
"ds_read_b64_tr_b16 a[208:209], v10 offset:12288\n"
"ds_read_b64_tr_b16 a[210:211], v10 offset:14336\n"
"v_mfma_f32_16x16x32_bf16 a[148:151], a[204:207], a[224:227], a[148:151]\n"
"ds_read_b128 a[224:227], v17 offset:768\n"
"v_mfma_f32_16x16x32_bf16 a[104:107], a[192:195], a[228:231], a[104:107]\n"
"ds_read_b64_tr_b16 a[212:213], v11 offset:12288\n"
"ds_read_b64_tr_b16 a[214:215], v11 offset:14336\n"
"v_mfma_f32_16x16x32_bf16 a[108:111], a[196:199], a[228:231], a[108:111]\n"
"v_mfma_f32_16x16x32_bf16 a[152:155], a[200:203], a[228:231], a[152:155]\n"
"ds_read_b64_tr_b16 a[216:217], v12 offset:12288\n"
"ds_read_b64_tr_b16 a[218:219], v12 offset:14336\n"
"v_mfma_f32_16x16x32_bf16 a[156:159], a[204:207], a[228:231], a[156:159]\n"
"ds_read_b128 a[228:231], v17 offset:17152\n"
"v_mfma_f32_16x16x32_bf16 a[112:115], a[192:195], a[232:235], a[112:115]\n"
"ds_read_b64_tr_b16 a[220:221], v13 offset:12288\n"
"ds_read_b64_tr_b16 a[222:223], v13 offset:14336\n"
"v_mfma_f32_16x16x32_bf16 a[116:119], a[196:199], a[232:235], a[116:119]\n"
"v_mfma_f32_16x16x32_bf16 a[160:163], a[200:203], a[232:235], a[160:163]\n"
"s_add_u32 s50, s46, 589824\n"
"s_add_u32 m0, s49, 8192\n"
"v_mfma_f32_16x16x32_bf16 a[164:167], a[204:207], a[232:235], a[164:167]\n"
"ds_read_b128 a[232:235], v17 offset:33536\n"
"buffer_load_dwordx4 v30, s[28:31], s50 offen offset:0 lds\n"
"s_waitcnt lgkmcnt(11)\n"
"v_mfma_f32_16x16x32_bf16 a[120:123], a[192:195], a[236:239], a[120:123]\n"
"buffer_load_dwordx4 v31, s[28:31], s50 offen offset:1024 lds\n"
"buffer_load_dwordx4 v32, s[28:31], s50 offen offset:2048 lds\n"
"v_mfma_f32_16x16x32_bf16 a[124:127], a[196:199], a[236:239], a[124:127]\n"
"buffer_load_dwordx4 v33, s[28:31], s50 offen offset:3072 lds\n"
"v_mfma_f32_16x16x32_bf16 a[168:171], a[200:203], a[236:239], a[168:171]\n"
"v_mfma_f32_16x16x32_bf16 a[172:175], a[204:207], a[236:239], a[172:175]\n"
"ds_read_b128 a[236:239], v17 offset:49920\n"
"v_mfma_f32_16x16x32_bf16 a[128:131], a[192:195], a[240:243], a[128:131]\n"
"v_mfma_f32_16x16x32_bf16 a[132:135], a[196:199], a[240:243], a[132:135]\n"
"v_mfma_f32_16x16x32_bf16 a[176:179], a[200:203], a[240:243], a[176:179]\n"
"v_mfma_f32_16x16x32_bf16 a[180:183], a[204:207], a[240:243], a[180:183]\n"
"ds_read_b128 a[240:243], v21 offset:768\n"
"v_mfma_f32_16x16x32_bf16 a[136:139], a[192:195], a[244:247], a[136:139]\n"
"v_mfma_f32_16x16x32_bf16 a[140:143], a[196:199], a[244:247], a[140:143]\n"
"v_mfma_f32_16x16x32_bf16 a[184:187], a[200:203], a[244:247], a[184:187]\n"
"v_mfma_f32_16x16x32_bf16 a[188:191], a[204:207], a[244:247], a[188:191]\n"
"ds_read_b128 a[244:247], v21 offset:17152\n"
"s_waitcnt lgkmcnt(3)\n"
"v_mfma_f32_16x16x32_bf16 a[96:99], a[208:211], a[224:227], a[96:99]\n"
"v_mfma_f32_16x16x32_bf16 a[100:103], a[212:215], a[224:227], a[100:103]\n"
"s_waitcnt vmcnt(8)\n"
"v_mfma_f32_16x16x32_bf16 a[144:147], a[216:219], a[224:227], a[144:147]\n"
"ds_read_b64_tr_b16 a[192:193], v10 offset:0\n"
"ds_read_b64_tr_b16 a[194:195], v10 offset:2048\n"
"v_mfma_f32_16x16x32_bf16 a[148:151], a[220:223], a[224:227], a[148:151]\n"
"v_mfma_f32_16x16x32_bf16 a[104:107], a[208:211], a[228:231], a[104:107]\n"
"ds_read_b64_tr_b16 a[196:197], v11 offset:0\n"
"ds_read_b64_tr_b16 a[198:199], v11 offset:2048\n"
"v_mfma_f32_16x16x32_bf16 a[108:111], a[212:215], a[228:231], a[108:111]\n"
"v_mfma_f32_16x16x32_bf16 a[152:155], a[216:219], a[228:231], a[152:155]\n"
"ds_read_b64_tr_b16 a[200:201], v12 offset:0\n"
"ds_read_b64_tr_b16 a[202:203], v12 offset:2048\n"
"v_mfma_f32_16x16x32_bf16 a[156:159], a[220:223], a[228:231], a[156:159]\n"
"v_mfma_f32_16x16x32_bf16 a[112:115], a[208:211], a[232:235], a[112:115]\n"
"ds_read_b64_tr_b16 a[204:205], v13 offset:0\n"
"ds_read_b64_tr_b16 a[206:207], v13 offset:2048\n"
"v_mfma_f32_16x16x32_bf16 a[116:119], a[212:215], a[232:235], a[116:119]\n"
"v_mfma_f32_16x16x32_bf16 a[160:163], a[216:219], a[232:235], a[160:163]\n"
"s_add_u32 s50, s46, 622592\n"
"s_add_u32 m0, s49, 12288\n"
"v_mfma_f32_16x16x32_bf16 a[164:167], a[220:223], a[232:235], a[164:167]\n"
"buffer_load_dwordx4 v30, s[28:31], s50 offen offset:0 lds\n"
"s_waitcnt lgkmcnt(8)\n"
"v_mfma_f32_16x16x32_bf16 a[120:123], a[208:211], a[236:239], a[120:123]\n"
"buffer_load_dwordx4 v31, s[28:31], s50 offen offset:1024 lds\n"
"buffer_load_dwordx4 v32, s[28:31], s50 offen offset:2048 lds\n"
"v_mfma_f32_16x16x32_bf16 a[124:127], a[212:215], a[236:239], a[124:127]\n"
"buffer_load_dwordx4 v33, s[28:31], s50 offen offset:3072 lds\n"
"v_mfma_f32_16x16x32_bf16 a[168:171], a[216:219], a[236:239], a[168:171]\n"
"v_mfma_f32_16x16x32_bf16 a[172:175], a[220:223], a[236:239], a[172:175]\n"
"v_mfma_f32_16x16x32_bf16 a[128:131], a[208:211], a[240:243], a[128:131]\n"
"v_mfma_f32_16x16x32_bf16 a[132:135], a[212:215], a[240:243], a[132:135]\n"
"v_mfma_f32_16x16x32_bf16 a[176:179], a[216:219], a[240:243], a[176:179]\n"
"v_mfma_f32_16x16x32_bf16 a[180:183], a[220:223], a[240:243], a[180:183]\n"
"v_mfma_f32_16x16x32_bf16 a[136:139], a[208:211], a[244:247], a[136:139]\n"
"v_mfma_f32_16x16x32_bf16 a[140:143], a[212:215], a[244:247], a[140:143]\n"
"v_mfma_f32_16x16x32_bf16 a[184:187], a[216:219], a[244:247], a[184:187]\n"
"v_mfma_f32_16x16x32_bf16 a[188:191], a[220:223], a[244:247], a[188:191]\n"
"; ---- end of section: r1\n"
"s_cmp_eq_u32 s43, 2\n"
"s_waitcnt lgkmcnt(0)\n"
"s_cbranch_scc1 LFE4_%=\n"
"s_waitcnt vmcnt(0) lgkmcnt(0)\n"
"s_barrier\n"
"ds_write_b128 v8, v[40:43] offset:0\n"
"ds_write_b128 v8, v[44:47] offset:16384\n"
"ds_write_b128 v8, v[48:51] offset:32768\n"
"ds_write_b128 v8, v[52:55] offset:49152\n"
"ds_write_b128 v9, v[56:59] offset:0\n"
"ds_write_b128 v9, v[60:63] offset:16384\n"
"s_waitcnt lgkmcnt(0)\n"
"s_barrier\n"
"ds_read_b128 a[224:227], v14 offset:0\n"
"ds_read_b128 a[228:231], v14 offset:16384\n"
"ds_read_b128 a[232:235], v14 offset:32768\n"
"ds_read_b128 a[236:239], v14 offset:49152\n"
"ds_read_b128 a[240:243], v18 offset:0\n"
"ds_read_b128 a[244:247], v18 offset:16384\n"
"s_waitcnt lgkmcnt(0)\n"
"s_cmp_lg_u32 s42, 0\n"
"s_cbranch_scc1 LHRF_%=\n"
"s_mul_i32 s53, s44, 11\n"
"s_add_u32 s53, s53, s43\n"
"s_add_u32 s53, s53, 8\n"
"s_mul_i32 s53, s53, 768\n"
"s_add_u32 s53, s53, 33554432\n"
"s_add_u32 s86, s80, s53\n"
"s_addc_u32 s87, s81, 0\n"
"v_lshlrev_b32 v60, 3, v1\n"
"s_mov_b32 exec_hi, 0\n"
"global_load_dwordx2 v[58:59], v60, s[86:87] sc1\n"
"s_mov_b64 exec, -1\n"
"LHRF_%=:\n"
"s_nop 3\n"
"v_mfma_f32_16x16x32_bf16 a[0:3], a[192:195], a[224:227], a[0:3]\n"
"v_mfma_f32_16x16x32_bf16 a[4:7], a[196:199], a[224:227], a[4:7]\n"
"v_mfma_f32_16x16x32_bf16 a[48:51], a[200:203], a[224:227], a[48:51]\n"
"ds_read_b64_tr_b16 a[208:209], v10 offset:4096\n"
"ds_read_b64_tr_b16 a[210:211], v10 offset:6144\n"
"v_mfma_f32_16x16x32_bf16 a[52:55], a[204:207], a[224:227], a[52:55]\n"
"ds_read_b128 a[224:227], v15 offset:0\n"
"v_mfma_f32_16x16x32_bf16 a[8:11], a[192:195], a[228:231], a[8:11]\n"
"ds_read_b64_tr_b16 a[212:213], v11 offset:4096\n"
"ds_read_b64_tr_b16 a[214:215], v11 offset:6144\n"
"v_mfma_f32_16x16x32_bf16 a[12:15], a[196:199], a[228:231], a[12:15]\n"
"v_mfma_f32_16x16x32_bf16 a[56:59], a[200:203], a[228:231], a[56:59]\n"
"ds_read_b64_tr_b16 a[216:217], v12 offset:4096\n"
"ds_read_b64_tr_b16 a[218:219], v12 offset:6144\n"
"v_mfma_f32_16x16x32_bf16 a[60:63], a[204:207], a[228:231], a[60:63]\n"
"ds_read_b128 a[228:231], v15 offset:16384\n"
"v_mfma_f32_16x16x32_bf16 a[16:19], a[192:195], a[232:235], a[16:19]\n"
"ds_read_b64_tr_b16 a[220:221], v13 offset:4096\n"
"ds_read_b64_tr_b16 a[222:223], v13 offset:6144\n"
"v_mfma_f32_16x16x32_bf16 a[20:23], a[196:199], a[232:235], a[20:23]\n"
"v_mfma_f32_16x16x32_bf16 a[64:67], a[200:203], a[232:235], a[64:67]\n"
"s_add_u32 s50, s46, 524288\n"
"s_add_u32 m0, s49, 0\n"
"v_mfma_f32_16x16x32_bf16 a[68:71], a[204:207], a[232:235], a[68:71]\n"
"ds_read_b128 a[232:235], v15 offset:32768\n"
"buffer_load_dwordx4 v34, s[28:31], s50 offen offset:0 lds\n"
"v_mfma_f32_16x16x32_bf16 a[24:27], a[192:195], a[236:239], a[24:27]\n"
"buffer_load_dwordx4 v35, s[28:31], s50 offen offset:1024 lds\n"
"buffer_load_dwordx4 v36, s[28:31], s50 offen offset:2048 lds\n"
"v_mfma_f32_16x16x32_bf16 a[28:31], a[196:199], a[236:239], a[28:31]\n"
"buffer_load_dwordx4 v37, s[28:31], s50 offen offset:3072 lds\n"
"v_mfma_f32_16x16x32_bf16 a[72:75], a[200:203], a[236:239], a[72:75]\n"
"v_mfma_f32_16x16x32_bf16 a[76:79], a[204:207], a[236:239], a[76:79]\n"
"ds_read_b128 a[236:239], v15 offset:49152\n"
"v_mfma_f32_16x16x32_bf16 a[32:35], a[192:195], a[240:243], a[32:35]\n"
"v_mfma_f32_16x16x32_bf16 a[36:39], a[196:199], a[240:243], a[36:39]\n"
"v_mfma_f32_16x16x32_bf16 a[80:83], a[200:203], a[240:243], a[80:83]\n"
"v_mfma_f32_16x16x32_bf16 a[84:87], a[204:207], a[240:243], a[84:87]\n"
"ds_read_b128 a[240:243], v19 offset:0\n"
"v_mfma_f32_16x16x32_bf16 a[40:43], a[192:195], a[244:247], a[40:43]\n"
"v_mfma_f32_16x16x32_bf16 a[44:47], a[196:199], a[244:247], a[44:47]\n"
"v_mfma_f32_16x16x32_bf16 a[88:91], a[200:203], a[244:247], a[88:91]\n"
"v_mfma_f32_16x16x32_bf16 a[92:95], a[204:207], a[244:247], a[92:95]\n"
"ds_read_b128 a[244:247], v19 offset:16384\n"
"s_waitcnt lgkmcnt(3)\n"
"v_mfma_f32_16x16x32_bf16 a[0:3], a[208:211], a[224:227], a[0:3]\n"
"v_mfma_f32_16x16x32_bf16 a[4:7], a[212:215], a[224:227], a[4:7]\n"
"v_mfma_f32_16x16x32_bf16 a[48:51], a[216:219], a[224:227], a[48:51]\n"
"ds_read_b64_tr_b16 a[192:193], v10 offset:8192\n"
"ds_read_b64_tr_b16 a[194:195], v10 offset:10240\n"
"v_mfma_f32_16x16x32_bf16 a[52:55], a[220:223], a[224:227], a[52:55]\n"
"ds_read_b128 a[224:227], v16 offset:0\n"
"v_mfma_f32_16x16x32_bf16 a[8:11], a[208:211], a[228:231], a[8:11]\n"
"ds_read_b64_tr_b16 a[196:197], v11 offset:8192\n"
"ds_read_b64_tr_b16 a[198:199], v11 offset:10240\n"
"v_mfma_f32_16x16x32_bf16 a[12:15], a[212:215], a[228:231], a[12:15]\n"
"v_mfma_f32_16x16x32_bf16 a[56:59], a[216:219], a[228:231], a[56:59]\n"
"ds_read_b64_tr_b16 a[200:201], v12 offset:8192\n"
"ds_read_b64_tr_b16 a[202:203], v12 offset:10240\n"
"v_mfma_f32_16x16x32_bf16 a[60:63], a[220:223], a[228:231], a[60:63]\n"
"ds_read_b128 a[228:231], v16 offset:16384\n"
"v_mfma_f32_16x16x32_bf16 a[16:19], a[208:211], a[232:235], a[16:19]\n"
"ds_read_b64_tr_b16 a[204:205], v13 offset:8192\n"
"ds_read_b64_tr_b16 a[206:207], v13 offset:10240\n"
"v_mfma_f32_16x16x32_bf16 a[20:23], a[212:215], a[232:235], a[20:23]\n"
"v_mfma_f32_16x16x32_bf16 a[64:67], a[216:219], a[232:235], a[64:67]\n"
"s_add_u32 s50, s46, 557056\n"
"s_add_u32 m0, s49, 4096\n"
"v_mfma_f32_16x16x32_bf16 a[68:71], a[220:223], a[232:235], a[68:71]\n"
"ds_read_b128 a[232:235], v16 offset:32768\n"
"buffer_load_dwordx4 v34, s[28:31], s50 offen offset:0 lds\n"
"s_waitcnt lgkmcnt(11)\n"
"v_mfma_f32_16x16x32_bf16 a[24:27], a[208:211], a[236:239], a[24:27]\n"
"buffer_load_dwordx4 v35, s[28:31], s50 offen offset:1024 lds\n"
"buffer_load_dwordx4 v36, s[28:31], s50 offen offset:2048 lds\n"
"v_mfma_f32_16x16x32_bf16 a[28:31], a[212:215], a[236:239], a[28:31]\n"
"buffer_load_dwordx4 v37, s[28:31], s50 offen offset:3072 lds\n"
"v_mfma_f32_16x16x32_bf16 a[72:75], a[216:219], a[236:239], a[72:75]\n"
"v_mfma_f32_16x16x32_bf16 a[76:79], a[220:223], a[236:239], a[76:79]\n"
"ds_read_b128 a[236:239], v16 offset:49152\n"
"v_mfma_f32_16x16x32_bf16 a[32:35], a[208:211], a[240:243], a[32:35]\n"
"v_mfma_f32_16x16x32_bf16 a[36:39], a[212:215], a[240:243], a[36:39]\n"
"v_mfma_f32_16x16x32_bf16 a[80:83], a[216:219], a[240:243], a[80:83]\n"
"v_mfma_f32_16x16x32_bf16 a[84:87], a[220:223], a[240:243], a[84:87]\n"
"ds_read_b128 a[240:243], v20 offset:0\n"
"v_mfma_f32_16x16x32_bf16 a[40:43], a[208:211], a[244:247], a[40:43]\n"
"v_mfma_f32_16x16x32_bf16 a[44:47], a[212:215], a[244:247], a[44:47]\n"
"v_mfma_f32_16x16x32_bf16 a[88:91], a[216:219], a[244:247], a[88:91]\n"
"v_mfma_f32_16x16x32_bf16 a[92:95], a[220:223], a[244:247], a[92:95]\n"
"ds_read_b128 a[244:247], v20 offset:16384\n"
"s_waitcnt lgkmcnt(3)\n"
"v_mfma_f32_16x16x32_bf16 a[0:3], a[192:195], a[224:227], a[0:3]\n"
"v_mfma_f32_16x16x32_bf16 a[4:7], a[196:199], a[224:227], a[4:7]\n"
"v_mfma_f32_16x16x32_bf16 a[48:51], a[200:203], a[224:227], a[48:51]\n"
"ds_read_b64_tr_b16 a[208:209], v10 offset:12288\n"
"ds_read_b64_tr_b16 a[210:211], v10 offset:14336\n"
"v_mfma_f32_16x16x32_bf16 a[52:55], a[204:207], a[224:227], a[52:55]\n"
"ds_read_b128 a[224:227], v17 offset:0\n"
"v_mfma_f32_16x16x32_bf16 a[8:11], a[192:195], a[228:231], a[8:11]\n"
"ds_read_b64_tr_b16 a[212:213], v11 offset:12288\n"
"ds_read_b64_tr_b16 a[214:215], v11 offset:14336\n"
"v_mfma_f32_16x16x32_bf16 a[12:15], a[196:199], a[228:231], a[12:15]\n"
"v_mfma_f32_16x16x32_bf16 a[56:59], a[200:203], a[228:231], a[56:59]\n"
"ds_read_b64_tr_b16 a[216:217], v12 offset:12288\n"
"ds_read_b64_tr_b16 a[218:219], v12 offset:14336\n"
"v_mfma_f32_16x16x32_bf16 a[60:63], a[204:207], a[228:231], a[60:63]\n"
"ds_read_b128 a[228:231], v17 offset:16384\n"
"v_mfma_f32_16x16x32_bf16 a[16:19], a[192:195], a[232:235], a[16:19]\n"
"ds_read_b64_tr_b16 a[220:221], v13 offset:12288\n"
"ds_read_b64_tr_b16 a[222:223], v13 offset:14336\n"
"v_mfma_f32_16x16x32_bf16 a[20:23], a[196:199], a[232:235], a[20:23]\n"
"v_mfma_f32_16x16x32_bf16 a[64:67], a[200:203], a[232:235], a[64:67]\n"
"s_add_u32 s50, s46, 589824\n"
"s_add_u32 m0, s49, 8192\n"
"v_mfma_f32_16x16x32_bf16 a[68:71], a[204:207], a[232:235], a[68:71]\n"
"ds_read_b128 a[232:235], v17 offset:32768\n"
"buffer_load_dwordx4 v34, s[28:31], s50 offen offset:0 lds\n"
"s_waitcnt lgkmcnt(11)\n"
"v_mfma_f32_16x16x32_bf16 a[24:27], a[192:195], a[236:239], a[24:27]\n"
"buffer_load_dwordx4 v35, s[28:31], s50 offen offset:1024 lds\n"
"buffer_load_dwordx4 v36, s[28:31], s50 offen offset:2048 lds\n"
"v_mfma_f32_16x16x32_bf16 a[28:31], a[196:199], a[236:239], a[28:31]\n"
"buffer_load_dwordx4 v37, s[28:31], s50 offen offset:3072 lds\n"
"v_mfma_f32_16x16x32_bf16 a[72:75], a[200:203], a[236:239], a[72:75]\n"
"v_mfma_f32_16x16x32_bf16 a[76:79], a[204:207], a[236:239], a[76:79]\n"
"ds_read_b128 a[236:239], v17 offset:49152\n"
"v_mfma_f32_16x16x32_bf16 a[32:35], a[192:195], a[240:243], a[32:35]\n"
"v_mfma_f32_16x16x32_bf16 a[36:39], a[196:199], a[240:243], a[36:39]\n"
"v_mfma_f32_16x16x32_bf16 a[80:83], a[200:203], a[240:243], a[80:83]\n"
"v_mfma_f32_16x16x32_bf16 a[84:87], a[204:207], a[240:243], a[84:87]\n"
"ds_read_b128 a[240:243], v21 offset:0\n"
"v_mfma_f32_16x16x32_bf16 a[40:43], a[192:195], a[244:247], a[40:43]\n"
"v_mfma_f32_16x16x32_bf16 a[44:47], a[196:199], a[244:247], a[44:47]\n"
"v_mfma_f32_16x16x32_bf16 a[88:91], a[200:203], a[244:247], a[88:91]\n"
"v_mfma_f32_16x16x32_bf16 a[92:95], a[204:207], a[244:247], a[92:95]\n"
"ds_read_b128 a[244:247], v21 offset:16384\n"
"s_waitcnt lgkmcnt(3)\n"
"v_mfma_f32_16x16x32_bf16 a[0:3], a[208:211], a[224:227], a[0:3]\n"
"v_mfma_f32_16x16x32_bf16 a[4:7], a[212:215], a[224:227], a[4:7]\n"
"s_waitcnt vmcnt(8)\n"
"v_mfma_f32_16x16x32_bf16 a[48:51], a[216:219], a[224:227], a[48:51]\n"
"ds_read_b64_tr_b16 a[192:193], v10 offset:0\n"
"ds_read_b64_tr_b16 a[194:195], v10 offset:2048\n"
"v_mfma_f32_16x16x32_bf16 a[52:55], a[220:223], a[224:227], a[52:55]\n"
"ds_read_b128 a[224:227], v14 offset:0\n"
"v_mfma_f32_16x16x32_bf16 a[8:11], a[208:211], a[228:231], a[8:11]\n"
"ds_read_b64_tr_b16 a[196:197], v11 offset:0\n"
"ds_read_b64_tr_b16 a[198:199], v11 offset:2048\n"
"v_mfma_f32_16x16x32_bf16 a[12:15], a[212:215], a[228:231], a[12:15]\n"
"v_mfma_f32_16x16x32_bf16 a[56:59], a[216:219], a[228:231], a[56:59]\n"
"ds_read_b64_tr_b16 a[200:201], v12 offset:0\n"
"ds_read_b64_tr_b16 a[202:203], v12 offset:2048\n"
"v_mfma_f32_16x16x32_bf16 a[60:63], a[220:223], a[228:231], a[60:63]\n"
"ds_read_b128 a[228:231], v14 offset:16384\n"
"v_mfma_f32_16x16x32_bf16 a[16:19], a[208:211], a[232:235], a[16:19]\n"
"ds_read_b64_tr_b16 a[204:205], v13 offset:0\n"
"ds_read_b64_tr_b16 a[206:207], v13 offset:2048\n"
"v_mfma_f32_16x16x32_bf16 a[20:23], a[212:215], a[232:235], a[20:23]\n"
"v_mfma_f32_16x16x32_bf16 a[64:67], a[216:219], a[232:235], a[64:67]\n"
"s_add_u32 s50, s46, 622592\n"
"s_add_u32 m0, s49, 12288\n"
"v_mfma_f32_16x16x32_bf16 a[68:71], a[220:223], a[232:235], a[68:71]\n"
"ds_read_b128 a[232:235], v14 offset:32768\n"
"buffer_load_dwordx4 v34, s[28:31], s50 offen offset:0 lds\n"
"s_waitcnt lgkmcnt(11)\n"
"v_mfma_f32_16x16x32_bf16 a[24:27], a[208:211], a[236:239], a[24:27]\n"
"buffer_load_dwordx4 v35, s[28:31], s50 offen offset:1024 lds\n"
"buffer_load_dwordx4 v36, s[28:31], s50 offen offset:2048 lds\n"
"v_mfma_f32_16x16x32_bf16 a[28:31], a[212:215], a[236:239], a[28:31]\n"
"buffer_load_dwordx4 v37, s[28:31], s50 offen offset:3072 lds\n"
"v_mfma_f32_16x16x32_bf16 a[72:75], a[216:219], a[236:239], a[72:75]\n"
"v_mfma_f32_16x16x32_bf16 a[76:79], a[220:223], a[236:239], a[76:79]\n"
"ds_read_b128 a[236:239], v14 offset:49152\n"
"v_mfma_f32_16x16x32_bf16 a[32:35], a[208:211], a[240:243], a[32:35]\n"
"v_mfma_f32_16x16x32_bf16 a[36:39], a[212:215], a[240:243], a[36:39]\n"
"v_mfma_f32_16x16x32_bf16 a[80:83], a[216:219], a[240:243], a[80:83]\n"
"v_mfma_f32_16x16x32_bf16 a[84:87], a[220:223], a[240:243], a[84:87]\n"
"ds_read_b128 a[240:243], v18 offset:0\n"
"v_mfma_f32_16x16x32_bf16 a[40:43], a[208:211], a[244:247], a[40:43]\n"
"v_mfma_f32_16x16x32_bf16 a[44:47], a[212:215], a[244:247], a[44:47]\n"
"v_mfma_f32_16x16x32_bf16 a[88:91], a[216:219], a[244:247], a[88:91]\n"
"v_mfma_f32_16x16x32_bf16 a[92:95], a[220:223], a[244:247], a[92:95]\n"
"ds_read_b128 a[244:247], v18 offset:16384\n"
"s_waitcnt lgkmcnt(3)\n"
"v_mfma_f32_16x16x32_bf16 a[96:99], a[192:195], a[224:227], a[96:99]\n"
"v_mfma_f32_16x16x32_bf16 a[100:103], a[196:199], a[224:227], a[100:103]\n"
"s_waitcnt vmcnt(8)\n"
"v_mfma_f32_16x16x32_bf16 a[144:147], a[200:203], a[224:227], a[144:147]\n"
"ds_read_b64_tr_b16 a[208:209], v10 offset:4096\n"
"ds_read_b64_tr_b16 a[210:211], v10 offset:6144\n"
"v_mfma_f32_16x16x32_bf16 a[148:151], a[204:207], a[224:227], a[148:151]\n"
"ds_read_b128 a[224:227], v15 offset:0\n"
"v_mfma_f32_16x16x32_bf16 a[104:107], a[192:195], a[228:231], a[104:107]\n"
"ds_read_b64_tr_b16 a[212:213], v11 offset:4096\n"
"ds_read_b64_tr_b16 a[214:215], v11 offset:6144\n"
"v_mfma_f32_16x16x32_bf16 a[108:111], a[196:199], a[228:231], a[108:111]\n"
"v_mfma_f32_16x16x32_bf16 a[152:155], a[200:203], a[228:231], a[152:155]\n"
"ds_read_b64_tr_b16 a[216:217], v12 offset:4096\n"
"ds_read_b64_tr_b16 a[218:219], v12 offset:6144\n"
"s_mul_i32 s53, s43, 22\n"
"v_mfma_f32_16x16x32_bf16 a[156:159], a[204:207], a[228:231], a[156:159]\n"
"ds_read_b128 a[228:231], v15 offset:16384\n"
"s_add_u32 s53, s53, s44\n"
"s_add_u32 s53, s53, 176\n"
"v_mfma_f32_16x16x32_bf16 a[112:115], a[192:195], a[232:235], a[112:115]\n"
"ds_read_b64_tr_b16 a[220:221], v13 offset:4096\n"
"ds_read_b64_tr_b16 a[222:223], v13 offset:6144\n"
"s_lshl_b32 s53, s53, 2\n"
"v_mfma_f32_16x16x32_bf16 a[116:119], a[196:199], a[232:235], a[116:119]\n"
"s_add_u32 s53, s53, s42\n"
"s_mul_i32 s53, s53, 24576\n"
"s_add_u32 s40, s80, s53\n"
"v_mfma_f32_16x16x32_bf16 a[160:163], a[200:203], a[232:235], a[160:163]\n"
"s_addc_u32 s41, s81, 0\n"
"v_lshlrev_b32 v2, 4, v1\n"
"v_mfma_f32_16x16x32_bf16 a[164:167], a[204:207], a[232:235], a[164:167]\n"
"ds_read_b128 a[232:235], v15 offset:32768\n"
"v_add_u32 v3, 4096, v2\n"
"v_add_u32 v4, 8192, v2\n"
"s_waitcnt lgkmcnt(11)\n"
"v_mfma_f32_16x16x32_bf16 a[120:123], a[192:195], a[236:239], a[120:123]\n"
"v_add_u32 v5, 12288, v2\n"
"v_add_u32 v6, 16384, v2\n"
"v_add_u32 v7, 20480, v2\n"
"v_mfma_f32_16x16x32_bf16 a[124:127], a[196:199], a[236:239], a[124:127]\n"
"v_accvgpr_read_b32 v64, a0\n"
"v_accvgpr_read_b32 v65, a1\n"
"v_accvgpr_read_b32 v66, a2\n"
"v_mfma_f32_16x16x32_bf16 a[168:171], a[200:203], a[236:239], a[168:171]\n"
"v_accvgpr_read_b32 v67, a3\n"
"v_accvgpr_read_b32 v68, a4\n"
"v_accvgpr_read_b32 v69, a5\n"
"v_mfma_f32_16x16x32_bf16 a[172:175], a[204:207], a[236:239], a[172:175]\n"
"ds_read_b128 a[236:239], v15 offset:49152\n"
"v_accvgpr_read_b32 v70, a6\n"
"v_accvgpr_read_b32 v71, a7\n"
"v_mfma_f32_16x16x32_bf16 a[128:131], a[192:195], a[240:243], a[128:131]\n"
"v_cvt_pk_bf16_f32 v72, v64, v65\n"
"v_cvt_pk_bf16_f32 v73, v66, v67\n"
"v_cvt_pk_bf16_f32 v74, v68, v69\n"
"v_mfma_f32_16x16x32_bf16 a[132:135], a[196:199], a[240:243], a[132:135]\n"
"v_cvt_pk_bf16_f32 v75, v70, v71\n"
"global_store_dwordx4 v2, v[72:75], s[40:41] offset:0 sc1\n"
"v_accvgpr_read_b32 v76, a8\n"
"v_mfma_f32_16x16x32_bf16 a[176:179], a[200:203], a[240:243], a[176:179]\n"
"v_accvgpr_read_b32 v77, a9\n"
"v_accvgpr_read_b32 v78, a10\n"
"v_accvgpr_read_b32 v79, a11\n"
"v_mfma_f32_16x16x32_bf16 a[180:183], a[204:207], a[240:243], a[180:183]\n"
"ds_read_b128 a[240:243], v19 offset:0\n"
"v_accvgpr_read_b32 v80, a12\n"
"v_mfma_f32_16x16x32_bf16 a[136:139], a[192:195], a[244:247], a[136:139]\n"
"v_accvgpr_read_b32 v81, a13\n"
"v_accvgpr_read_b32 v82, a14\n"
"v_accvgpr_read_b32 v83, a15\n"
"v_mfma_f32_16x16x32_bf16 a[140:143], a[196:199], a[244:247], a[140:143]\n"
"v_cvt_pk_bf16_f32 v84, v76, v77\n"
"v_cvt_pk_bf16_f32 v85, v78, v79\n"
"v_cvt_pk_bf16_f32 v86, v80, v81\n"
"v_mfma_f32_16x16x32_bf16 a[184:187], a[200:203], a[244:247], a[184:187]\n"
"v_cvt_pk_bf16_f32 v87, v82, v83\n"
"global_store_dwordx4 v2, v[84:87], s[40:41] offset:1024 sc1\n"
"v_accvgpr_read_b32 v88, a16\n"
"v_mfma_f32_16x16x32_bf16 a[188:191], a[204:207], a[244:247], a[188:191]\n"
"ds_read_b128 a[244:247], v19 offset:16384\n"
"v_accvgpr_read_b32 v89, a17\n"
"v_accvgpr_read_b32 v90, a18\n"
"s_waitcnt lgkmcnt(3)\n"
"v_mfma_f32_16x16x32_bf16 a[96:99], a[208:211], a[224:227], a[96:99]\n"
"v_accvgpr_read_b32 v91, a19\n"
"v_accvgpr_read_b32 v92, a20\n"
"v_mfma_f32_16x16x32_bf16 a[100:103], a[212:215], a[224:227], a[100:103]\n"
"v_accvgpr_read_b32 v93, a21\n"
"v_accvgpr_read_b32 v94, a22\n"
"s_waitcnt vmcnt(6)\n"
"v_mfma_f32_16x16x32_bf16 a[144:147], a[216:219], a[224:227], a[144:147]\n"
"ds_read_b64_tr_b16 a[192:193], v10 offset:8192\n"
"ds_read_b64_tr_b16 a[194:195], v10 offset:10240\n"
"v_mfma_f32_16x16x32_bf16 a[148:151], a[220:223], a[224:227], a[148:151]\n"
"ds_read_b128 a[224:227], v16 offset:0\n"
"v_accvgpr_read_b32 v95, a23\n"
"v_cvt_pk_bf16_f32 v96, v88, v89\n"
"v_mfma_f32_16x16x32_bf16 a[104:107], a[208:211], a[228:231], a[104:107]\n"
"ds_read_b64_tr_b16 a[196:197], v11 offset:8192\n"
"ds_read_b64_tr_b16 a[198:199], v11 offset:10240\n"
"v_mfma_f32_16x16x32_bf16 a[108:111], a[212:215], a[228:231], a[108:111]\n"
"v_cvt_pk_bf16_f32 v97, v90, v91\n"
"v_cvt_pk_bf16_f32 v98, v92, v93\n"
"v_cvt_pk_bf16_f32 v99, v94, v95\n"
"v_mfma_f32_16x16x32_bf16 a[152:155], a[216:219], a[228:231], a[152:155]\n"
"ds_read_b64_tr_b16 a[200:201], v12 offset:8192\n"
"ds_read_b64_tr_b16 a[202:203], v12 offset:10240\n"
"v_mfma_f32_16x16x32_bf16 a[156:159], a[220:223], a[228:231], a[156:159]\n"
"ds_read_b128 a[228:231], v16 offset:16384\n"
"global_store_dwordx4 v2, v[96:99], s[40:41] offset:2048 sc1\n"
"v_mfma_f32_16x16x32_bf16 a[112:115], a[208:211], a[232:235], a[112:115]\n"
"ds_read_b64_tr_b16 a[204:205], v13 offset:8192\n"
"ds_read_b64_tr_b16 a[206:207], v13 offset:10240\n"
"v_accvgpr_read_b32 v100, a24\n"
"v_mfma_f32_16x16x32_bf16 a[116:119], a[212:215], a[232:235], a[116:119]\n"
"v_accvgpr_read_b32 v101, a25\n"
"v_accvgpr_read_b32 v102, a26\n"
"v_mfma_f32_16x16x32_bf16 a[160:163], a[216:219], a[232:235], a[160:163]\n"
"v_accvgpr_read_b32 v103, a27\n"
"v_accvgpr_read_b32 v104, a28\n"
"v_accvgpr_read_b32 v105, a29\n"
"v_mfma_f32_16x16x32_bf16 a[164:167], a[220:223], a[232:235], a[164:167]\n"
"ds_read_b128 a[232:235], v16 offset:32768\n"
"v_accvgpr_read_b32 v106, a30\n"
"s_waitcnt lgkmcnt(11)\n"
"v_mfma_f32_16x16x32_bf16 a[120:123], a[208:211], a[236:239], a[120:123]\n"
"v_accvgpr_read_b32 v107, a31\n"
"v_cvt_pk_bf16_f32 v108, v100, v101\n"
"v_mfma_f32_16x16x32_bf16 a[124:127], a[212:215], a[236:239], a[124:127]\n"
"v_cvt_pk_bf16_f32 v109, v102, v103\n"
"v_cvt_pk_bf16_f32 v110, v104, v105\n"
"v_cvt_pk_bf16_f32 v111, v106, v107\n"
"v_mfma_f32_16x16x32_bf16 a[168:171], a[216:219], a[236:239], a[168:171]\n"
"global_store_dwordx4 v2, v[108:111], s[40:41] offset:3072 sc1\n"
"v_accvgpr_read_b32 v64, a32\n"
"v_mfma_f32_16x16x32_bf16 a[172:175], a[220:223], a[236:239], a[172:175]\n"
"ds_read_b128 a[236:239], v16 offset:49152\n"
"v_accvgpr_read_b32 v65, a33\n"
"v_accvgpr_read_b32 v66, a34\n"
"v_mfma_f32_16x16x32_bf16 a[128:131], a[208:211], a[240:243], a[128:131]\n"
"v_accvgpr_read_b32 v67, a35\n"
"v_accvgpr_read_b32 v68, a36\n"
"v_mfma_f32_16x16x32_bf16 a[132:135], a[212:215], a[240:243], a[132:135]\n"
"v_accvgpr_read_b32 v69, a37\n"
"v_accvgpr_read_b32 v70, a38\n"
"v_accvgpr_read_b32 v71, a39\n"
"v_mfma_f32_16x16x32_bf16 a[176:179], a[216:219], a[240:243], a[176:179]\n"
"v_cvt_pk_bf16_f32 v72, v64, v65\n"
"v_cvt_pk_bf16_f32 v73, v66, v67\n"
"v_mfma_f32_16x16x32_bf16 a[180:183], a[220:223], a[240:243], a[180:183]\n"
"ds_read_b128 a[240:243], v20 offset:0\n"
"v_cvt_pk_bf16_f32 v74, v68, v69\n"
"v_mfma_f32_16x16x32_bf16 a[136:139], a[208:211], a[244:247], a[136:139]\n"
"v_cvt_pk_bf16_f32 v75, v70, v71\n"
"global_store_dwordx4 v3, v[72:75], s[40:41] offset:0 sc1\n"
"v_accvgpr_read_b32 v76, a40\n"
"v_mfma_f32_16x16x32_bf16 a[140:143], a[212:215], a[244:247], a[140:143]\n"
"v_accvgpr_read_b32 v77, a41\n"
"v_accvgpr_read_b32 v78, a42\n"
"v_mfma_f32_16x16x32_bf16 a[184:187], a[216:219], a[244:247], a[184:187]\n"
"v_accvgpr_read_b32 v79, a43\n"
"v_accvgpr_read_b32 v80, a44\n"
"v_accvgpr_read_b32 v81, a45\n"
"v_mfma_f32_16x16x32_bf16 a[188:191], a[220:223], a[244:247], a[188:191]\n"
"ds_read_b128 a[244:247], v20 offset:16384\n"
"v_accvgpr_read_b32 v82, a46\n"
"s_waitcnt lgkmcnt(3)\n"
"v_mfma_f32_16x16x32_bf16 a[96:99], a[192:195], a[224:227], a[96:99]\n"
"v_accvgpr_read_b32 v83, a47\n"
"v_cvt_pk_bf16_f32 v84, v76, v77\n"
"v_mfma_f32_16x16x32_bf16 a[100:103], a[196:199], a[224:227], a[100:103]\n"
"v_cvt_pk_bf16_f32 v85, v78, v79\n"
"v_cvt_pk_bf16_f32 v86, v80, v81\n"
"s_waitcnt vmcnt(5)\n"
"v_mfma_f32_16x16x32_bf16 a[144:147], a[200:203], a[224:227], a[144:147]\n"
"ds_read_b64_tr_b16 a[208:209], v10 offset:12288\n"
"ds_read_b64_tr_b16 a[210:211], v10 offset:14336\n"
"v_mfma_f32_16x16x32_bf16 a[148:151], a[204:207], a[224:227], a[148:151]\n"
"ds_read_b128 a[224:227], v17 offset:0\n"
"v_cvt_pk_bf16_f32 v87, v82, v83\n"
"global_store_dwordx4 v3, v[84:87], s[40:41] offset:1024 sc1\n"
"v_mfma_f32_16x16x32_bf16 a[104:107], a[192:195], a[228:231], a[104:107]\n"
"ds_read_b64_tr_b16 a[212:213], v11 offset:12288\n"
"ds_read_b64_tr_b16 a[214:215], v11 offset:14336\n"
"v_mfma_f32_16x16x32_bf16 a[108:111], a[196:199], a[228:231], a[108:111]\n"
"v_accvgpr_read_b32 v88, a48\n"
"v_accvgpr_read_b32 v89, a49\n"
"v_accvgpr_read_b32 v90, a50\n"
"v_mfma_f32_16x16x32_bf16 a[152:155], a[200:203], a[228:231], a[152:155]\n"
"ds_read_b64_tr_b16 a[216:217], v12 offset:12288\n"
"ds_read_b64_tr_b16 a[218:219], v12 offset:14336\n"
"v_mfma_f32_16x16x32_bf16 a[156:159], a[204:207], a[228:231], a[156:159]\n"
"ds_read_b128 a[228:231], v17 offset:16384\n"
"v_accvgpr_read_b32 v91, a51\n"
"v_mfma_f32_16x16x32_bf16 a[112:115], a[192:195], a[232:235], a[112:115]\n"
"ds_read_b64_tr_b16 a[220:221], v13 offset:12288\n"
"ds_read_b64_tr_b16 a[222:223], v13 offset:14336\n"
"v_accvgpr_read_b32 v92, a52\n"
"v_mfma_f32_16x16x32_bf16 a[116:119], a[196:199], a[232:235], a[116:119]\n"
"v_accvgpr_read_b32 v93, a53\n"
"v_accvgpr_read_b32 v94, a54\n"
"v_mfma_f32_16x16x32_bf16 a[160:163], a[200:203], a[232:235], a[160:163]\n"
"v_accvgpr_read_b32 v95, a55\n"
"v_cvt_pk_bf16_f32 v96, v88, v89\n"
"v_cvt_pk_bf16_f32 v97, v90, v91\n"
"v_mfma_f32_16x16x32_bf16 a[164:167], a[204:207], a[232:235], a[164:167]\n"
"ds_read_b128 a[232:235], v17 offset:32768\n"
"v_cvt_pk_bf16_f32 v98, v92, v93\n"
"s_waitcnt lgkmcnt(11)\n"
"v_mfma_f32_16x16x32_bf16 a[120:123], a[192:195], a[236:239], a[120:123]\n"
"v_cvt_pk_bf16_f32 v99, v94, v95\n"
"global_store_dwordx4 v3, v[96:99], s[40:41] offset:2048 sc1\n"
"v_mfma_f32_16x16x32_bf16 a[124:127], a[196:199], a[236:239], a[124:127]\n"
"v_accvgpr_read_b32 v100, a56\n"
"v_accvgpr_read_b32 v101, a57\n"
"v_accvgpr_read_b32 v102, a58\n"
"v_mfma_f32_16x16x32_bf16 a[168:171], a[200:203], a[236:239], a[168:171]\n"
"v_accvgpr_read_b32 v103, a59\n"
"v_accvgpr_read_b32 v104, a60\n"
"v_mfma_f32_16x16x32_bf16 a[172:175], a[204:207], a[236:239], a[172:175]\n"
"ds_read_b128 a[236:239], v17 offset:49152\n"
"v_accvgpr_read_b32 v105, a61\n"
"v_accvgpr_read_b32 v106, a62\n"
"v_mfma_f32_16x16x32_bf16 a[128:131], a[192:195], a[240:243], a[128:131]\n"
"v_accvgpr_read_b32 v107, a63\n"
"v_cvt_pk_bf16_f32 v108, v100, v101\n"
"v_mfma_f32_16x16x32_bf16 a[132:135], a[196:199], a[240:243], a[132:135]\n"
"v_cvt_pk_bf16_f32 v109, v102, v103\n"
"v_cvt_pk_bf16_f32 v110, v104, v105\n"
"v_cvt_pk_bf16_f32 v111, v106, v107\n"
"v_mfma_f32_16x16x32_bf16 a[176:179], a[200:203], a[240:243], a[176:179]\n"
"global_store_dwordx4 v3, v[108:111], s[40:41] offset:3072 sc1\n"
"v_accvgpr_read_b32 v64, a64\n"
"v_mfma_f32_16x16x32_bf16 a[180:183], a[204:207], a[240:243], a[180:183]\n"
"ds_read_b128 a[240:243], v21 offset:0\n"
"v_accvgpr_read_b32 v65, a65\n"
"v_mfma_f32_16x16x32_bf16 a[136:139], a[192:195], a[244:247], a[136:139]\n"
"v_accvgpr_read_b32 v66, a66\n"
"v_accvgpr_read_b32 v67, a67\n"
"v_accvgpr_read_b32 v68, a68\n"
"v_mfma_f32_16x16x32_bf16 a[140:143], a[196:199], a[244:247], a[140:143]\n"
"v_accvgpr_read_b32 v69, a69\n"
"v_accvgpr_read_b32 v70, a70\n"
"v_mfma_f32_16x16x32_bf16 a[184:187], a[200:203], a[244:247], a[184:187]\n"
"v_accvgpr_read_b32 v71, a71\n"
"v_cvt_pk_bf16_f32 v72, v64, v65\n"
"v_cvt_pk_bf16_f32 v73, v66, v67\n"
"v_mfma_f32_16x16x32_bf16 a[188:191], a[204:207], a[244:247], a[188:191]\n"
"ds_read_b128 a[244:247], v21 offset:16384\n"
"v_cvt_pk_bf16_f32 v74, v68, v69\n"
"s_waitcnt lgkmcnt(3)\n"
"v_mfma_f32_16x16x32_bf16 a[96:99], a[208:211], a[224:227], a[96:99]\n"
"v_cvt_pk_bf16_f32 v75, v70, v71\n"
"global_store_dwordx4 v4, v[72:75], s[40:41] offset:0 sc1\n"
"v_mfma_f32_16x16x32_bf16 a[100:103], a[212:215], a[224:227], a[100:103]\n"
"v_accvgpr_read_b32 v76, a72\n"
"v_mfma_f32_16x16x32_bf16 a[144:147], a[216:219], a[224:227], a[144:147]\n"
"v_accvgpr_read_b32 v77, a73\n"
"v_accvgpr_read_b32 v78, a74\n"
"v_mfma_f32_16x16x32_bf16 a[148:151], a[220:223], a[224:227], a[148:151]\n"
"v_accvgpr_read_b32 v79, a75\n"
"v_accvgpr_read_b32 v80, a76\n"
"v_mfma_f32_16x16x32_bf16 a[104:107], a[208:211], a[228:231], a[104:107]\n"
"v_accvgpr_read_b32 v81, a77\n"
"v_accvgpr_read_b32 v82, a78\n"
"v_mfma_f32_16x16x32_bf16 a[108:111], a[212:215], a[228:231], a[108:111]\n"
"v_accvgpr_read_b32 v83, a79\n"
"v_mfma_f32_16x16x32_bf16 a[152:155], a[216:219], a[228:231], a[152:155]\n"
"v_cvt_pk_bf16_f32 v84, v76, v77\n"
"v_cvt_pk_bf16_f32 v85, v78, v79\n"
"v_mfma_f32_16x16x32_bf16 a[156:159], a[220:223], a[228:231], a[156:159]\n"
"v_cvt_pk_bf16_f32 v86, v80, v81\n"
"v_cvt_pk_bf16_f32 v87, v82, v83\n"
"v_mfma_f32_16x16x32_bf16 a[112:115], a[208:211], a[232:235], a[112:115]\n"
"global_store_dwordx4 v4, v[84:87], s[40:41] offset:1024 sc1\n"
"v_mfma_f32_16x16x32_bf16 a[116:119], a[212:215], a[232:235], a[116:119]\n"
"v_accvgpr_read_b32 v88, a80\n"
"v_accvgpr_read_b32 v89, a81\n"
"v_mfma_f32_16x16x32_bf16 a[160:163], a[216:219], a[232:235], a[160:163]\n"
"v_accvgpr_read_b32 v90, a82\n"
"v_accvgpr_read_b32 v91, a83\n"
"v_mfma_f32_16x16x32_bf16 a[164:167], a[220:223], a[232:235], a[164:167]\n"
"v_accvgpr_read_b32 v92, a84\n"
"v_accvgpr_read_b32 v93, a85\n"
"s_waitcnt lgkmcnt(0)\n"
"v_mfma_f32_16x16x32_bf16 a[120:123], a[208:211], a[236:239], a[120:123]\n"
"v_accvgpr_read_b32 v94, a86\n"
"v_mfma_f32_16x16x32_bf16 a[124:127], a[212:215], a[236:239], a[124:127]\n"
"v_accvgpr_read_b32 v95, a87\n"
"v_cvt_pk_bf16_f32 v96, v88, v89\n"
"v_mfma_f32_16x16x32_bf16 a[168:171], a[216:219], a[236:239], a[168:171]\n"
"v_cvt_pk_bf16_f32 v97, v90, v91\n"
"v_cvt_pk_bf16_f32 v98, v92, v93\n"
"v_mfma_f32_16x16x32_bf16 a[172:175], a[220:223], a[236:239], a[172:175]\n"
"v_cvt_pk_bf16_f32 v99, v94, v95\n"
"v_mfma_f32_16x16x32_bf16 a[128:131], a[208:211], a[240:243], a[128:131]\n"
"global_store_dwordx4 v4, v[96:99], s[40:41] offset:2048 sc1\n"
"v_accvgpr_read_b32 v100, a88\n"
"v_mfma_f32_16x16x32_bf16 a[132:135], a[212:215], a[240:243], a[132:135]\n"
"v_accvgpr_read_b32 v101, a89\n"
"v_accvgpr_read_b32 v102, a90\n"
"v_mfma_f32_16x16x32_bf16 a[176:179], a[216:219], a[240:243], a[176:179]\n"
"v_accvgpr_read_b32 v103, a91\n"
"v_mfma_f32_16x16x32_bf16 a[180:183], a[220:223], a[240:243], a[180:183]\n"
"v_accvgpr_read_b32 v104, a92\n"
"v_accvgpr_read_b32 v105, a93\n"
"v_mfma_f32_16x16x32_bf16 a[136:139], a[208:211], a[244:247], a[136:139]\n"
"v_accvgpr_read_b32 v106, a94\n"
"v_accvgpr_read_b32 v107, a95\n"
"v_mfma_f32_16x16x32_bf16 a[140:143], a[212:215], a[244:247], a[140:143]\n"
"v_cvt_pk_bf16_f32 v108, v100, v101\n"
"v_cvt_pk_bf16_f32 v109, v102, v103\n"
"v_mfma_f32_16x16x32_bf16 a[184:187], a[216:219], a[244:247], a[184:187]\n"
"v_cvt_pk_bf16_f32 v110, v104, v105\n"
"v_mfma_f32_16x16x32_bf16 a[188:191], a[220:223], a[244:247], a[188:191]\n"
"v_cvt_pk_bf16_f32 v111, v106, v107\n"
"global_store_dwordx4 v4, v[108:111], s[40:41] offset:3072 sc1\n"
"v_accvgpr_read_b32 v64, a96\n"
"v_accvgpr_read_b32 v65, a97\n"
"v_accvgpr_read_b32 v66, a98\n"
"v_accvgpr_read_b32 v67, a99\n"
"v_accvgpr_read_b32 v68, a100\n"
"v_accvgpr_read_b32 v69, a101\n"
"v_accvgpr_read_b32 v70, a102\n"
"v_accvgpr_read_b32 v71, a103\n"
"v_cvt_pk_bf16_f32 v72, v64, v65\n"
"v_cvt_pk_bf16_f32 v73, v66, v67\n"
"v_cvt_pk_bf16_f32 v74, v68, v69\n"
"v_cvt_pk_bf16_f32 v75, v70, v71\n"
"global_store_dwordx4 v5, v[72:75], s[40:41] offset:0 sc1\n"
"v_accvgpr_read_b32 v76, a104\n"
"v_accvgpr_read_b32 v77, a105\n"
"v_accvgpr_read_b32 v78, a106\n"
"v_accvgpr_read_b32 v79, a107\n"
"v_accvgpr_read_b32 v80, a108\n"
"v_accvgpr_read_b32 v81, a109\n"
"v_accvgpr_read_b32 v82, a110\n"
"v_accvgpr_read_b32 v83, a111\n"
"v_cvt_pk_bf16_f32 v84, v76, v77\n"
"v_cvt_pk_bf16_f32 v85, v78, v79\n"
"v_cvt_pk_bf16_f32 v86, v80, v81\n"
"v_cvt_pk_bf16_f32 v87, v82, v83\n"
"global_store_dwordx4 v5, v[84:87], s[40:41] offset:1024 sc1\n"
"v_accvgpr_read_b32 v88, a112\n"
"v_accvgpr_read_b32 v89, a113\n"
"v_accvgpr_read_b32 v90, a114\n"
"v_accvgpr_read_b32 v91, a115\n"
"v_accvgpr_read_b32 v92, a116\n"
"v_accvgpr_read_b32 v93, a117\n"
"v_accvgpr_read_b32 v94, a118\n"
"v_accvgpr_read_b32 v95, a119\n"
"v_cvt_pk_bf16_f32 v96, v88, v89\n"
"v_cvt_pk_bf16_f32 v97, v90, v91\n"
"v_cvt_pk_bf16_f32 v98, v92, v93\n"
"v_cvt_pk_bf16_f32 v99, v94, v95\n"
"global_store_dwordx4 v5, v[96:99], s[40:41] offset:2048 sc1\n"
"v_accvgpr_read_b32 v100, a120\n"
"v_accvgpr_read_b32 v101, a121\n"
"v_accvgpr_read_b32 v102, a122\n"
"v_accvgpr_read_b32 v103, a123\n"
"v_accvgpr_read_b32 v104, a124\n"
"v_accvgpr_read_b32 v105, a125\n"
"v_accvgpr_read_b32 v106, a126\n"
"v_accvgpr_read_b32 v107, a127\n"
"v_cvt_pk_bf16_f32 v108, v100, v101\n"
"v_cvt_pk_bf16_f32 v109, v102, v103\n"
"v_cvt_pk_bf16_f32 v110, v104, v105\n"
"v_cvt_pk_bf16_f32 v111, v106, v107\n"
"global_store_dwordx4 v5, v[108:111], s[40:41] offset:3072 sc1\n"
"v_accvgpr_read_b32 v64, a128\n"
"v_accvgpr_read_b32 v65, a129\n"
"v_accvgpr_read_b32 v66, a130\n"
"v_accvgpr_read_b32 v67, a131\n"
"v_accvgpr_read_b32 v68, a132\n"
"v_accvgpr_read_b32 v69, a133\n"
"v_accvgpr_read_b32 v70, a134\n"
"v_accvgpr_read_b32 v71, a135\n"
"v_cvt_pk_bf16_f32 v72, v64, v65\n"
"v_cvt_pk_bf16_f32 v73, v66, v67\n"
"v_cvt_pk_bf16_f32 v74, v68, v69\n"
"v_cvt_pk_bf16_f32 v75, v70, v71\n"
"global_store_dwordx4 v6, v[72:75], s[40:41] offset:0 sc1\n"
"v_accvgpr_read_b32 v76, a136\n"
"v_accvgpr_read_b32 v77, a137\n"
"v_accvgpr_read_b32 v78, a138\n"
"v_accvgpr_read_b32 v79, a139\n"
"v_accvgpr_read_b32 v80, a140\n"
"v_accvgpr_read_b32 v81, a141\n"
"v_accvgpr_read_b32 v82, a142\n"
"v_accvgpr_read_b32 v83, a143\n"
"v_cvt_pk_bf16_f32 v84, v76, v77\n"
"v_cvt_pk_bf16_f32 v85, v78, v79\n"
"v_cvt_pk_bf16_f32 v86, v80, v81\n"
"v_cvt_pk_bf16_f32 v87, v82, v83\n"
"global_store_dwordx4 v6, v[84:87], s[40:41] offset:1024 sc1\n"
"v_accvgpr_read_b32 v88, a144\n"
"v_accvgpr_read_b32 v89, a145\n"
"v_accvgpr_read_b32 v90, a146\n"
"v_accvgpr_read_b32 v91, a147\n"
"v_accvgpr_read_b32 v92, a148\n"
"v_accvgpr_read_b32 v93, a149\n"
"v_accvgpr_read_b32 v94, a150\n"
"v_accvgpr_read_b32 v95, a151\n"
"v_cvt_pk_bf16_f32 v96, v88, v89\n"
"v_cvt_pk_bf16_f32 v97, v90, v91\n"
"v_cvt_pk_bf16_f32 v98, v92, v93\n"
"v_cvt_pk_bf16_f32 v99, v94, v95\n"
"global_store_dwordx4 v6, v[96:99], s[40:41] offset:2048 sc1\n"
"v_accvgpr_read_b32 v100, a152\n"
"v_accvgpr_read_b32 v101, a153\n"
"v_accvgpr_read_b32 v102, a154\n"
"v_accvgpr_read_b32 v103, a155\n"
"v_accvgpr_read_b32 v104, a156\n"
"v_accvgpr_read_b32 v105, a157\n"
"v_accvgpr_read_b32 v106, a158\n"
"v_accvgpr_read_b32 v107, a159\n"
"v_cvt_pk_bf16_f32 v108, v100, v101\n"
"v_cvt_pk_bf16_f32 v109, v102, v103\n"
"v_cvt_pk_bf16_f32 v110, v104, v105\n"
"v_cvt_pk_bf16_f32 v111, v106, v107\n"
"global_store_dwordx4 v6, v[108:111], s[40:41] offset:3072 sc1\n"
"v_accvgpr_read_b32 v64, a160\n"
"v_accvgpr_read_b32 v65, a161\n"
"v_accvgpr_read_b32 v66, a162\n"
"v_accvgpr_read_b32 v67, a163\n"
"v_accvgpr_read_b32 v68, a164\n"
"v_accvgpr_read_b32 v69, a165\n"
"v_accvgpr_read_b32 v70, a166\n"
"v_accvgpr_read_b32 v71, a167\n"
"v_cvt_pk_bf16_f32 v72, v64, v65\n"
"v_cvt_pk_bf16_f32 v73, v66, v67\n"
"v_cvt_pk_bf16_f32 v74, v68, v69\n"
"v_cvt_pk_bf16_f32 v75, v70, v71\n"
"global_store_dwordx4 v7, v[72:75], s[40:41] offset:0 sc1\n"
"v_accvgpr_read_b32 v76, a168\n"
"v_accvgpr_read_b32 v77, a169\n"
"v_accvgpr_read_b32 v78, a170\n"
"v_accvgpr_read_b32 v79, a171\n"
"v_accvgpr_read_b32 v80, a172\n"
"v_accvgpr_read_b32 v81, a173\n"
"v_accvgpr_read_b32 v82, a174\n"
"v_accvgpr_read_b32 v83, a175\n"
"v_cvt_pk_bf16_f32 v84, v76, v77\n"
"v_cvt_pk_bf16_f32 v85, v78, v79\n"
"v_cvt_pk_bf16_f32 v86, v80, v81\n"
"v_cvt_pk_bf16_f32 v87, v82, v83\n"
"global_store_dwordx4 v7, v[84:87], s[40:41] offset:1024 sc1\n"
"v_accvgpr_read_b32 v88, a176\n"
"v_accvgpr_read_b32 v89, a177\n"
"v_accvgpr_read_b32 v90, a178\n"
"v_accvgpr_read_b32 v91, a179\n"
"v_accvgpr_read_b32 v92, a180\n"
"v_accvgpr_read_b32 v93, a181\n"
"v_accvgpr_read_b32 v94, a182\n"
"v_accvgpr_read_b32 v95, a183\n"
"v_cvt_pk_bf16_f32 v96, v88, v89\n"
"v_cvt_pk_bf16_f32 v97, v90, v91\n"
"v_cvt_pk_bf16_f32 v98, v92, v93\n"
"v_cvt_pk_bf16_f32 v99, v94, v95\n"
"global_store_dwordx4 v7, v[96:99], s[40:41] offset:2048 sc1\n"
"v_accvgpr_read_b32 v100, a184\n"
"v_accvgpr_read_b32 v101, a185\n"
"v_accvgpr_read_b32 v102, a186\n"
"v_accvgpr_read_b32 v103, a187\n"
"v_accvgpr_read_b32 v104, a188\n"
"v_accvgpr_read_b32 v105, a189\n"
"v_accvgpr_read_b32 v106, a190\n"
"v_accvgpr_read_b32 v107, a191\n"
"v_cvt_pk_bf16_f32 v108, v100, v101\n"
"v_cvt_pk_bf16_f32 v109, v102, v103\n"
"v_cvt_pk_bf16_f32 v110, v104, v105\n"
"v_cvt_pk_bf16_f32 v111, v106, v107\n"
"global_store_dwordx4 v7, v[108:111], s[40:41] offset:3072 sc1\n"
"s_branch LFE_%=\n"
"LFE4_%=:\n"
"s_nop 7\n"
"s_nop 7\n"
"s_cmp_lg_u32 s42, 0\n"
"s_cbranch_scc1 LHRG_%=\n"
"s_mul_i32 s53, s44, 11\n"
"s_add_u32 s53, s53, s43\n"
"s_add_u32 s53, s53, 8\n"
"s_mul_i32 s53, s53, 768\n"
"s_add_u32 s53, s53, 33554432\n"
"s_add_u32 s86, s80, s53\n"
"s_addc_u32 s87, s81, 0\n"
"v_lshlrev_b32 v60, 3, v1\n"
"s_mov_b32 exec_hi, 0\n"
"global_load_dwordx2 v[58:59], v60, s[86:87] sc1\n"
"s_mov_b64 exec, -1\n"
"LHRG_%=:\n"
"s_waitcnt vmcnt(0)\n"
"s_mul_i32 s53, s43, 22\n"
"s_add_u32 s53, s53, s44\n"
"s_add_u32 s53, s53, 176\n"
"s_lshl_b32 s53, s53, 2\n"
"s_add_u32 s53, s53, s42\n"
"s_mul_i32 s53, s53, 24576\n"
"s_add_u32 s40, s80, s53\n"
"s_addc_u32 s41, s81, 0\n"
"v_lshlrev_b32 v2, 4, v1\n"
"v_add_u32 v3, 4096, v2\n"
"v_add_u32 v4, 8192, v2\n"
"v_add_u32 v5, 12288, v2\n"
"v_add_u32 v6, 16384, v2\n"
"v_add_u32 v7, 20480, v2\n"
"v_accvgpr_read_b32 v64, a0\n"
"v_accvgpr_read_b32 v65, a1\n"
"v_accvgpr_read_b32 v66, a2\n"
"v_accvgpr_read_b32 v67, a3\n"
"v_accvgpr_read_b32 v68, a4\n"
"v_accvgpr_read_b32 v69, a5\n"
"v_accvgpr_read_b32 v70, a6\n"
"v_accvgpr_read_b32 v71, a7\n"
"v_cvt_pk_bf16_f32 v72, v64, v65\n"
"v_cvt_pk_bf16_f32 v73, v66, v67\n"
"v_cvt_pk_bf16_f32 v74, v68, v69\n"
"v_cvt_pk_bf16_f32 v75, v70, v71\n"
"global_store_dwordx4 v2, v[72:75], s[40:41] offset:0 sc1\n"
"v_accvgpr_read_b32 v76, a8\n"
"v_accvgpr_read_b32 v77, a9\n"
"v_accvgpr_read_b32 v78, a10\n"
"v_accvgpr_read_b32 v79, a11\n"
"v_accvgpr_read_b32 v80, a12\n"
"v_accvgpr_read_b32 v81, a13\n"
"v_accvgpr_read_b32 v82, a14\n"
"v_accvgpr_read_b32 v83, a15\n"
"v_cvt_pk_bf16_f32 v84, v76, v77\n"
"v_cvt_pk_bf16_f32 v85, v78, v79\n"
"v_cvt_pk_bf16_f32 v86, v80, v81\n"
"v_cvt_pk_bf16_f32 v87, v82, v83\n"
"global_store_dwordx4 v2, v[84:87], s[40:41] offset:1024 sc1\n"
"v_accvgpr_read_b32 v88, a16\n"
"v_accvgpr_read_b32 v89, a17\n"
"v_accvgpr_read_b32 v90, a18\n"
"v_accvgpr_read_b32 v91, a19\n"
"v_accvgpr_read_b32 v92, a20\n"
"v_accvgpr_read_b32 v93, a21\n"
"v_accvgpr_read_b32 v94, a22\n"
"v_accvgpr_read_b32 v95, a23\n"
"v_cvt_pk_bf16_f32 v96, v88, v89\n"
"v_cvt_pk_bf16_f32 v97, v90, v91\n"
"v_cvt_pk_bf16_f32 v98, v92, v93\n"
"v_cvt_pk_bf16_f32 v99, v94, v95\n"
"global_store_dwordx4 v2, v[96:99], s[40:41] offset:2048 sc1\n"
"v_accvgpr_read_b32 v100, a24\n"
"v_accvgpr_read_b32 v101, a25\n"
"v_accvgpr_read_b32 v102, a26\n"
"v_accvgpr_read_b32 v103, a27\n"
"v_accvgpr_read_b32 v104, a28\n"
"v_accvgpr_read_b32 v105, a29\n"
"v_accvgpr_read_b32 v106, a30\n"
"v_accvgpr_read_b32 v107, a31\n"
"v_cvt_pk_bf16_f32 v108, v100, v101\n"
"v_cvt_pk_bf16_f32 v109, v102, v103\n"
"v_cvt_pk_bf16_f32 v110, v104, v105\n"
"v_cvt_pk_bf16_f32 v111, v106, v107\n"
"global_store_dwordx4 v2, v[108:111], s[40:41] offset:3072 sc1\n"
"v_accvgpr_read_b32 v64, a32\n"
"v_accvgpr_read_b32 v65, a33\n"
"v_accvgpr_read_b32 v66, a34\n"
"v_accvgpr_read_b32 v67, a35\n"
"v_accvgpr_read_b32 v68, a36\n"
"v_accvgpr_read_b32 v69, a37\n"
"v_accvgpr_read_b32 v70, a38\n"
"v_accvgpr_read_b32 v71, a39\n"
"v_cvt_pk_bf16_f32 v72, v64, v65\n"
"v_cvt_pk_bf16_f32 v73, v66, v67\n"
"v_cvt_pk_bf16_f32 v74, v68, v69\n"
"v_cvt_pk_bf16_f32 v75, v70, v71\n"
"global_store_dwordx4 v3, v[72:75], s[40:41] offset:0 sc1\n"
"v_accvgpr_read_b32 v76, a40\n"
"v_accvgpr_read_b32 v77, a41\n"
"v_accvgpr_read_b32 v78, a42\n"
"v_accvgpr_read_b32 v79, a43\n"
"v_accvgpr_read_b32 v80, a44\n"
"v_accvgpr_read_b32 v81, a45\n"
"v_accvgpr_read_b32 v82, a46\n"
"v_accvgpr_read_b32 v83, a47\n"
"v_cvt_pk_bf16_f32 v84, v76, v77\n"
"v_cvt_pk_bf16_f32 v85, v78, v79\n"
"v_cvt_pk_bf16_f32 v86, v80, v81\n"
"v_cvt_pk_bf16_f32 v87, v82, v83\n"
"global_store_dwordx4 v3, v[84:87], s[40:41] offset:1024 sc1\n"
"v_accvgpr_read_b32 v88, a48\n"
"v_accvgpr_read_b32 v89, a49\n"
"v_accvgpr_read_b32 v90, a50\n"
"v_accvgpr_read_b32 v91, a51\n"
"v_accvgpr_read_b32 v92, a52\n"
"v_accvgpr_read_b32 v93, a53\n"
"v_accvgpr_read_b32 v94, a54\n"
"v_accvgpr_read_b32 v95, a55\n"
"v_cvt_pk_bf16_f32 v96, v88, v89\n"
"v_cvt_pk_bf16_f32 v97, v90, v91\n"
"v_cvt_pk_bf16_f32 v98, v92, v93\n"
"v_cvt_pk_bf16_f32 v99, v94, v95\n"
"global_store_dwordx4 v3, v[96:99], s[40:41] offset:2048 sc1\n"
"v_accvgpr_read_b32 v100, a56\n"
"v_accvgpr_read_b32 v101, a57\n"
"v_accvgpr_read_b32 v102, a58\n"
"v_accvgpr_read_b32 v103, a59\n"
"v_accvgpr_read_b32 v104, a60\n"
"v_accvgpr_read_b32 v105, a61\n"
"v_accvgpr_read_b32 v106, a62\n"
"v_accvgpr_read_b32 v107, a63\n"
"v_cvt_pk_bf16_f32 v108, v100, v101\n"
"v_cvt_pk_bf16_f32 v109, v102, v103\n"
"v_cvt_pk_bf16_f32 v110, v104, v105\n"
"v_cvt_pk_bf16_f32 v111, v106, v107\n"
"global_store_dwordx4 v3, v[108:111], s[40:41] offset:3072 sc1\n"
"v_accvgpr_read_b32 v64, a64\n"
"v_accvgpr_read_b32 v65, a65\n"
"v_accvgpr_read_b32 v66, a66\n"
"v_accvgpr_read_b32 v67, a67\n"
"v_accvgpr_read_b32 v68, a68\n"
"v_accvgpr_read_b32 v69, a69\n"
"v_accvgpr_read_b32 v70, a70\n"
"v_accvgpr_read_b32 v71, a71\n"
"v_cvt_pk_bf16_f32 v72, v64, v65\n"
"v_cvt_pk_bf16_f32 v73, v66, v67\n"
"v_cvt_pk_bf16_f32 v74, v68, v69\n"
"v_cvt_pk_bf16_f32 v75, v70, v71\n"
"global_store_dwordx4 v4, v[72:75], s[40:41] offset:0 sc1\n"
"v_accvgpr_read_b32 v76, a72\n"
"v_accvgpr_read_b32 v77, a73\n"
"v_accvgpr_read_b32 v78, a74\n"
"v_accvgpr_read_b32 v79, a75\n"
"v_accvgpr_read_b32 v80, a76\n"
"v_accvgpr_read_b32 v81, a77\n"
"v_accvgpr_read_b32 v82, a78\n"
"v_accvgpr_read_b32 v83, a79\n"
"v_cvt_pk_bf16_f32 v84, v76, v77\n"
"v_cvt_pk_bf16_f32 v85, v78, v79\n"
"v_cvt_pk_bf16_f32 v86, v80, v81\n"
"v_cvt_pk_bf16_f32 v87, v82, v83\n"
"global_store_dwordx4 v4, v[84:87], s[40:41] offset:1024 sc1\n"
"v_accvgpr_read_b32 v88, a80\n"
"v_accvgpr_read_b32 v89, a81\n"
"v_accvgpr_read_b32 v90, a82\n"
"v_accvgpr_read_b32 v91, a83\n"
"v_accvgpr_read_b32 v92, a84\n"
"v_accvgpr_read_b32 v93, a85\n"
"v_accvgpr_read_b32 v94, a86\n"
"v_accvgpr_read_b32 v95, a87\n"
"v_cvt_pk_bf16_f32 v96, v88, v89\n"
"v_cvt_pk_bf16_f32 v97, v90, v91\n"
"v_cvt_pk_bf16_f32 v98, v92, v93\n"
"v_cvt_pk_bf16_f32 v99, v94, v95\n"
"global_store_dwordx4 v4, v[96:99], s[40:41] offset:2048 sc1\n"
"v_accvgpr_read_b32 v100, a88\n"
"v_accvgpr_read_b32 v101, a89\n"
"v_accvgpr_read_b32 v102, a90\n"
"v_accvgpr_read_b32 v103, a91\n"
"v_accvgpr_read_b32 v104, a92\n"
"v_accvgpr_read_b32 v105, a93\n"
"v_accvgpr_read_b32 v106, a94\n"
"v_accvgpr_read_b32 v107, a95\n"
"v_cvt_pk_bf16_f32 v108, v100, v101\n"
"v_cvt_pk_bf16_f32 v109, v102, v103\n"
"v_cvt_pk_bf16_f32 v110, v104, v105\n"
"v_cvt_pk_bf16_f32 v111, v106, v107\n"
"global_store_dwordx4 v4, v[108:111], s[40:41] offset:3072 sc1\n"
"v_accvgpr_read_b32 v64, a96\n"
"v_accvgpr_read_b32 v65, a97\n"
"v_accvgpr_read_b32 v66, a98\n"
"v_accvgpr_read_b32 v67, a99\n"
"v_accvgpr_read_b32 v68, a100\n"
"v_accvgpr_read_b32 v69, a101\n"
"v_accvgpr_read_b32 v70, a102\n"
"v_accvgpr_read_b32 v71, a103\n"
"v_cvt_pk_bf16_f32 v72, v64, v65\n"
"v_cvt_pk_bf16_f32 v73, v66, v67\n"
"v_cvt_pk_bf16_f32 v74, v68, v69\n"
"v_cvt_pk_bf16_f32 v75, v70, v71\n"
"global_store_dwordx4 v5, v[72:75], s[40:41] offset:0 sc1\n"
"v_accvgpr_read_b32 v76, a104\n"
"v_accvgpr_read_b32 v77, a105\n"
"v_accvgpr_read_b32 v78, a106\n"
"v_accvgpr_read_b32 v79, a107\n"
"v_accvgpr_read_b32 v80, a108\n"
"v_accvgpr_read_b32 v81, a109\n"
"v_accvgpr_read_b32 v82, a110\n"
"v_accvgpr_read_b32 v83, a111\n"
"v_cvt_pk_bf16_f32 v84, v76, v77\n"
"v_cvt_pk_bf16_f32 v85, v78, v79\n"
"v_cvt_pk_bf16_f32 v86, v80, v81\n"
"v_cvt_pk_bf16_f32 v87, v82, v83\n"
"global_store_dwordx4 v5, v[84:87], s[40:41] offset:1024 sc1\n"
"v_accvgpr_read_b32 v88, a112\n"
"v_accvgpr_read_b32 v89, a113\n"
"v_accvgpr_read_b32 v90, a114\n"
"v_accvgpr_read_b32 v91, a115\n"
"v_accvgpr_read_b32 v92, a116\n"
"v_accvgpr_read_b32 v93, a117\n"
"v_accvgpr_read_b32 v94, a118\n"
"v_accvgpr_read_b32 v95, a119\n"
"v_cvt_pk_bf16_f32 v96, v88, v89\n"
"v_cvt_pk_bf16_f32 v97, v90, v91\n"
"v_cvt_pk_bf16_f32 v98, v92, v93\n"
"v_cvt_pk_bf16_f32 v99, v94, v95\n"
"global_store_dwordx4 v5, v[96:99], s[40:41] offset:2048 sc1\n"
"v_accvgpr_read_b32 v100, a120\n"
"v_accvgpr_read_b32 v101, a121\n"
"v_accvgpr_read_b32 v102, a122\n"
"v_accvgpr_read_b32 v103, a123\n"
"v_accvgpr_read_b32 v104, a124\n"
"v_accvgpr_read_b32 v105, a125\n"
"v_accvgpr_read_b32 v106, a126\n"
"v_accvgpr_read_b32 v107, a127\n"
"v_cvt_pk_bf16_f32 v108, v100, v101\n"
"v_cvt_pk_bf16_f32 v109, v102, v103\n"
"v_cvt_pk_bf16_f32 v110, v104, v105\n"
"v_cvt_pk_bf16_f32 v111, v106, v107\n"
"global_store_dwordx4 v5, v[108:111], s[40:41] offset:3072 sc1\n"
"v_accvgpr_read_b32 v64, a128\n"
"v_accvgpr_read_b32 v65, a129\n"
"v_accvgpr_read_b32 v66, a130\n"
"v_accvgpr_read_b32 v67, a131\n"
"v_accvgpr_read_b32 v68, a132\n"
"v_accvgpr_read_b32 v69, a133\n"
"v_accvgpr_read_b32 v70, a134\n"
"v_accvgpr_read_b32 v71, a135\n"
"v_cvt_pk_bf16_f32 v72, v64, v65\n"
"v_cvt_pk_bf16_f32 v73, v66, v67\n"
"v_cvt_pk_bf16_f32 v74, v68, v69\n"
"v_cvt_pk_bf16_f32 v75, v70, v71\n"
"global_store_dwordx4 v6, v[72:75], s[40:41] offset:0 sc1\n"
"v_accvgpr_read_b32 v76, a136\n"
"v_accvgpr_read_b32 v77, a137\n"
"v_accvgpr_read_b32 v78, a138\n"
"v_accvgpr_read_b32 v79, a139\n"
"v_accvgpr_read_b32 v80, a140\n"
"v_accvgpr_read_b32 v81, a141\n"
"v_accvgpr_read_b32 v82, a142\n"
"v_accvgpr_read_b32 v83, a143\n"
"v_cvt_pk_bf16_f32 v84, v76, v77\n"
"v_cvt_pk_bf16_f32 v85, v78, v79\n"
"v_cvt_pk_bf16_f32 v86, v80, v81\n"
"v_cvt_pk_bf16_f32 v87, v82, v83\n"
"global_store_dwordx4 v6, v[84:87], s[40:41] offset:1024 sc1\n"
"v_accvgpr_read_b32 v88, a144\n"
"v_accvgpr_read_b32 v89, a145\n"
"v_accvgpr_read_b32 v90, a146\n"
"v_accvgpr_read_b32 v91, a147\n"
"v_accvgpr_read_b32 v92, a148\n"
"v_accvgpr_read_b32 v93, a149\n"
"v_accvgpr_read_b32 v94, a150\n"
"v_accvgpr_read_b32 v95, a151\n"
"v_cvt_pk_bf16_f32 v96, v88, v89\n"
"v_cvt_pk_bf16_f32 v97, v90, v91\n"
"v_cvt_pk_bf16_f32 v98, v92, v93\n"
"v_cvt_pk_bf16_f32 v99, v94, v95\n"
"global_store_dwordx4 v6, v[96:99], s[40:41] offset:2048 sc1\n"
"v_accvgpr_read_b32 v100, a152\n"
"v_accvgpr_read_b32 v101, a153\n"
"v_accvgpr_read_b32 v102, a154\n"
"v_accvgpr_read_b32 v103, a155\n"
"v_accvgpr_read_b32 v104, a156\n"
"v_accvgpr_read_b32 v105, a157\n"
"v_accvgpr_read_b32 v106, a158\n"
"v_accvgpr_read_b32 v107, a159\n"
"v_cvt_pk_bf16_f32 v108, v100, v101\n"
"v_cvt_pk_bf16_f32 v109, v102, v103\n"
"v_cvt_pk_bf16_f32 v110, v104, v105\n"
"v_cvt_pk_bf16_f32 v111, v106, v107\n"
"global_store_dwordx4 v6, v[108:111], s[40:41] offset:3072 sc1\n"
"v_accvgpr_read_b32 v64, a160\n"
"v_accvgpr_read_b32 v65, a161\n"
"v_accvgpr_read_b32 v66, a162\n"
"v_accvgpr_read_b32 v67, a163\n"
"v_accvgpr_read_b32 v68, a164\n"
"v_accvgpr_read_b32 v69, a165\n"
"v_accvgpr_read_b32 v70, a166\n"
"v_accvgpr_read_b32 v71, a167\n"
"v_cvt_pk_bf16_f32 v72, v64, v65\n"
"v_cvt_pk_bf16_f32 v73, v66, v67\n"
"v_cvt_pk_bf16_f32 v74, v68, v69\n"
"v_cvt_pk_bf16_f32 v75, v70, v71\n"
"global_store_dwordx4 v7, v[72:75], s[40:41] offset:0 sc1\n"
"v_accvgpr_read_b32 v76, a168\n"
"v_accvgpr_read_b32 v77, a169\n"
"v_accvgpr_read_b32 v78, a170\n"
"v_accvgpr_read_b32 v79, a171\n"
"v_accvgpr_read_b32 v80, a172\n"
"v_accvgpr_read_b32 v81, a173\n"
"v_accvgpr_read_b32 v82, a174\n"
"v_accvgpr_read_b32 v83, a175\n"
"v_cvt_pk_bf16_f32 v84, v76, v77\n"
"v_cvt_pk_bf16_f32 v85, v78, v79\n"
"v_cvt_pk_bf16_f32 v86, v80, v81\n"
"v_cvt_pk_bf16_f32 v87, v82, v83\n"
"global_store_dwordx4 v7, v[84:87], s[40:41] offset:1024 sc1\n"
"v_accvgpr_read_b32 v88, a176\n"
"v_accvgpr_read_b32 v89, a177\n"
"v_accvgpr_read_b32 v90, a178\n"
"v_accvgpr_read_b32 v91, a179\n"
"v_accvgpr_read_b32 v92, a180\n"
"v_accvgpr_read_b32 v93, a181\n"
"v_accvgpr_read_b32 v94, a182\n"
"v_accvgpr_read_b32 v95, a183\n"
"v_cvt_pk_bf16_f32 v96, v88, v89\n"
"v_cvt_pk_bf16_f32 v97, v90, v91\n"
"v_cvt_pk_bf16_f32 v98, v92, v93\n"
"v_cvt_pk_bf16_f32 v99, v94, v95\n"
"global_store_dwordx4 v7, v[96:99], s[40:41] offset:2048 sc1\n"
"v_accvgpr_read_b32 v100, a184\n"
"v_accvgpr_read_b32 v101, a185\n"
"v_accvgpr_read_b32 v102, a186\n"
"v_accvgpr_read_b32 v103, a187\n"
"v_accvgpr_read_b32 v104, a188\n"
"v_accvgpr_read_b32 v105, a189\n"
"v_accvgpr_read_b32 v106, a190\n"
"v_accvgpr_read_b32 v107, a191\n"
"v_cvt_pk_bf16_f32 v108, v100, v101\n"
"v_cvt_pk_bf16_f32 v109, v102, v103\n"
"v_cvt_pk_bf16_f32 v110, v104, v105\n"
"v_cvt_pk_bf16_f32 v111, v106, v107\n"
"global_store_dwordx4 v7, v[108:111], s[40:41] offset:3072 sc1\n"
"LFE_%=:\n"
"s_nop 7\n"
"s_nop 7\n"
"; ---- end of section: r2\n"
"; ---- end of section: end\n"
"s_waitcnt vmcnt(0)\n"
"s_barrier\n"
"s_cmp_lg_u32 s42, 0\n"
"s_cbranch_scc1 LHPF_%=\n"
"s_mov_b32 exec_hi, 0\n"
"global_store_dwordx2 v60, v[58:59], s[86:87] offset:256 sc1\n"
"s_mov_b64 exec, -1\n"
"LHPF_%=:\n"
"s_cmp_lg_u32 s42, 0\n"
"s_cbranch_scc1 LEND_%=\n"
"s_mov_b32 s47, 131072\n"
"s_mov_b32 exec_hi, 0\n"
"LHFF_%=:\n"
"global_load_dwordx2 v[56:57], v60, s[86:87] offset:512 sc1\n"
"global_load_dwordx2 v[62:63], v60, s[86:87] sc1\n"
"s_waitcnt vmcnt(0)\n"
"v_cmp_ne_u64 vcc, v[56:57], v[58:59]\n"
"s_nop 4\n"
"s_cbranch_vccz LEND_%=\n"
"v_mov_b32 v58, v62\n"
"v_mov_b32 v59, v63\n"
"global_store_dwordx2 v60, v[58:59], s[86:87] offset:256 sc1\n"
"s_sleep 8\n"
"s_sub_u32 s47, s47, 1\n"
"s_cmp_lg_u32 s47, 0\n"
"s_cbranch_scc1 LHFF_%=\n"
"s_branch LEND_%=\n"
"s_branch LEND_%=\n"
"LPPROG_%=:\n"
"s_nop 7\n"
"s_nop 7\n"
"s_mov_b32 s39, 0x20000\n"
"s_sub_u32 s47, s58, 242\n"
"s_lshl_b32 s47, s47, 2\n"
"s_add_u32 s47, s47, s42\n"
"s_add_u32 s48, s47, 0\n"
"LPPLa_%=:\n"
"s_mov_b32 s53, s48\n"
"s_cmp_lt_u32 s53, 512\n"
"s_cbranch_scc1 LPQpa%=\n"
"s_cmp_lt_u32 s53, 768\n"
"s_cbranch_scc1 LPKpa%=\n"
"s_cmp_lt_u32 s53, 1248\n"
"s_cbranch_scc1 LPGpa%=\n"
"s_cmp_lt_u32 s53, 1472\n"
"s_cbranch_scc1 LPDpa%=\n"
"s_sub_u32 s53, s53, 1472\n"
"s_mov_b32 s36, s72\n"
"s_and_b32 s37, s73, 0xffff\n"
"s_mov_b32 s38, 4194304\n"
"s_lshr_b32 s54, s53, 5\n"
"s_lshl_b32 s54, s54, 15\n"
"s_bfe_u32 s55, s53, 0x30002\n"
"s_lshl_b32 s55, s55, 19\n"
"s_add_u32 s54, s54, s55\n"
"s_and_b32 s53, s53, 3\n"
"s_lshl_b32 s53, s53, 13\n"
"s_add_u32 s54, s54, s53\n"
"v_lshlrev_b32 v56, 7, v1\n"
"s_branch LPLpa%=\n"
"LPQpa%=:\n"
"s_mov_b32 s36, s70\n"
"s_and_b32 s37, s71, 0xffff\n"
"s_mov_b32 s38, 4194304\n"
"s_lshl_b32 s54, s53, 13\n"
"v_lshlrev_b32 v56, 7, v1\n"
"s_branch LPLpa%=\n"
"LPKpa%=:\n"
"s_sub_u32 s53, s53, 512\n"
"s_bitcmp0_b32 s53, 0\n"
"s_cselect_b32 s36, s66, s68\n"
"s_cselect_b32 s37, s67, s69\n"
"s_and_b32 s37, s37, 0xffff\n"
"s_mov_b32 s38, 4194304\n"
"s_lshr_b32 s54, s53, 4\n"
"s_lshl_b32 s54, s54, 13\n"
"s_bfe_u32 s55, s53, 0x30001\n"
"s_lshl_b32 s55, s55, 19\n"
"s_add_u32 s54, s54, s55\n"
"s_add_u32 s54, s54, 393216\n"
"v_lshlrev_b32 v56, 7, v1\n"
"s_branch LPLpa%=\n"
"LPGpa%=:\n"
"s_sub_u32 s53, s53, 768\n"
"s_bitcmp0_b32 s53, 4\n"
"s_cselect_b32 s36, s74, s76\n"
"s_cselect_b32 s37, s75, s77\n"
"s_and_b32 s37, s37, 0xffff\n"
"s_mov_b32 s38, 1835008\n"
"s_and_b32 s54, s53, 15\n"
"s_mul_i32 s54, s54, 114688\n"
"s_lshr_b32 s53, s53, 5\n"
"s_mul_i32 s55, s53, 22\n"
"s_lshr_b32 s55, s55, 6\n"
"s_mul_i32 s56, s55, 3\n"
"s_sub_u32 s53, s53, s56\n"
"s_mul_i32 s53, s53, 1280\n"
"s_lshl_b32 s55, s55, 8\n"
"s_add_u32 s54, s54, s53\n"
"s_add_u32 s54, s54, s55\n"
"v_lshrrev_b32 v56, 1, v1\n"
"v_and_b32 v57, 1, v1\n"
"v_mul_u32_u24 v56, 3584, v56\n"
"v_lshl_add_u32 v56, v57, 7, v56\n"
"s_branch LPLpa%=\n"
"LPDpa%=:\n"
"s_sub_u32 s53, s53, 1248\n"
"s_mov_b32 s36, s78\n"
"s_and_b32 s37, s79, 0xffff\n"
"s_mov_b32 s38, 1835008\n"
"s_lshl_b32 s54, s53, 13\n"
"v_lshlrev_b32 v56, 7, v1\n"
"LPLpa%=:\n"
"buffer_load_dword v63, v56, s[36:39], s54 offen\n"
"s_waitcnt vmcnt(16)\n"
"s_add_u32 s48, s48, 56\n"
"s_cmp_lt_u32 s48, 512\n"
"s_cbranch_scc1 LPPLa_%=\n"
"s_add_u32 s48, s47, 960\n"
"LPPLb_%=:\n"
"s_mov_b32 s53, s48\n"
"s_cmp_lt_u32 s53, 512\n"
"s_cbranch_scc1 LPQpb%=\n"
"s_cmp_lt_u32 s53, 768\n"
"s_cbranch_scc1 LPKpb%=\n"
"s_cmp_lt_u32 s53, 1248\n"
"s_cbranch_scc1 LPGpb%=\n"
"s_cmp_lt_u32 s53, 1472\n"
"s_cbranch_scc1 LPDpb%=\n"
"s_sub_u32 s53, s53, 1472\n"
"s_mov_b32 s36, s72\n"
"s_and_b32 s37, s73, 0xffff\n"
"s_mov_b32 s38, 4194304\n"
"s_lshr_b32 s54, s53, 5\n"
"s_lshl_b32 s54, s54, 15\n"
"s_bfe_u32 s55, s53, 0x30002\n"
"s_lshl_b32 s55, s55, 19\n"
"s_add_u32 s54, s54, s55\n"
"s_and_b32 s53, s53, 3\n"
"s_lshl_b32 s53, s53, 13\n"
"s_add_u32 s54, s54, s53\n"
"v_lshlrev_b32 v56, 7, v1\n"
"s_branch LPLpb%=\n"
"LPQpb%=:\n"
"s_mov_b32 s36, s70\n"
"s_and_b32 s37, s71, 0xffff\n"
"s_mov_b32 s38, 4194304\n"
"s_lshl_b32 s54, s53, 13\n"
"v_lshlrev_b32 v56, 7, v1\n"
"s_branch LPLpb%=\n"
"LPKpb%=:\n"
"s_sub_u32 s53, s53, 512\n"
"s_bitcmp0_b32 s53, 0\n"
"s_cselect_b32 s36, s66, s68\n"
"s_cselect_b32 s37, s67, s69\n"
"s_and_b32 s37, s37, 0xffff\n"
"s_mov_b32 s38, 4194304\n"
"s_lshr_b32 s54, s53, 4\n"
"s_lshl_b32 s54, s54, 13\n"
"s_bfe_u32 s55, s53, 0x30001\n"
"s_lshl_b32 s55, s55, 19\n"
"s_add_u32 s54, s54, s55\n"
"s_add_u32 s54, s54, 393216\n"
"v_lshlrev_b32 v56, 7, v1\n"
"s_branch LPLpb%=\n"
"LPGpb%=:\n"
"s_sub_u32 s53, s53, 768\n"
"s_bitcmp0_b32 s53, 4\n"
"s_cselect_b32 s36, s74, s76\n"
"s_cselect_b32 s37, s75, s77\n"
"s_and_b32 s37, s37, 0xffff\n"
"s_mov_b32 s38, 1835008\n"
"s_and_b32 s54, s53, 15\n"
"s_mul_i32 s54, s54, 114688\n"
"s_lshr_b32 s53, s53, 5\n"
"s_mul_i32 s55, s53, 22\n"
"s_lshr_b32 s55, s55, 6\n"
"s_mul_i32 s56, s55, 3\n"
"s_sub_u32 s53, s53, s56\n"
"s_mul_i32 s53, s53, 1280\n"
"s_lshl_b32 s55, s55, 8\n"
"s_add_u32 s54, s54, s53\n"
"s_add_u32 s54, s54, s55\n"
"v_lshrrev_b32 v56, 1, v1\n"
"v_and_b32 v57, 1, v1\n"
"v_mul_u32_u24 v56, 3584, v56\n"
"v_lshl_add_u32 v56, v57, 7, v56\n"
"s_branch LPLpb%=\n"
"LPDpb%=:\n"
"s_sub_u32 s53, s53, 1248\n"
"s_mov_b32 s36, s78\n"
"s_and_b32 s37, s79, 0xffff\n"
"s_mov_b32 s38, 1835008\n"
"s_lshl_b32 s54, s53, 13\n"
"v_lshlrev_b32 v56, 7, v1\n"
"LPLpb%=:\n"
"buffer_load_dword v63, v56, s[36:39], s54 offen\n"
"s_waitcnt vmcnt(16)\n"
"s_add_u32 s48, s48, 56\n"
"s_cmp_lt_u32 s48, 1984\n"
"s_cbranch_scc1 LPPLb_%=\n"
"LEND_%=:\n"
"s_nop 7\n"
"s_nop 7\n"
      : : "s"(ka), "s"(blk), "v"(tid) : "v1","v2","v3","v4","v5","v6","v7","v8","v9","v10","v11","v12","v13","v14","v15","v16","v17","v18","v19","v20","v21","v22","v23","v24","v25","v26","v27","v28","v29","v30","v31","v32","v33","v34","v35","v36","v37","v38","v39","v40","v41","v42","v43","v44","v45","v46","v47","v48","v49","v50","v51","v52","v53","v54","v55","v56","v57","v58","v59","v60","v61","v62","v63","v64","v65","v66","v67","v68","v69","v70","v71","v72","v73","v74","v75","v76","v77","v78","v79","v80","v81","v82","v83","v84","v85","v86","v87","v88","v89","v90","v91","v92","v93","v94","v95","v96","v97","v98","v99","v100","v101","v102","v103","v104","v105","v106","v107","v108","v109","v110","v111","v112","v113","v114","v115","v116","v117","v118","v119","v120","v121","v122","v123","v124","v125","v126","v127","v128","v129","v130","v131","v132","v133","v134","v135","v136","v137","v138","v139","v140","v141","v142","v143","v144","v145","v146","v147","v148","v149","v150","v151","v152","v153","v154","v155","v156","v157","v158","v159","v160","v161","v162","v163","v164","v165","v166","v167","v168","v169","v170","v171","v172","v173","v174","v175","v176","v177","v178","v179","v180","v181","v182","v183","v184","v185","v186","v187","v188","v189","v190","v191","v192","v193","v194","v195","v196","v197","v198","v199","v200","v201","v202","v203","v204","v205","v206","v207","v208","v209","v210","v211","v212","v213","v214","v215","v216","v217","v218","v219","v220","v221","v222","v223","v224","v225","v226","v227","v228","v229","v230","v231","v232","v233","v234","v235","v236","v237","v238","v239","v240","v241","v242","v243","v244","v245","v246","v247","v248","v249","v250","v251","v252","v253","v254","v255","a0","a1","a2","a3","a4","a5","a6","a7","a8","a9","a10","a11","a12","a13","a14","a15","a16","a17","a18","a19","a20","a21","a22","a23","a24","a25","a26","a27","a28","a29","a30","a31","a32","a33","a34","a35","a36","a37","a38","a39","a40","a41","a42","a43","a44","a45","a46","a47","a48","a49","a50","a51","a52","a53","a54","a55","a56","a57","a58","a59","a60","a61","a62","a63","a64","a65","a66","a67","a68","a69","a70","a71","a72","a73","a74","a75","a76","a77","a78","a79","a80","a81","a82","a83","a84","a85","a86","a87","a88","a89","a90","a91","a92","a93","a94","a95","a96","a97","a98","a99","a100","a101","a102","a103","a104","a105","a106","a107","a108","a109","a110","a111","a112","a113","a114","a115","a116","a117","a118","a119","a120","a121","a122","a123","a124","a125","a126","a127","a128","a129","a130","a131","a132","a133","a134","a135","a136","a137","a138","a139","a140","a141","a142","a143","a144","a145","a146","a147","a148","a149","a150","a151","a152","a153","a154","a155","a156","a157","a158","a159","a160","a161","a162","a163","a164","a165","a166","a167","a168","a169","a170","a171","a172","a173","a174","a175","a176","a177","a178","a179","a180","a181","a182","a183","a184","a185","a186","a187","a188","a189","a190","a191","a192","a193","a194","a195","a196","a197","a198","a199","a200","a201","a202","a203","a204","a205","a206","a207","a208","a209","a210","a211","a212","a213","a214","a215","a216","a217","a218","a219","a220","a221","a222","a223","a224","a225","a226","a227","a228","a229","a230","a231","a232","a233","a234","a235","a236","a237","a238","a239","a240","a241","a242","a243","a244","a245","a246","a247","a248","a249","a250","a251","a252","a253","a254","a255","s20","s21","s22","s23","s24","s25","s26","s27","s28","s29","s30","s31","s33","s34","s35","s36","s37","s38","s39","s40","s41","s42","s43","s44","s45","s46","s47","s48","s49","s50","s51","s52","s53","s54","s55","s56","s57","s58","s59","s60","s61","s62","s63","s64","s65","s66","s67","s68","s69","s70","s71","s72","s73","s74","s75","s76","s77","s78","s79","s80","s81","s82","s83","s84","s85","s86","s87","s88","s89","s90","s91","s92","s93","s94","s95","s96","s97","s98","s99","memory","scc","vcc");
}

static void launch_all(void* const* d_in, void* d_out, void* d_ws, hipStream_t stream){
  (void)hipFuncSetAttribute((const void*)fused_main, hipFuncAttributeMaxDynamicSharedMemorySize, 163840);
  fused_main<<<GRID,256,163840,stream>>>(d_in[0],d_in[1],d_in[2],d_in[3],d_in[4],d_in[6],d_in[7],d_in[8],d_ws,d_out);
}
extern "C" void kernel_launch(void* const* d_in, const int* in_sizes, int n_in, void* d_out, int out_size, void* d_ws, size_t ws_size, hipStream_t stream){
  launch_all(d_in,d_out,d_ws,stream);
}
```
